```python
import torch

def _softmax(x, axis=-1):
    return torch.softmax(x, dim=axis)
def _gelu(x):
    return torch.nn.functional.gelu(x, approximate="tanh")
def _silu(x):
    return torch.nn.functional.silu(x)
def _sigmoid(x):
    return torch.sigmoid(x)
def _layernorm(x, axis=-1, eps=1e-5):
    m = x.mean(dim=axis, keepdim=True)
    v = x.var(dim=axis, keepdim=True, unbiased=False)
    return (x - m) / torch.sqrt(v + eps)
def _scatter_add(tgt, idx, src, axis=0):
    return tgt.clone().index_add_(axis, idx, src)
def _segment_sum(src, seg, n):
    out = torch.zeros((n,) + src.shape[1:], dtype=src.dtype)
    return out.index_add_(0, seg, src)
def _pad(x, pw):
    flat = tuple(v for pair in reversed(pw) for v in pair)
    return torch.nn.functional.pad(x, flat)

kernel_name = 'recurrent_scan__mha_block_cached__mixer_s_mlp__prompt'

def setup_inputs():

    _w = (lambda a: (lambda t: t.to(torch.bfloat16) if t.is_floating_point() else t)(torch.as_tensor(a)))
    def _A_setup():
        B, Sq, D = 1, 2048, 640
        H, Sk, Dh = 32, 2048, 192
        return {
            "x": _w((lambda *s: torch.randn(*s, dtype=torch.bfloat16) * 0.05)(B, Sq, D)),
            "k_cache": _w((lambda *s: torch.randn(*s, dtype=torch.bfloat16) * 0.8)(B, H, Sk, Dh)),
            "v_cache": _w((lambda *s: torch.randn(*s, dtype=torch.bfloat16) * 0.8)(B, H, Sk, Dh)),
            "wq": _w((lambda *s: torch.randn(*s, dtype=torch.bfloat16) * 0.8)(D, H * Dh) * (D ** -0.5)),
            "wo": _w((lambda *s: torch.randn(*s, dtype=torch.bfloat16) * 0.8)(H * Dh, D) * ((H * Dh) ** -0.5)),
        }
    _A_in = _A_setup()

    def _B_setup():
        B, S, D, F = 1, 2048, 640, 1536
        return {
            "x":  _w((lambda *s: torch.randn(*s, dtype=torch.bfloat16) * 0.05)(B, S, D)),
            "w1": _w((lambda *s: torch.randn(*s, dtype=torch.bfloat16) * 3.2)(D, D) * (D ** -0.5)),
            "w2": _w((lambda *s: torch.randn(*s, dtype=torch.bfloat16) * 3.2)(D, D) * (D ** -0.5)),
            "w3": _w((lambda *s: torch.randn(*s, dtype=torch.bfloat16) * 3.2)(D, F) * (D ** -0.5)),
            "w4": _w((lambda *s: torch.randn(*s, dtype=torch.bfloat16) * 3.2)(F, D) * (F ** -0.5)),
        }
    _B_in = _B_setup()

    return {
        "A_x": _A_in["x"],
        "A_k_cache": _A_in["k_cache"],
        "A_v_cache": _A_in["v_cache"],
        "A_wq": _A_in["wq"],
        "A_wo": _A_in["wo"],
        "B_x": _B_in["x"],
        "B_w1": _B_in["w1"],
        "B_w2": _B_in["w2"],
        "B_w3": _B_in["w3"],
        "B_w4": _B_in["w4"],
    }

def reference(A_x, A_k_cache, A_v_cache, A_wq, A_wo, B_x, B_w1, B_w2, B_w3, B_w4):
    def _A_ref(x, k_cache, v_cache, wq, wo):
        B, Sq, D = x.shape
        H, Dh = 32, 192
        q = (lambda x, *d: x.permute(*d))((x @ wq).reshape(B, Sq, H, Dh), 0, 2, 1, 3)
        scores = torch.einsum("bhqd,bhkd->bhqk", q, k_cache) * (Dh ** -0.5)
        p = _softmax(scores, axis=-1)
        o = torch.einsum("bhqk,bhkd->bhqd", p, v_cache)
        return (lambda x, *d: x.permute(*d))(o, 0, 2, 1, 3).reshape(B, Sq, H * Dh) @ wo

    def _B_ref(x, w1, w2, w3, w4):
        B, S, D = x.shape
        h = _gelu(_layernorm(x, axis=-1) @ w1)
        c = D // 4
        g0 = _pad(h[:, :, 0*c:1*c], ((0, 0), (1, 0), (0, 0)))[:, :S, :]
        g1 = _pad(h[:, :, 1*c:2*c], ((0, 0), (0, 1), (0, 0)))[:, 1:, :]
        g2 = _pad(h[:, :, 2*c:3*c], ((0, 0), (2, 0), (0, 0)))[:, :S, :]
        g3 = _pad(h[:, :, 3*c:   ], ((0, 0), (0, 2), (0, 0)))[:, 2:, :]
        hs = torch.concatenate([g0, g1, g2, g3], axis=-1) @ w2
        x1 = x + hs
        h2 = _gelu(_layernorm(x1, axis=-1) @ w3) @ w4
        return x1 + h2

    x = A_x
    xn = _layernorm(x, axis=-1)
    s = _A_ref(xn, A_k_cache, A_v_cache, A_wq, A_wo)
    g = _silu(_B_ref(xn, B_w1, B_w2, B_w3, B_w4))
    return x + s * g
```

```cpp
#include <hip/hip_runtime.h>
#include <hip/hip_bf16.h>
typedef unsigned short u16;
typedef __attribute__((ext_vector_type(8))) short bf16x8;
typedef __attribute__((ext_vector_type(4))) float f32x4;
constexpr int S=2048, D=640, H=32, DH=192, F=1536;
__device__ __forceinline__ float bf2f(u16 x){ return __uint_as_float(((unsigned)x)<<16); }
__device__ __forceinline__ u16 f2bf(float x){ unsigned u=__float_as_uint(x); u += 0x7fffu + ((u>>16)&1u); return (u16)(u>>16); }
__device__ __forceinline__ float gelu(float x){ float u=0.7978845608028654f*(x+0.044715f*x*x*x); return 0.5f*x*(1.f+tanhf(u)); }

__global__ void ln_kernel(const u16* x, u16* y){
  int row=blockIdx.x, l=threadIdx.x; float v[10]; float s=0;
  for(int i=0;i<10;i++){ v[i]=bf2f(x[row*D+l+64*i]); s+=v[i]; }
  for(int o=32;o;o>>=1) s+=__shfl_xor(s,o);
  float m=s/D, q=0; for(int i=0;i<10;i++){ q+=(v[i]-m)*(v[i]-m);}
  for(int o=32;o;o>>=1) q+=__shfl_xor(q,o);
  float r=1.f/sqrtf(q/D+1e-5f);
  for(int i=0;i<10;i++) y[row*D+l+64*i]=f2bf((v[i]-m)*r);
}
template<int MODE>
__global__ void gemm(const u16* A, const u16* B, u16* C, const u16* R, const u16* G, int M, int N, int K){
  int l=threadIdx.x; int m0=blockIdx.y*16, n0=blockIdx.x*16; f32x4 acc={0,0,0,0};
  int r=l&15, g=l>>4;
  for(int k0=0;k0<K;k0+=32){
    bf16x8 a,b; int row=m0+r; int k=k0+8*g;
    if(MODE==2){ int grp=k/160; int sh = grp==0?-1: grp==1?1: grp==2?-2:2; row+=sh; }
    if(row>=0 && row<M) a=*(const bf16x8*)(A+(long)row*K+k); else a=bf16x8{0,0,0,0,0,0,0,0};
    for(int j=0;j<8;j++) b[j]=B[(long)(k+j)*N+n0+r];
    acc=__builtin_amdgcn_mfma_f32_16x16x32_bf16(a,b,acc,0,0,0);
  }
  for(int j=0;j<4;j++){ long o=(long)(m0+g*4+j)*N+n0+r; float v=acc[j];
    if(MODE==1) v=gelu(v);
    if(MODE==2) v=bf2f(R[o])+v;
    if(MODE==3){ v=bf2f(R[o])+v; v=v/(1.f+expf(-v)); }
    if(MODE==4) v=bf2f(R[o])+v*bf2f(G[o]);
    C[o]=f2bf(v); }
}
__global__ __launch_bounds__(64) void attn(const u16* Q, const u16* Kc, const u16* Vc, u16* O){
  int l=threadIdx.x, h=blockIdx.y, q0=blockIdx.x*16; int r=l&15,g=l>>4;
  bf16x8 qf[6]; for(int s=0;s<6;s++) qf[s]=*(const bf16x8*)(Q+(long)(q0+r)*(H*DH)+h*DH+32*s+8*g);
  f32x4 o[12]; for(int i=0;i<12;i++) o[i]=f32x4{0,0,0,0};
  float m=-1e30f, lsum=0; const float c=0.07216878364870322f*1.4426950408889634f;
  const u16* K=Kc+(long)h*S*DH; const u16* V=Vc+(long)h*S*DH;
  for(int k0=0;k0<S;k0+=32){
    f32x4 s0={0,0,0,0}, s1={0,0,0,0};
    for(int s=0;s<6;s++){ bf16x8 a0=*(const bf16x8*)(K+(long)(k0+r)*DH+32*s+8*g), a1=*(const bf16x8*)(K+(long)(k0+16+r)*DH+32*s+8*g);
      s0=__builtin_amdgcn_mfma_f32_16x16x32_bf16(a0,qf[s],s0,0,0,0); s1=__builtin_amdgcn_mfma_f32_16x16x32_bf16(a1,qf[s],s1,0,0,0);}
    float mx=m; for(int j=0;j<4;j++){ mx=fmaxf(mx,s0[j]*c); mx=fmaxf(mx,s1[j]*c);}
    mx=fmaxf(mx,__shfl_xor(mx,16)); mx=fmaxf(mx,__shfl_xor(mx,32));
    float al=exp2f(m-mx); m=mx; lsum*=al; bf16x8 p;
    for(int j=0;j<4;j++){ float e0=exp2f(s0[j]*c-m), e1=exp2f(s1[j]*c-m); lsum+=e0+e1; p[j]=f2bf(e0); p[4+j]=f2bf(e1);}
    for(int i=0;i<12;i++){ bf16x8 a; for(int j=0;j<4;j++){ a[j]=V[(long)(k0+4*g+j)*DH+16*i+r]; a[4+j]=V[(long)(k0+16+4*g+j)*DH+16*i+r]; }
      for(int j=0;j<4;j++) o[i][j]*=al;
      o[i]=__builtin_amdgcn_mfma_f32_16x16x32_bf16(a,p,o[i],0,0,0);}
  }
  lsum+=__shfl_xor(lsum,16); lsum+=__shfl_xor(lsum,32); float il=1.f/lsum;
  for(int i=0;i<12;i++) for(int j=0;j<4;j++) O[(long)(q0+r)*(H*DH)+h*DH+16*i+4*g+j]=f2bf(o[i][j]*il);
}
extern "C" void kernel_launch(void* const* d_in, const int* in_sizes, int n_in, void* d_out, int out_size, void* d_ws, size_t ws_size, hipStream_t stream){
  const u16 *x=(const u16*)d_in[0], *kc=(const u16*)d_in[1], *vc=(const u16*)d_in[2], *wq=(const u16*)d_in[3], *wo=(const u16*)d_in[4];
  const u16 *w1=(const u16*)d_in[6], *w2=(const u16*)d_in[7], *w3=(const u16*)d_in[8], *w4=(const u16*)d_in[9];
  u16* ws=(u16*)d_ws; u16 *xn=ws, *xnn=xn+S*D, *h1=xnn+S*D, *x1=h1+S*D, *x1n=x1+S*D, *g=x1n+S*D, *h3=g+S*D, *q=h3+S*F, *o=q+S*H*DH;
  ln_kernel<<<S,64,0,stream>>>(x,xn); ln_kernel<<<S,64,0,stream>>>(xn,xnn);
  gemm<1><<<dim3(D/16,S/16),64,0,stream>>>(xnn,w1,h1,0,0,S,D,D);
  gemm<2><<<dim3(D/16,S/16),64,0,stream>>>(h1,w2,x1,xn,0,S,D,D);
  ln_kernel<<<S,64,0,stream>>>(x1,x1n);
  gemm<1><<<dim3(F/16,S/16),64,0,stream>>>(x1n,w3,h3,0,0,S,F,D);
  gemm<3><<<dim3(D/16,S/16),64,0,stream>>>(h3,w4,g,x1,0,S,D,F);
  gemm<0><<<dim3(H*DH/16,S/16),64,0,stream>>>(xn,wq,q,0,0,S,H*DH,D);
  attn<<<dim3(S/16,H),64,0,stream>>>(q,kc,vc,o);
  gemm<4><<<dim3(D/16,S/16),64,0,stream>>>(o,wo,(u16*)d_out,x,g,S,D,H*DH);
}
```

```cpp
#include <hip/hip_runtime.h>
#include <hip/hip_bf16.h>

__global__ __launch_bounds__(256, 1) void fused_block_kernel(const void* x, const void* kc, const void* vc, const void* wq, const void* wo,
                                                             const void* w1, const void* w2, const void* w3, const void* w4, void* out, void* ws) {
  __shared__ __attribute__((aligned(16))) char lds[163840];
  asm volatile(
    "L_begin:\n"
    "s_load_dwordx16 s[4:19], %0, 0x0\n"
    "s_load_dwordx4 s[20:23], %0, 0x40\n"
    "s_load_dwordx2 s[24:25], %0, 0x50\n"
    "v_lshrrev_b32 v17, 6, %2\n"
    "v_and_b32 v16, 63, %2\n"
    "v_readfirstlane_b32 s76, v17\n"
    "v_lshlrev_b32 v9, 4, v16\n"
    "s_and_b32 s77, %1, 7\n"
    "s_lshr_b32 s78, %1, 3\n"
    "s_and_b32 s78, s78, 7\n"
    "s_lshr_b32 s79, %1, 6\n"
    "s_lshl_b32 s82, s77, 2\n"
    "s_add_u32 s82, s82, s79\n"
    "s_lshl_b32 s83, s82, 2\n"
    "s_add_u32 s83, s83, s76\n"
    "s_getpc_b64 s[84:85]\n"
    "L_pc0:\n"
    "s_add_u32 s84, s84, L_c0-L_pc0\n"
    "s_addc_u32 s85, s85, 0\n"
    "s_mov_b32 s86, L_tail-L_c0\n"
    "s_mov_b32 s87, 0x20000\n"
    "s_lshr_b32 s95, %1, 3\n"
    "s_lshl_b32 s95, s95, 7\n"
    "v_lshl_add_u32 v17, %2, 12, s95\n"
    "buffer_load_dword v51, v17, s[84:87], 0 offen\n"
    "v_lshrrev_b32 v17, 4, v16\n"
    "v_and_b32 v18, 15, v16\n"
    "v_xor_b32 v18, v18, v17\n"
    "v_mul_u32_u24 v17, 1280, v17\n"
    "v_xor_b32 v19, 0, v18\n"
    "v_lshl_add_u32 v84, v19, 4, v17\n"
    "v_xor_b32 v19, 4, v18\n"
    "v_lshl_add_u32 v85, v19, 4, v17\n"
    "v_add_u32 v85, 4096, v85\n"
    "v_xor_b32 v19, 8, v18\n"
    "v_lshl_add_u32 v86, v19, 4, v17\n"
    "v_add_u32 v86, 8192, v86\n"
    "v_xor_b32 v19, 12, v18\n"
    "v_lshl_add_u32 v87, v19, 4, v17\n"
    "v_add_u32 v87, 12288, v87\n"
    "s_mul_i32 s89, s76, 20480\n"
    "v_mul_u32_u24 v17, 21846, v16\n"
    "v_lshrrev_b32 v17, 16, v17\n"
    "v_mul_u32_u24 v17, 11904, v17\n"
    "v_lshl_add_u32 v50, v16, 7, v17\n"
    "s_lshl_b32 s73, s78, 2\n"
    "s_add_u32 s73, s73, s76\n"
    "s_mul_i32 s73, s73, 49152\n"
    "v_min_u32 v49, 37120, v50\n"
    "v_add_u32 v49, s73, v49\n"
    "s_waitcnt lgkmcnt(0)\n"
    "s_load_dwordx2 s[80:81], s[24:25], 0x0\n"
    "s_lshl_b32 s73, s82, 3\n"
    "s_add_u32 s73, s73, s78\n"
    "s_mul_i32 s73, s73, 10240\n"
    "s_mov_b64 s[84:85], s[4:5]\n"
    "s_add_u32 s84, s84, s73\n"
    "s_addc_u32 s85, s85, 0\n"
    "s_mov_b32 s86, 10240\n"
    "s_mov_b32 s87, 0x20000\n"
    "v_lshlrev_b32 v17, 7, %2\n"
    "buffer_load_dword v51, v17, s[84:87], 0 offen\n"
    "s_mul_i32 s73, %1, 3200\n"
    "s_mov_b64 s[84:85], s[14:15]\n"
    "s_add_u32 s84, s84, s73\n"
    "s_addc_u32 s85, s85, 0\n"
    "s_mov_b32 s86, 3200\n"
    "s_mov_b32 s87, 0x20000\n"
    "v_lshlrev_b32 v17, 7, %2\n"
    "buffer_load_dword v51, v17, s[84:87], 0 offen\n"
    "s_mul_i32 s73, s82, 384\n"
    "s_mov_b64 s[44:45], s[10:11]\n"
    "s_add_u32 s44, s44, s73\n"
    "s_addc_u32 s45, s45, 0\n"
    "s_mov_b32 s46, 7864320\n"
    "s_mov_b32 s47, 0x20000\n"
    "s_sub_u32 s46, s46, s73\n"
    "buffer_load_dword v51, v49, s[44:47], 0 offen\n"
    "s_mul_i32 s73, s83, 20480\n"
    "s_mov_b64 s[84:85], s[4:5]\n"
    "s_add_u32 s84, s84, s73\n"
    "s_addc_u32 s85, s85, 0\n"
    "s_mov_b32 s86, 20480\n"
    "s_mov_b32 s87, 0x20000\n"
    "s_add_u32 m0, s89, 0\n"
    "s_mov_b32 s92, 0\n"
    "buffer_load_dwordx4 v84, s[84:87], s92 offen offset:0 lds\n"
    "buffer_load_dwordx4 v85, s[84:87], s92 offen offset:1024 lds\n"
    "buffer_load_dwordx4 v86, s[84:87], s92 offen offset:2048 lds\n"
    "buffer_load_dwordx4 v87, s[84:87], s92 offen offset:3072 lds\n"
    "L_c0:\n"
    "s_mul_i32 s73, s78, 160\n"
    "s_mov_b64 s[48:49], s[14:15]\n"
    "s_add_u32 s48, s48, s73\n"
    "s_addc_u32 s49, s49, 0\n"
    "s_mov_b32 s50, 819200\n"
    "s_mov_b32 s51, 0x20000\n"
    "s_sub_u32 s50, s50, s73\n"
    "v_lshrrev_b32 v16, 4, v9\n"
    "v_add_u32 v16, 0, v16\n"
    "v_mul_u32_u24 v17, 6554, v16\n"
    "v_lshrrev_b32 v17, 16, v17\n"
    "v_mul_u32_u24 v18, 10, v17\n"
    "v_sub_u32 v16, v16, v18\n"
    "v_and_b32 v18, 12, v17\n"
    "v_lshrrev_b32 v19, 4, v17\n"
    "v_and_b32 v17, 3, v17\n"
    "v_lshl_add_u32 v17, v18, 1, v17\n"
    "v_lshl_add_u32 v17, v19, 2, v17\n"
    "v_mul_u32_u24 v17, 1280, v17\n"
    "v_lshl_add_u32 v11, v16, 4, v17\n"
    "v_lshrrev_b32 v16, 4, v9\n"
    "v_add_u32 v16, 64, v16\n"
    "v_mul_u32_u24 v17, 6554, v16\n"
    "v_lshrrev_b32 v17, 16, v17\n"
    "v_mul_u32_u24 v18, 10, v17\n"
    "v_sub_u32 v16, v16, v18\n"
    "v_and_b32 v18, 12, v17\n"
    "v_lshrrev_b32 v19, 4, v17\n"
    "v_and_b32 v17, 3, v17\n"
    "v_lshl_add_u32 v17, v18, 1, v17\n"
    "v_lshl_add_u32 v17, v19, 2, v17\n"
    "v_mul_u32_u24 v17, 1280, v17\n"
    "v_lshl_add_u32 v12, v16, 4, v17\n"
    "v_subrev_u32 v12, 1024, v12\n"
    "v_lshrrev_b32 v16, 4, v9\n"
    "v_add_u32 v16, 128, v16\n"
    "v_mul_u32_u24 v17, 6554, v16\n"
    "v_lshrrev_b32 v17, 16, v17\n"
    "v_mul_u32_u24 v18, 10, v17\n"
    "v_sub_u32 v16, v16, v18\n"
    "v_and_b32 v18, 12, v17\n"
    "v_lshrrev_b32 v19, 4, v17\n"
    "v_and_b32 v17, 3, v17\n"
    "v_lshl_add_u32 v17, v18, 1, v17\n"
    "v_lshl_add_u32 v17, v19, 2, v17\n"
    "v_mul_u32_u24 v17, 1280, v17\n"
    "v_lshl_add_u32 v13, v16, 4, v17\n"
    "v_subrev_u32 v13, 2048, v13\n"
    "v_lshrrev_b32 v16, 4, v9\n"
    "v_add_u32 v16, 192, v16\n"
    "v_mul_u32_u24 v17, 6554, v16\n"
    "v_lshrrev_b32 v17, 16, v17\n"
    "v_mul_u32_u24 v18, 10, v17\n"
    "v_sub_u32 v16, v16, v18\n"
    "v_and_b32 v18, 12, v17\n"
    "v_lshrrev_b32 v19, 4, v17\n"
    "v_and_b32 v17, 3, v17\n"
    "v_lshl_add_u32 v17, v18, 1, v17\n"
    "v_lshl_add_u32 v17, v19, 2, v17\n"
    "v_mul_u32_u24 v17, 1280, v17\n"
    "v_lshl_add_u32 v14, v16, 4, v17\n"
    "v_subrev_u32 v14, 3072, v14\n"
    "v_lshrrev_b32 v16, 4, v9\n"
    "v_add_u32 v16, 256, v16\n"
    "v_mul_u32_u24 v17, 6554, v16\n"
    "v_lshrrev_b32 v17, 16, v17\n"
    "v_mul_u32_u24 v18, 10, v17\n"
    "v_sub_u32 v16, v16, v18\n"
    "v_and_b32 v18, 12, v17\n"
    "v_lshrrev_b32 v19, 4, v17\n"
    "v_and_b32 v17, 3, v17\n"
    "v_lshl_add_u32 v17, v18, 1, v17\n"
    "v_lshl_add_u32 v17, v19, 2, v17\n"
    "v_mul_u32_u24 v17, 1280, v17\n"
    "v_lshl_add_u32 v15, v16, 4, v17\n"
    "s_mul_i32 s91, s76, 5120\n"
    "s_mul_i32 s90, s76, 40960\n"
    "v_lshrrev_b32 v16, 4, v9\n"
    "s_mul_i32 s73, s82, 786432\n"
    "s_mov_b64 s[36:37], s[6:7]\n"
    "s_add_u32 s36, s36, s73\n"
    "s_addc_u32 s37, s37, 0\n"
    "s_mov_b32 s38, 786432\n"
    "s_mov_b32 s39, 0x20000\n"
    "s_mov_b64 s[40:41], s[8:9]\n"
    "s_add_u32 s40, s40, s73\n"
    "s_addc_u32 s41, s41, 0\n"
    "s_mov_b32 s42, 786432\n"
    "s_mov_b32 s43, 0x20000\n"
    "s_mov_b32 s56, 0\n"
    "s_mov_b32 s62, 0\n"
    "s_mov_b32 s57, 2048\n"
    "s_mov_b32 s63, 195584\n"
    "s_mov_b32 s58, 128\n"
    "s_mov_b32 s64, 128\n"
    "s_mov_b32 s59, 2176\n"
    "s_mov_b32 s65, 195712\n"
    "s_mov_b32 s60, 256\n"
    "s_mov_b32 s66, 256\n"
    "s_mov_b32 s61, 2304\n"
    "s_mov_b32 s67, 195840\n"
    "s_mul_i32 s68, s76, 6144\n"
    "v_lshrrev_b32 v17, 3, v16\n"
    "v_and_b32 v18, 6, v17\n"
    "v_and_b32 v19, 7, v16\n"
    "v_xor_b32 v18, v18, v19\n"
    "v_lshlrev_b32 v18, 4, v18\n"
    "v_lshrrev_b32 v19, 2, v17\n"
    "v_lshlrev_b32 v19, 3, v19\n"
    "v_and_b32 v20, 3, v17\n"
    "v_add_u32 v19, v19, v20\n"
    "s_lshr_b32 s73, s76, 1\n"
    "s_lshl_b32 s73, s73, 5\n"
    "s_and_b32 s74, s76, 1\n"
    "s_lshl_b32 s74, s74, 2\n"
    "s_add_u32 s73, s73, s74\n"
    "v_add_u32 v19, s73, v19\n"
    "v_mul_u32_u24 v19, 12288, v19\n"
    "v_add_u32 v59, v19, v18\n"
    "s_add_u32 s73, s78, 2\n"
    "s_lshl_b32 s73, s73, 2\n"
    "s_add_u32 s73, s73, s76\n"
    "s_mul_i32 s73, s73, 196608\n"
    "v_min_u32 v50, 184576, v50\n"
    "v_add_u32 v50, s73, v50\n"
    "v_mul_u32_u24 v19, 384, v17\n"
    "v_add3_u32 v8, v19, v18, s68\n"
    "v_and_b32 v17, 15, v16\n"
    "v_lshrrev_b32 v18, 4, v16\n"
    "v_and_b32 v19, 6, v16\n"
    "v_xor_b32 v19, v19, v18\n"
    "v_lshlrev_b32 v19, 4, v19\n"
    "v_lshl_add_u32 v19, v17, 7, v19\n"
    "v_xor_b32 v20, 64, v19\n"
    "v_add_u32 v2, 49152, v19\n"
    "v_add_u32 v3, 49152, v20\n"
    "v_xor_b32 v19, v17, v18\n"
    "v_lshlrev_b32 v19, 4, v19\n"
    "v_lshl_add_u32 v19, v17, 8, v19\n"
    "v_xor_b32 v76, 0, v19\n"
    "v_add_u32 v76, s89, v76\n"
    "v_xor_b32 v77, 64, v19\n"
    "v_add_u32 v77, s89, v77\n"
    "v_xor_b32 v78, 128, v19\n"
    "v_add_u32 v78, s89, v78\n"
    "v_xor_b32 v79, 192, v19\n"
    "v_add_u32 v79, s89, v79\n"
    "s_mul_i32 s73, s78, 160\n"
    "v_lshl_add_u32 v20, v18, 3, s73\n"
    "v_lshlrev_b32 v101, 4, v17\n"
    "v_lshl_add_u32 v102, v17, 8, s89\n"
    "v_and_b32 v96, 0xf8, v20\n"
    "v_xor_b32 v96, v96, v101\n"
    "v_lshrrev_b32 v103, 8, v20\n"
    "v_lshl_add_u32 v96, v103, 12, v96\n"
    "v_add_u32 v96, v96, v102\n"
    "v_add_u32 v20, 32, v20\n"
    "v_and_b32 v97, 0xf8, v20\n"
    "v_xor_b32 v97, v97, v101\n"
    "v_lshrrev_b32 v103, 8, v20\n"
    "v_lshl_add_u32 v97, v103, 12, v97\n"
    "v_add_u32 v97, v97, v102\n"
    "v_add_u32 v20, 32, v20\n"
    "v_and_b32 v98, 0xf8, v20\n"
    "v_xor_b32 v98, v98, v101\n"
    "v_lshrrev_b32 v103, 8, v20\n"
    "v_lshl_add_u32 v98, v103, 12, v98\n"
    "v_add_u32 v98, v98, v102\n"
    "v_add_u32 v20, 32, v20\n"
    "v_and_b32 v99, 0xf8, v20\n"
    "v_xor_b32 v99, v99, v101\n"
    "v_lshrrev_b32 v103, 8, v20\n"
    "v_lshl_add_u32 v99, v103, 12, v99\n"
    "v_add_u32 v99, v99, v102\n"
    "v_add_u32 v20, 32, v20\n"
    "v_and_b32 v100, 0xf8, v20\n"
    "v_xor_b32 v100, v100, v101\n"
    "v_lshrrev_b32 v103, 8, v20\n"
    "v_lshl_add_u32 v100, v103, 12, v100\n"
    "v_add_u32 v100, v100, v102\n"
    "s_mul_i32 s88, s78, 5\n"
    "s_lshr_b32 s88, s88, 1\n"
    "s_add_u32 s73, s88, 0\n"
    "s_and_b32 s74, s73, 3\n"
    "s_lshl_b32 s74, s74, 6\n"
    "s_lshr_b32 s73, s73, 2\n"
    "s_lshl_b32 s73, s73, 12\n"
    "s_add_u32 s73, s73, s89\n"
    "v_xor_b32 v80, s74, v19\n"
    "v_add_u32 v80, s73, v80\n"
    "s_add_u32 s73, s88, 1\n"
    "s_and_b32 s74, s73, 3\n"
    "s_lshl_b32 s74, s74, 6\n"
    "s_lshr_b32 s73, s73, 2\n"
    "s_lshl_b32 s73, s73, 12\n"
    "s_add_u32 s73, s73, s89\n"
    "v_xor_b32 v81, s74, v19\n"
    "v_add_u32 v81, s73, v81\n"
    "s_add_u32 s73, s88, 2\n"
    "s_and_b32 s74, s73, 3\n"
    "s_lshl_b32 s74, s74, 6\n"
    "s_lshr_b32 s73, s73, 2\n"
    "s_lshl_b32 s73, s73, 12\n"
    "s_add_u32 s73, s73, s89\n"
    "v_xor_b32 v82, s74, v19\n"
    "v_add_u32 v82, s73, v82\n"
    "v_lshrrev_b32 v17, 2, v16\n"
    "v_and_b32 v18, 6, v17\n"
    "v_and_b32 v19, 3, v16\n"
    "v_xor_b32 v20, v19, v18\n"
    "v_lshlrev_b32 v20, 4, v20\n"
    "v_lshl_add_u32 v20, v17, 7, v20\n"
    "v_add_u32 v57, 49152, v20\n"
    "v_xor_b32 v58, 64, v57\n"
    "v_lshlrev_b32 v19, 3, v19\n"
    "v_mul_u32_u24 v20, 160, v17\n"
    "v_add_u32 v61, v20, v19\n"
    "v_add_u32 v60, 98304, v61\n"
    "v_lshlrev_b32 v18, 4, v18\n"
    "v_xor_b32 v20, v19, v18\n"
    "v_lshl_add_u32 v20, v17, 7, v20\n"
    "v_xor_b32 v4, 0, v20\n"
    "v_add_u32 v4, 98304, v4\n"
    "v_xor_b32 v5, 32, v20\n"
    "v_add_u32 v5, 98304, v5\n"
    "v_xor_b32 v6, 64, v20\n"
    "v_add_u32 v6, 98304, v6\n"
    "v_xor_b32 v7, 96, v20\n"
    "v_add_u32 v7, 98304, v7\n"
    "v_cmp_eq_u32 vcc, 0, %2\n"
    "v_bfrev_b32 v17, 1\n"
    "s_nop 0\n"
    "v_cndmask_b32 v10, v17, 0, vcc\n"
    "v_mov_b32 v92, 0x3f803f80\n"
    "v_mov_b32 v93, 0x3f803f80\n"
    "v_mov_b32 v94, 0x3f803f80\n"
    "v_mov_b32 v95, 0x3f803f80\n"
    "s_mov_b64 s[52:53], s[24:25]\n"
    "s_mov_b32 s54, 71303168\n"
    "s_mov_b32 s55, 0x20000\n"
    "s_mov_b32 s75, 0\n"
    "v_and_b32 v17, 15, v16\n"
    "v_lshrrev_b32 v18, 4, v16\n"
    "v_lshlrev_b32 v18, 2, v18\n"
    "v_sub_u32 v17, v17, v18\n"
    "v_mov_b32 v18, 0\n"
    "s_waitcnt lgkmcnt(0)\n"
    "s_add_u32 s80, s80, 1\n"
    "s_addc_u32 s81, s81, 0\n"
    "s_add_u32 m0, s89, 4096\n"
    "s_mov_b32 s92, 256\n"
    "buffer_load_dwordx4 v84, s[84:87], s92 offen offset:0 lds\n"
    "buffer_load_dwordx4 v85, s[84:87], s92 offen offset:1024 lds\n"
    "buffer_load_dwordx4 v86, s[84:87], s92 offen offset:2048 lds\n"
    "buffer_load_dwordx4 v87, s[84:87], s92 offen offset:3072 lds\n"
    "s_add_u32 m0, s89, 8192\n"
    "s_mov_b32 s92, 512\n"
    "buffer_load_dwordx4 v84, s[84:87], s92 offen offset:0 lds\n"
    "buffer_load_dwordx4 v85, s[84:87], s92 offen offset:1024 lds\n"
    "buffer_load_dwordx4 v86, s[84:87], s92 offen offset:2048 lds\n"
    "buffer_load_dwordx4 v87, s[84:87], s92 offen offset:3072 lds\n"
    "s_add_u32 m0, s89, 12288\n"
    "s_mov_b32 s92, 768\n"
    "buffer_load_dwordx4 v84, s[84:87], s92 offen offset:0 lds\n"
    "buffer_load_dwordx4 v85, s[84:87], s92 offen offset:1024 lds\n"
    "buffer_load_dwordx4 v86, s[84:87], s92 offen offset:2048 lds\n"
    "buffer_load_dwordx4 v87, s[84:87], s92 offen offset:3072 lds\n"
    "s_add_u32 m0, s89, 16384\n"
    "s_mov_b32 s92, 1024\n"
    "buffer_load_dwordx4 v84, s[84:87], s92 offen offset:0 lds\n"
    "buffer_load_dwordx4 v85, s[84:87], s92 offen offset:1024 lds\n"
    "buffer_load_dwordx4 v86, s[84:87], s92 offen offset:2048 lds\n"
    "buffer_load_dwordx4 v87, s[84:87], s92 offen offset:3072 lds\n"
    "s_waitcnt vmcnt(12)\n"
    "ds_read_b128 v[176:179], v76 offset:0\n"
    "ds_read_b128 v[180:183], v77 offset:0\n"
    "ds_read_b128 v[184:187], v78 offset:0\n"
    "ds_read_b128 v[188:191], v79 offset:0\n"
    "ds_read_b128 v[192:195], v76 offset:4096\n"
    "ds_read_b128 v[196:199], v77 offset:4096\n"
    "ds_read_b128 v[200:203], v78 offset:4096\n"
    "ds_read_b128 v[204:207], v79 offset:4096\n"
    "s_waitcnt lgkmcnt(7)\n"
    "v_mfma_f32_16x16x32_bf16 v[64:67], v[92:95], v[176:179], 0\n"
    "s_waitcnt vmcnt(8)\n"
    "ds_read_b128 v[208:211], v76 offset:8192\n"
    "v_mfma_f32_16x16x32_bf16 v[68:71], v[176:179], v[176:179], 0\n"
    "s_waitcnt lgkmcnt(7)\n"
    "v_mfma_f32_16x16x32_bf16 v[64:67], v[92:95], v[180:183], v[64:67]\n"
    "ds_read_b128 v[212:215], v77 offset:8192\n"
    "v_mfma_f32_16x16x32_bf16 v[68:71], v[180:183], v[180:183], v[68:71]\n"
    "s_waitcnt lgkmcnt(7)\n"
    "v_mfma_f32_16x16x32_bf16 v[64:67], v[92:95], v[184:187], v[64:67]\n"
    "ds_read_b128 v[216:219], v78 offset:8192\n"
    "v_mfma_f32_16x16x32_bf16 v[68:71], v[184:187], v[184:187], v[68:71]\n"
    "s_waitcnt lgkmcnt(7)\n"
    "v_mfma_f32_16x16x32_bf16 v[64:67], v[92:95], v[188:191], v[64:67]\n"
    "ds_read_b128 v[220:223], v79 offset:8192\n"
    "v_mfma_f32_16x16x32_bf16 v[68:71], v[188:191], v[188:191], v[68:71]\n"
    "s_waitcnt lgkmcnt(7)\n"
    "v_mfma_f32_16x16x32_bf16 v[64:67], v[92:95], v[192:195], v[64:67]\n"
    "s_waitcnt vmcnt(4)\n"
    "ds_read_b128 v[224:227], v76 offset:12288\n"
    "v_mfma_f32_16x16x32_bf16 v[68:71], v[192:195], v[192:195], v[68:71]\n"
    "s_waitcnt lgkmcnt(7)\n"
    "v_mfma_f32_16x16x32_bf16 v[64:67], v[92:95], v[196:199], v[64:67]\n"
    "ds_read_b128 v[228:231], v77 offset:12288\n"
    "v_mfma_f32_16x16x32_bf16 v[68:71], v[196:199], v[196:199], v[68:71]\n"
    "s_waitcnt lgkmcnt(7)\n"
    "v_mfma_f32_16x16x32_bf16 v[64:67], v[92:95], v[200:203], v[64:67]\n"
    "ds_read_b128 v[232:235], v78 offset:12288\n"
    "v_mfma_f32_16x16x32_bf16 v[68:71], v[200:203], v[200:203], v[68:71]\n"
    "s_waitcnt lgkmcnt(7)\n"
    "v_mfma_f32_16x16x32_bf16 v[64:67], v[92:95], v[204:207], v[64:67]\n"
    "ds_read_b128 v[236:239], v79 offset:12288\n"
    "v_mfma_f32_16x16x32_bf16 v[68:71], v[204:207], v[204:207], v[68:71]\n"
    "s_waitcnt lgkmcnt(7)\n"
    "v_mfma_f32_16x16x32_bf16 v[64:67], v[92:95], v[208:211], v[64:67]\n"
    "s_waitcnt vmcnt(0)\n"
    "ds_read_b128 v[240:243], v76 offset:16384\n"
    "v_mfma_f32_16x16x32_bf16 v[68:71], v[208:211], v[208:211], v[68:71]\n"
    "s_waitcnt lgkmcnt(7)\n"
    "s_add_u32 m0, s91, 98304\n"
    "s_nop 0\n"
    "buffer_load_dwordx4 v11, s[48:51], s90 offen offset:0 lds\n"
    "v_mfma_f32_16x16x32_bf16 v[64:67], v[92:95], v[212:215], v[64:67]\n"
    "ds_read_b128 v[244:247], v77 offset:16384\n"
    "v_mfma_f32_16x16x32_bf16 v[68:71], v[212:215], v[212:215], v[68:71]\n"
    "s_waitcnt lgkmcnt(7)\n"
    "v_mfma_f32_16x16x32_bf16 v[64:67], v[92:95], v[216:219], v[64:67]\n"
    "ds_read_b128 v[248:251], v78 offset:16384\n"
    "v_mfma_f32_16x16x32_bf16 v[68:71], v[216:219], v[216:219], v[68:71]\n"
    "s_waitcnt lgkmcnt(7)\n"
    "buffer_load_dwordx4 v12, s[48:51], s90 offen offset:1024 lds\n"
    "v_mfma_f32_16x16x32_bf16 v[64:67], v[92:95], v[220:223], v[64:67]\n"
    "ds_read_b128 v[252:255], v79 offset:16384\n"
    "v_mfma_f32_16x16x32_bf16 v[68:71], v[220:223], v[220:223], v[68:71]\n"
    "s_waitcnt lgkmcnt(7)\n"
    "v_mfma_f32_16x16x32_bf16 v[64:67], v[92:95], v[224:227], v[64:67]\n"
    "ds_read_b128 v[164:167], v80\n"
    "v_mfma_f32_16x16x32_bf16 v[68:71], v[224:227], v[224:227], v[68:71]\n"
    "s_waitcnt lgkmcnt(7)\n"
    "ds_read_b128 v[168:171], v81\n"
    "buffer_load_dwordx4 v13, s[48:51], s90 offen offset:2048 lds\n"
    "v_mfma_f32_16x16x32_bf16 v[64:67], v[92:95], v[228:231], v[64:67]\n"
    "ds_read_b128 v[172:175], v82\n"
    "v_mfma_f32_16x16x32_bf16 v[68:71], v[228:231], v[228:231], v[68:71]\n"
    "s_waitcnt lgkmcnt(8)\n"
    "ds_read_b64 v[104:105], v96\n"
    "v_mfma_f32_16x16x32_bf16 v[64:67], v[92:95], v[232:235], v[64:67]\n"
    "ds_read_b64 v[106:107], v97\n"
    "v_mfma_f32_16x16x32_bf16 v[68:71], v[232:235], v[232:235], v[68:71]\n"
    "s_waitcnt lgkmcnt(9)\n"
    "ds_read_b64 v[108:109], v98\n"
    "buffer_load_dwordx4 v14, s[48:51], s90 offen offset:3072 lds\n"
    "v_mfma_f32_16x16x32_bf16 v[64:67], v[92:95], v[236:239], v[64:67]\n"
    "ds_read_b64 v[110:111], v99\n"
    "v_mfma_f32_16x16x32_bf16 v[68:71], v[236:239], v[236:239], v[68:71]\n"
    "s_waitcnt lgkmcnt(10)\n"
    "ds_read_b64 v[112:113], v100\n"
    "v_mfma_f32_16x16x32_bf16 v[64:67], v[92:95], v[240:243], v[64:67]\n"
    "v_mfma_f32_16x16x32_bf16 v[68:71], v[240:243], v[240:243], v[68:71]\n"
    "s_waitcnt lgkmcnt(10)\n"
    "s_add_u32 m0, s91, 102400\n"
    "s_nop 0\n"
    "buffer_load_dwordx4 v15, s[48:51], s90 offen offset:0 lds\n"
    "v_mfma_f32_16x16x32_bf16 v[64:67], v[92:95], v[244:247], v[64:67]\n"
    "v_mfma_f32_16x16x32_bf16 v[68:71], v[244:247], v[244:247], v[68:71]\n"
    "s_waitcnt lgkmcnt(9)\n"
    "v_mfma_f32_16x16x32_bf16 v[64:67], v[92:95], v[248:251], v[64:67]\n"
    "v_mfma_f32_16x16x32_bf16 v[68:71], v[248:251], v[248:251], v[68:71]\n"
    "s_waitcnt lgkmcnt(8)\n"
    "s_add_u32 s90, s90, 163840\n"
    "v_mfma_f32_16x16x32_bf16 v[64:67], v[92:95], v[252:255], v[64:67]\n"
    "v_mfma_f32_16x16x32_bf16 v[68:71], v[252:255], v[252:255], v[68:71]\n"
    "s_waitcnt lgkmcnt(0)\n"
    "s_barrier\n"
    "s_add_u32 m0, s91, 118784\n"
    "s_nop 0\n"
    "buffer_load_dwordx4 v11, s[48:51], s90 offen offset:0 lds\n"
    "s_nop 2\n"
    "v_cmp_eq_u32 vcc, 0, v17\n"
    "s_nop 1\n"
    "v_cndmask_b32 v18, v18, v68, vcc\n"
    "v_cmp_eq_u32 vcc, 1, v17\n"
    "s_nop 1\n"
    "v_cndmask_b32 v18, v18, v69, vcc\n"
    "v_cmp_eq_u32 vcc, 2, v17\n"
    "s_nop 1\n"
    "v_cndmask_b32 v18, v18, v70, vcc\n"
    "v_cmp_eq_u32 vcc, 3, v17\n"
    "s_nop 1\n"
    "v_cndmask_b32 v18, v18, v71, vcc\n"
    "v_mov_b32 v19, v18\n"
    "s_nop 1\n"
    "v_permlane16_swap_b32 v18, v19\n"
    "v_add_f32 v18, v18, v19\n"
    "v_mov_b32 v19, v18\n"
    "s_nop 1\n"
    "v_permlane32_swap_b32 v18, v19\n"
    "v_add_f32 v18, v18, v19\n"
    "buffer_load_dwordx4 v12, s[48:51], s90 offen offset:1024 lds\n"
    "v_mul_f32 v17, 0x3acccccd, v64\n"
    "v_mul_f32 v19, v17, v17\n"
    "v_mul_f32 v18, 0x3acccccd, v18\n"
    "v_sub_f32 v18, v18, v19\n"
    "v_max_f32 v18, 0, v18\n"
    "v_add_f32 v19, 0x3727c5ac, v18\n"
    "v_rsq_f32 v52, v19\n"
    "s_nop 0\n"
    "v_mov_b32 v53, v52\n"
    "v_mul_f32 v54, v17, v52\n"
    "v_sub_f32 v54, 0, v54\n"
    "v_mov_b32 v55, v54\n"
    "v_mul_f32 v19, v52, v52\n"
    "v_mul_f32 v19, v19, v18\n"
    "v_add_f32 v19, 0x3727c5ac, v19\n"
    "v_rsq_f32 v88, v19\n"
    "v_mul_f32 v72, 0x3dd53b95, v52\n"
    "v_mul_f32 v74, 0x3dd53b95, v54\n"
    "v_mul_f32 v73, 0x3dd53b95, v52\n"
    "v_mul_f32 v75, 0x3dd53b95, v54\n"
    "s_mul_i32 s92, s82, 81920\n"
    "s_lshl_b32 s73, s76, 11\n"
    "s_add_u32 s92, s92, s73\n"
    "s_add_u32 s92, s92, 1048576\n"
    "buffer_load_dwordx4 v13, s[48:51], s90 offen offset:2048 lds\n"
    "v_lshlrev_b32 v16, 16, v164\n"
    "v_and_b32 v17, 0xffff0000, v164\n"
    "v_lshlrev_b32 v18, 16, v165\n"
    "v_and_b32 v19, 0xffff0000, v165\n"
    "v_lshlrev_b32 v20, 16, v166\n"
    "v_and_b32 v21, 0xffff0000, v166\n"
    "v_lshlrev_b32 v22, 16, v167\n"
    "v_and_b32 v23, 0xffff0000, v167\n"
    "v_pk_fma_f32 v[16:17], v[16:17], v[72:73], v[74:75]\n"
    "v_pk_fma_f32 v[18:19], v[18:19], v[72:73], v[74:75]\n"
    "v_pk_fma_f32 v[20:21], v[20:21], v[72:73], v[74:75]\n"
    "v_pk_fma_f32 v[22:23], v[22:23], v[72:73], v[74:75]\n"
    "v_cvt_pk_bf16_f32 v164, v16, v17\n"
    "v_cvt_pk_bf16_f32 v165, v18, v19\n"
    "v_cvt_pk_bf16_f32 v166, v20, v21\n"
    "v_cvt_pk_bf16_f32 v167, v22, v23\n"
    "s_add_u32 s73, s88, 0\n"
    "s_lshr_b32 s74, s73, 1\n"
    "s_and_b32 s73, s73, 1\n"
    "s_lshl_b32 s74, s74, 3\n"
    "s_add_u32 s74, s74, s73\n"
    "s_lshl_b32 s74, s74, 10\n"
    "s_add_u32 s74, s74, s92\n"
    "buffer_store_dwordx4 v[164:167], v9, s[52:55], s74 offen sc1\n"
    "v_lshlrev_b32 v16, 16, v168\n"
    "v_and_b32 v17, 0xffff0000, v168\n"
    "v_lshlrev_b32 v18, 16, v169\n"
    "v_and_b32 v19, 0xffff0000, v169\n"
    "v_lshlrev_b32 v20, 16, v170\n"
    "v_and_b32 v21, 0xffff0000, v170\n"
    "v_lshlrev_b32 v22, 16, v171\n"
    "v_and_b32 v23, 0xffff0000, v171\n"
    "v_pk_fma_f32 v[16:17], v[16:17], v[72:73], v[74:75]\n"
    "v_pk_fma_f32 v[18:19], v[18:19], v[72:73], v[74:75]\n"
    "v_pk_fma_f32 v[20:21], v[20:21], v[72:73], v[74:75]\n"
    "v_pk_fma_f32 v[22:23], v[22:23], v[72:73], v[74:75]\n"
    "v_cvt_pk_bf16_f32 v168, v16, v17\n"
    "v_cvt_pk_bf16_f32 v169, v18, v19\n"
    "v_cvt_pk_bf16_f32 v170, v20, v21\n"
    "v_cvt_pk_bf16_f32 v171, v22, v23\n"
    "s_add_u32 s73, s88, 1\n"
    "s_lshr_b32 s74, s73, 1\n"
    "s_and_b32 s73, s73, 1\n"
    "s_lshl_b32 s74, s74, 3\n"
    "s_add_u32 s74, s74, s73\n"
    "s_lshl_b32 s74, s74, 10\n"
    "s_add_u32 s74, s74, s92\n"
    "buffer_store_dwordx4 v[168:171], v9, s[52:55], s74 offen sc1\n"
    "v_lshlrev_b32 v16, 16, v172\n"
    "v_and_b32 v17, 0xffff0000, v172\n"
    "v_lshlrev_b32 v18, 16, v173\n"
    "v_and_b32 v19, 0xffff0000, v173\n"
    "v_lshlrev_b32 v20, 16, v174\n"
    "v_and_b32 v21, 0xffff0000, v174\n"
    "v_lshlrev_b32 v22, 16, v175\n"
    "v_and_b32 v23, 0xffff0000, v175\n"
    "v_pk_fma_f32 v[16:17], v[16:17], v[72:73], v[74:75]\n"
    "v_pk_fma_f32 v[18:19], v[18:19], v[72:73], v[74:75]\n"
    "v_pk_fma_f32 v[20:21], v[20:21], v[72:73], v[74:75]\n"
    "v_pk_fma_f32 v[22:23], v[22:23], v[72:73], v[74:75]\n"
    "v_cvt_pk_bf16_f32 v172, v16, v17\n"
    "v_cvt_pk_bf16_f32 v173, v18, v19\n"
    "v_cvt_pk_bf16_f32 v174, v20, v21\n"
    "v_cvt_pk_bf16_f32 v175, v22, v23\n"
    "s_add_u32 s73, s88, 2\n"
    "s_lshr_b32 s74, s73, 1\n"
    "s_and_b32 s73, s73, 1\n"
    "s_lshl_b32 s74, s74, 3\n"
    "s_add_u32 s74, s74, s73\n"
    "s_lshl_b32 s74, s74, 10\n"
    "s_add_u32 s74, s74, s92\n"
    "buffer_store_dwordx4 v[172:175], v9, s[52:55], s74 offen sc1\n"
    "v_lshlrev_b32 v16, 16, v176\n"
    "v_and_b32 v17, 0xffff0000, v176\n"
    "v_lshlrev_b32 v18, 16, v177\n"
    "v_and_b32 v19, 0xffff0000, v177\n"
    "v_lshlrev_b32 v20, 16, v178\n"
    "v_and_b32 v21, 0xffff0000, v178\n"
    "v_lshlrev_b32 v22, 16, v179\n"
    "v_and_b32 v23, 0xffff0000, v179\n"
    "v_pk_fma_f32 v[16:17], v[16:17], v[52:53], v[54:55]\n"
    "v_pk_fma_f32 v[18:19], v[18:19], v[52:53], v[54:55]\n"
    "v_pk_fma_f32 v[20:21], v[20:21], v[52:53], v[54:55]\n"
    "v_pk_fma_f32 v[22:23], v[22:23], v[52:53], v[54:55]\n"
    "v_cvt_pk_bf16_f32 v176, v16, v17\n"
    "v_cvt_pk_bf16_f32 v177, v18, v19\n"
    "v_cvt_pk_bf16_f32 v178, v20, v21\n"
    "v_cvt_pk_bf16_f32 v179, v22, v23\n"
    "v_lshlrev_b32 v16, 16, v180\n"
    "v_and_b32 v17, 0xffff0000, v180\n"
    "v_lshlrev_b32 v18, 16, v181\n"
    "v_and_b32 v19, 0xffff0000, v181\n"
    "v_lshlrev_b32 v20, 16, v182\n"
    "v_and_b32 v21, 0xffff0000, v182\n"
    "v_lshlrev_b32 v22, 16, v183\n"
    "v_and_b32 v23, 0xffff0000, v183\n"
    "v_pk_fma_f32 v[16:17], v[16:17], v[52:53], v[54:55]\n"
    "v_pk_fma_f32 v[18:19], v[18:19], v[52:53], v[54:55]\n"
    "v_pk_fma_f32 v[20:21], v[20:21], v[52:53], v[54:55]\n"
    "v_pk_fma_f32 v[22:23], v[22:23], v[52:53], v[54:55]\n"
    "v_cvt_pk_bf16_f32 v180, v16, v17\n"
    "v_cvt_pk_bf16_f32 v181, v18, v19\n"
    "v_cvt_pk_bf16_f32 v182, v20, v21\n"
    "v_cvt_pk_bf16_f32 v183, v22, v23\n"
    "v_lshlrev_b32 v16, 16, v184\n"
    "v_and_b32 v17, 0xffff0000, v184\n"
    "v_lshlrev_b32 v18, 16, v185\n"
    "v_and_b32 v19, 0xffff0000, v185\n"
    "v_lshlrev_b32 v20, 16, v186\n"
    "v_and_b32 v21, 0xffff0000, v186\n"
    "v_lshlrev_b32 v22, 16, v187\n"
    "v_and_b32 v23, 0xffff0000, v187\n"
    "v_pk_fma_f32 v[16:17], v[16:17], v[52:53], v[54:55]\n"
    "v_pk_fma_f32 v[18:19], v[18:19], v[52:53], v[54:55]\n"
    "v_pk_fma_f32 v[20:21], v[20:21], v[52:53], v[54:55]\n"
    "v_pk_fma_f32 v[22:23], v[22:23], v[52:53], v[54:55]\n"
    "v_cvt_pk_bf16_f32 v184, v16, v17\n"
    "v_cvt_pk_bf16_f32 v185, v18, v19\n"
    "v_cvt_pk_bf16_f32 v186, v20, v21\n"
    "v_cvt_pk_bf16_f32 v187, v22, v23\n"
    "buffer_load_dwordx4 v14, s[48:51], s90 offen offset:3072 lds\n"
    "v_lshlrev_b32 v16, 16, v188\n"
    "v_and_b32 v17, 0xffff0000, v188\n"
    "v_lshlrev_b32 v18, 16, v189\n"
    "v_and_b32 v19, 0xffff0000, v189\n"
    "v_lshlrev_b32 v20, 16, v190\n"
    "v_and_b32 v21, 0xffff0000, v190\n"
    "v_lshlrev_b32 v22, 16, v191\n"
    "v_and_b32 v23, 0xffff0000, v191\n"
    "v_pk_fma_f32 v[16:17], v[16:17], v[52:53], v[54:55]\n"
    "v_pk_fma_f32 v[18:19], v[18:19], v[52:53], v[54:55]\n"
    "v_pk_fma_f32 v[20:21], v[20:21], v[52:53], v[54:55]\n"
    "v_pk_fma_f32 v[22:23], v[22:23], v[52:53], v[54:55]\n"
    "v_cvt_pk_bf16_f32 v188, v16, v17\n"
    "v_cvt_pk_bf16_f32 v189, v18, v19\n"
    "v_cvt_pk_bf16_f32 v190, v20, v21\n"
    "v_cvt_pk_bf16_f32 v191, v22, v23\n"
    "s_add_u32 m0, s91, 122880\n"
    "s_nop 0\n"
    "buffer_load_dwordx4 v15, s[48:51], s90 offen offset:0 lds\n"
    "v_lshlrev_b32 v16, 16, v192\n"
    "v_and_b32 v17, 0xffff0000, v192\n"
    "v_lshlrev_b32 v18, 16, v193\n"
    "v_and_b32 v19, 0xffff0000, v193\n"
    "v_lshlrev_b32 v20, 16, v194\n"
    "v_and_b32 v21, 0xffff0000, v194\n"
    "v_lshlrev_b32 v22, 16, v195\n"
    "v_and_b32 v23, 0xffff0000, v195\n"
    "v_pk_fma_f32 v[16:17], v[16:17], v[52:53], v[54:55]\n"
    "v_pk_fma_f32 v[18:19], v[18:19], v[52:53], v[54:55]\n"
    "v_pk_fma_f32 v[20:21], v[20:21], v[52:53], v[54:55]\n"
    "v_pk_fma_f32 v[22:23], v[22:23], v[52:53], v[54:55]\n"
    "v_cvt_pk_bf16_f32 v192, v16, v17\n"
    "v_cvt_pk_bf16_f32 v193, v18, v19\n"
    "v_cvt_pk_bf16_f32 v194, v20, v21\n"
    "v_cvt_pk_bf16_f32 v195, v22, v23\n"
    "s_add_u32 s90, s90, 163840\n"
    "v_lshlrev_b32 v16, 16, v196\n"
    "v_and_b32 v17, 0xffff0000, v196\n"
    "v_lshlrev_b32 v18, 16, v197\n"
    "v_and_b32 v19, 0xffff0000, v197\n"
    "v_lshlrev_b32 v20, 16, v198\n"
    "v_and_b32 v21, 0xffff0000, v198\n"
    "v_lshlrev_b32 v22, 16, v199\n"
    "v_and_b32 v23, 0xffff0000, v199\n"
    "v_pk_fma_f32 v[16:17], v[16:17], v[52:53], v[54:55]\n"
    "v_pk_fma_f32 v[18:19], v[18:19], v[52:53], v[54:55]\n"
    "v_pk_fma_f32 v[20:21], v[20:21], v[52:53], v[54:55]\n"
    "v_pk_fma_f32 v[22:23], v[22:23], v[52:53], v[54:55]\n"
    "v_cvt_pk_bf16_f32 v196, v16, v17\n"
    "v_cvt_pk_bf16_f32 v197, v18, v19\n"
    "v_cvt_pk_bf16_f32 v198, v20, v21\n"
    "v_cvt_pk_bf16_f32 v199, v22, v23\n"
    "s_add_u32 m0, s91, 139264\n"
    "s_nop 0\n"
    "buffer_load_dwordx4 v11, s[48:51], s90 offen offset:0 lds\n"
    "v_lshlrev_b32 v16, 16, v200\n"
    "v_and_b32 v17, 0xffff0000, v200\n"
    "v_lshlrev_b32 v18, 16, v201\n"
    "v_and_b32 v19, 0xffff0000, v201\n"
    "v_lshlrev_b32 v20, 16, v202\n"
    "v_and_b32 v21, 0xffff0000, v202\n"
    "v_lshlrev_b32 v22, 16, v203\n"
    "v_and_b32 v23, 0xffff0000, v203\n"
    "v_pk_fma_f32 v[16:17], v[16:17], v[52:53], v[54:55]\n"
    "v_pk_fma_f32 v[18:19], v[18:19], v[52:53], v[54:55]\n"
    "v_pk_fma_f32 v[20:21], v[20:21], v[52:53], v[54:55]\n"
    "v_pk_fma_f32 v[22:23], v[22:23], v[52:53], v[54:55]\n"
    "v_cvt_pk_bf16_f32 v200, v16, v17\n"
    "v_cvt_pk_bf16_f32 v201, v18, v19\n"
    "v_cvt_pk_bf16_f32 v202, v20, v21\n"
    "v_cvt_pk_bf16_f32 v203, v22, v23\n"
    "buffer_load_dwordx4 v12, s[48:51], s90 offen offset:1024 lds\n"
    "v_lshlrev_b32 v16, 16, v204\n"
    "v_and_b32 v17, 0xffff0000, v204\n"
    "v_lshlrev_b32 v18, 16, v205\n"
    "v_and_b32 v19, 0xffff0000, v205\n"
    "v_lshlrev_b32 v20, 16, v206\n"
    "v_and_b32 v21, 0xffff0000, v206\n"
    "v_lshlrev_b32 v22, 16, v207\n"
    "v_and_b32 v23, 0xffff0000, v207\n"
    "v_pk_fma_f32 v[16:17], v[16:17], v[52:53], v[54:55]\n"
    "v_pk_fma_f32 v[18:19], v[18:19], v[52:53], v[54:55]\n"
    "v_pk_fma_f32 v[20:21], v[20:21], v[52:53], v[54:55]\n"
    "v_pk_fma_f32 v[22:23], v[22:23], v[52:53], v[54:55]\n"
    "v_cvt_pk_bf16_f32 v204, v16, v17\n"
    "v_cvt_pk_bf16_f32 v205, v18, v19\n"
    "v_cvt_pk_bf16_f32 v206, v20, v21\n"
    "v_cvt_pk_bf16_f32 v207, v22, v23\n"
    "buffer_load_dwordx4 v13, s[48:51], s90 offen offset:2048 lds\n"
    "v_lshlrev_b32 v16, 16, v208\n"
    "v_and_b32 v17, 0xffff0000, v208\n"
    "v_lshlrev_b32 v18, 16, v209\n"
    "v_and_b32 v19, 0xffff0000, v209\n"
    "v_lshlrev_b32 v20, 16, v210\n"
    "v_and_b32 v21, 0xffff0000, v210\n"
    "v_lshlrev_b32 v22, 16, v211\n"
    "v_and_b32 v23, 0xffff0000, v211\n"
    "v_pk_fma_f32 v[16:17], v[16:17], v[52:53], v[54:55]\n"
    "v_pk_fma_f32 v[18:19], v[18:19], v[52:53], v[54:55]\n"
    "v_pk_fma_f32 v[20:21], v[20:21], v[52:53], v[54:55]\n"
    "v_pk_fma_f32 v[22:23], v[22:23], v[52:53], v[54:55]\n"
    "v_cvt_pk_bf16_f32 v208, v16, v17\n"
    "v_cvt_pk_bf16_f32 v209, v18, v19\n"
    "v_cvt_pk_bf16_f32 v210, v20, v21\n"
    "v_cvt_pk_bf16_f32 v211, v22, v23\n"
    "buffer_load_dwordx4 v14, s[48:51], s90 offen offset:3072 lds\n"
    "v_lshlrev_b32 v16, 16, v212\n"
    "v_and_b32 v17, 0xffff0000, v212\n"
    "v_lshlrev_b32 v18, 16, v213\n"
    "v_and_b32 v19, 0xffff0000, v213\n"
    "v_lshlrev_b32 v20, 16, v214\n"
    "v_and_b32 v21, 0xffff0000, v214\n"
    "v_lshlrev_b32 v22, 16, v215\n"
    "v_and_b32 v23, 0xffff0000, v215\n"
    "v_pk_fma_f32 v[16:17], v[16:17], v[52:53], v[54:55]\n"
    "v_pk_fma_f32 v[18:19], v[18:19], v[52:53], v[54:55]\n"
    "v_pk_fma_f32 v[20:21], v[20:21], v[52:53], v[54:55]\n"
    "v_pk_fma_f32 v[22:23], v[22:23], v[52:53], v[54:55]\n"
    "v_cvt_pk_bf16_f32 v212, v16, v17\n"
    "v_cvt_pk_bf16_f32 v213, v18, v19\n"
    "v_cvt_pk_bf16_f32 v214, v20, v21\n"
    "v_cvt_pk_bf16_f32 v215, v22, v23\n"
    "s_add_u32 m0, s91, 143360\n"
    "s_nop 0\n"
    "buffer_load_dwordx4 v15, s[48:51], s90 offen offset:0 lds\n"
    "v_lshlrev_b32 v16, 16, v216\n"
    "v_and_b32 v17, 0xffff0000, v216\n"
    "v_lshlrev_b32 v18, 16, v217\n"
    "v_and_b32 v19, 0xffff0000, v217\n"
    "v_lshlrev_b32 v20, 16, v218\n"
    "v_and_b32 v21, 0xffff0000, v218\n"
    "v_lshlrev_b32 v22, 16, v219\n"
    "v_and_b32 v23, 0xffff0000, v219\n"
    "v_pk_fma_f32 v[16:17], v[16:17], v[52:53], v[54:55]\n"
    "v_pk_fma_f32 v[18:19], v[18:19], v[52:53], v[54:55]\n"
    "v_pk_fma_f32 v[20:21], v[20:21], v[52:53], v[54:55]\n"
    "v_pk_fma_f32 v[22:23], v[22:23], v[52:53], v[54:55]\n"
    "v_cvt_pk_bf16_f32 v216, v16, v17\n"
    "v_cvt_pk_bf16_f32 v217, v18, v19\n"
    "v_cvt_pk_bf16_f32 v218, v20, v21\n"
    "v_cvt_pk_bf16_f32 v219, v22, v23\n"
    "v_lshlrev_b32 v16, 16, v220\n"
    "v_and_b32 v17, 0xffff0000, v220\n"
    "v_lshlrev_b32 v18, 16, v221\n"
    "v_and_b32 v19, 0xffff0000, v221\n"
    "v_lshlrev_b32 v20, 16, v222\n"
    "v_and_b32 v21, 0xffff0000, v222\n"
    "v_lshlrev_b32 v22, 16, v223\n"
    "v_and_b32 v23, 0xffff0000, v223\n"
    "v_pk_fma_f32 v[16:17], v[16:17], v[52:53], v[54:55]\n"
    "v_pk_fma_f32 v[18:19], v[18:19], v[52:53], v[54:55]\n"
    "v_pk_fma_f32 v[20:21], v[20:21], v[52:53], v[54:55]\n"
    "v_pk_fma_f32 v[22:23], v[22:23], v[52:53], v[54:55]\n"
    "v_cvt_pk_bf16_f32 v220, v16, v17\n"
    "v_cvt_pk_bf16_f32 v221, v18, v19\n"
    "v_cvt_pk_bf16_f32 v222, v20, v21\n"
    "v_cvt_pk_bf16_f32 v223, v22, v23\n"
    "s_add_u32 s90, s90, 163840\n"
    "v_lshlrev_b32 v16, 16, v224\n"
    "v_and_b32 v17, 0xffff0000, v224\n"
    "v_lshlrev_b32 v18, 16, v225\n"
    "v_and_b32 v19, 0xffff0000, v225\n"
    "v_lshlrev_b32 v20, 16, v226\n"
    "v_and_b32 v21, 0xffff0000, v226\n"
    "v_lshlrev_b32 v22, 16, v227\n"
    "v_and_b32 v23, 0xffff0000, v227\n"
    "v_pk_fma_f32 v[16:17], v[16:17], v[52:53], v[54:55]\n"
    "v_pk_fma_f32 v[18:19], v[18:19], v[52:53], v[54:55]\n"
    "v_pk_fma_f32 v[20:21], v[20:21], v[52:53], v[54:55]\n"
    "v_pk_fma_f32 v[22:23], v[22:23], v[52:53], v[54:55]\n"
    "v_cvt_pk_bf16_f32 v224, v16, v17\n"
    "v_cvt_pk_bf16_f32 v225, v18, v19\n"
    "v_cvt_pk_bf16_f32 v226, v20, v21\n"
    "v_cvt_pk_bf16_f32 v227, v22, v23\n"
    "s_add_u32 m0, s91, 0\n"
    "s_nop 0\n"
    "buffer_load_dwordx4 v11, s[48:51], s90 offen offset:0 lds\n"
    "v_lshlrev_b32 v16, 16, v228\n"
    "v_and_b32 v17, 0xffff0000, v228\n"
    "v_lshlrev_b32 v18, 16, v229\n"
    "v_and_b32 v19, 0xffff0000, v229\n"
    "v_lshlrev_b32 v20, 16, v230\n"
    "v_and_b32 v21, 0xffff0000, v230\n"
    "v_lshlrev_b32 v22, 16, v231\n"
    "v_and_b32 v23, 0xffff0000, v231\n"
    "v_pk_fma_f32 v[16:17], v[16:17], v[52:53], v[54:55]\n"
    "v_pk_fma_f32 v[18:19], v[18:19], v[52:53], v[54:55]\n"
    "v_pk_fma_f32 v[20:21], v[20:21], v[52:53], v[54:55]\n"
    "v_pk_fma_f32 v[22:23], v[22:23], v[52:53], v[54:55]\n"
    "v_cvt_pk_bf16_f32 v228, v16, v17\n"
    "v_cvt_pk_bf16_f32 v229, v18, v19\n"
    "v_cvt_pk_bf16_f32 v230, v20, v21\n"
    "v_cvt_pk_bf16_f32 v231, v22, v23\n"
    "buffer_load_dwordx4 v12, s[48:51], s90 offen offset:1024 lds\n"
    "v_lshlrev_b32 v16, 16, v232\n"
    "v_and_b32 v17, 0xffff0000, v232\n"
    "v_lshlrev_b32 v18, 16, v233\n"
    "v_and_b32 v19, 0xffff0000, v233\n"
    "v_lshlrev_b32 v20, 16, v234\n"
    "v_and_b32 v21, 0xffff0000, v234\n"
    "v_lshlrev_b32 v22, 16, v235\n"
    "v_and_b32 v23, 0xffff0000, v235\n"
    "v_pk_fma_f32 v[16:17], v[16:17], v[52:53], v[54:55]\n"
    "v_pk_fma_f32 v[18:19], v[18:19], v[52:53], v[54:55]\n"
    "v_pk_fma_f32 v[20:21], v[20:21], v[52:53], v[54:55]\n"
    "v_pk_fma_f32 v[22:23], v[22:23], v[52:53], v[54:55]\n"
    "v_cvt_pk_bf16_f32 v232, v16, v17\n"
    "v_cvt_pk_bf16_f32 v233, v18, v19\n"
    "v_cvt_pk_bf16_f32 v234, v20, v21\n"
    "v_cvt_pk_bf16_f32 v235, v22, v23\n"
    "buffer_load_dwordx4 v13, s[48:51], s90 offen offset:2048 lds\n"
    "v_lshlrev_b32 v16, 16, v236\n"
    "v_and_b32 v17, 0xffff0000, v236\n"
    "v_lshlrev_b32 v18, 16, v237\n"
    "v_and_b32 v19, 0xffff0000, v237\n"
    "v_lshlrev_b32 v20, 16, v238\n"
    "v_and_b32 v21, 0xffff0000, v238\n"
    "v_lshlrev_b32 v22, 16, v239\n"
    "v_and_b32 v23, 0xffff0000, v239\n"
    "v_pk_fma_f32 v[16:17], v[16:17], v[52:53], v[54:55]\n"
    "v_pk_fma_f32 v[18:19], v[18:19], v[52:53], v[54:55]\n"
    "v_pk_fma_f32 v[20:21], v[20:21], v[52:53], v[54:55]\n"
    "v_pk_fma_f32 v[22:23], v[22:23], v[52:53], v[54:55]\n"
    "v_cvt_pk_bf16_f32 v236, v16, v17\n"
    "v_cvt_pk_bf16_f32 v237, v18, v19\n"
    "v_cvt_pk_bf16_f32 v238, v20, v21\n"
    "v_cvt_pk_bf16_f32 v239, v22, v23\n"
    "buffer_load_dwordx4 v14, s[48:51], s90 offen offset:3072 lds\n"
    "v_lshlrev_b32 v16, 16, v240\n"
    "v_and_b32 v17, 0xffff0000, v240\n"
    "v_lshlrev_b32 v18, 16, v241\n"
    "v_and_b32 v19, 0xffff0000, v241\n"
    "v_lshlrev_b32 v20, 16, v242\n"
    "v_and_b32 v21, 0xffff0000, v242\n"
    "v_lshlrev_b32 v22, 16, v243\n"
    "v_and_b32 v23, 0xffff0000, v243\n"
    "v_pk_fma_f32 v[16:17], v[16:17], v[52:53], v[54:55]\n"
    "v_pk_fma_f32 v[18:19], v[18:19], v[52:53], v[54:55]\n"
    "v_pk_fma_f32 v[20:21], v[20:21], v[52:53], v[54:55]\n"
    "v_pk_fma_f32 v[22:23], v[22:23], v[52:53], v[54:55]\n"
    "v_cvt_pk_bf16_f32 v240, v16, v17\n"
    "v_cvt_pk_bf16_f32 v241, v18, v19\n"
    "v_cvt_pk_bf16_f32 v242, v20, v21\n"
    "v_cvt_pk_bf16_f32 v243, v22, v23\n"
    "s_add_u32 m0, s91, 4096\n"
    "s_nop 0\n"
    "buffer_load_dwordx4 v15, s[48:51], s90 offen offset:0 lds\n"
    "v_lshlrev_b32 v16, 16, v244\n"
    "v_and_b32 v17, 0xffff0000, v244\n"
    "v_lshlrev_b32 v18, 16, v245\n"
    "v_and_b32 v19, 0xffff0000, v245\n"
    "v_lshlrev_b32 v20, 16, v246\n"
    "v_and_b32 v21, 0xffff0000, v246\n"
    "v_lshlrev_b32 v22, 16, v247\n"
    "v_and_b32 v23, 0xffff0000, v247\n"
    "v_pk_fma_f32 v[16:17], v[16:17], v[52:53], v[54:55]\n"
    "v_pk_fma_f32 v[18:19], v[18:19], v[52:53], v[54:55]\n"
    "v_pk_fma_f32 v[20:21], v[20:21], v[52:53], v[54:55]\n"
    "v_pk_fma_f32 v[22:23], v[22:23], v[52:53], v[54:55]\n"
    "v_cvt_pk_bf16_f32 v244, v16, v17\n"
    "v_cvt_pk_bf16_f32 v245, v18, v19\n"
    "v_cvt_pk_bf16_f32 v246, v20, v21\n"
    "v_cvt_pk_bf16_f32 v247, v22, v23\n"
    "s_add_u32 s90, s90, 163840\n"
    "v_lshlrev_b32 v16, 16, v248\n"
    "v_and_b32 v17, 0xffff0000, v248\n"
    "v_lshlrev_b32 v18, 16, v249\n"
    "v_and_b32 v19, 0xffff0000, v249\n"
    "v_lshlrev_b32 v20, 16, v250\n"
    "v_and_b32 v21, 0xffff0000, v250\n"
    "v_lshlrev_b32 v22, 16, v251\n"
    "v_and_b32 v23, 0xffff0000, v251\n"
    "v_pk_fma_f32 v[16:17], v[16:17], v[52:53], v[54:55]\n"
    "v_pk_fma_f32 v[18:19], v[18:19], v[52:53], v[54:55]\n"
    "v_pk_fma_f32 v[20:21], v[20:21], v[52:53], v[54:55]\n"
    "v_pk_fma_f32 v[22:23], v[22:23], v[52:53], v[54:55]\n"
    "v_cvt_pk_bf16_f32 v248, v16, v17\n"
    "v_cvt_pk_bf16_f32 v249, v18, v19\n"
    "v_cvt_pk_bf16_f32 v250, v20, v21\n"
    "v_cvt_pk_bf16_f32 v251, v22, v23\n"
    "v_lshlrev_b32 v16, 16, v252\n"
    "v_and_b32 v17, 0xffff0000, v252\n"
    "v_lshlrev_b32 v18, 16, v253\n"
    "v_and_b32 v19, 0xffff0000, v253\n"
    "v_lshlrev_b32 v20, 16, v254\n"
    "v_and_b32 v21, 0xffff0000, v254\n"
    "v_lshlrev_b32 v22, 16, v255\n"
    "v_and_b32 v23, 0xffff0000, v255\n"
    "v_pk_fma_f32 v[16:17], v[16:17], v[52:53], v[54:55]\n"
    "v_pk_fma_f32 v[18:19], v[18:19], v[52:53], v[54:55]\n"
    "v_pk_fma_f32 v[20:21], v[20:21], v[52:53], v[54:55]\n"
    "v_pk_fma_f32 v[22:23], v[22:23], v[52:53], v[54:55]\n"
    "v_cvt_pk_bf16_f32 v252, v16, v17\n"
    "v_cvt_pk_bf16_f32 v253, v18, v19\n"
    "v_cvt_pk_bf16_f32 v254, v20, v21\n"
    "v_cvt_pk_bf16_f32 v255, v22, v23\n"
    "s_waitcnt vmcnt(10)\n"
    "s_barrier\n"
    "L_g1:\n"
    "ds_read_b64_tr_b16 a[192:193], v60 offset:0\n"
    "ds_read_b64_tr_b16 a[194:195], v60 offset:2560\n"
    "ds_read_b64_tr_b16 a[196:197], v60 offset:5120\n"
    "ds_read_b64_tr_b16 a[198:199], v60 offset:7680\n"
    "ds_read_b64_tr_b16 a[200:201], v60 offset:32\n"
    "ds_read_b64_tr_b16 a[202:203], v60 offset:2592\n"
    "ds_read_b64_tr_b16 a[204:205], v60 offset:5152\n"
    "ds_read_b64_tr_b16 a[206:207], v60 offset:7712\n"
    "ds_read_b64_tr_b16 a[208:209], v60 offset:64\n"
    "ds_read_b64_tr_b16 a[210:211], v60 offset:2624\n"
    "ds_read_b64_tr_b16 a[212:213], v60 offset:5184\n"
    "ds_read_b64_tr_b16 a[214:215], v60 offset:7744\n"
    "s_lshl_b32 s73, s82, 3\n"
    "s_add_u32 s73, s73, s78\n"
    "s_lshl_b32 s95, s73, 6\n"
    "s_add_u32 s95, s95, 4096\n"
    "v_mov_b32 v62, s80\n"
    "v_mov_b32 v63, s81\n"
    "buffer_store_dwordx2 v[62:63], v10, s[52:55], s95 offen sc1\n"
    "s_lshl_b32 s73, s78, 5\n"
    "s_add_u32 s73, s73, s82\n"
    "s_mul_i32 s73, s73, 3200\n"
    "s_mov_b64 s[84:85], s[16:17]\n"
    "s_add_u32 s84, s84, s73\n"
    "s_addc_u32 s85, s85, 0\n"
    "s_mov_b32 s86, 3200\n"
    "s_mov_b32 s87, 0x20000\n"
    "s_waitcnt lgkmcnt(10)\n"
    "v_mfma_f32_16x16x32_bf16 v[64:67], a[192:195], v[176:179], 0\n"
    "s_waitcnt lgkmcnt(8)\n"
    "ds_read_b64_tr_b16 a[216:217], v60 offset:96\n"
    "ds_read_b64_tr_b16 a[218:219], v60 offset:2656\n"
    "v_mfma_f32_16x16x32_bf16 v[64:67], a[196:199], v[180:183], v[64:67]\n"
    "s_waitcnt lgkmcnt(8)\n"
    "ds_read_b64_tr_b16 a[220:221], v60 offset:5216\n"
    "ds_read_b64_tr_b16 a[222:223], v60 offset:7776\n"
    "s_add_u32 m0, s91, 20480\n"
    "s_nop 0\n"
    "buffer_load_dwordx4 v11, s[48:51], s90 offen offset:0 lds\n"
    "v_mfma_f32_16x16x32_bf16 v[68:71], a[200:203], v[176:179], 0\n"
    "s_waitcnt lgkmcnt(8)\n"
    "ds_read_b64_tr_b16 a[224:225], v60 offset:128\n"
    "ds_read_b64_tr_b16 a[226:227], v60 offset:2688\n"
    "v_mfma_f32_16x16x32_bf16 v[68:71], a[204:207], v[180:183], v[68:71]\n"
    "s_waitcnt lgkmcnt(8)\n"
    "ds_read_b64_tr_b16 a[228:229], v60 offset:5248\n"
    "ds_read_b64_tr_b16 a[230:231], v60 offset:7808\n"
    "v_mfma_f32_16x16x32_bf16 v[72:75], a[208:211], v[176:179], 0\n"
    "s_waitcnt lgkmcnt(8)\n"
    "ds_read_b64_tr_b16 a[232:233], v60 offset:10240\n"
    "ds_read_b64_tr_b16 a[234:235], v60 offset:12800\n"
    "v_mfma_f32_16x16x32_bf16 v[72:75], a[212:215], v[180:183], v[72:75]\n"
    "s_waitcnt lgkmcnt(8)\n"
    "ds_read_b64_tr_b16 a[236:237], v60 offset:15360\n"
    "ds_read_b64_tr_b16 a[238:239], v60 offset:17920\n"
    "buffer_load_dwordx4 v12, s[48:51], s90 offen offset:1024 lds\n"
    "v_mfma_f32_16x16x32_bf16 v[76:79], a[216:219], v[176:179], 0\n"
    "s_waitcnt lgkmcnt(8)\n"
    "ds_read_b64_tr_b16 a[192:193], v60 offset:10272\n"
    "ds_read_b64_tr_b16 a[194:195], v60 offset:12832\n"
    "v_mfma_f32_16x16x32_bf16 v[76:79], a[220:223], v[180:183], v[76:79]\n"
    "s_waitcnt lgkmcnt(8)\n"
    "ds_read_b64_tr_b16 a[196:197], v60 offset:15392\n"
    "ds_read_b64_tr_b16 a[198:199], v60 offset:17952\n"
    "v_mfma_f32_16x16x32_bf16 v[80:83], a[224:227], v[176:179], 0\n"
    "s_waitcnt lgkmcnt(8)\n"
    "ds_read_b64_tr_b16 a[200:201], v60 offset:10304\n"
    "ds_read_b64_tr_b16 a[202:203], v60 offset:12864\n"
    "v_mfma_f32_16x16x32_bf16 v[80:83], a[228:231], v[180:183], v[80:83]\n"
    "s_waitcnt lgkmcnt(8)\n"
    "ds_read_b64_tr_b16 a[204:205], v60 offset:15424\n"
    "ds_read_b64_tr_b16 a[206:207], v60 offset:17984\n"
    "buffer_load_dwordx4 v13, s[48:51], s90 offen offset:2048 lds\n"
    "v_mfma_f32_16x16x32_bf16 v[64:67], a[232:235], v[184:187], v[64:67]\n"
    "s_waitcnt lgkmcnt(8)\n"
    "ds_read_b64_tr_b16 a[208:209], v60 offset:10336\n"
    "ds_read_b64_tr_b16 a[210:211], v60 offset:12896\n"
    "v_mfma_f32_16x16x32_bf16 v[64:67], a[236:239], v[188:191], v[64:67]\n"
    "s_waitcnt lgkmcnt(8)\n"
    "ds_read_b64_tr_b16 a[212:213], v60 offset:15456\n"
    "ds_read_b64_tr_b16 a[214:215], v60 offset:18016\n"
    "v_mfma_f32_16x16x32_bf16 v[68:71], a[192:195], v[184:187], v[68:71]\n"
    "s_waitcnt lgkmcnt(8)\n"
    "ds_read_b64_tr_b16 a[216:217], v60 offset:10368\n"
    "ds_read_b64_tr_b16 a[218:219], v60 offset:12928\n"
    "v_mfma_f32_16x16x32_bf16 v[68:71], a[196:199], v[188:191], v[68:71]\n"
    "s_waitcnt lgkmcnt(8)\n"
    "ds_read_b64_tr_b16 a[220:221], v60 offset:15488\n"
    "ds_read_b64_tr_b16 a[222:223], v60 offset:18048\n"
    "buffer_load_dwordx4 v14, s[48:51], s90 offen offset:3072 lds\n"
    "v_mfma_f32_16x16x32_bf16 v[72:75], a[200:203], v[184:187], v[72:75]\n"
    "s_waitcnt lgkmcnt(8)\n"
    "ds_read_b64_tr_b16 a[224:225], v60 offset:20480\n"
    "ds_read_b64_tr_b16 a[226:227], v60 offset:23040\n"
    "v_mfma_f32_16x16x32_bf16 v[72:75], a[204:207], v[188:191], v[72:75]\n"
    "s_waitcnt lgkmcnt(8)\n"
    "ds_read_b64_tr_b16 a[228:229], v60 offset:25600\n"
    "ds_read_b64_tr_b16 a[230:231], v60 offset:28160\n"
    "v_mfma_f32_16x16x32_bf16 v[76:79], a[208:211], v[184:187], v[76:79]\n"
    "s_waitcnt lgkmcnt(8)\n"
    "ds_read_b64_tr_b16 a[232:233], v60 offset:20512\n"
    "ds_read_b64_tr_b16 a[234:235], v60 offset:23072\n"
    "v_mfma_f32_16x16x32_bf16 v[76:79], a[212:215], v[188:191], v[76:79]\n"
    "s_waitcnt lgkmcnt(8)\n"
    "ds_read_b64_tr_b16 a[236:237], v60 offset:25632\n"
    "ds_read_b64_tr_b16 a[238:239], v60 offset:28192\n"
    "s_add_u32 m0, s91, 24576\n"
    "s_nop 0\n"
    "buffer_load_dwordx4 v15, s[48:51], s90 offen offset:0 lds\n"
    "v_mfma_f32_16x16x32_bf16 v[80:83], a[216:219], v[184:187], v[80:83]\n"
    "s_waitcnt lgkmcnt(8)\n"
    "ds_read_b64_tr_b16 a[192:193], v60 offset:20544\n"
    "ds_read_b64_tr_b16 a[194:195], v60 offset:23104\n"
    "v_mfma_f32_16x16x32_bf16 v[80:83], a[220:223], v[188:191], v[80:83]\n"
    "s_waitcnt lgkmcnt(8)\n"
    "ds_read_b64_tr_b16 a[196:197], v60 offset:25664\n"
    "ds_read_b64_tr_b16 a[198:199], v60 offset:28224\n"
    "v_mfma_f32_16x16x32_bf16 v[64:67], a[224:227], v[192:195], v[64:67]\n"
    "s_waitcnt lgkmcnt(8)\n"
    "ds_read_b64_tr_b16 a[200:201], v60 offset:20576\n"
    "ds_read_b64_tr_b16 a[202:203], v60 offset:23136\n"
    "v_mfma_f32_16x16x32_bf16 v[64:67], a[228:231], v[196:199], v[64:67]\n"
    "s_waitcnt lgkmcnt(8)\n"
    "ds_read_b64_tr_b16 a[204:205], v60 offset:25696\n"
    "ds_read_b64_tr_b16 a[206:207], v60 offset:28256\n"
    "s_cmp_eq_u32 s78, 0\n"
    "s_cselect_b32 s93, 0, 0x80000000\n"
    "v_or_b32 v48, s93, v50\n"
    "buffer_load_dword v51, v48, s[44:47], 0 offen\n"
    "v_mfma_f32_16x16x32_bf16 v[68:71], a[232:235], v[192:195], v[68:71]\n"
    "s_waitcnt lgkmcnt(8)\n"
    "ds_read_b64_tr_b16 a[208:209], v60 offset:20608\n"
    "ds_read_b64_tr_b16 a[210:211], v60 offset:23168\n"
    "v_mfma_f32_16x16x32_bf16 v[68:71], a[236:239], v[196:199], v[68:71]\n"
    "s_waitcnt lgkmcnt(8)\n"
    "ds_read_b64_tr_b16 a[212:213], v60 offset:25728\n"
    "ds_read_b64_tr_b16 a[214:215], v60 offset:28288\n"
    "v_mfma_f32_16x16x32_bf16 v[72:75], a[192:195], v[192:195], v[72:75]\n"
    "s_waitcnt lgkmcnt(8)\n"
    "ds_read_b64_tr_b16 a[216:217], v60 offset:30720\n"
    "ds_read_b64_tr_b16 a[218:219], v60 offset:33280\n"
    "v_mfma_f32_16x16x32_bf16 v[72:75], a[196:199], v[196:199], v[72:75]\n"
    "s_waitcnt lgkmcnt(8)\n"
    "ds_read_b64_tr_b16 a[220:221], v60 offset:35840\n"
    "ds_read_b64_tr_b16 a[222:223], v60 offset:38400\n"
    "v_lshl_or_b32 v48, %2, 7, s93\n"
    "buffer_load_dword v51, v48, s[84:87], 0 offen\n"
    "v_mfma_f32_16x16x32_bf16 v[76:79], a[200:203], v[192:195], v[76:79]\n"
    "s_waitcnt lgkmcnt(8)\n"
    "ds_read_b64_tr_b16 a[224:225], v60 offset:30752\n"
    "ds_read_b64_tr_b16 a[226:227], v60 offset:33312\n"
    "v_mfma_f32_16x16x32_bf16 v[76:79], a[204:207], v[196:199], v[76:79]\n"
    "s_waitcnt lgkmcnt(8)\n"
    "ds_read_b64_tr_b16 a[228:229], v60 offset:35872\n"
    "ds_read_b64_tr_b16 a[230:231], v60 offset:38432\n"
    "v_mfma_f32_16x16x32_bf16 v[80:83], a[208:211], v[192:195], v[80:83]\n"
    "s_waitcnt lgkmcnt(8)\n"
    "ds_read_b64_tr_b16 a[232:233], v60 offset:30784\n"
    "ds_read_b64_tr_b16 a[234:235], v60 offset:33344\n"
    "v_mfma_f32_16x16x32_bf16 v[80:83], a[212:215], v[196:199], v[80:83]\n"
    "s_waitcnt lgkmcnt(8)\n"
    "ds_read_b64_tr_b16 a[236:237], v60 offset:35904\n"
    "ds_read_b64_tr_b16 a[238:239], v60 offset:38464\n"
    "s_cmp_eq_u32 s78, 1\n"
    "s_cselect_b32 s93, 0, 0x80000000\n"
    "v_or_b32 v48, s93, v50\n"
    "buffer_load_dword v51, v48, s[44:47], 0 offen\n"
    "v_mfma_f32_16x16x32_bf16 v[64:67], a[216:219], v[200:203], v[64:67]\n"
    "s_waitcnt lgkmcnt(8)\n"
    "ds_read_b64_tr_b16 a[192:193], v60 offset:30816\n"
    "ds_read_b64_tr_b16 a[194:195], v60 offset:33376\n"
    "v_mfma_f32_16x16x32_bf16 v[64:67], a[220:223], v[204:207], v[64:67]\n"
    "s_waitcnt lgkmcnt(8)\n"
    "ds_read_b64_tr_b16 a[196:197], v60 offset:35936\n"
    "ds_read_b64_tr_b16 a[198:199], v60 offset:38496\n"
    "v_mfma_f32_16x16x32_bf16 v[68:71], a[224:227], v[200:203], v[68:71]\n"
    "s_waitcnt lgkmcnt(8)\n"
    "ds_read_b64_tr_b16 a[200:201], v60 offset:30848\n"
    "ds_read_b64_tr_b16 a[202:203], v60 offset:33408\n"
    "v_mfma_f32_16x16x32_bf16 v[68:71], a[228:231], v[204:207], v[68:71]\n"
    "s_waitcnt lgkmcnt(8)\n"
    "ds_read_b64_tr_b16 a[204:205], v60 offset:35968\n"
    "ds_read_b64_tr_b16 a[206:207], v60 offset:38528\n"
    "v_lshl_or_b32 v48, %2, 7, s93\n"
    "buffer_load_dword v51, v48, s[84:87], 0 offen\n"
    "s_waitcnt vmcnt(10)\n"
    "s_barrier\n"
    "v_mfma_f32_16x16x32_bf16 v[72:75], a[232:235], v[200:203], v[72:75]\n"
    "s_waitcnt lgkmcnt(8)\n"
    "ds_read_b64_tr_b16 a[208:209], v60 offset:40960\n"
    "ds_read_b64_tr_b16 a[210:211], v60 offset:43520\n"
    "v_mfma_f32_16x16x32_bf16 v[72:75], a[236:239], v[204:207], v[72:75]\n"
    "s_waitcnt lgkmcnt(8)\n"
    "ds_read_b64_tr_b16 a[212:213], v60 offset:46080\n"
    "ds_read_b64_tr_b16 a[214:215], v60 offset:48640\n"
    "v_mfma_f32_16x16x32_bf16 v[76:79], a[192:195], v[200:203], v[76:79]\n"
    "s_waitcnt lgkmcnt(8)\n"
    "ds_read_b64_tr_b16 a[216:217], v60 offset:40992\n"
    "ds_read_b64_tr_b16 a[218:219], v60 offset:43552\n"
    "v_mfma_f32_16x16x32_bf16 v[76:79], a[196:199], v[204:207], v[76:79]\n"
    "s_waitcnt lgkmcnt(8)\n"
    "ds_read_b64_tr_b16 a[220:221], v60 offset:46112\n"
    "ds_read_b64_tr_b16 a[222:223], v60 offset:48672\n"
    "s_add_u32 m0, s68, 49152\n"
    "s_nop 0\n"
    "buffer_load_dwordx4 v59, s[44:47], s62 offen offset:0 lds\n"
    "v_mfma_f32_16x16x32_bf16 v[80:83], a[200:203], v[200:203], v[80:83]\n"
    "s_waitcnt lgkmcnt(8)\n"
    "ds_read_b64_tr_b16 a[224:225], v60 offset:41024\n"
    "ds_read_b64_tr_b16 a[226:227], v60 offset:43584\n"
    "v_mfma_f32_16x16x32_bf16 v[80:83], a[204:207], v[204:207], v[80:83]\n"
    "s_waitcnt lgkmcnt(8)\n"
    "ds_read_b64_tr_b16 a[228:229], v60 offset:46144\n"
    "ds_read_b64_tr_b16 a[230:231], v60 offset:48704\n"
    "v_mfma_f32_16x16x32_bf16 v[64:67], a[208:211], v[208:211], v[64:67]\n"
    "s_waitcnt lgkmcnt(8)\n"
    "ds_read_b64_tr_b16 a[232:233], v60 offset:41056\n"
    "ds_read_b64_tr_b16 a[234:235], v60 offset:43616\n"
    "v_mfma_f32_16x16x32_bf16 v[64:67], a[212:215], v[212:215], v[64:67]\n"
    "s_waitcnt lgkmcnt(8)\n"
    "ds_read_b64_tr_b16 a[236:237], v60 offset:46176\n"
    "ds_read_b64_tr_b16 a[238:239], v60 offset:48736\n"
    "buffer_load_dwordx4 v59, s[44:47], s63 offen offset:1024 lds\n"
    "v_mfma_f32_16x16x32_bf16 v[68:71], a[216:219], v[208:211], v[68:71]\n"
    "s_waitcnt lgkmcnt(8)\n"
    "ds_read_b64_tr_b16 a[192:193], v60 offset:41088\n"
    "ds_read_b64_tr_b16 a[194:195], v60 offset:43648\n"
    "v_mfma_f32_16x16x32_bf16 v[68:71], a[220:223], v[212:215], v[68:71]\n"
    "s_waitcnt lgkmcnt(8)\n"
    "ds_read_b64_tr_b16 a[196:197], v60 offset:46208\n"
    "ds_read_b64_tr_b16 a[198:199], v60 offset:48768\n"
    "v_mfma_f32_16x16x32_bf16 v[72:75], a[224:227], v[208:211], v[72:75]\n"
    "s_waitcnt lgkmcnt(8)\n"
    "ds_read_b64_tr_b16 a[200:201], v60 offset:51200\n"
    "ds_read_b64_tr_b16 a[202:203], v60 offset:53760\n"
    "v_mfma_f32_16x16x32_bf16 v[72:75], a[228:231], v[212:215], v[72:75]\n"
    "s_waitcnt lgkmcnt(8)\n"
    "ds_read_b64_tr_b16 a[204:205], v60 offset:56320\n"
    "ds_read_b64_tr_b16 a[206:207], v60 offset:58880\n"
    "s_add_u32 m0, s68, 51200\n"
    "s_nop 0\n"
    "buffer_load_dwordx4 v59, s[44:47], s64 offen offset:0 lds\n"
    "v_mfma_f32_16x16x32_bf16 v[76:79], a[232:235], v[208:211], v[76:79]\n"
    "s_waitcnt lgkmcnt(8)\n"
    "ds_read_b64_tr_b16 a[208:209], v60 offset:51232\n"
    "ds_read_b64_tr_b16 a[210:211], v60 offset:53792\n"
    "v_mfma_f32_16x16x32_bf16 v[76:79], a[236:239], v[212:215], v[76:79]\n"
    "s_waitcnt lgkmcnt(8)\n"
    "ds_read_b64_tr_b16 a[212:213], v60 offset:56352\n"
    "ds_read_b64_tr_b16 a[214:215], v60 offset:58912\n"
    "v_mfma_f32_16x16x32_bf16 v[80:83], a[192:195], v[208:211], v[80:83]\n"
    "s_waitcnt lgkmcnt(8)\n"
    "ds_read_b64_tr_b16 a[216:217], v60 offset:51264\n"
    "ds_read_b64_tr_b16 a[218:219], v60 offset:53824\n"
    "v_mfma_f32_16x16x32_bf16 v[80:83], a[196:199], v[212:215], v[80:83]\n"
    "s_waitcnt lgkmcnt(8)\n"
    "ds_read_b64_tr_b16 a[220:221], v60 offset:56384\n"
    "ds_read_b64_tr_b16 a[222:223], v60 offset:58944\n"
    "buffer_load_dwordx4 v59, s[44:47], s65 offen offset:1024 lds\n"
    "v_mfma_f32_16x16x32_bf16 v[64:67], a[200:203], v[216:219], v[64:67]\n"
    "s_waitcnt lgkmcnt(8)\n"
    "ds_read_b64_tr_b16 a[224:225], v60 offset:51296\n"
    "ds_read_b64_tr_b16 a[226:227], v60 offset:53856\n"
    "v_mfma_f32_16x16x32_bf16 v[64:67], a[204:207], v[220:223], v[64:67]\n"
    "s_waitcnt lgkmcnt(8)\n"
    "ds_read_b64_tr_b16 a[228:229], v60 offset:56416\n"
    "ds_read_b64_tr_b16 a[230:231], v60 offset:58976\n"
    "v_mfma_f32_16x16x32_bf16 v[68:71], a[208:211], v[216:219], v[68:71]\n"
    "s_waitcnt lgkmcnt(8)\n"
    "ds_read_b64_tr_b16 a[232:233], v60 offset:51328\n"
    "ds_read_b64_tr_b16 a[234:235], v60 offset:53888\n"
    "v_mfma_f32_16x16x32_bf16 v[68:71], a[212:215], v[220:223], v[68:71]\n"
    "s_waitcnt lgkmcnt(8)\n"
    "ds_read_b64_tr_b16 a[236:237], v60 offset:56448\n"
    "ds_read_b64_tr_b16 a[238:239], v60 offset:59008\n"
    "s_add_u32 m0, s68, 53248\n"
    "s_nop 0\n"
    "buffer_load_dwordx4 v59, s[44:47], s66 offen offset:0 lds\n"
    "v_mfma_f32_16x16x32_bf16 v[72:75], a[216:219], v[216:219], v[72:75]\n"
    "s_waitcnt lgkmcnt(8)\n"
    "ds_read_b64_tr_b16 a[192:193], v61 offset:0\n"
    "ds_read_b64_tr_b16 a[194:195], v61 offset:2560\n"
    "v_mfma_f32_16x16x32_bf16 v[72:75], a[220:223], v[220:223], v[72:75]\n"
    "s_waitcnt lgkmcnt(8)\n"
    "ds_read_b64_tr_b16 a[196:197], v61 offset:5120\n"
    "ds_read_b64_tr_b16 a[198:199], v61 offset:7680\n"
    "v_mfma_f32_16x16x32_bf16 v[76:79], a[224:227], v[216:219], v[76:79]\n"
    "s_waitcnt lgkmcnt(8)\n"
    "ds_read_b64_tr_b16 a[200:201], v61 offset:32\n"
    "ds_read_b64_tr_b16 a[202:203], v61 offset:2592\n"
    "v_mfma_f32_16x16x32_bf16 v[76:79], a[228:231], v[220:223], v[76:79]\n"
    "s_waitcnt lgkmcnt(8)\n"
    "ds_read_b64_tr_b16 a[204:205], v61 offset:5152\n"
    "ds_read_b64_tr_b16 a[206:207], v61 offset:7712\n"
    "buffer_load_dwordx4 v59, s[44:47], s67 offen offset:1024 lds\n"
    "v_mfma_f32_16x16x32_bf16 v[80:83], a[232:235], v[216:219], v[80:83]\n"
    "s_waitcnt lgkmcnt(8)\n"
    "ds_read_b64_tr_b16 a[208:209], v61 offset:64\n"
    "ds_read_b64_tr_b16 a[210:211], v61 offset:2624\n"
    "v_mfma_f32_16x16x32_bf16 v[80:83], a[236:239], v[220:223], v[80:83]\n"
    "s_waitcnt lgkmcnt(8)\n"
    "ds_read_b64_tr_b16 a[212:213], v61 offset:5184\n"
    "ds_read_b64_tr_b16 a[214:215], v61 offset:7744\n"
    "v_mfma_f32_16x16x32_bf16 v[64:67], a[192:195], v[224:227], v[64:67]\n"
    "s_waitcnt lgkmcnt(8)\n"
    "ds_read_b64_tr_b16 a[216:217], v61 offset:96\n"
    "ds_read_b64_tr_b16 a[218:219], v61 offset:2656\n"
    "v_mfma_f32_16x16x32_bf16 v[64:67], a[196:199], v[228:231], v[64:67]\n"
    "s_waitcnt lgkmcnt(8)\n"
    "ds_read_b64_tr_b16 a[220:221], v61 offset:5216\n"
    "ds_read_b64_tr_b16 a[222:223], v61 offset:7776\n"
    "v_add_u32 v59, 786432, v59\n"
    "v_mfma_f32_16x16x32_bf16 v[68:71], a[200:203], v[224:227], v[68:71]\n"
    "s_waitcnt lgkmcnt(8)\n"
    "ds_read_b64_tr_b16 a[224:225], v61 offset:128\n"
    "ds_read_b64_tr_b16 a[226:227], v61 offset:2688\n"
    "s_lshl_b32 s73, s78, 2\n"
    "v_mfma_f32_16x16x32_bf16 v[68:71], a[204:207], v[228:231], v[68:71]\n"
    "s_waitcnt lgkmcnt(8)\n"
    "ds_read_b64_tr_b16 a[228:229], v61 offset:5248\n"
    "ds_read_b64_tr_b16 a[230:231], v61 offset:7808\n"
    "s_add_u32 s73, s73, s76\n"
    "v_mfma_f32_16x16x32_bf16 v[72:75], a[208:211], v[224:227], v[72:75]\n"
    "s_waitcnt lgkmcnt(8)\n"
    "ds_read_b64_tr_b16 a[232:233], v61 offset:10240\n"
    "ds_read_b64_tr_b16 a[234:235], v61 offset:12800\n"
    "s_mul_i32 s88, s73, 81920\n"
    "v_mfma_f32_16x16x32_bf16 v[72:75], a[212:215], v[228:231], v[72:75]\n"
    "s_waitcnt lgkmcnt(8)\n"
    "ds_read_b64_tr_b16 a[236:237], v61 offset:15360\n"
    "ds_read_b64_tr_b16 a[238:239], v61 offset:17920\n"
    "s_lshl_b32 s73, s73, 3\n"
    "v_mfma_f32_16x16x32_bf16 v[76:79], a[216:219], v[224:227], v[76:79]\n"
    "s_waitcnt lgkmcnt(8)\n"
    "ds_read_b64_tr_b16 a[192:193], v61 offset:10272\n"
    "ds_read_b64_tr_b16 a[194:195], v61 offset:12832\n"
    "v_lshrrev_b32 v56, 4, v9\n"
    "v_mfma_f32_16x16x32_bf16 v[76:79], a[220:223], v[228:231], v[76:79]\n"
    "s_waitcnt lgkmcnt(8)\n"
    "ds_read_b64_tr_b16 a[196:197], v61 offset:15392\n"
    "ds_read_b64_tr_b16 a[198:199], v61 offset:17952\n"
    "v_min_u32 v56, 7, v56\n"
    "v_mfma_f32_16x16x32_bf16 v[80:83], a[224:227], v[224:227], v[80:83]\n"
    "s_waitcnt lgkmcnt(8)\n"
    "ds_read_b64_tr_b16 a[200:201], v61 offset:10304\n"
    "ds_read_b64_tr_b16 a[202:203], v61 offset:12864\n"
    "v_add_u32 v56, s73, v56\n"
    "v_mfma_f32_16x16x32_bf16 v[80:83], a[228:231], v[228:231], v[80:83]\n"
    "s_waitcnt lgkmcnt(8)\n"
    "ds_read_b64_tr_b16 a[204:205], v61 offset:15424\n"
    "ds_read_b64_tr_b16 a[206:207], v61 offset:17984\n"
    "v_lshlrev_b32 v56, 6, v56\n"
    "v_mfma_f32_16x16x32_bf16 v[64:67], a[232:235], v[232:235], v[64:67]\n"
    "s_waitcnt lgkmcnt(8)\n"
    "ds_read_b64_tr_b16 a[208:209], v61 offset:10336\n"
    "ds_read_b64_tr_b16 a[210:211], v61 offset:12896\n"
    "v_add_u32 v56, 4096, v56\n"
    "v_mfma_f32_16x16x32_bf16 v[64:67], a[236:239], v[236:239], v[64:67]\n"
    "s_waitcnt lgkmcnt(8)\n"
    "ds_read_b64_tr_b16 a[212:213], v61 offset:15456\n"
    "ds_read_b64_tr_b16 a[214:215], v61 offset:18016\n"
    "buffer_load_dwordx2 v[62:63], v56, s[52:55], 0 offen sc1\n"
    "v_mfma_f32_16x16x32_bf16 v[68:71], a[192:195], v[232:235], v[68:71]\n"
    "s_waitcnt lgkmcnt(8)\n"
    "ds_read_b64_tr_b16 a[216:217], v61 offset:10368\n"
    "ds_read_b64_tr_b16 a[218:219], v61 offset:12928\n"
    "v_mfma_f32_16x16x32_bf16 v[68:71], a[196:199], v[236:239], v[68:71]\n"
    "s_waitcnt lgkmcnt(8)\n"
    "ds_read_b64_tr_b16 a[220:221], v61 offset:15488\n"
    "ds_read_b64_tr_b16 a[222:223], v61 offset:18048\n"
    "s_waitcnt vmcnt(11)\n"
    "s_barrier\n"
    "v_mfma_f32_16x16x32_bf16 v[72:75], a[200:203], v[232:235], v[72:75]\n"
    "s_waitcnt lgkmcnt(8)\n"
    "ds_read_b64_tr_b16 a[224:225], v61 offset:20480\n"
    "ds_read_b64_tr_b16 a[226:227], v61 offset:23040\n"
    "v_mfma_f32_16x16x32_bf16 v[72:75], a[204:207], v[236:239], v[72:75]\n"
    "s_waitcnt lgkmcnt(8)\n"
    "ds_read_b64_tr_b16 a[228:229], v61 offset:25600\n"
    "ds_read_b64_tr_b16 a[230:231], v61 offset:28160\n"
    "v_mfma_f32_16x16x32_bf16 v[76:79], a[208:211], v[232:235], v[76:79]\n"
    "s_waitcnt lgkmcnt(8)\n"
    "ds_read_b64_tr_b16 a[232:233], v61 offset:20512\n"
    "ds_read_b64_tr_b16 a[234:235], v61 offset:23072\n"
    "v_mfma_f32_16x16x32_bf16 v[76:79], a[212:215], v[236:239], v[76:79]\n"
    "s_waitcnt lgkmcnt(8)\n"
    "ds_read_b64_tr_b16 a[236:237], v61 offset:25632\n"
    "ds_read_b64_tr_b16 a[238:239], v61 offset:28192\n"
    "v_mfma_f32_16x16x32_bf16 v[80:83], a[216:219], v[232:235], v[80:83]\n"
    "s_waitcnt lgkmcnt(8)\n"
    "ds_read_b64_tr_b16 a[192:193], v61 offset:20544\n"
    "ds_read_b64_tr_b16 a[194:195], v61 offset:23104\n"
    "v_mfma_f32_16x16x32_bf16 v[80:83], a[220:223], v[236:239], v[80:83]\n"
    "s_waitcnt lgkmcnt(8)\n"
    "ds_read_b64_tr_b16 a[196:197], v61 offset:25664\n"
    "ds_read_b64_tr_b16 a[198:199], v61 offset:28224\n"
    "v_mfma_f32_16x16x32_bf16 v[64:67], a[224:227], v[240:243], v[64:67]\n"
    "s_waitcnt lgkmcnt(8)\n"
    "ds_read_b64_tr_b16 a[200:201], v61 offset:20576\n"
    "ds_read_b64_tr_b16 a[202:203], v61 offset:23136\n"
    "v_mfma_f32_16x16x32_bf16 v[64:67], a[228:231], v[244:247], v[64:67]\n"
    "s_waitcnt lgkmcnt(8)\n"
    "ds_read_b64_tr_b16 a[204:205], v61 offset:25696\n"
    "ds_read_b64_tr_b16 a[206:207], v61 offset:28256\n"
    "v_mfma_f32_16x16x32_bf16 v[68:71], a[232:235], v[240:243], v[68:71]\n"
    "s_waitcnt lgkmcnt(8)\n"
    "ds_read_b64_tr_b16 a[208:209], v61 offset:20608\n"
    "ds_read_b64_tr_b16 a[210:211], v61 offset:23168\n"
    "v_mfma_f32_16x16x32_bf16 v[68:71], a[236:239], v[244:247], v[68:71]\n"
    "s_waitcnt lgkmcnt(8)\n"
    "ds_read_b64_tr_b16 a[212:213], v61 offset:25728\n"
    "ds_read_b64_tr_b16 a[214:215], v61 offset:28288\n"
    "v_mfma_f32_16x16x32_bf16 v[72:75], a[192:195], v[240:243], v[72:75]\n"
    "s_waitcnt lgkmcnt(8)\n"
    "ds_read_b64_tr_b16 a[216:217], v61 offset:30720\n"
    "ds_read_b64_tr_b16 a[218:219], v61 offset:33280\n"
    "v_mfma_f32_16x16x32_bf16 v[72:75], a[196:199], v[244:247], v[72:75]\n"
    "s_waitcnt lgkmcnt(8)\n"
    "ds_read_b64_tr_b16 a[220:221], v61 offset:35840\n"
    "ds_read_b64_tr_b16 a[222:223], v61 offset:38400\n"
    "v_mfma_f32_16x16x32_bf16 v[76:79], a[200:203], v[240:243], v[76:79]\n"
    "s_waitcnt lgkmcnt(8)\n"
    "ds_read_b64_tr_b16 a[224:225], v61 offset:30752\n"
    "ds_read_b64_tr_b16 a[226:227], v61 offset:33312\n"
    "v_mfma_f32_16x16x32_bf16 v[76:79], a[204:207], v[244:247], v[76:79]\n"
    "s_waitcnt lgkmcnt(8)\n"
    "ds_read_b64_tr_b16 a[228:229], v61 offset:35872\n"
    "ds_read_b64_tr_b16 a[230:231], v61 offset:38432\n"
    "v_mfma_f32_16x16x32_bf16 v[80:83], a[208:211], v[240:243], v[80:83]\n"
    "s_waitcnt lgkmcnt(8)\n"
    "ds_read_b64_tr_b16 a[232:233], v61 offset:30784\n"
    "ds_read_b64_tr_b16 a[234:235], v61 offset:33344\n"
    "v_mfma_f32_16x16x32_bf16 v[80:83], a[212:215], v[244:247], v[80:83]\n"
    "s_waitcnt lgkmcnt(8)\n"
    "ds_read_b64_tr_b16 a[236:237], v61 offset:35904\n"
    "ds_read_b64_tr_b16 a[238:239], v61 offset:38464\n"
    "v_mfma_f32_16x16x32_bf16 v[64:67], a[216:219], v[248:251], v[64:67]\n"
    "s_waitcnt lgkmcnt(8)\n"
    "ds_read_b64_tr_b16 a[192:193], v61 offset:30816\n"
    "ds_read_b64_tr_b16 a[194:195], v61 offset:33376\n"
    "v_mfma_f32_16x16x32_bf16 v[64:67], a[220:223], v[252:255], v[64:67]\n"
    "s_waitcnt lgkmcnt(8)\n"
    "ds_read_b64_tr_b16 a[196:197], v61 offset:35936\n"
    "ds_read_b64_tr_b16 a[198:199], v61 offset:38496\n"
    "v_mfma_f32_16x16x32_bf16 v[68:71], a[224:227], v[248:251], v[68:71]\n"
    "s_waitcnt lgkmcnt(8)\n"
    "ds_read_b64_tr_b16 a[200:201], v61 offset:30848\n"
    "ds_read_b64_tr_b16 a[202:203], v61 offset:33408\n"
    "v_mfma_f32_16x16x32_bf16 v[68:71], a[228:231], v[252:255], v[68:71]\n"
    "s_waitcnt lgkmcnt(8)\n"
    "ds_read_b64_tr_b16 a[204:205], v61 offset:35968\n"
    "ds_read_b64_tr_b16 a[206:207], v61 offset:38528\n"
    "v_mfma_f32_16x16x32_bf16 v[72:75], a[232:235], v[248:251], v[72:75]\n"
    "s_waitcnt lgkmcnt(8)\n"
    "v_mfma_f32_16x16x32_bf16 v[72:75], a[236:239], v[252:255], v[72:75]\n"
    "s_waitcnt lgkmcnt(6)\n"
    "v_mfma_f32_16x16x32_bf16 v[76:79], a[192:195], v[248:251], v[76:79]\n"
    "s_waitcnt lgkmcnt(4)\n"
    "v_mfma_f32_16x16x32_bf16 v[76:79], a[196:199], v[252:255], v[76:79]\n"
    "s_waitcnt lgkmcnt(2)\n"
    "v_mfma_f32_16x16x32_bf16 v[80:83], a[200:203], v[248:251], v[80:83]\n"
    "s_waitcnt lgkmcnt(0)\n"
    "v_mfma_f32_16x16x32_bf16 v[80:83], a[204:207], v[252:255], v[80:83]\n"
    "s_waitcnt vmcnt(0)\n"
    "v_cmp_ne_u64 vcc, s[80:81], v[62:63]\n"
    "s_nop 1\n"
    "s_cbranch_vccnz L_slow0\n"
    "L_back0:\n"
    "s_barrier\n"
    "s_add_u32 s88, s88, 1048576\n"
    "s_add_u32 s89, s88, 4096\n"
    "buffer_load_dwordx4 a[0:3], v9, s[52:55], s88 offen offset:0 sc1\n"
    "buffer_load_dwordx4 a[4:7], v9, s[52:55], s88 offen offset:1024 sc1\n"
    "buffer_load_dwordx4 a[8:11], v9, s[52:55], s88 offen offset:2048 sc1\n"
    "buffer_load_dwordx4 a[12:15], v9, s[52:55], s88 offen offset:3072 sc1\n"
    "buffer_load_dwordx4 a[16:19], v9, s[52:55], s89 offen offset:0 sc1\n"
    "buffer_load_dwordx4 a[20:23], v9, s[52:55], s89 offen offset:1024 sc1\n"
    "buffer_load_dwordx4 a[24:27], v9, s[52:55], s89 offen offset:2048 sc1\n"
    "buffer_load_dwordx4 a[28:31], v9, s[52:55], s89 offen offset:3072 sc1\n"
    "s_add_u32 s88, s88, 8192\n"
    "s_add_u32 s89, s89, 8192\n"
    "ds_read_b64_tr_b16 a[192:193], v57 offset:0\n"
    "ds_read_b64_tr_b16 a[194:195], v57 offset:6144\n"
    "ds_read_b64_tr_b16 a[196:197], v57 offset:12288\n"
    "ds_read_b64_tr_b16 a[198:199], v57 offset:18432\n"
    "ds_read_b64_tr_b16 a[200:201], v57 offset:8\n"
    "ds_read_b64_tr_b16 a[202:203], v57 offset:6152\n"
    "ds_read_b64_tr_b16 a[204:205], v57 offset:12296\n"
    "ds_read_b64_tr_b16 a[206:207], v57 offset:18440\n"
    "ds_read_b64_tr_b16 a[208:209], v58 offset:0\n"
    "ds_read_b64_tr_b16 a[210:211], v58 offset:6144\n"
    "ds_read_b64_tr_b16 a[212:213], v58 offset:12288\n"
    "ds_read_b64_tr_b16 a[214:215], v58 offset:18432\n"
    "s_mov_b32 s92, 0xbdd2d3e8\n"
    "v_mov_b32 v20, 0xc0135761\n"
    "v_mul_f32 v64, v88, v64\n"
    "v_mul_f32 v65, v88, v65\n"
    "v_mul_f32 v66, v88, v66\n"
    "v_mul_f32 v67, v88, v67\n"
    "s_add_u32 m0, s68, 73728\n"
    "s_nop 0\n"
    "buffer_load_dwordx4 v59, s[44:47], s62 offen offset:0 lds\n"
    "v_mul_f32 v16, v64, v64\n"
    "v_mul_f32 v17, v65, v65\n"
    "v_mul_f32 v18, v66, v66\n"
    "v_mul_f32 v19, v67, v67\n"
    "v_fma_f32 v16, v16, s92, v20\n"
    "v_fma_f32 v17, v17, s92, v20\n"
    "v_fma_f32 v18, v18, s92, v20\n"
    "v_fma_f32 v19, v19, s92, v20\n"
    "v_mul_f32 v16, v16, v64\n"
    "v_mul_f32 v17, v17, v65\n"
    "buffer_load_dwordx4 v59, s[44:47], s63 offen offset:1024 lds\n"
    "v_mul_f32 v18, v18, v66\n"
    "v_mul_f32 v19, v19, v67\n"
    "v_exp_f32 v16, v16\n"
    "v_exp_f32 v17, v17\n"
    "v_exp_f32 v18, v18\n"
    "v_exp_f32 v19, v19\n"
    "v_add_f32 v16, 1.0, v16\n"
    "v_add_f32 v17, 1.0, v17\n"
    "v_add_f32 v18, 1.0, v18\n"
    "v_add_f32 v19, 1.0, v19\n"
    "s_add_u32 m0, s68, 75776\n"
    "s_nop 0\n"
    "buffer_load_dwordx4 v59, s[44:47], s64 offen offset:0 lds\n"
    "v_rcp_f32 v16, v16\n"
    "v_rcp_f32 v17, v17\n"
    "v_rcp_f32 v18, v18\n"
    "v_rcp_f32 v19, v19\n"
    "v_mul_f32 v64, v64, v16\n"
    "v_mul_f32 v65, v65, v17\n"
    "v_mul_f32 v66, v66, v18\n"
    "v_mul_f32 v67, v67, v19\n"
    "v_mul_f32 v68, v88, v68\n"
    "v_mul_f32 v69, v88, v69\n"
    "buffer_load_dwordx4 v59, s[44:47], s65 offen offset:1024 lds\n"
    "v_mul_f32 v70, v88, v70\n"
    "v_mul_f32 v71, v88, v71\n"
    "v_mul_f32 v16, v68, v68\n"
    "v_mul_f32 v17, v69, v69\n"
    "v_mul_f32 v18, v70, v70\n"
    "v_mul_f32 v19, v71, v71\n"
    "v_fma_f32 v16, v16, s92, v20\n"
    "v_fma_f32 v17, v17, s92, v20\n"
    "v_fma_f32 v18, v18, s92, v20\n"
    "v_fma_f32 v19, v19, s92, v20\n"
    "s_add_u32 m0, s68, 77824\n"
    "s_nop 0\n"
    "buffer_load_dwordx4 v59, s[44:47], s66 offen offset:0 lds\n"
    "v_mul_f32 v16, v16, v68\n"
    "v_mul_f32 v17, v17, v69\n"
    "v_mul_f32 v18, v18, v70\n"
    "v_mul_f32 v19, v19, v71\n"
    "v_exp_f32 v16, v16\n"
    "v_exp_f32 v17, v17\n"
    "v_exp_f32 v18, v18\n"
    "v_exp_f32 v19, v19\n"
    "v_add_f32 v16, 1.0, v16\n"
    "v_add_f32 v17, 1.0, v17\n"
    "buffer_load_dwordx4 v59, s[44:47], s67 offen offset:1024 lds\n"
    "v_add_f32 v18, 1.0, v18\n"
    "v_add_f32 v19, 1.0, v19\n"
    "v_rcp_f32 v16, v16\n"
    "v_rcp_f32 v17, v17\n"
    "v_rcp_f32 v18, v18\n"
    "v_rcp_f32 v19, v19\n"
    "v_mul_f32 v68, v68, v16\n"
    "v_mul_f32 v69, v69, v17\n"
    "v_mul_f32 v70, v70, v18\n"
    "v_mul_f32 v71, v71, v19\n"
    "v_add_u32 v59, 786432, v59\n"
    "buffer_load_dwordx4 a[32:35], v9, s[52:55], s88 offen offset:0 sc1\n"
    "v_mul_f32 v72, v88, v72\n"
    "v_mul_f32 v73, v88, v73\n"
    "v_mul_f32 v74, v88, v74\n"
    "v_mul_f32 v75, v88, v75\n"
    "v_mul_f32 v16, v72, v72\n"
    "v_mul_f32 v17, v73, v73\n"
    "v_mul_f32 v18, v74, v74\n"
    "v_mul_f32 v19, v75, v75\n"
    "v_fma_f32 v16, v16, s92, v20\n"
    "v_fma_f32 v17, v17, s92, v20\n"
    "buffer_load_dwordx4 a[36:39], v9, s[52:55], s88 offen offset:1024 sc1\n"
    "v_fma_f32 v18, v18, s92, v20\n"
    "v_fma_f32 v19, v19, s92, v20\n"
    "v_mul_f32 v16, v16, v72\n"
    "v_mul_f32 v17, v17, v73\n"
    "v_mul_f32 v18, v18, v74\n"
    "v_mul_f32 v19, v19, v75\n"
    "v_exp_f32 v16, v16\n"
    "v_exp_f32 v17, v17\n"
    "v_exp_f32 v18, v18\n"
    "v_exp_f32 v19, v19\n"
    "buffer_load_dwordx4 a[40:43], v9, s[52:55], s88 offen offset:2048 sc1\n"
    "v_add_f32 v16, 1.0, v16\n"
    "v_add_f32 v17, 1.0, v17\n"
    "v_add_f32 v18, 1.0, v18\n"
    "v_add_f32 v19, 1.0, v19\n"
    "v_rcp_f32 v16, v16\n"
    "v_rcp_f32 v17, v17\n"
    "v_rcp_f32 v18, v18\n"
    "v_rcp_f32 v19, v19\n"
    "v_mul_f32 v72, v72, v16\n"
    "v_mul_f32 v73, v73, v17\n"
    "buffer_load_dwordx4 a[44:47], v9, s[52:55], s88 offen offset:3072 sc1\n"
    "v_mul_f32 v74, v74, v18\n"
    "v_mul_f32 v75, v75, v19\n"
    "v_mul_f32 v76, v88, v76\n"
    "v_mul_f32 v77, v88, v77\n"
    "v_mul_f32 v78, v88, v78\n"
    "v_mul_f32 v79, v88, v79\n"
    "v_mul_f32 v16, v76, v76\n"
    "v_mul_f32 v17, v77, v77\n"
    "v_mul_f32 v18, v78, v78\n"
    "v_mul_f32 v19, v79, v79\n"
    "buffer_load_dwordx4 a[48:51], v9, s[52:55], s89 offen offset:0 sc1\n"
    "v_fma_f32 v16, v16, s92, v20\n"
    "v_fma_f32 v17, v17, s92, v20\n"
    "v_fma_f32 v18, v18, s92, v20\n"
    "v_fma_f32 v19, v19, s92, v20\n"
    "v_mul_f32 v16, v16, v76\n"
    "v_mul_f32 v17, v17, v77\n"
    "v_mul_f32 v18, v18, v78\n"
    "v_mul_f32 v19, v19, v79\n"
    "v_exp_f32 v16, v16\n"
    "v_exp_f32 v17, v17\n"
    "buffer_load_dwordx4 a[52:55], v9, s[52:55], s89 offen offset:1024 sc1\n"
    "v_exp_f32 v18, v18\n"
    "v_exp_f32 v19, v19\n"
    "v_add_f32 v16, 1.0, v16\n"
    "v_add_f32 v17, 1.0, v17\n"
    "v_add_f32 v18, 1.0, v18\n"
    "v_add_f32 v19, 1.0, v19\n"
    "v_rcp_f32 v16, v16\n"
    "v_rcp_f32 v17, v17\n"
    "v_rcp_f32 v18, v18\n"
    "v_rcp_f32 v19, v19\n"
    "buffer_load_dwordx4 a[56:59], v9, s[52:55], s89 offen offset:2048 sc1\n"
    "v_mul_f32 v76, v76, v16\n"
    "v_mul_f32 v77, v77, v17\n"
    "v_mul_f32 v78, v78, v18\n"
    "v_mul_f32 v79, v79, v19\n"
    "v_mul_f32 v80, v88, v80\n"
    "v_mul_f32 v81, v88, v81\n"
    "v_mul_f32 v82, v88, v82\n"
    "v_mul_f32 v83, v88, v83\n"
    "v_mul_f32 v16, v80, v80\n"
    "v_mul_f32 v17, v81, v81\n"
    "buffer_load_dwordx4 a[60:63], v9, s[52:55], s89 offen offset:3072 sc1\n"
    "v_mul_f32 v18, v82, v82\n"
    "v_mul_f32 v19, v83, v83\n"
    "v_fma_f32 v16, v16, s92, v20\n"
    "v_fma_f32 v17, v17, s92, v20\n"
    "v_fma_f32 v18, v18, s92, v20\n"
    "v_fma_f32 v19, v19, s92, v20\n"
    "v_mul_f32 v16, v16, v80\n"
    "v_mul_f32 v17, v17, v81\n"
    "v_mul_f32 v18, v18, v82\n"
    "v_mul_f32 v19, v19, v83\n"
    "s_add_u32 s88, s88, 8192\n"
    "s_add_u32 s89, s89, 8192\n"
    "v_exp_f32 v16, v16\n"
    "v_exp_f32 v17, v17\n"
    "v_exp_f32 v18, v18\n"
    "v_exp_f32 v19, v19\n"
    "v_add_f32 v16, 1.0, v16\n"
    "v_add_f32 v17, 1.0, v17\n"
    "v_add_f32 v18, 1.0, v18\n"
    "v_add_f32 v19, 1.0, v19\n"
    "v_rcp_f32 v16, v16\n"
    "v_rcp_f32 v17, v17\n"
    "v_rcp_f32 v18, v18\n"
    "v_rcp_f32 v19, v19\n"
    "v_mul_f32 v80, v80, v16\n"
    "v_mul_f32 v81, v81, v17\n"
    "v_mul_f32 v82, v82, v18\n"
    "v_mul_f32 v83, v83, v19\n"
    "s_lshl_b32 s73, s83, 3\n"
    "s_add_u32 s73, s73, s78\n"
    "s_mul_i32 s73, s73, 2560\n"
    "s_add_u32 s73, s73, 4194304\n"
    "v_cvt_pk_bf16_f32 v16, v64, v65\n"
    "v_cvt_pk_bf16_f32 v17, v66, v67\n"
    "v_cvt_pk_bf16_f32 v18, v68, v69\n"
    "v_cvt_pk_bf16_f32 v19, v70, v71\n"
    "v_cvt_pk_bf16_f32 v20, v72, v73\n"
    "v_cvt_pk_bf16_f32 v21, v74, v75\n"
    "v_cvt_pk_bf16_f32 v22, v76, v77\n"
    "v_cvt_pk_bf16_f32 v23, v78, v79\n"
    "v_cvt_pk_bf16_f32 v24, v80, v81\n"
    "v_cvt_pk_bf16_f32 v25, v82, v83\n"
    "v_lshrrev_b32 v26, 1, v9\n"
    "buffer_store_dwordx4 v[16:19], v9, s[52:55], s73 offen sc1\n"
    "buffer_store_dwordx4 v[20:23], v9, s[52:55], s73 offen offset:1024 sc1\n"
    "buffer_store_dwordx2 v[24:25], v26, s[52:55], s73 offen offset:2048 sc1\n"
    "s_nop 1\n"
    "s_add_u32 s73, s73, 62914560\n"
    "buffer_store_dwordx4 v[104:107], v9, s[52:55], s73 offen\n"
    "buffer_store_dwordx4 v[108:111], v9, s[52:55], s73 offen offset:1024\n"
    "buffer_store_dwordx2 v[112:113], v26, s[52:55], s73 offen offset:2048\n"
    "v_lshlrev_b32 v28, 16, v104\n"
    "v_and_b32 v29, 0xffff0000, v104\n"
    "v_lshlrev_b32 v30, 16, v105\n"
    "v_and_b32 v31, 0xffff0000, v105\n"
    "v_lshlrev_b32 v32, 16, v106\n"
    "v_and_b32 v33, 0xffff0000, v106\n"
    "v_lshlrev_b32 v34, 16, v107\n"
    "v_and_b32 v35, 0xffff0000, v107\n"
    "v_lshlrev_b32 v36, 16, v108\n"
    "v_and_b32 v37, 0xffff0000, v108\n"
    "v_lshlrev_b32 v38, 16, v109\n"
    "v_and_b32 v39, 0xffff0000, v109\n"
    "v_lshlrev_b32 v40, 16, v110\n"
    "v_and_b32 v41, 0xffff0000, v110\n"
    "v_lshlrev_b32 v42, 16, v111\n"
    "v_and_b32 v43, 0xffff0000, v111\n"
    "v_lshlrev_b32 v44, 16, v112\n"
    "v_and_b32 v45, 0xffff0000, v112\n"
    "v_lshlrev_b32 v46, 16, v113\n"
    "v_and_b32 v47, 0xffff0000, v113\n"
    "v_pk_fma_f32 v[28:29], v[28:29], v[52:53], v[54:55]\n"
    "v_pk_fma_f32 v[30:31], v[30:31], v[52:53], v[54:55]\n"
    "v_pk_fma_f32 v[32:33], v[32:33], v[52:53], v[54:55]\n"
    "v_pk_fma_f32 v[34:35], v[34:35], v[52:53], v[54:55]\n"
    "v_pk_fma_f32 v[36:37], v[36:37], v[52:53], v[54:55]\n"
    "v_pk_fma_f32 v[38:39], v[38:39], v[52:53], v[54:55]\n"
    "v_pk_fma_f32 v[40:41], v[40:41], v[52:53], v[54:55]\n"
    "v_pk_fma_f32 v[42:43], v[42:43], v[52:53], v[54:55]\n"
    "v_pk_fma_f32 v[44:45], v[44:45], v[52:53], v[54:55]\n"
    "v_pk_fma_f32 v[46:47], v[46:47], v[52:53], v[54:55]\n"
    "s_waitcnt vmcnt(20)\n"
    "s_waitcnt lgkmcnt(10)\n"
    "v_mfma_f32_16x16x32_bf16 v[64:67], a[192:195], a[0:3], 0\n"
    "s_waitcnt lgkmcnt(8)\n"
    "ds_read_b64_tr_b16 a[216:217], v58 offset:8\n"
    "v_mfma_f32_16x16x32_bf16 v[64:67], a[196:199], a[4:7], v[64:67]\n"
    "ds_read_b64_tr_b16 a[218:219], v58 offset:6152\n"
    "v_mfma_f32_16x16x32_bf16 v[68:71], a[196:199], a[12:15], 0\n"
    "s_cmp_eq_u32 s78, 2\n"
    "s_cselect_b32 s93, 0, 0x80000000\n"
    "v_or_b32 v48, s93, v50\n"
    "v_mfma_f32_16x16x32_bf16 v[68:71], a[192:195], a[8:11], v[68:71]\n"
    "buffer_load_dword v51, v48, s[44:47], 0 offen\n"
    "ds_read_b64_tr_b16 a[220:221], v58 offset:12296\n"
    "v_mfma_f32_16x16x32_bf16 v[72:75], a[192:195], a[16:19], 0\n"
    "ds_read_b64_tr_b16 a[222:223], v58 offset:18440\n"
    "v_mfma_f32_16x16x32_bf16 v[72:75], a[196:199], a[20:23], v[72:75]\n"
    "v_lshl_or_b32 v48, %2, 7, s93\n"
    "v_mfma_f32_16x16x32_bf16 v[76:79], a[196:199], a[28:31], 0\n"
    "buffer_load_dword v51, v48, s[84:87], 0 offen\n"
    "ds_read_b64_tr_b16 a[224:225], v57 offset:2048\n"
    "v_mfma_f32_16x16x32_bf16 v[76:79], a[192:195], a[24:27], v[76:79]\n"
    "s_waitcnt lgkmcnt(11)\n"
    "ds_read_b64_tr_b16 a[226:227], v57 offset:8192\n"
    "v_mfma_f32_16x16x32_bf16 v[92:95], a[200:203], a[24:27], 0\n"
    "s_waitcnt lgkmcnt(10)\n"
    "v_mfma_f32_16x16x32_bf16 v[92:95], a[204:207], a[28:31], v[92:95]\n"
    "s_add_u32 m0, s68, 0\n"
    "ds_read_b64_tr_b16 a[228:229], v57 offset:14336\n"
    "v_mfma_f32_16x16x32_bf16 v[88:91], a[204:207], a[20:23], 0\n"
    "buffer_load_dwordx4 v59, s[44:47], s62 offen offset:0 lds\n"
    "ds_read_b64_tr_b16 a[230:231], v57 offset:20480\n"
    "v_mfma_f32_16x16x32_bf16 v[88:91], a[200:203], a[16:19], v[88:91]\n"
    "v_mfma_f32_16x16x32_bf16 v[84:87], a[200:203], a[8:11], 0\n"
    "ds_read_b64_tr_b16 a[232:233], v57 offset:2056\n"
    "v_mfma_f32_16x16x32_bf16 v[84:87], a[204:207], a[12:15], v[84:87]\n"
    "buffer_load_dwordx4 v59, s[44:47], s63 offen offset:1024 lds\n"
    "ds_read_b64_tr_b16 a[234:235], v57 offset:8200\n"
    "v_mfma_f32_16x16x32_bf16 v[80:83], a[204:207], a[4:7], 0\n"
    "v_mfma_f32_16x16x32_bf16 v[80:83], a[200:203], a[0:3], v[80:83]\n"
    "s_waitcnt lgkmcnt(12)\n"
    "ds_read_b64_tr_b16 a[236:237], v57 offset:14344\n"
    "v_mfma_f32_16x16x32_bf16 v[96:99], a[208:211], a[0:3], 0\n"
    "s_waitcnt lgkmcnt(11)\n"
    "ds_read_b64_tr_b16 a[238:239], v57 offset:20488\n"
    "v_mfma_f32_16x16x32_bf16 v[96:99], a[212:215], a[4:7], v[96:99]\n"
    "s_add_u32 m0, s68, 2048\n"
    "v_mfma_f32_16x16x32_bf16 v[100:103], a[212:215], a[12:15], 0\n"
    "buffer_load_dwordx4 v59, s[44:47], s64 offen offset:0 lds\n"
    "v_mfma_f32_16x16x32_bf16 v[100:103], a[208:211], a[8:11], v[100:103]\n"
    "v_mfma_f32_16x16x32_bf16 v[104:107], a[208:211], a[16:19], 0\n"
    "v_mfma_f32_16x16x32_bf16 v[104:107], a[212:215], a[20:23], v[104:107]\n"
    "buffer_load_dwordx4 v59, s[44:47], s65 offen offset:1024 lds\n"
    "v_mfma_f32_16x16x32_bf16 v[108:111], a[212:215], a[28:31], 0\n"
    "s_waitcnt lgkmcnt(0)\n"
    "v_mfma_f32_16x16x32_bf16 v[108:111], a[208:211], a[24:27], v[108:111]\n"
    "v_mfma_f32_16x16x32_bf16 v[112:115], a[216:219], a[0:3], 0\n"
    "ds_read_b64_tr_b16 a[192:193], v58 offset:2048\n"
    "v_mfma_f32_16x16x32_bf16 v[112:115], a[220:223], a[4:7], v[112:115]\n"
    "s_add_u32 m0, s68, 4096\n"
    "ds_read_b64_tr_b16 a[194:195], v58 offset:8192\n"
    "v_mfma_f32_16x16x32_bf16 v[116:119], a[220:223], a[12:15], 0\n"
    "buffer_load_dwordx4 v59, s[44:47], s66 offen offset:0 lds\n"
    "v_mfma_f32_16x16x32_bf16 v[116:119], a[216:219], a[8:11], v[116:119]\n"
    "ds_read_b64_tr_b16 a[196:197], v58 offset:14336\n"
    "v_mfma_f32_16x16x32_bf16 v[120:123], a[216:219], a[16:19], 0\n"
    "ds_read_b64_tr_b16 a[198:199], v58 offset:20480\n"
    "v_mfma_f32_16x16x32_bf16 v[120:123], a[220:223], a[20:23], v[120:123]\n"
    "buffer_load_dwordx4 v59, s[44:47], s67 offen offset:1024 lds\n"
    "v_mfma_f32_16x16x32_bf16 v[124:127], a[220:223], a[28:31], 0\n"
    "ds_read_b64_tr_b16 a[200:201], v58 offset:2056\n"
    "v_mfma_f32_16x16x32_bf16 v[124:127], a[216:219], a[24:27], v[124:127]\n"
    "ds_read_b64_tr_b16 a[202:203], v58 offset:8200\n"
    "v_mfma_f32_16x16x32_bf16 v[140:143], a[224:227], a[24:27], 0\n"
    "v_mfma_f32_16x16x32_bf16 v[140:143], a[228:231], a[28:31], v[140:143]\n"
    "ds_read_b64_tr_b16 a[204:205], v58 offset:14344\n"
    "v_add_u32 v59, 786432, v59\n"
    "v_mfma_f32_16x16x32_bf16 v[136:139], a[228:231], a[20:23], 0\n"
    "buffer_load_dwordx4 a[64:67], v9, s[52:55], s88 offen offset:0 sc1\n"
    "ds_read_b64_tr_b16 a[206:207], v58 offset:20488\n"
    "v_mfma_f32_16x16x32_bf16 v[136:139], a[224:227], a[16:19], v[136:139]\n"
    "v_mfma_f32_16x16x32_bf16 v[132:135], a[224:227], a[8:11], 0\n"
    "ds_read_b64_tr_b16 a[208:209], v57 offset:4096\n"
    "v_mfma_f32_16x16x32_bf16 v[132:135], a[228:231], a[12:15], v[132:135]\n"
    "buffer_load_dwordx4 a[68:71], v9, s[52:55], s88 offen offset:1024 sc1\n"
    "ds_read_b64_tr_b16 a[210:211], v57 offset:10240\n"
    "v_mfma_f32_16x16x32_bf16 v[128:131], a[228:231], a[4:7], 0\n"
    "v_mfma_f32_16x16x32_bf16 v[128:131], a[224:227], a[0:3], v[128:131]\n"
    "ds_read_b64_tr_b16 a[212:213], v57 offset:16384\n"
    "v_mfma_f32_16x16x32_bf16 v[144:147], a[232:235], a[0:3], 0\n"
    "ds_read_b64_tr_b16 a[214:215], v57 offset:22528\n"
    "v_mfma_f32_16x16x32_bf16 v[144:147], a[236:239], a[4:7], v[144:147]\n"
    "buffer_load_dwordx4 a[72:75], v9, s[52:55], s88 offen offset:2048 sc1\n"
    "v_mfma_f32_16x16x32_bf16 v[148:151], a[236:239], a[12:15], 0\n"
    "v_mfma_f32_16x16x32_bf16 v[148:151], a[232:235], a[8:11], v[148:151]\n"
    "v_mfma_f32_16x16x32_bf16 v[152:155], a[232:235], a[16:19], 0\n"
    "v_mfma_f32_16x16x32_bf16 v[152:155], a[236:239], a[20:23], v[152:155]\n"
    "buffer_load_dwordx4 a[76:79], v9, s[52:55], s88 offen offset:3072 sc1\n"
    "v_mfma_f32_16x16x32_bf16 v[156:159], a[236:239], a[28:31], 0\n"
    "s_waitcnt lgkmcnt(0)\n"
    "v_mfma_f32_16x16x32_bf16 v[156:159], a[232:235], a[24:27], v[156:159]\n"
    "v_mfma_f32_16x16x32_bf16 v[160:163], a[192:195], a[0:3], 0\n"
    "ds_read_b64_tr_b16 a[216:217], v57 offset:4104\n"
    "v_mfma_f32_16x16x32_bf16 v[160:163], a[196:199], a[4:7], v[160:163]\n"
    "buffer_load_dwordx4 a[80:83], v9, s[52:55], s89 offen offset:0 sc1\n"
    "ds_read_b64_tr_b16 a[218:219], v57 offset:10248\n"
    "v_mfma_f32_16x16x32_bf16 v[164:167], a[196:199], a[12:15], 0\n"
    "v_mfma_f32_16x16x32_bf16 v[164:167], a[192:195], a[8:11], v[164:167]\n"
    "ds_read_b64_tr_b16 a[220:221], v57 offset:16392\n"
    "v_mfma_f32_16x16x32_bf16 v[168:171], a[192:195], a[16:19], 0\n"
    "ds_read_b64_tr_b16 a[222:223], v57 offset:22536\n"
    "v_mfma_f32_16x16x32_bf16 v[168:171], a[196:199], a[20:23], v[168:171]\n"
    "buffer_load_dwordx4 a[84:87], v9, s[52:55], s89 offen offset:1024 sc1\n"
    "v_mfma_f32_16x16x32_bf16 v[172:175], a[196:199], a[28:31], 0\n"
    "ds_read_b64_tr_b16 a[224:225], v58 offset:4096\n"
    "v_mfma_f32_16x16x32_bf16 v[172:175], a[192:195], a[24:27], v[172:175]\n"
    "ds_read_b64_tr_b16 a[226:227], v58 offset:10240\n"
    "v_mfma_f32_16x16x32_bf16 v[188:191], a[200:203], a[24:27], 0\n"
    "v_mfma_f32_16x16x32_bf16 v[188:191], a[204:207], a[28:31], v[188:191]\n"
    "buffer_load_dwordx4 a[88:91], v9, s[52:55], s89 offen offset:2048 sc1\n"
    "ds_read_b64_tr_b16 a[228:229], v58 offset:16384\n"
    "v_mfma_f32_16x16x32_bf16 v[184:187], a[204:207], a[20:23], 0\n"
    "ds_read_b64_tr_b16 a[230:231], v58 offset:22528\n"
    "v_mfma_f32_16x16x32_bf16 v[184:187], a[200:203], a[16:19], v[184:187]\n"
    "v_mfma_f32_16x16x32_bf16 v[180:183], a[200:203], a[8:11], 0\n"
    "ds_read_b64_tr_b16 a[232:233], v58 offset:4104\n"
    "v_mfma_f32_16x16x32_bf16 v[180:183], a[204:207], a[12:15], v[180:183]\n"
    "buffer_load_dwordx4 a[92:95], v9, s[52:55], s89 offen offset:3072 sc1\n"
    "ds_read_b64_tr_b16 a[234:235], v58 offset:10248\n"
    "v_mfma_f32_16x16x32_bf16 v[176:179], a[204:207], a[4:7], 0\n"
    "v_mfma_f32_16x16x32_bf16 v[176:179], a[200:203], a[0:3], v[176:179]\n"
    "ds_read_b64_tr_b16 a[236:237], v58 offset:16392\n"
    "v_mfma_f32_16x16x32_bf16 v[192:195], a[208:211], a[0:3], 0\n"
    "ds_read_b64_tr_b16 a[238:239], v58 offset:22536\n"
    "v_mfma_f32_16x16x32_bf16 v[192:195], a[212:215], a[4:7], v[192:195]\n"
    "s_add_u32 s88, s88, 8192\n"
    "v_mfma_f32_16x16x32_bf16 v[196:199], a[212:215], a[12:15], 0\n"
    "s_add_u32 s89, s89, 8192\n"
    "v_mfma_f32_16x16x32_bf16 v[196:199], a[208:211], a[8:11], v[196:199]\n"
    "s_add_u32 m0, s68, 24576\n"
    "v_mfma_f32_16x16x32_bf16 v[200:203], a[208:211], a[16:19], 0\n"
    "buffer_load_dwordx4 v59, s[44:47], s62 offen offset:0 lds\n"
    "v_mfma_f32_16x16x32_bf16 v[200:203], a[212:215], a[20:23], v[200:203]\n"
    "buffer_load_dwordx4 v59, s[44:47], s63 offen offset:1024 lds\n"
    "v_mfma_f32_16x16x32_bf16 v[204:207], a[212:215], a[28:31], 0\n"
    "s_waitcnt vmcnt(21) lgkmcnt(0)\n"
    "s_barrier\n"
    "v_mfma_f32_16x16x32_bf16 v[204:207], a[208:211], a[24:27], v[204:207]\n"
    "v_mfma_f32_16x16x32_bf16 v[208:211], a[216:219], a[0:3], 0\n"
    "ds_read_b64_tr_b16 a[192:193], v57 offset:24576\n"
    "v_mfma_f32_16x16x32_bf16 v[208:211], a[220:223], a[4:7], v[208:211]\n"
    "s_add_u32 m0, s68, 26624\n"
    "ds_read_b64_tr_b16 a[194:195], v57 offset:30720\n"
    "v_mfma_f32_16x16x32_bf16 v[212:215], a[220:223], a[12:15], 0\n"
    "buffer_load_dwordx4 v59, s[44:47], s64 offen offset:0 lds\n"
    "v_mfma_f32_16x16x32_bf16 v[212:215], a[216:219], a[8:11], v[212:215]\n"
    "ds_read_b64_tr_b16 a[196:197], v57 offset:36864\n"
    "v_mfma_f32_16x16x32_bf16 v[216:219], a[216:219], a[16:19], 0\n"
    "ds_read_b64_tr_b16 a[198:199], v57 offset:43008\n"
    "v_mfma_f32_16x16x32_bf16 v[216:219], a[220:223], a[20:23], v[216:219]\n"
    "buffer_load_dwordx4 v59, s[44:47], s65 offen offset:1024 lds\n"
    "v_mfma_f32_16x16x32_bf16 v[220:223], a[220:223], a[28:31], 0\n"
    "ds_read_b64_tr_b16 a[200:201], v57 offset:24584\n"
    "v_mfma_f32_16x16x32_bf16 v[220:223], a[216:219], a[24:27], v[220:223]\n"
    "ds_read_b64_tr_b16 a[202:203], v57 offset:30728\n"
    "v_mfma_f32_16x16x32_bf16 v[236:239], a[224:227], a[24:27], 0\n"
    "v_mfma_f32_16x16x32_bf16 v[236:239], a[228:231], a[28:31], v[236:239]\n"
    "s_add_u32 m0, s68, 28672\n"
    "ds_read_b64_tr_b16 a[204:205], v57 offset:36872\n"
    "v_mfma_f32_16x16x32_bf16 v[232:235], a[228:231], a[20:23], 0\n"
    "buffer_load_dwordx4 v59, s[44:47], s66 offen offset:0 lds\n"
    "ds_read_b64_tr_b16 a[206:207], v57 offset:43016\n"
    "v_mfma_f32_16x16x32_bf16 v[232:235], a[224:227], a[16:19], v[232:235]\n"
    "v_mfma_f32_16x16x32_bf16 v[228:231], a[224:227], a[8:11], 0\n"
    "ds_read_b64_tr_b16 a[208:209], v58 offset:24576\n"
    "v_mfma_f32_16x16x32_bf16 v[228:231], a[228:231], a[12:15], v[228:231]\n"
    "buffer_load_dwordx4 v59, s[44:47], s67 offen offset:1024 lds\n"
    "ds_read_b64_tr_b16 a[210:211], v58 offset:30720\n"
    "v_mfma_f32_16x16x32_bf16 v[224:227], a[228:231], a[4:7], 0\n"
    "v_mfma_f32_16x16x32_bf16 v[224:227], a[224:227], a[0:3], v[224:227]\n"
    "ds_read_b64_tr_b16 a[212:213], v58 offset:36864\n"
    "v_mfma_f32_16x16x32_bf16 v[240:243], a[232:235], a[0:3], 0\n"
    "ds_read_b64_tr_b16 a[214:215], v58 offset:43008\n"
    "v_mfma_f32_16x16x32_bf16 v[240:243], a[236:239], a[4:7], v[240:243]\n"
    "v_add_u32 v59, 786432, v59\n"
    "v_mfma_f32_16x16x32_bf16 v[244:247], a[236:239], a[12:15], 0\n"
    "v_mfma_f32_16x16x32_bf16 v[244:247], a[232:235], a[8:11], v[244:247]\n"
    "v_mfma_f32_16x16x32_bf16 v[248:251], a[232:235], a[16:19], 0\n"
    "v_mfma_f32_16x16x32_bf16 v[248:251], a[236:239], a[20:23], v[248:251]\n"
    "v_mfma_f32_16x16x32_bf16 v[252:255], a[236:239], a[28:31], 0\n"
    "s_waitcnt lgkmcnt(0)\n"
    "v_mfma_f32_16x16x32_bf16 v[252:255], a[232:235], a[24:27], v[252:255]\n"
    "v_mfma_f32_16x16x32_bf16 v[64:67], a[192:195], a[32:35], v[64:67]\n"
    "ds_read_b64_tr_b16 a[216:217], v58 offset:24584\n"
    "v_mfma_f32_16x16x32_bf16 v[64:67], a[196:199], a[36:39], v[64:67]\n"
    "ds_read_b64_tr_b16 a[218:219], v58 offset:30728\n"
    "v_mfma_f32_16x16x32_bf16 v[68:71], a[196:199], a[44:47], v[68:71]\n"
    "s_cmp_eq_u32 s78, 3\n"
    "s_cselect_b32 s93, 0, 0x80000000\n"
    "v_or_b32 v48, s93, v50\n"
    "v_mfma_f32_16x16x32_bf16 v[68:71], a[192:195], a[40:43], v[68:71]\n"
    "buffer_load_dword v51, v48, s[44:47], 0 offen\n"
    "ds_read_b64_tr_b16 a[220:221], v58 offset:36872\n"
    "v_mfma_f32_16x16x32_bf16 v[72:75], a[192:195], a[48:51], v[72:75]\n"
    "ds_read_b64_tr_b16 a[222:223], v58 offset:43016\n"
    "v_lshl_or_b32 v48, %2, 7, s93\n"
    "v_mfma_f32_16x16x32_bf16 v[72:75], a[196:199], a[52:55], v[72:75]\n"
    "buffer_load_dword v51, v48, s[84:87], 0 offen\n"
    "v_mfma_f32_16x16x32_bf16 v[76:79], a[196:199], a[60:63], v[76:79]\n"
    "ds_read_b64_tr_b16 a[224:225], v57 offset:26624\n"
    "v_mfma_f32_16x16x32_bf16 v[76:79], a[192:195], a[56:59], v[76:79]\n"
    "ds_read_b64_tr_b16 a[226:227], v57 offset:32768\n"
    "v_mfma_f32_16x16x32_bf16 v[92:95], a[200:203], a[56:59], v[92:95]\n"
    "s_lshl_b32 s73, s82, 3\n"
    "v_mfma_f32_16x16x32_bf16 v[92:95], a[204:207], a[60:63], v[92:95]\n"
    "s_add_u32 s73, s73, s78\n"
    "ds_read_b64_tr_b16 a[228:229], v57 offset:38912\n"
    "v_mfma_f32_16x16x32_bf16 v[88:91], a[204:207], a[52:55], v[88:91]\n"
    "s_lshl_b32 s95, s73, 6\n"
    "ds_read_b64_tr_b16 a[230:231], v57 offset:45056\n"
    "v_mfma_f32_16x16x32_bf16 v[88:91], a[200:203], a[48:51], v[88:91]\n"
    "s_add_u32 s95, s95, 20480\n"
    "v_mov_b32 v62, s80\n"
    "v_mov_b32 v63, s81\n"
    "v_mfma_f32_16x16x32_bf16 v[84:87], a[200:203], a[40:43], v[84:87]\n"
    "buffer_store_dwordx2 v[62:63], v10, s[52:55], s95 offen sc1\n"
    "ds_read_b64_tr_b16 a[232:233], v57 offset:26632\n"
    "v_mfma_f32_16x16x32_bf16 v[84:87], a[204:207], a[44:47], v[84:87]\n"
    "buffer_load_dwordx4 a[96:99], v9, s[52:55], s88 offen offset:0 sc1\n"
    "ds_read_b64_tr_b16 a[234:235], v57 offset:32776\n"
    "v_mfma_f32_16x16x32_bf16 v[80:83], a[204:207], a[36:39], v[80:83]\n"
    "v_mfma_f32_16x16x32_bf16 v[80:83], a[200:203], a[32:35], v[80:83]\n"
    "buffer_load_dwordx4 a[100:103], v9, s[52:55], s88 offen offset:1024 sc1\n"
    "ds_read_b64_tr_b16 a[236:237], v57 offset:38920\n"
    "v_mfma_f32_16x16x32_bf16 v[96:99], a[208:211], a[32:35], v[96:99]\n"
    "ds_read_b64_tr_b16 a[238:239], v57 offset:45064\n"
    "v_mfma_f32_16x16x32_bf16 v[96:99], a[212:215], a[36:39], v[96:99]\n"
    "v_mfma_f32_16x16x32_bf16 v[100:103], a[212:215], a[44:47], v[100:103]\n"
    "buffer_load_dwordx4 a[104:107], v9, s[52:55], s88 offen offset:2048 sc1\n"
    "v_mfma_f32_16x16x32_bf16 v[100:103], a[208:211], a[40:43], v[100:103]\n"
    "v_mfma_f32_16x16x32_bf16 v[104:107], a[208:211], a[48:51], v[104:107]\n"
    "v_mfma_f32_16x16x32_bf16 v[104:107], a[212:215], a[52:55], v[104:107]\n"
    "v_mfma_f32_16x16x32_bf16 v[108:111], a[212:215], a[60:63], v[108:111]\n"
    "s_waitcnt lgkmcnt(0)\n"
    "buffer_load_dwordx4 a[108:111], v9, s[52:55], s88 offen offset:3072 sc1\n"
    "v_mfma_f32_16x16x32_bf16 v[108:111], a[208:211], a[56:59], v[108:111]\n"
    "v_mfma_f32_16x16x32_bf16 v[112:115], a[216:219], a[32:35], v[112:115]\n"
    "ds_read_b64_tr_b16 a[192:193], v58 offset:26624\n"
    "v_mfma_f32_16x16x32_bf16 v[112:115], a[220:223], a[36:39], v[112:115]\n"
    "buffer_load_dwordx4 a[112:115], v9, s[52:55], s89 offen offset:0 sc1\n"
    "ds_read_b64_tr_b16 a[194:195], v58 offset:32768\n"
    "v_mfma_f32_16x16x32_bf16 v[116:119], a[220:223], a[44:47], v[116:119]\n"
    "v_mfma_f32_16x16x32_bf16 v[116:119], a[216:219], a[40:43], v[116:119]\n"
    "ds_read_b64_tr_b16 a[196:197], v58 offset:38912\n"
    "v_mfma_f32_16x16x32_bf16 v[120:123], a[216:219], a[48:51], v[120:123]\n"
    "ds_read_b64_tr_b16 a[198:199], v58 offset:45056\n"
    "v_mfma_f32_16x16x32_bf16 v[120:123], a[220:223], a[52:55], v[120:123]\n"
    "buffer_load_dwordx4 a[116:119], v9, s[52:55], s89 offen offset:1024 sc1\n"
    "v_mfma_f32_16x16x32_bf16 v[124:127], a[220:223], a[60:63], v[124:127]\n"
    "ds_read_b64_tr_b16 a[200:201], v58 offset:26632\n"
    "v_mfma_f32_16x16x32_bf16 v[124:127], a[216:219], a[56:59], v[124:127]\n"
    "ds_read_b64_tr_b16 a[202:203], v58 offset:32776\n"
    "v_mfma_f32_16x16x32_bf16 v[140:143], a[224:227], a[56:59], v[140:143]\n"
    "buffer_load_dwordx4 a[120:123], v9, s[52:55], s89 offen offset:2048 sc1\n"
    "v_mfma_f32_16x16x32_bf16 v[140:143], a[228:231], a[60:63], v[140:143]\n"
    "ds_read_b64_tr_b16 a[204:205], v58 offset:38920\n"
    "v_mfma_f32_16x16x32_bf16 v[136:139], a[228:231], a[52:55], v[136:139]\n"
    "ds_read_b64_tr_b16 a[206:207], v58 offset:45064\n"
    "v_mfma_f32_16x16x32_bf16 v[136:139], a[224:227], a[48:51], v[136:139]\n"
    "v_mfma_f32_16x16x32_bf16 v[132:135], a[224:227], a[40:43], v[132:135]\n"
    "buffer_load_dwordx4 a[124:127], v9, s[52:55], s89 offen offset:3072 sc1\n"
    "ds_read_b64_tr_b16 a[208:209], v57 offset:28672\n"
    "v_mfma_f32_16x16x32_bf16 v[132:135], a[228:231], a[44:47], v[132:135]\n"
    "ds_read_b64_tr_b16 a[210:211], v57 offset:34816\n"
    "v_mfma_f32_16x16x32_bf16 v[128:131], a[228:231], a[36:39], v[128:131]\n"
    "v_mfma_f32_16x16x32_bf16 v[128:131], a[224:227], a[32:35], v[128:131]\n"
    "s_add_u32 s88, s88, 8192\n"
    "ds_read_b64_tr_b16 a[212:213], v57 offset:40960\n"
    "v_mfma_f32_16x16x32_bf16 v[144:147], a[232:235], a[32:35], v[144:147]\n"
    "s_add_u32 s89, s89, 8192\n"
    "ds_read_b64_tr_b16 a[214:215], v57 offset:47104\n"
    "v_mfma_f32_16x16x32_bf16 v[144:147], a[236:239], a[36:39], v[144:147]\n"
    "s_add_u32 m0, s68, 49152\n"
    "v_mfma_f32_16x16x32_bf16 v[148:151], a[236:239], a[44:47], v[148:151]\n"
    "buffer_load_dwordx4 v59, s[44:47], s62 offen offset:0 lds\n"
    "v_mfma_f32_16x16x32_bf16 v[148:151], a[232:235], a[40:43], v[148:151]\n"
    "buffer_load_dwordx4 v59, s[44:47], s63 offen offset:1024 lds\n"
    "v_mfma_f32_16x16x32_bf16 v[152:155], a[232:235], a[48:51], v[152:155]\n"
    "v_mfma_f32_16x16x32_bf16 v[152:155], a[236:239], a[52:55], v[152:155]\n"
    "v_mfma_f32_16x16x32_bf16 v[156:159], a[236:239], a[60:63], v[156:159]\n"
    "s_waitcnt lgkmcnt(0)\n"
    "v_mfma_f32_16x16x32_bf16 v[156:159], a[232:235], a[56:59], v[156:159]\n"
    "s_add_u32 m0, s68, 51200\n"
    "v_mfma_f32_16x16x32_bf16 v[160:163], a[192:195], a[32:35], v[160:163]\n"
    "buffer_load_dwordx4 v59, s[44:47], s64 offen offset:0 lds\n"
    "ds_read_b64_tr_b16 a[216:217], v57 offset:28680\n"
    "v_mfma_f32_16x16x32_bf16 v[160:163], a[196:199], a[36:39], v[160:163]\n"
    "ds_read_b64_tr_b16 a[218:219], v57 offset:34824\n"
    "v_mfma_f32_16x16x32_bf16 v[164:167], a[196:199], a[44:47], v[164:167]\n"
    "buffer_load_dwordx4 v59, s[44:47], s65 offen offset:1024 lds\n"
    "v_mfma_f32_16x16x32_bf16 v[164:167], a[192:195], a[40:43], v[164:167]\n"
    "ds_read_b64_tr_b16 a[220:221], v57 offset:40968\n"
    "v_mfma_f32_16x16x32_bf16 v[168:171], a[192:195], a[48:51], v[168:171]\n"
    "ds_read_b64_tr_b16 a[222:223], v57 offset:47112\n"
    "v_mfma_f32_16x16x32_bf16 v[168:171], a[196:199], a[52:55], v[168:171]\n"
    "v_mfma_f32_16x16x32_bf16 v[172:175], a[196:199], a[60:63], v[172:175]\n"
    "s_add_u32 m0, s68, 53248\n"
    "ds_read_b64_tr_b16 a[224:225], v58 offset:28672\n"
    "v_mfma_f32_16x16x32_bf16 v[172:175], a[192:195], a[56:59], v[172:175]\n"
    "buffer_load_dwordx4 v59, s[44:47], s66 offen offset:0 lds\n"
    "ds_read_b64_tr_b16 a[226:227], v58 offset:34816\n"
    "v_mfma_f32_16x16x32_bf16 v[188:191], a[200:203], a[56:59], v[188:191]\n"
    "v_mfma_f32_16x16x32_bf16 v[188:191], a[204:207], a[60:63], v[188:191]\n"
    "buffer_load_dwordx4 v59, s[44:47], s67 offen offset:1024 lds\n"
    "ds_read_b64_tr_b16 a[228:229], v58 offset:40960\n"
    "v_mfma_f32_16x16x32_bf16 v[184:187], a[204:207], a[52:55], v[184:187]\n"
    "ds_read_b64_tr_b16 a[230:231], v58 offset:47104\n"
    "v_mfma_f32_16x16x32_bf16 v[184:187], a[200:203], a[48:51], v[184:187]\n"
    "v_mfma_f32_16x16x32_bf16 v[180:183], a[200:203], a[40:43], v[180:183]\n"
    "ds_read_b64_tr_b16 a[232:233], v58 offset:28680\n"
    "v_mfma_f32_16x16x32_bf16 v[180:183], a[204:207], a[44:47], v[180:183]\n"
    "ds_read_b64_tr_b16 a[234:235], v58 offset:34824\n"
    "v_add_u32 v59, 786432, v59\n"
    "v_mfma_f32_16x16x32_bf16 v[176:179], a[204:207], a[36:39], v[176:179]\n"
    "buffer_load_dwordx4 a[0:3], v9, s[52:55], s88 offen offset:0 sc1\n"
    "v_mfma_f32_16x16x32_bf16 v[176:179], a[200:203], a[32:35], v[176:179]\n"
    "ds_read_b64_tr_b16 a[236:237], v58 offset:40968\n"
    "v_mfma_f32_16x16x32_bf16 v[192:195], a[208:211], a[32:35], v[192:195]\n"
    "buffer_load_dwordx4 a[4:7], v9, s[52:55], s88 offen offset:1024 sc1\n"
    "ds_read_b64_tr_b16 a[238:239], v58 offset:47112\n"
    "v_mfma_f32_16x16x32_bf16 v[192:195], a[212:215], a[36:39], v[192:195]\n"
    "v_mfma_f32_16x16x32_bf16 v[196:199], a[212:215], a[44:47], v[196:199]\n"
    "v_mfma_f32_16x16x32_bf16 v[196:199], a[208:211], a[40:43], v[196:199]\n"
    "v_mfma_f32_16x16x32_bf16 v[200:203], a[208:211], a[48:51], v[200:203]\n"
    "buffer_load_dwordx4 a[8:11], v9, s[52:55], s88 offen offset:2048 sc1\n"
    "v_mfma_f32_16x16x32_bf16 v[200:203], a[212:215], a[52:55], v[200:203]\n"
    "v_mfma_f32_16x16x32_bf16 v[204:207], a[212:215], a[60:63], v[204:207]\n"
    "s_waitcnt vmcnt(26) lgkmcnt(0)\n"
    "s_barrier\n"
    "v_mfma_f32_16x16x32_bf16 v[204:207], a[208:211], a[56:59], v[204:207]\n"
    "v_add_u32 v57, 4294918144, v57\n"
    "v_add_u32 v58, 4294918144, v58\n"
    "v_mfma_f32_16x16x32_bf16 v[208:211], a[216:219], a[32:35], v[208:211]\n"
    "buffer_load_dwordx4 a[12:15], v9, s[52:55], s88 offen offset:3072 sc1\n"
    "ds_read_b64_tr_b16 a[192:193], v57 offset:0\n"
    "v_mfma_f32_16x16x32_bf16 v[208:211], a[220:223], a[36:39], v[208:211]\n"
    "ds_read_b64_tr_b16 a[194:195], v57 offset:6144\n"
    "v_mfma_f32_16x16x32_bf16 v[212:215], a[220:223], a[44:47], v[212:215]\n"
    "v_mfma_f32_16x16x32_bf16 v[212:215], a[216:219], a[40:43], v[212:215]\n"
    "buffer_load_dwordx4 a[16:19], v9, s[52:55], s89 offen offset:0 sc1\n"
    "ds_read_b64_tr_b16 a[196:197], v57 offset:12288\n"
    "v_mfma_f32_16x16x32_bf16 v[216:219], a[216:219], a[48:51], v[216:219]\n"
    "ds_read_b64_tr_b16 a[198:199], v57 offset:18432\n"
    "v_mfma_f32_16x16x32_bf16 v[216:219], a[220:223], a[52:55], v[216:219]\n"
    "v_mfma_f32_16x16x32_bf16 v[220:223], a[220:223], a[60:63], v[220:223]\n"
    "buffer_load_dwordx4 a[20:23], v9, s[52:55], s89 offen offset:1024 sc1\n"
    "ds_read_b64_tr_b16 a[200:201], v57 offset:8\n"
    "v_mfma_f32_16x16x32_bf16 v[220:223], a[216:219], a[56:59], v[220:223]\n"
    "ds_read_b64_tr_b16 a[202:203], v57 offset:6152\n"
    "v_mfma_f32_16x16x32_bf16 v[236:239], a[224:227], a[56:59], v[236:239]\n"
    "v_mfma_f32_16x16x32_bf16 v[236:239], a[228:231], a[60:63], v[236:239]\n"
    "ds_read_b64_tr_b16 a[204:205], v57 offset:12296\n"
    "v_mfma_f32_16x16x32_bf16 v[232:235], a[228:231], a[52:55], v[232:235]\n"
    "buffer_load_dwordx4 a[24:27], v9, s[52:55], s89 offen offset:2048 sc1\n"
    "ds_read_b64_tr_b16 a[206:207], v57 offset:18440\n"
    "v_mfma_f32_16x16x32_bf16 v[232:235], a[224:227], a[48:51], v[232:235]\n"
    "v_mfma_f32_16x16x32_bf16 v[228:231], a[224:227], a[40:43], v[228:231]\n"
    "ds_read_b64_tr_b16 a[208:209], v58 offset:0\n"
    "v_mfma_f32_16x16x32_bf16 v[228:231], a[228:231], a[44:47], v[228:231]\n"
    "buffer_load_dwordx4 a[28:31], v9, s[52:55], s89 offen offset:3072 sc1\n"
    "ds_read_b64_tr_b16 a[210:211], v58 offset:6144\n"
    "v_mfma_f32_16x16x32_bf16 v[224:227], a[228:231], a[36:39], v[224:227]\n"
    "v_mfma_f32_16x16x32_bf16 v[224:227], a[224:227], a[32:35], v[224:227]\n"
    "ds_read_b64_tr_b16 a[212:213], v58 offset:12288\n"
    "v_mfma_f32_16x16x32_bf16 v[240:243], a[232:235], a[32:35], v[240:243]\n"
    "ds_read_b64_tr_b16 a[214:215], v58 offset:18432\n"
    "v_mfma_f32_16x16x32_bf16 v[240:243], a[236:239], a[36:39], v[240:243]\n"
    "s_add_u32 s88, s88, 8192\n"
    "v_mfma_f32_16x16x32_bf16 v[244:247], a[236:239], a[44:47], v[244:247]\n"
    "s_add_u32 s89, s89, 8192\n"
    "v_mfma_f32_16x16x32_bf16 v[244:247], a[232:235], a[40:43], v[244:247]\n"
    "v_mfma_f32_16x16x32_bf16 v[248:251], a[232:235], a[48:51], v[248:251]\n"
    "v_mfma_f32_16x16x32_bf16 v[248:251], a[236:239], a[52:55], v[248:251]\n"
    "v_mfma_f32_16x16x32_bf16 v[252:255], a[236:239], a[60:63], v[252:255]\n"
    "s_waitcnt lgkmcnt(0)\n"
    "v_mfma_f32_16x16x32_bf16 v[252:255], a[232:235], a[56:59], v[252:255]\n"
    "v_mfma_f32_16x16x32_bf16 v[64:67], a[192:195], a[64:67], v[64:67]\n"
    "ds_read_b64_tr_b16 a[216:217], v58 offset:8\n"
    "v_mfma_f32_16x16x32_bf16 v[64:67], a[196:199], a[68:71], v[64:67]\n"
    "ds_read_b64_tr_b16 a[218:219], v58 offset:6152\n"
    "v_mfma_f32_16x16x32_bf16 v[68:71], a[196:199], a[76:79], v[68:71]\n"
    "s_mul_i32 s74, s78, 160\n"
    "s_mov_b64 s[48:49], s[16:17]\n"
    "s_add_u32 s48, s48, s74\n"
    "s_addc_u32 s49, s49, 0\n"
    "s_mov_b32 s50, 819200\n"
    "s_mov_b32 s51, 0x20000\n"
    "s_sub_u32 s50, s50, s74\n"
    "s_mul_i32 s90, s76, 40960\n"
    "s_cmp_eq_u32 s78, 4\n"
    "s_cselect_b32 s93, 0, 0x80000000\n"
    "v_or_b32 v48, s93, v50\n"
    "v_mfma_f32_16x16x32_bf16 v[68:71], a[192:195], a[72:75], v[68:71]\n"
    "buffer_load_dword v51, v48, s[44:47], 0 offen\n"
    "ds_read_b64_tr_b16 a[220:221], v58 offset:12296\n"
    "v_mfma_f32_16x16x32_bf16 v[72:75], a[192:195], a[80:83], v[72:75]\n"
    "ds_read_b64_tr_b16 a[222:223], v58 offset:18440\n"
    "v_mfma_f32_16x16x32_bf16 v[72:75], a[196:199], a[84:87], v[72:75]\n"
    "v_mfma_f32_16x16x32_bf16 v[76:79], a[196:199], a[92:95], v[76:79]\n"
    "ds_read_b64_tr_b16 a[224:225], v57 offset:2048\n"
    "v_lshl_or_b32 v48, %2, 7, s93\n"
    "v_mfma_f32_16x16x32_bf16 v[76:79], a[192:195], a[88:91], v[76:79]\n"
    "buffer_load_dword v51, v48, s[84:87], 0 offen\n"
    "ds_read_b64_tr_b16 a[226:227], v57 offset:8192\n"
    "v_mfma_f32_16x16x32_bf16 v[92:95], a[200:203], a[88:91], v[92:95]\n"
    "v_lshrrev_b32 v16, 4, v9\n"
    "v_add_u32 v16, 0, v16\n"
    "v_mfma_f32_16x16x32_bf16 v[92:95], a[204:207], a[92:95], v[92:95]\n"
    "ds_read_b64_tr_b16 a[228:229], v57 offset:14336\n"
    "v_mul_u32_u24 v17, 6554, v16\n"
    "v_mfma_f32_16x16x32_bf16 v[88:91], a[204:207], a[84:87], v[88:91]\n"
    "ds_read_b64_tr_b16 a[230:231], v57 offset:20480\n"
    "v_lshrrev_b32 v17, 16, v17\n"
    "v_mfma_f32_16x16x32_bf16 v[88:91], a[200:203], a[80:83], v[88:91]\n"
    "s_add_u32 m0, s68, 73728\n"
    "v_mul_u32_u24 v18, 10, v17\n"
    "v_sub_u32 v16, v16, v18\n"
    "v_mfma_f32_16x16x32_bf16 v[84:87], a[200:203], a[72:75], v[84:87]\n"
    "buffer_load_dwordx4 v59, s[44:47], s62 offen offset:0 lds\n"
    "ds_read_b64_tr_b16 a[232:233], v57 offset:2056\n"
    "v_mfma_f32_16x16x32_bf16 v[84:87], a[204:207], a[76:79], v[84:87]\n"
    "ds_read_b64_tr_b16 a[234:235], v57 offset:8200\n"
    "v_mul_u32_u24 v17, 1280, v17\n"
    "v_mfma_f32_16x16x32_bf16 v[80:83], a[204:207], a[68:71], v[80:83]\n"
    "v_lshl_add_u32 v11, v16, 4, v17\n"
    "v_lshrrev_b32 v16, 4, v9\n"
    "v_mfma_f32_16x16x32_bf16 v[80:83], a[200:203], a[64:67], v[80:83]\n"
    "ds_read_b64_tr_b16 a[236:237], v57 offset:14344\n"
    "v_add_u32 v16, 64, v16\n"
    "v_mfma_f32_16x16x32_bf16 v[96:99], a[208:211], a[64:67], v[96:99]\n"
    "ds_read_b64_tr_b16 a[238:239], v57 offset:20488\n"
    "v_mul_u32_u24 v17, 6554, v16\n"
    "v_mfma_f32_16x16x32_bf16 v[96:99], a[212:215], a[68:71], v[96:99]\n"
    "buffer_load_dwordx4 v59, s[44:47], s63 offen offset:1024 lds\n"
    "v_lshrrev_b32 v17, 16, v17\n"
    "v_mfma_f32_16x16x32_bf16 v[100:103], a[212:215], a[76:79], v[100:103]\n"
    "v_mul_u32_u24 v18, 10, v17\n"
    "v_sub_u32 v16, v16, v18\n"
    "v_mfma_f32_16x16x32_bf16 v[100:103], a[208:211], a[72:75], v[100:103]\n"
    "v_mul_u32_u24 v17, 1280, v17\n"
    "v_lshl_add_u32 v12, v16, 4, v17\n"
    "v_mfma_f32_16x16x32_bf16 v[104:107], a[208:211], a[80:83], v[104:107]\n"
    "v_subrev_u32 v12, 1024, v12\n"
    "v_lshrrev_b32 v16, 4, v9\n"
    "v_mfma_f32_16x16x32_bf16 v[104:107], a[212:215], a[84:87], v[104:107]\n"
    "v_add_u32 v16, 128, v16\n"
    "v_mul_u32_u24 v17, 6554, v16\n"
    "v_mfma_f32_16x16x32_bf16 v[108:111], a[212:215], a[92:95], v[108:111]\n"
    "s_waitcnt lgkmcnt(0)\n"
    "v_lshrrev_b32 v17, 16, v17\n"
    "v_mul_u32_u24 v18, 10, v17\n"
    "v_mfma_f32_16x16x32_bf16 v[108:111], a[208:211], a[88:91], v[108:111]\n"
    "s_add_u32 m0, s68, 75776\n"
    "v_sub_u32 v16, v16, v18\n"
    "v_mul_u32_u24 v17, 1280, v17\n"
    "v_mfma_f32_16x16x32_bf16 v[112:115], a[216:219], a[64:67], v[112:115]\n"
    "buffer_load_dwordx4 v59, s[44:47], s64 offen offset:0 lds\n"
    "ds_read_b64_tr_b16 a[192:193], v58 offset:2048\n"
    "v_mfma_f32_16x16x32_bf16 v[112:115], a[220:223], a[68:71], v[112:115]\n"
    "ds_read_b64_tr_b16 a[194:195], v58 offset:8192\n"
    "v_lshl_add_u32 v13, v16, 4, v17\n"
    "v_mfma_f32_16x16x32_bf16 v[116:119], a[220:223], a[76:79], v[116:119]\n"
    "v_subrev_u32 v13, 2048, v13\n"
    "v_lshrrev_b32 v16, 4, v9\n"
    "v_mfma_f32_16x16x32_bf16 v[116:119], a[216:219], a[72:75], v[116:119]\n"
    "ds_read_b64_tr_b16 a[196:197], v58 offset:14336\n"
    "v_add_u32 v16, 192, v16\n"
    "v_mfma_f32_16x16x32_bf16 v[120:123], a[216:219], a[80:83], v[120:123]\n"
    "buffer_load_dwordx4 v59, s[44:47], s65 offen offset:1024 lds\n"
    "ds_read_b64_tr_b16 a[198:199], v58 offset:20480\n"
    "v_mfma_f32_16x16x32_bf16 v[120:123], a[220:223], a[84:87], v[120:123]\n"
    "v_mul_u32_u24 v17, 6554, v16\n"
    "v_lshrrev_b32 v17, 16, v17\n"
    "v_mfma_f32_16x16x32_bf16 v[124:127], a[220:223], a[92:95], v[124:127]\n"
    "ds_read_b64_tr_b16 a[200:201], v58 offset:2056\n"
    "v_mul_u32_u24 v18, 10, v17\n"
    "v_mfma_f32_16x16x32_bf16 v[124:127], a[216:219], a[88:91], v[124:127]\n"
    "ds_read_b64_tr_b16 a[202:203], v58 offset:8200\n"
    "v_sub_u32 v16, v16, v18\n"
    "v_mfma_f32_16x16x32_bf16 v[140:143], a[224:227], a[88:91], v[140:143]\n"
    "v_mul_u32_u24 v17, 1280, v17\n"
    "v_lshl_add_u32 v14, v16, 4, v17\n"
    "v_mfma_f32_16x16x32_bf16 v[140:143], a[228:231], a[92:95], v[140:143]\n"
    "s_add_u32 m0, s68, 77824\n"
    "ds_read_b64_tr_b16 a[204:205], v58 offset:14344\n"
    "v_subrev_u32 v14, 3072, v14\n"
    "v_mfma_f32_16x16x32_bf16 v[136:139], a[228:231], a[84:87], v[136:139]\n"
    "buffer_load_dwordx4 v59, s[44:47], s66 offen offset:0 lds\n"
    "ds_read_b64_tr_b16 a[206:207], v58 offset:20488\n"
    "v_mfma_f32_16x16x32_bf16 v[136:139], a[224:227], a[80:83], v[136:139]\n"
    "v_lshrrev_b32 v16, 4, v9\n"
    "v_add_u32 v16, 256, v16\n"
    "v_mfma_f32_16x16x32_bf16 v[132:135], a[224:227], a[72:75], v[132:135]\n"
    "ds_read_b64_tr_b16 a[208:209], v57 offset:4096\n"
    "v_mul_u32_u24 v17, 6554, v16\n"
    "v_mfma_f32_16x16x32_bf16 v[132:135], a[228:231], a[76:79], v[132:135]\n"
    "ds_read_b64_tr_b16 a[210:211], v57 offset:10240\n"
    "v_lshrrev_b32 v17, 16, v17\n"
    "v_mfma_f32_16x16x32_bf16 v[128:131], a[228:231], a[68:71], v[128:131]\n"
    "buffer_load_dwordx4 v59, s[44:47], s67 offen offset:1024 lds\n"
    "v_mul_u32_u24 v18, 10, v17\n"
    "v_mfma_f32_16x16x32_bf16 v[128:131], a[224:227], a[64:67], v[128:131]\n"
    "ds_read_b64_tr_b16 a[212:213], v57 offset:16384\n"
    "v_sub_u32 v16, v16, v18\n"
    "v_mfma_f32_16x16x32_bf16 v[144:147], a[232:235], a[64:67], v[144:147]\n"
    "ds_read_b64_tr_b16 a[214:215], v57 offset:22528\n"
    "v_mul_u32_u24 v17, 1280, v17\n"
    "v_mfma_f32_16x16x32_bf16 v[144:147], a[236:239], a[68:71], v[144:147]\n"
    "v_lshl_add_u32 v15, v16, 4, v17\n"
    "v_mfma_f32_16x16x32_bf16 v[148:151], a[236:239], a[76:79], v[148:151]\n"
    "v_mfma_f32_16x16x32_bf16 v[148:151], a[232:235], a[72:75], v[148:151]\n"
    "v_mfma_f32_16x16x32_bf16 v[152:155], a[232:235], a[80:83], v[152:155]\n"
    "v_add_u32 v59, 786432, v59\n"
    "v_mfma_f32_16x16x32_bf16 v[152:155], a[236:239], a[84:87], v[152:155]\n"
    "buffer_load_dwordx4 a[32:35], v9, s[52:55], s88 offen offset:0 sc1\n"
    "v_mfma_f32_16x16x32_bf16 v[156:159], a[236:239], a[92:95], v[156:159]\n"
    "s_waitcnt lgkmcnt(0)\n"
    "v_mfma_f32_16x16x32_bf16 v[156:159], a[232:235], a[88:91], v[156:159]\n"
    "v_mfma_f32_16x16x32_bf16 v[160:163], a[192:195], a[64:67], v[160:163]\n"
    "ds_read_b64_tr_b16 a[216:217], v57 offset:4104\n"
    "v_mfma_f32_16x16x32_bf16 v[160:163], a[196:199], a[68:71], v[160:163]\n"
    "buffer_load_dwordx4 a[36:39], v9, s[52:55], s88 offen offset:1024 sc1\n"
    "ds_read_b64_tr_b16 a[218:219], v57 offset:10248\n"
    "v_mfma_f32_16x16x32_bf16 v[164:167], a[196:199], a[76:79], v[164:167]\n"
    "v_mfma_f32_16x16x32_bf16 v[164:167], a[192:195], a[72:75], v[164:167]\n"
    "ds_read_b64_tr_b16 a[220:221], v57 offset:16392\n"
    "v_mfma_f32_16x16x32_bf16 v[168:171], a[192:195], a[80:83], v[168:171]\n"
    "ds_read_b64_tr_b16 a[222:223], v57 offset:22536\n"
    "v_mfma_f32_16x16x32_bf16 v[168:171], a[196:199], a[84:87], v[168:171]\n"
    "v_mfma_f32_16x16x32_bf16 v[172:175], a[196:199], a[92:95], v[172:175]\n"
    "ds_read_b64_tr_b16 a[224:225], v58 offset:4096\n"
    "v_mfma_f32_16x16x32_bf16 v[172:175], a[192:195], a[88:91], v[172:175]\n"
    "buffer_load_dwordx4 a[40:43], v9, s[52:55], s88 offen offset:2048 sc1\n"
    "ds_read_b64_tr_b16 a[226:227], v58 offset:10240\n"
    "v_mfma_f32_16x16x32_bf16 v[188:191], a[200:203], a[88:91], v[188:191]\n"
    "v_mfma_f32_16x16x32_bf16 v[188:191], a[204:207], a[92:95], v[188:191]\n"
    "ds_read_b64_tr_b16 a[228:229], v58 offset:16384\n"
    "v_mfma_f32_16x16x32_bf16 v[184:187], a[204:207], a[84:87], v[184:187]\n"
    "ds_read_b64_tr_b16 a[230:231], v58 offset:22528\n"
    "v_mfma_f32_16x16x32_bf16 v[184:187], a[200:203], a[80:83], v[184:187]\n"
    "v_mfma_f32_16x16x32_bf16 v[180:183], a[200:203], a[72:75], v[180:183]\n"
    "buffer_load_dwordx4 a[44:47], v9, s[52:55], s88 offen offset:3072 sc1\n"
    "ds_read_b64_tr_b16 a[232:233], v58 offset:4104\n"
    "v_mfma_f32_16x16x32_bf16 v[180:183], a[204:207], a[76:79], v[180:183]\n"
    "ds_read_b64_tr_b16 a[234:235], v58 offset:10248\n"
    "v_mfma_f32_16x16x32_bf16 v[176:179], a[204:207], a[68:71], v[176:179]\n"
    "v_mfma_f32_16x16x32_bf16 v[176:179], a[200:203], a[64:67], v[176:179]\n"
    "ds_read_b64_tr_b16 a[236:237], v58 offset:16392\n"
    "v_mfma_f32_16x16x32_bf16 v[192:195], a[208:211], a[64:67], v[192:195]\n"
    "ds_read_b64_tr_b16 a[238:239], v58 offset:22536\n"
    "v_mfma_f32_16x16x32_bf16 v[192:195], a[212:215], a[68:71], v[192:195]\n"
    "buffer_load_dwordx4 a[48:51], v9, s[52:55], s89 offen offset:0 sc1\n"
    "v_mfma_f32_16x16x32_bf16 v[196:199], a[212:215], a[76:79], v[196:199]\n"
    "v_mfma_f32_16x16x32_bf16 v[196:199], a[208:211], a[72:75], v[196:199]\n"
    "v_mfma_f32_16x16x32_bf16 v[200:203], a[208:211], a[80:83], v[200:203]\n"
    "v_mfma_f32_16x16x32_bf16 v[200:203], a[212:215], a[84:87], v[200:203]\n"
    "v_mfma_f32_16x16x32_bf16 v[204:207], a[212:215], a[92:95], v[204:207]\n"
    "s_waitcnt vmcnt(27) lgkmcnt(0)\n"
    "s_barrier\n"
    "v_mfma_f32_16x16x32_bf16 v[204:207], a[208:211], a[88:91], v[204:207]\n"
    "buffer_load_dwordx4 a[52:55], v9, s[52:55], s89 offen offset:1024 sc1\n"
    "v_mfma_f32_16x16x32_bf16 v[208:211], a[216:219], a[64:67], v[208:211]\n"
    "ds_read_b64_tr_b16 a[192:193], v57 offset:24576\n"
    "v_mfma_f32_16x16x32_bf16 v[208:211], a[220:223], a[68:71], v[208:211]\n"
    "ds_read_b64_tr_b16 a[194:195], v57 offset:30720\n"
    "v_mfma_f32_16x16x32_bf16 v[212:215], a[220:223], a[76:79], v[212:215]\n"
    "v_mfma_f32_16x16x32_bf16 v[212:215], a[216:219], a[72:75], v[212:215]\n"
    "ds_read_b64_tr_b16 a[196:197], v57 offset:36864\n"
    "v_mfma_f32_16x16x32_bf16 v[216:219], a[216:219], a[80:83], v[216:219]\n"
    "buffer_load_dwordx4 a[56:59], v9, s[52:55], s89 offen offset:2048 sc1\n"
    "ds_read_b64_tr_b16 a[198:199], v57 offset:43008\n"
    "v_mfma_f32_16x16x32_bf16 v[216:219], a[220:223], a[84:87], v[216:219]\n"
    "v_mfma_f32_16x16x32_bf16 v[220:223], a[220:223], a[92:95], v[220:223]\n"
    "ds_read_b64_tr_b16 a[200:201], v57 offset:24584\n"
    "v_mfma_f32_16x16x32_bf16 v[220:223], a[216:219], a[88:91], v[220:223]\n"
    "ds_read_b64_tr_b16 a[202:203], v57 offset:30728\n"
    "v_mfma_f32_16x16x32_bf16 v[236:239], a[224:227], a[88:91], v[236:239]\n"
    "v_mfma_f32_16x16x32_bf16 v[236:239], a[228:231], a[92:95], v[236:239]\n"
    "ds_read_b64_tr_b16 a[204:205], v57 offset:36872\n"
    "v_mfma_f32_16x16x32_bf16 v[232:235], a[228:231], a[84:87], v[232:235]\n"
    "buffer_load_dwordx4 a[60:63], v9, s[52:55], s89 offen offset:3072 sc1\n"
    "ds_read_b64_tr_b16 a[206:207], v57 offset:43016\n"
    "v_mfma_f32_16x16x32_bf16 v[232:235], a[224:227], a[80:83], v[232:235]\n"
    "v_mfma_f32_16x16x32_bf16 v[228:231], a[224:227], a[72:75], v[228:231]\n"
    "ds_read_b64_tr_b16 a[208:209], v58 offset:24576\n"
    "v_mfma_f32_16x16x32_bf16 v[228:231], a[228:231], a[76:79], v[228:231]\n"
    "ds_read_b64_tr_b16 a[210:211], v58 offset:30720\n"
    "v_mfma_f32_16x16x32_bf16 v[224:227], a[228:231], a[68:71], v[224:227]\n"
    "v_mfma_f32_16x16x32_bf16 v[224:227], a[224:227], a[64:67], v[224:227]\n"
    "s_add_u32 s88, s88, 8192\n"
    "ds_read_b64_tr_b16 a[212:213], v58 offset:36864\n"
    "v_mfma_f32_16x16x32_bf16 v[240:243], a[232:235], a[64:67], v[240:243]\n"
    "s_add_u32 s89, s89, 8192\n"
    "ds_read_b64_tr_b16 a[214:215], v58 offset:43008\n"
    "v_mfma_f32_16x16x32_bf16 v[240:243], a[236:239], a[68:71], v[240:243]\n"
    "v_mfma_f32_16x16x32_bf16 v[244:247], a[236:239], a[76:79], v[244:247]\n"
    "v_mfma_f32_16x16x32_bf16 v[244:247], a[232:235], a[72:75], v[244:247]\n"
    "v_mfma_f32_16x16x32_bf16 v[248:251], a[232:235], a[80:83], v[248:251]\n"
    "v_mfma_f32_16x16x32_bf16 v[248:251], a[236:239], a[84:87], v[248:251]\n"
    "v_mfma_f32_16x16x32_bf16 v[252:255], a[236:239], a[92:95], v[252:255]\n"
    "s_waitcnt lgkmcnt(0)\n"
    "v_mfma_f32_16x16x32_bf16 v[252:255], a[232:235], a[88:91], v[252:255]\n"
    "v_mfma_f32_16x16x32_bf16 v[64:67], a[192:195], a[96:99], v[64:67]\n"
    "ds_read_b64_tr_b16 a[216:217], v58 offset:24584\n"
    "v_mfma_f32_16x16x32_bf16 v[64:67], a[196:199], a[100:103], v[64:67]\n"
    "ds_read_b64_tr_b16 a[218:219], v58 offset:30728\n"
    "v_mfma_f32_16x16x32_bf16 v[68:71], a[196:199], a[108:111], v[68:71]\n"
    "s_cmp_eq_u32 s78, 5\n"
    "s_cselect_b32 s93, 0, 0x80000000\n"
    "v_or_b32 v48, s93, v50\n"
    "v_mfma_f32_16x16x32_bf16 v[68:71], a[192:195], a[104:107], v[68:71]\n"
    "buffer_load_dword v51, v48, s[44:47], 0 offen\n"
    "ds_read_b64_tr_b16 a[220:221], v58 offset:36872\n"
    "v_mfma_f32_16x16x32_bf16 v[72:75], a[192:195], a[112:115], v[72:75]\n"
    "ds_read_b64_tr_b16 a[222:223], v58 offset:43016\n"
    "v_mfma_f32_16x16x32_bf16 v[72:75], a[196:199], a[116:119], v[72:75]\n"
    "v_lshl_or_b32 v48, %2, 7, s93\n"
    "v_mfma_f32_16x16x32_bf16 v[76:79], a[196:199], a[124:127], v[76:79]\n"
    "buffer_load_dword v51, v48, s[84:87], 0 offen\n"
    "ds_read_b64_tr_b16 a[224:225], v57 offset:26624\n"
    "v_mfma_f32_16x16x32_bf16 v[76:79], a[192:195], a[120:123], v[76:79]\n"
    "ds_read_b64_tr_b16 a[226:227], v57 offset:32768\n"
    "v_mfma_f32_16x16x32_bf16 v[92:95], a[200:203], a[120:123], v[92:95]\n"
    "v_mfma_f32_16x16x32_bf16 v[92:95], a[204:207], a[124:127], v[92:95]\n"
    "s_add_u32 m0, s68, 0\n"
    "ds_read_b64_tr_b16 a[228:229], v57 offset:38912\n"
    "v_mfma_f32_16x16x32_bf16 v[88:91], a[204:207], a[116:119], v[88:91]\n"
    "buffer_load_dwordx4 v59, s[44:47], s62 offen offset:0 lds\n"
    "ds_read_b64_tr_b16 a[230:231], v57 offset:45056\n"
    "v_mfma_f32_16x16x32_bf16 v[88:91], a[200:203], a[112:115], v[88:91]\n"
    "v_mfma_f32_16x16x32_bf16 v[84:87], a[200:203], a[104:107], v[84:87]\n"
    "ds_read_b64_tr_b16 a[232:233], v57 offset:26632\n"
    "v_mfma_f32_16x16x32_bf16 v[84:87], a[204:207], a[108:111], v[84:87]\n"
    "buffer_load_dwordx4 v59, s[44:47], s63 offen offset:1024 lds\n"
    "ds_read_b64_tr_b16 a[234:235], v57 offset:32776\n"
    "v_mfma_f32_16x16x32_bf16 v[80:83], a[204:207], a[100:103], v[80:83]\n"
    "v_mfma_f32_16x16x32_bf16 v[80:83], a[200:203], a[96:99], v[80:83]\n"
    "ds_read_b64_tr_b16 a[236:237], v57 offset:38920\n"
    "v_mfma_f32_16x16x32_bf16 v[96:99], a[208:211], a[96:99], v[96:99]\n"
    "ds_read_b64_tr_b16 a[238:239], v57 offset:45064\n"
    "v_mfma_f32_16x16x32_bf16 v[96:99], a[212:215], a[100:103], v[96:99]\n"
    "s_add_u32 m0, s68, 2048\n"
    "v_mfma_f32_16x16x32_bf16 v[100:103], a[212:215], a[108:111], v[100:103]\n"
    "buffer_load_dwordx4 v59, s[44:47], s64 offen offset:0 lds\n"
    "v_mfma_f32_16x16x32_bf16 v[100:103], a[208:211], a[104:107], v[100:103]\n"
    "v_mfma_f32_16x16x32_bf16 v[104:107], a[208:211], a[112:115], v[104:107]\n"
    "v_mfma_f32_16x16x32_bf16 v[104:107], a[212:215], a[116:119], v[104:107]\n"
    "buffer_load_dwordx4 v59, s[44:47], s65 offen offset:1024 lds\n"
    "v_mfma_f32_16x16x32_bf16 v[108:111], a[212:215], a[124:127], v[108:111]\n"
    "s_waitcnt lgkmcnt(0)\n"
    "v_mfma_f32_16x16x32_bf16 v[108:111], a[208:211], a[120:123], v[108:111]\n"
    "v_mfma_f32_16x16x32_bf16 v[112:115], a[216:219], a[96:99], v[112:115]\n"
    "ds_read_b64_tr_b16 a[192:193], v58 offset:26624\n"
    "v_mfma_f32_16x16x32_bf16 v[112:115], a[220:223], a[100:103], v[112:115]\n"
    "ds_read_b64_tr_b16 a[194:195], v58 offset:32768\n"
    "v_mfma_f32_16x16x32_bf16 v[116:119], a[220:223], a[108:111], v[116:119]\n"
    "s_add_u32 m0, s68, 4096\n"
    "v_mfma_f32_16x16x32_bf16 v[116:119], a[216:219], a[104:107], v[116:119]\n"
    "buffer_load_dwordx4 v59, s[44:47], s66 offen offset:0 lds\n"
    "ds_read_b64_tr_b16 a[196:197], v58 offset:38912\n"
    "v_mfma_f32_16x16x32_bf16 v[120:123], a[216:219], a[112:115], v[120:123]\n"
    "ds_read_b64_tr_b16 a[198:199], v58 offset:45056\n"
    "v_mfma_f32_16x16x32_bf16 v[120:123], a[220:223], a[116:119], v[120:123]\n"
    "v_mfma_f32_16x16x32_bf16 v[124:127], a[220:223], a[124:127], v[124:127]\n"
    "buffer_load_dwordx4 v59, s[44:47], s67 offen offset:1024 lds\n"
    "ds_read_b64_tr_b16 a[200:201], v58 offset:26632\n"
    "v_mfma_f32_16x16x32_bf16 v[124:127], a[216:219], a[120:123], v[124:127]\n"
    "ds_read_b64_tr_b16 a[202:203], v58 offset:32776\n"
    "v_mfma_f32_16x16x32_bf16 v[140:143], a[224:227], a[120:123], v[140:143]\n"
    "v_mfma_f32_16x16x32_bf16 v[140:143], a[228:231], a[124:127], v[140:143]\n"
    "ds_read_b64_tr_b16 a[204:205], v58 offset:38920\n"
    "v_mfma_f32_16x16x32_bf16 v[136:139], a[228:231], a[116:119], v[136:139]\n"
    "ds_read_b64_tr_b16 a[206:207], v58 offset:45064\n"
    "v_add_u32 v59, 786432, v59\n"
    "v_mfma_f32_16x16x32_bf16 v[136:139], a[224:227], a[112:115], v[136:139]\n"
    "buffer_load_dwordx4 a[64:67], v9, s[52:55], s88 offen offset:0 sc1\n"
    "v_mfma_f32_16x16x32_bf16 v[132:135], a[224:227], a[104:107], v[132:135]\n"
    "ds_read_b64_tr_b16 a[208:209], v57 offset:28672\n"
    "v_mfma_f32_16x16x32_bf16 v[132:135], a[228:231], a[108:111], v[132:135]\n"
    "ds_read_b64_tr_b16 a[210:211], v57 offset:34816\n"
    "v_mfma_f32_16x16x32_bf16 v[128:131], a[228:231], a[100:103], v[128:131]\n"
    "buffer_load_dwordx4 a[68:71], v9, s[52:55], s88 offen offset:1024 sc1\n"
    "v_mfma_f32_16x16x32_bf16 v[128:131], a[224:227], a[96:99], v[128:131]\n"
    "ds_read_b64_tr_b16 a[212:213], v57 offset:40960\n"
    "v_mfma_f32_16x16x32_bf16 v[144:147], a[232:235], a[96:99], v[144:147]\n"
    "ds_read_b64_tr_b16 a[214:215], v57 offset:47104\n"
    "v_mfma_f32_16x16x32_bf16 v[144:147], a[236:239], a[100:103], v[144:147]\n"
    "v_mfma_f32_16x16x32_bf16 v[148:151], a[236:239], a[108:111], v[148:151]\n"
    "buffer_load_dwordx4 a[72:75], v9, s[52:55], s88 offen offset:2048 sc1\n"
    "v_mfma_f32_16x16x32_bf16 v[148:151], a[232:235], a[104:107], v[148:151]\n"
    "v_mfma_f32_16x16x32_bf16 v[152:155], a[232:235], a[112:115], v[152:155]\n"
    "v_mfma_f32_16x16x32_bf16 v[152:155], a[236:239], a[116:119], v[152:155]\n"
    "v_mfma_f32_16x16x32_bf16 v[156:159], a[236:239], a[124:127], v[156:159]\n"
    "s_waitcnt lgkmcnt(0)\n"
    "v_mfma_f32_16x16x32_bf16 v[156:159], a[232:235], a[120:123], v[156:159]\n"
    "buffer_load_dwordx4 a[76:79], v9, s[52:55], s88 offen offset:3072 sc1\n"
    "v_mfma_f32_16x16x32_bf16 v[160:163], a[192:195], a[96:99], v[160:163]\n"
    "ds_read_b64_tr_b16 a[216:217], v57 offset:28680\n"
    "v_mfma_f32_16x16x32_bf16 v[160:163], a[196:199], a[100:103], v[160:163]\n"
    "ds_read_b64_tr_b16 a[218:219], v57 offset:34824\n"
    "v_mfma_f32_16x16x32_bf16 v[164:167], a[196:199], a[108:111], v[164:167]\n"
    "v_mfma_f32_16x16x32_bf16 v[164:167], a[192:195], a[104:107], v[164:167]\n"
    "buffer_load_dwordx4 a[80:83], v9, s[52:55], s89 offen offset:0 sc1\n"
    "ds_read_b64_tr_b16 a[220:221], v57 offset:40968\n"
    "v_mfma_f32_16x16x32_bf16 v[168:171], a[192:195], a[112:115], v[168:171]\n"
    "ds_read_b64_tr_b16 a[222:223], v57 offset:47112\n"
    "v_mfma_f32_16x16x32_bf16 v[168:171], a[196:199], a[116:119], v[168:171]\n"
    "v_mfma_f32_16x16x32_bf16 v[172:175], a[196:199], a[124:127], v[172:175]\n"
    "ds_read_b64_tr_b16 a[224:225], v58 offset:28672\n"
    "v_mfma_f32_16x16x32_bf16 v[172:175], a[192:195], a[120:123], v[172:175]\n"
    "buffer_load_dwordx4 a[84:87], v9, s[52:55], s89 offen offset:1024 sc1\n"
    "ds_read_b64_tr_b16 a[226:227], v58 offset:34816\n"
    "v_mfma_f32_16x16x32_bf16 v[188:191], a[200:203], a[120:123], v[188:191]\n"
    "v_mfma_f32_16x16x32_bf16 v[188:191], a[204:207], a[124:127], v[188:191]\n"
    "ds_read_b64_tr_b16 a[228:229], v58 offset:40960\n"
    "v_mfma_f32_16x16x32_bf16 v[184:187], a[204:207], a[116:119], v[184:187]\n"
    "ds_read_b64_tr_b16 a[230:231], v58 offset:47104\n"
    "v_mfma_f32_16x16x32_bf16 v[184:187], a[200:203], a[112:115], v[184:187]\n"
    "buffer_load_dwordx4 a[88:91], v9, s[52:55], s89 offen offset:2048 sc1\n"
    "v_mfma_f32_16x16x32_bf16 v[180:183], a[200:203], a[104:107], v[180:183]\n"
    "ds_read_b64_tr_b16 a[232:233], v58 offset:28680\n"
    "v_mfma_f32_16x16x32_bf16 v[180:183], a[204:207], a[108:111], v[180:183]\n"
    "ds_read_b64_tr_b16 a[234:235], v58 offset:34824\n"
    "v_mfma_f32_16x16x32_bf16 v[176:179], a[204:207], a[100:103], v[176:179]\n"
    "v_mfma_f32_16x16x32_bf16 v[176:179], a[200:203], a[96:99], v[176:179]\n"
    "buffer_load_dwordx4 a[92:95], v9, s[52:55], s89 offen offset:3072 sc1\n"
    "ds_read_b64_tr_b16 a[236:237], v58 offset:40968\n"
    "v_mfma_f32_16x16x32_bf16 v[192:195], a[208:211], a[96:99], v[192:195]\n"
    "ds_read_b64_tr_b16 a[238:239], v58 offset:47112\n"
    "v_mfma_f32_16x16x32_bf16 v[192:195], a[212:215], a[100:103], v[192:195]\n"
    "v_mfma_f32_16x16x32_bf16 v[196:199], a[212:215], a[108:111], v[196:199]\n"
    "v_mfma_f32_16x16x32_bf16 v[196:199], a[208:211], a[104:107], v[196:199]\n"
    "s_add_u32 s88, s88, 8192\n"
    "v_mfma_f32_16x16x32_bf16 v[200:203], a[208:211], a[112:115], v[200:203]\n"
    "s_add_u32 s89, s89, 8192\n"
    "v_mfma_f32_16x16x32_bf16 v[200:203], a[212:215], a[116:119], v[200:203]\n"
    "s_add_u32 m0, s91, 98304\n"
    "v_mfma_f32_16x16x32_bf16 v[204:207], a[212:215], a[124:127], v[204:207]\n"
    "s_waitcnt vmcnt(32) lgkmcnt(0)\n"
    "s_barrier\n"
    "v_mfma_f32_16x16x32_bf16 v[204:207], a[208:211], a[120:123], v[204:207]\n"
    "v_add_u32 v57, 49152, v57\n"
    "v_add_u32 v58, 49152, v58\n"
    "v_mfma_f32_16x16x32_bf16 v[208:211], a[216:219], a[96:99], v[208:211]\n"
    "buffer_load_dwordx4 v11, s[48:51], s90 offen offset:0 lds\n"
    "ds_read_b64_tr_b16 a[192:193], v57 offset:0\n"
    "v_mfma_f32_16x16x32_bf16 v[208:211], a[220:223], a[100:103], v[208:211]\n"
    "buffer_load_dwordx4 v12, s[48:51], s90 offen offset:1024 lds\n"
    "ds_read_b64_tr_b16 a[194:195], v57 offset:6144\n"
    "v_mfma_f32_16x16x32_bf16 v[212:215], a[220:223], a[108:111], v[212:215]\n"
    "v_mfma_f32_16x16x32_bf16 v[212:215], a[216:219], a[104:107], v[212:215]\n"
    "ds_read_b64_tr_b16 a[196:197], v57 offset:12288\n"
    "v_mfma_f32_16x16x32_bf16 v[216:219], a[216:219], a[112:115], v[216:219]\n"
    "buffer_load_dwordx4 v13, s[48:51], s90 offen offset:2048 lds\n"
    "ds_read_b64_tr_b16 a[198:199], v57 offset:18432\n"
    "v_mfma_f32_16x16x32_bf16 v[216:219], a[220:223], a[116:119], v[216:219]\n"
    "v_mfma_f32_16x16x32_bf16 v[220:223], a[220:223], a[124:127], v[220:223]\n"
    "ds_read_b64_tr_b16 a[200:201], v57 offset:8\n"
    "v_mfma_f32_16x16x32_bf16 v[220:223], a[216:219], a[120:123], v[220:223]\n"
    "ds_read_b64_tr_b16 a[202:203], v57 offset:6152\n"
    "v_mfma_f32_16x16x32_bf16 v[236:239], a[224:227], a[120:123], v[236:239]\n"
    "buffer_load_dwordx4 v14, s[48:51], s90 offen offset:3072 lds\n"
    "v_mfma_f32_16x16x32_bf16 v[236:239], a[228:231], a[124:127], v[236:239]\n"
    "ds_read_b64_tr_b16 a[204:205], v57 offset:12296\n"
    "v_mfma_f32_16x16x32_bf16 v[232:235], a[228:231], a[116:119], v[232:235]\n"
    "ds_read_b64_tr_b16 a[206:207], v57 offset:18440\n"
    "v_mfma_f32_16x16x32_bf16 v[232:235], a[224:227], a[112:115], v[232:235]\n"
    "v_mfma_f32_16x16x32_bf16 v[228:231], a[224:227], a[104:107], v[228:231]\n"
    "s_add_u32 m0, s91, 102400\n"
    "ds_read_b64_tr_b16 a[208:209], v58 offset:0\n"
    "v_mfma_f32_16x16x32_bf16 v[228:231], a[228:231], a[108:111], v[228:231]\n"
    "buffer_load_dwordx4 v15, s[48:51], s90 offen offset:0 lds\n"
    "ds_read_b64_tr_b16 a[210:211], v58 offset:6144\n"
    "v_mfma_f32_16x16x32_bf16 v[224:227], a[228:231], a[100:103], v[224:227]\n"
    "v_mfma_f32_16x16x32_bf16 v[224:227], a[224:227], a[96:99], v[224:227]\n"
    "ds_read_b64_tr_b16 a[212:213], v58 offset:12288\n"
    "v_mfma_f32_16x16x32_bf16 v[240:243], a[232:235], a[96:99], v[240:243]\n"
    "s_add_u32 s90, s90, 163840\n"
    "ds_read_b64_tr_b16 a[214:215], v58 offset:18432\n"
    "v_mfma_f32_16x16x32_bf16 v[240:243], a[236:239], a[100:103], v[240:243]\n"
    "v_mfma_f32_16x16x32_bf16 v[244:247], a[236:239], a[108:111], v[244:247]\n"
    "v_mfma_f32_16x16x32_bf16 v[244:247], a[232:235], a[104:107], v[244:247]\n"
    "v_mfma_f32_16x16x32_bf16 v[248:251], a[232:235], a[112:115], v[248:251]\n"
    "v_mfma_f32_16x16x32_bf16 v[248:251], a[236:239], a[116:119], v[248:251]\n"
    "v_mfma_f32_16x16x32_bf16 v[252:255], a[236:239], a[124:127], v[252:255]\n"
    "s_waitcnt lgkmcnt(0)\n"
    "v_mfma_f32_16x16x32_bf16 v[252:255], a[232:235], a[120:123], v[252:255]\n"
    "v_mfma_f32_16x16x32_bf16 v[64:67], a[192:195], a[0:3], v[64:67]\n"
    "ds_read_b64_tr_b16 a[216:217], v58 offset:8\n"
    "v_mfma_f32_16x16x32_bf16 v[64:67], a[196:199], a[4:7], v[64:67]\n"
    "ds_read_b64_tr_b16 a[218:219], v58 offset:6152\n"
    "v_mfma_f32_16x16x32_bf16 v[68:71], a[196:199], a[12:15], v[68:71]\n"
    "s_cmp_eq_u32 s78, 6\n"
    "s_cselect_b32 s93, 0, 0x80000000\n"
    "v_or_b32 v48, s93, v50\n"
    "v_mfma_f32_16x16x32_bf16 v[68:71], a[192:195], a[8:11], v[68:71]\n"
    "buffer_load_dword v51, v48, s[44:47], 0 offen\n"
    "ds_read_b64_tr_b16 a[220:221], v58 offset:12296\n"
    "v_mfma_f32_16x16x32_bf16 v[72:75], a[192:195], a[16:19], v[72:75]\n"
    "ds_read_b64_tr_b16 a[222:223], v58 offset:18440\n"
    "v_mfma_f32_16x16x32_bf16 v[72:75], a[196:199], a[20:23], v[72:75]\n"
    "v_lshl_or_b32 v48, %2, 7, s93\n"
    "v_mfma_f32_16x16x32_bf16 v[76:79], a[196:199], a[28:31], v[76:79]\n"
    "buffer_load_dword v51, v48, s[84:87], 0 offen\n"
    "ds_read_b64_tr_b16 a[224:225], v57 offset:2048\n"
    "v_mfma_f32_16x16x32_bf16 v[76:79], a[192:195], a[24:27], v[76:79]\n"
    "ds_read_b64_tr_b16 a[226:227], v57 offset:8192\n"
    "v_mfma_f32_16x16x32_bf16 v[92:95], a[200:203], a[24:27], v[92:95]\n"
    "v_mfma_f32_16x16x32_bf16 v[92:95], a[204:207], a[28:31], v[92:95]\n"
    "s_lshl_b32 s73, s82, 3\n"
    "ds_read_b64_tr_b16 a[228:229], v57 offset:14336\n"
    "v_mfma_f32_16x16x32_bf16 v[88:91], a[204:207], a[20:23], v[88:91]\n"
    "s_sub_u32 s73, s73, 8\n"
    "ds_read_b64_tr_b16 a[230:231], v57 offset:20480\n"
    "v_lshrrev_b32 v56, 4, v9\n"
    "v_mfma_f32_16x16x32_bf16 v[88:91], a[200:203], a[16:19], v[88:91]\n"
    "v_min_u32 v56, 23, v56\n"
    "v_add_u32 v56, s73, v56\n"
    "v_mfma_f32_16x16x32_bf16 v[84:87], a[200:203], a[8:11], v[84:87]\n"
    "ds_read_b64_tr_b16 a[232:233], v57 offset:2056\n"
    "v_max_i32 v56, 0, v56\n"
    "v_mfma_f32_16x16x32_bf16 v[84:87], a[204:207], a[12:15], v[84:87]\n"
    "ds_read_b64_tr_b16 a[234:235], v57 offset:8200\n"
    "v_min_i32 v56, 0xff, v56\n"
    "v_mfma_f32_16x16x32_bf16 v[80:83], a[204:207], a[4:7], v[80:83]\n"
    "v_lshlrev_b32 v56, 6, v56\n"
    "v_add_u32 v56, 20480, v56\n"
    "v_mfma_f32_16x16x32_bf16 v[80:83], a[200:203], a[0:3], v[80:83]\n"
    "buffer_load_dwordx2 v[62:63], v56, s[52:55], 0 offen sc1\n"
    "ds_read_b64_tr_b16 a[236:237], v57 offset:14344\n"
    "v_mfma_f32_16x16x32_bf16 v[96:99], a[208:211], a[0:3], v[96:99]\n"
    "s_add_u32 m0, s68, 24576\n"
    "ds_read_b64_tr_b16 a[238:239], v57 offset:20488\n"
    "v_mfma_f32_16x16x32_bf16 v[96:99], a[212:215], a[4:7], v[96:99]\n"
    "buffer_load_dwordx4 v59, s[44:47], s62 offen offset:0 lds\n"
    "v_mfma_f32_16x16x32_bf16 v[100:103], a[212:215], a[12:15], v[100:103]\n"
    "buffer_load_dwordx4 v59, s[44:47], s63 offen offset:1024 lds\n"
    "v_mfma_f32_16x16x32_bf16 v[100:103], a[208:211], a[8:11], v[100:103]\n"
    "v_mfma_f32_16x16x32_bf16 v[104:107], a[208:211], a[16:19], v[104:107]\n"
    "v_mfma_f32_16x16x32_bf16 v[104:107], a[212:215], a[20:23], v[104:107]\n"
    "s_add_u32 m0, s68, 26624\n"
    "v_mfma_f32_16x16x32_bf16 v[108:111], a[212:215], a[28:31], v[108:111]\n"
    "s_waitcnt lgkmcnt(0)\n"
    "buffer_load_dwordx4 v59, s[44:47], s64 offen offset:0 lds\n"
    "v_mfma_f32_16x16x32_bf16 v[108:111], a[208:211], a[24:27], v[108:111]\n"
    "v_mfma_f32_16x16x32_bf16 v[112:115], a[216:219], a[0:3], v[112:115]\n"
    "ds_read_b64_tr_b16 a[192:193], v58 offset:2048\n"
    "v_mfma_f32_16x16x32_bf16 v[112:115], a[220:223], a[4:7], v[112:115]\n"
    "buffer_load_dwordx4 v59, s[44:47], s65 offen offset:1024 lds\n"
    "ds_read_b64_tr_b16 a[194:195], v58 offset:8192\n"
    "v_mfma_f32_16x16x32_bf16 v[116:119], a[220:223], a[12:15], v[116:119]\n"
    "v_mfma_f32_16x16x32_bf16 v[116:119], a[216:219], a[8:11], v[116:119]\n"
    "ds_read_b64_tr_b16 a[196:197], v58 offset:14336\n"
    "v_mfma_f32_16x16x32_bf16 v[120:123], a[216:219], a[16:19], v[120:123]\n"
    "ds_read_b64_tr_b16 a[198:199], v58 offset:20480\n"
    "v_mfma_f32_16x16x32_bf16 v[120:123], a[220:223], a[20:23], v[120:123]\n"
    "s_add_u32 m0, s68, 28672\n"
    "v_mfma_f32_16x16x32_bf16 v[124:127], a[220:223], a[28:31], v[124:127]\n"
    "buffer_load_dwordx4 v59, s[44:47], s66 offen offset:0 lds\n"
    "ds_read_b64_tr_b16 a[200:201], v58 offset:2056\n"
    "v_mfma_f32_16x16x32_bf16 v[124:127], a[216:219], a[24:27], v[124:127]\n"
    "ds_read_b64_tr_b16 a[202:203], v58 offset:8200\n"
    "v_mfma_f32_16x16x32_bf16 v[140:143], a[224:227], a[24:27], v[140:143]\n"
    "v_mfma_f32_16x16x32_bf16 v[140:143], a[228:231], a[28:31], v[140:143]\n"
    "buffer_load_dwordx4 v59, s[44:47], s67 offen offset:1024 lds\n"
    "ds_read_b64_tr_b16 a[204:205], v58 offset:14344\n"
    "v_mfma_f32_16x16x32_bf16 v[136:139], a[228:231], a[20:23], v[136:139]\n"
    "ds_read_b64_tr_b16 a[206:207], v58 offset:20488\n"
    "v_mfma_f32_16x16x32_bf16 v[136:139], a[224:227], a[16:19], v[136:139]\n"
    "v_mfma_f32_16x16x32_bf16 v[132:135], a[224:227], a[8:11], v[132:135]\n"
    "ds_read_b64_tr_b16 a[208:209], v57 offset:4096\n"
    "v_mfma_f32_16x16x32_bf16 v[132:135], a[228:231], a[12:15], v[132:135]\n"
    "ds_read_b64_tr_b16 a[210:211], v57 offset:10240\n"
    "v_add_u32 v59, 786432, v59\n"
    "v_mfma_f32_16x16x32_bf16 v[128:131], a[228:231], a[4:7], v[128:131]\n"
    "buffer_load_dwordx4 a[96:99], v9, s[52:55], s88 offen offset:0 sc1\n"
    "v_mfma_f32_16x16x32_bf16 v[128:131], a[224:227], a[0:3], v[128:131]\n"
    "ds_read_b64_tr_b16 a[212:213], v57 offset:16384\n"
    "v_mfma_f32_16x16x32_bf16 v[144:147], a[232:235], a[0:3], v[144:147]\n"
    "ds_read_b64_tr_b16 a[214:215], v57 offset:22528\n"
    "v_mfma_f32_16x16x32_bf16 v[144:147], a[236:239], a[4:7], v[144:147]\n"
    "buffer_load_dwordx4 a[100:103], v9, s[52:55], s88 offen offset:1024 sc1\n"
    "v_mfma_f32_16x16x32_bf16 v[148:151], a[236:239], a[12:15], v[148:151]\n"
    "v_mfma_f32_16x16x32_bf16 v[148:151], a[232:235], a[8:11], v[148:151]\n"
    "v_mfma_f32_16x16x32_bf16 v[152:155], a[232:235], a[16:19], v[152:155]\n"
    "v_mfma_f32_16x16x32_bf16 v[152:155], a[236:239], a[20:23], v[152:155]\n"
    "buffer_load_dwordx4 a[104:107], v9, s[52:55], s88 offen offset:2048 sc1\n"
    "v_mfma_f32_16x16x32_bf16 v[156:159], a[236:239], a[28:31], v[156:159]\n"
    "s_waitcnt lgkmcnt(0)\n"
    "v_mfma_f32_16x16x32_bf16 v[156:159], a[232:235], a[24:27], v[156:159]\n"
    "v_mfma_f32_16x16x32_bf16 v[160:163], a[192:195], a[0:3], v[160:163]\n"
    "ds_read_b64_tr_b16 a[216:217], v57 offset:4104\n"
    "v_mfma_f32_16x16x32_bf16 v[160:163], a[196:199], a[4:7], v[160:163]\n"
    "buffer_load_dwordx4 a[108:111], v9, s[52:55], s88 offen offset:3072 sc1\n"
    "ds_read_b64_tr_b16 a[218:219], v57 offset:10248\n"
    "v_mfma_f32_16x16x32_bf16 v[164:167], a[196:199], a[12:15], v[164:167]\n"
    "v_mfma_f32_16x16x32_bf16 v[164:167], a[192:195], a[8:11], v[164:167]\n"
    "ds_read_b64_tr_b16 a[220:221], v57 offset:16392\n"
    "v_mfma_f32_16x16x32_bf16 v[168:171], a[192:195], a[16:19], v[168:171]\n"
    "ds_read_b64_tr_b16 a[222:223], v57 offset:22536\n"
    "v_mfma_f32_16x16x32_bf16 v[168:171], a[196:199], a[20:23], v[168:171]\n"
    "buffer_load_dwordx4 a[112:115], v9, s[52:55], s89 offen offset:0 sc1\n"
    "v_mfma_f32_16x16x32_bf16 v[172:175], a[196:199], a[28:31], v[172:175]\n"
    "ds_read_b64_tr_b16 a[224:225], v58 offset:4096\n"
    "v_mfma_f32_16x16x32_bf16 v[172:175], a[192:195], a[24:27], v[172:175]\n"
    "ds_read_b64_tr_b16 a[226:227], v58 offset:10240\n"
    "v_mfma_f32_16x16x32_bf16 v[188:191], a[200:203], a[24:27], v[188:191]\n"
    "v_mfma_f32_16x16x32_bf16 v[188:191], a[204:207], a[28:31], v[188:191]\n"
    "buffer_load_dwordx4 a[116:119], v9, s[52:55], s89 offen offset:1024 sc1\n"
    "ds_read_b64_tr_b16 a[228:229], v58 offset:16384\n"
    "v_mfma_f32_16x16x32_bf16 v[184:187], a[204:207], a[20:23], v[184:187]\n"
    "ds_read_b64_tr_b16 a[230:231], v58 offset:22528\n"
    "v_mfma_f32_16x16x32_bf16 v[184:187], a[200:203], a[16:19], v[184:187]\n"
    "v_mfma_f32_16x16x32_bf16 v[180:183], a[200:203], a[8:11], v[180:183]\n"
    "ds_read_b64_tr_b16 a[232:233], v58 offset:4104\n"
    "v_mfma_f32_16x16x32_bf16 v[180:183], a[204:207], a[12:15], v[180:183]\n"
    "buffer_load_dwordx4 a[120:123], v9, s[52:55], s89 offen offset:2048 sc1\n"
    "ds_read_b64_tr_b16 a[234:235], v58 offset:10248\n"
    "v_mfma_f32_16x16x32_bf16 v[176:179], a[204:207], a[4:7], v[176:179]\n"
    "v_mfma_f32_16x16x32_bf16 v[176:179], a[200:203], a[0:3], v[176:179]\n"
    "ds_read_b64_tr_b16 a[236:237], v58 offset:16392\n"
    "v_mfma_f32_16x16x32_bf16 v[192:195], a[208:211], a[0:3], v[192:195]\n"
    "ds_read_b64_tr_b16 a[238:239], v58 offset:22536\n"
    "v_mfma_f32_16x16x32_bf16 v[192:195], a[212:215], a[4:7], v[192:195]\n"
    "buffer_load_dwordx4 a[124:127], v9, s[52:55], s89 offen offset:3072 sc1\n"
    "v_mfma_f32_16x16x32_bf16 v[196:199], a[212:215], a[12:15], v[196:199]\n"
    "v_mfma_f32_16x16x32_bf16 v[196:199], a[208:211], a[8:11], v[196:199]\n"
    "v_mfma_f32_16x16x32_bf16 v[200:203], a[208:211], a[16:19], v[200:203]\n"
    "v_mfma_f32_16x16x32_bf16 v[200:203], a[212:215], a[20:23], v[200:203]\n"
    "s_add_u32 s88, s88, 8192\n"
    "v_mfma_f32_16x16x32_bf16 v[204:207], a[212:215], a[28:31], v[204:207]\n"
    "s_waitcnt vmcnt(38) lgkmcnt(0)\n"
    "s_barrier\n"
    "v_mfma_f32_16x16x32_bf16 v[204:207], a[208:211], a[24:27], v[204:207]\n"
    "s_add_u32 s89, s89, 8192\n"
    "v_mfma_f32_16x16x32_bf16 v[208:211], a[216:219], a[0:3], v[208:211]\n"
    "s_add_u32 m0, s91, 118784\n"
    "ds_read_b64_tr_b16 a[192:193], v57 offset:24576\n"
    "v_mfma_f32_16x16x32_bf16 v[208:211], a[220:223], a[4:7], v[208:211]\n"
    "buffer_load_dwordx4 v11, s[48:51], s90 offen offset:0 lds\n"
    "ds_read_b64_tr_b16 a[194:195], v57 offset:30720\n"
    "v_mfma_f32_16x16x32_bf16 v[212:215], a[220:223], a[12:15], v[212:215]\n"
    "buffer_load_dwordx4 v12, s[48:51], s90 offen offset:1024 lds\n"
    "v_mfma_f32_16x16x32_bf16 v[212:215], a[216:219], a[8:11], v[212:215]\n"
    "ds_read_b64_tr_b16 a[196:197], v57 offset:36864\n"
    "v_mfma_f32_16x16x32_bf16 v[216:219], a[216:219], a[16:19], v[216:219]\n"
    "ds_read_b64_tr_b16 a[198:199], v57 offset:43008\n"
    "v_mfma_f32_16x16x32_bf16 v[216:219], a[220:223], a[20:23], v[216:219]\n"
    "buffer_load_dwordx4 v13, s[48:51], s90 offen offset:2048 lds\n"
    "v_mfma_f32_16x16x32_bf16 v[220:223], a[220:223], a[28:31], v[220:223]\n"
    "ds_read_b64_tr_b16 a[200:201], v57 offset:24584\n"
    "v_mfma_f32_16x16x32_bf16 v[220:223], a[216:219], a[24:27], v[220:223]\n"
    "ds_read_b64_tr_b16 a[202:203], v57 offset:30728\n"
    "v_mfma_f32_16x16x32_bf16 v[236:239], a[224:227], a[24:27], v[236:239]\n"
    "v_mfma_f32_16x16x32_bf16 v[236:239], a[228:231], a[28:31], v[236:239]\n"
    "buffer_load_dwordx4 v14, s[48:51], s90 offen offset:3072 lds\n"
    "ds_read_b64_tr_b16 a[204:205], v57 offset:36872\n"
    "v_mfma_f32_16x16x32_bf16 v[232:235], a[228:231], a[20:23], v[232:235]\n"
    "ds_read_b64_tr_b16 a[206:207], v57 offset:43016\n"
    "v_mfma_f32_16x16x32_bf16 v[232:235], a[224:227], a[16:19], v[232:235]\n"
    "v_mfma_f32_16x16x32_bf16 v[228:231], a[224:227], a[8:11], v[228:231]\n"
    "ds_read_b64_tr_b16 a[208:209], v58 offset:24576\n"
    "v_mfma_f32_16x16x32_bf16 v[228:231], a[228:231], a[12:15], v[228:231]\n"
    "s_add_u32 m0, s91, 122880\n"
    "ds_read_b64_tr_b16 a[210:211], v58 offset:30720\n"
    "v_mfma_f32_16x16x32_bf16 v[224:227], a[228:231], a[4:7], v[224:227]\n"
    "buffer_load_dwordx4 v15, s[48:51], s90 offen offset:0 lds\n"
    "v_mfma_f32_16x16x32_bf16 v[224:227], a[224:227], a[0:3], v[224:227]\n"
    "ds_read_b64_tr_b16 a[212:213], v58 offset:36864\n"
    "v_mfma_f32_16x16x32_bf16 v[240:243], a[232:235], a[0:3], v[240:243]\n"
    "ds_read_b64_tr_b16 a[214:215], v58 offset:43008\n"
    "v_mfma_f32_16x16x32_bf16 v[240:243], a[236:239], a[4:7], v[240:243]\n"
    "s_add_u32 s90, s90, 163840\n"
    "v_mfma_f32_16x16x32_bf16 v[244:247], a[236:239], a[12:15], v[244:247]\n"
    "v_mfma_f32_16x16x32_bf16 v[244:247], a[232:235], a[8:11], v[244:247]\n"
    "v_mfma_f32_16x16x32_bf16 v[248:251], a[232:235], a[16:19], v[248:251]\n"
    "v_mfma_f32_16x16x32_bf16 v[248:251], a[236:239], a[20:23], v[248:251]\n"
    "v_mfma_f32_16x16x32_bf16 v[252:255], a[236:239], a[28:31], v[252:255]\n"
    "s_waitcnt lgkmcnt(0)\n"
    "v_mfma_f32_16x16x32_bf16 v[252:255], a[232:235], a[24:27], v[252:255]\n"
    "v_mfma_f32_16x16x32_bf16 v[64:67], a[192:195], a[32:35], v[64:67]\n"
    "ds_read_b64_tr_b16 a[216:217], v58 offset:24584\n"
    "v_mfma_f32_16x16x32_bf16 v[64:67], a[196:199], a[36:39], v[64:67]\n"
    "ds_read_b64_tr_b16 a[218:219], v58 offset:30728\n"
    "v_mfma_f32_16x16x32_bf16 v[68:71], a[196:199], a[44:47], v[68:71]\n"
    "s_cmp_eq_u32 s78, 7\n"
    "s_cselect_b32 s93, 0, 0x80000000\n"
    "v_or_b32 v48, s93, v50\n"
    "v_mfma_f32_16x16x32_bf16 v[68:71], a[192:195], a[40:43], v[68:71]\n"
    "buffer_load_dword v51, v48, s[44:47], 0 offen\n"
    "ds_read_b64_tr_b16 a[220:221], v58 offset:36872\n"
    "v_mfma_f32_16x16x32_bf16 v[72:75], a[192:195], a[48:51], v[72:75]\n"
    "ds_read_b64_tr_b16 a[222:223], v58 offset:43016\n"
    "v_mfma_f32_16x16x32_bf16 v[72:75], a[196:199], a[52:55], v[72:75]\n"
    "v_lshl_or_b32 v48, %2, 7, s93\n"
    "v_mfma_f32_16x16x32_bf16 v[76:79], a[196:199], a[60:63], v[76:79]\n"
    "buffer_load_dword v51, v48, s[84:87], 0 offen\n"
    "ds_read_b64_tr_b16 a[224:225], v57 offset:26624\n"
    "v_mfma_f32_16x16x32_bf16 v[76:79], a[192:195], a[56:59], v[76:79]\n"
    "ds_read_b64_tr_b16 a[226:227], v57 offset:32768\n"
    "v_mfma_f32_16x16x32_bf16 v[92:95], a[200:203], a[56:59], v[92:95]\n"
    "v_mfma_f32_16x16x32_bf16 v[92:95], a[204:207], a[60:63], v[92:95]\n"
    "s_add_u32 m0, s68, 49152\n"
    "ds_read_b64_tr_b16 a[228:229], v57 offset:38912\n"
    "v_mfma_f32_16x16x32_bf16 v[88:91], a[204:207], a[52:55], v[88:91]\n"
    "buffer_load_dwordx4 v59, s[44:47], s62 offen offset:0 lds\n"
    "ds_read_b64_tr_b16 a[230:231], v57 offset:45056\n"
    "v_mfma_f32_16x16x32_bf16 v[88:91], a[200:203], a[48:51], v[88:91]\n"
    "v_mfma_f32_16x16x32_bf16 v[84:87], a[200:203], a[40:43], v[84:87]\n"
    "ds_read_b64_tr_b16 a[232:233], v57 offset:26632\n"
    "v_mfma_f32_16x16x32_bf16 v[84:87], a[204:207], a[44:47], v[84:87]\n"
    "buffer_load_dwordx4 v59, s[44:47], s63 offen offset:1024 lds\n"
    "ds_read_b64_tr_b16 a[234:235], v57 offset:32776\n"
    "v_mfma_f32_16x16x32_bf16 v[80:83], a[204:207], a[36:39], v[80:83]\n"
    "v_mfma_f32_16x16x32_bf16 v[80:83], a[200:203], a[32:35], v[80:83]\n"
    "ds_read_b64_tr_b16 a[236:237], v57 offset:38920\n"
    "v_mfma_f32_16x16x32_bf16 v[96:99], a[208:211], a[32:35], v[96:99]\n"
    "ds_read_b64_tr_b16 a[238:239], v57 offset:45064\n"
    "v_mfma_f32_16x16x32_bf16 v[96:99], a[212:215], a[36:39], v[96:99]\n"
    "s_add_u32 m0, s68, 51200\n"
    "v_mfma_f32_16x16x32_bf16 v[100:103], a[212:215], a[44:47], v[100:103]\n"
    "buffer_load_dwordx4 v59, s[44:47], s64 offen offset:0 lds\n"
    "v_mfma_f32_16x16x32_bf16 v[100:103], a[208:211], a[40:43], v[100:103]\n"
    "v_mfma_f32_16x16x32_bf16 v[104:107], a[208:211], a[48:51], v[104:107]\n"
    "v_mfma_f32_16x16x32_bf16 v[104:107], a[212:215], a[52:55], v[104:107]\n"
    "buffer_load_dwordx4 v59, s[44:47], s65 offen offset:1024 lds\n"
    "v_mfma_f32_16x16x32_bf16 v[108:111], a[212:215], a[60:63], v[108:111]\n"
    "s_waitcnt lgkmcnt(0)\n"
    "v_mfma_f32_16x16x32_bf16 v[108:111], a[208:211], a[56:59], v[108:111]\n"
    "v_mfma_f32_16x16x32_bf16 v[112:115], a[216:219], a[32:35], v[112:115]\n"
    "ds_read_b64_tr_b16 a[192:193], v58 offset:26624\n"
    "v_mfma_f32_16x16x32_bf16 v[112:115], a[220:223], a[36:39], v[112:115]\n"
    "ds_read_b64_tr_b16 a[194:195], v58 offset:32768\n"
    "v_mfma_f32_16x16x32_bf16 v[116:119], a[220:223], a[44:47], v[116:119]\n"
    "s_add_u32 m0, s68, 53248\n"
    "v_mfma_f32_16x16x32_bf16 v[116:119], a[216:219], a[40:43], v[116:119]\n"
    "buffer_load_dwordx4 v59, s[44:47], s66 offen offset:0 lds\n"
    "ds_read_b64_tr_b16 a[196:197], v58 offset:38912\n"
    "v_mfma_f32_16x16x32_bf16 v[120:123], a[216:219], a[48:51], v[120:123]\n"
    "ds_read_b64_tr_b16 a[198:199], v58 offset:45056\n"
    "v_mfma_f32_16x16x32_bf16 v[120:123], a[220:223], a[52:55], v[120:123]\n"
    "v_mfma_f32_16x16x32_bf16 v[124:127], a[220:223], a[60:63], v[124:127]\n"
    "buffer_load_dwordx4 v59, s[44:47], s67 offen offset:1024 lds\n"
    "ds_read_b64_tr_b16 a[200:201], v58 offset:26632\n"
    "v_mfma_f32_16x16x32_bf16 v[124:127], a[216:219], a[56:59], v[124:127]\n"
    "ds_read_b64_tr_b16 a[202:203], v58 offset:32776\n"
    "v_mfma_f32_16x16x32_bf16 v[140:143], a[224:227], a[56:59], v[140:143]\n"
    "v_mfma_f32_16x16x32_bf16 v[140:143], a[228:231], a[60:63], v[140:143]\n"
    "ds_read_b64_tr_b16 a[204:205], v58 offset:38920\n"
    "v_mfma_f32_16x16x32_bf16 v[136:139], a[228:231], a[52:55], v[136:139]\n"
    "ds_read_b64_tr_b16 a[206:207], v58 offset:45064\n"
    "v_add_u32 v59, 786432, v59\n"
    "v_mfma_f32_16x16x32_bf16 v[136:139], a[224:227], a[48:51], v[136:139]\n"
    "buffer_load_dwordx4 a[0:3], v9, s[52:55], s88 offen offset:0 sc1\n"
    "v_mfma_f32_16x16x32_bf16 v[132:135], a[224:227], a[40:43], v[132:135]\n"
    "ds_read_b64_tr_b16 a[208:209], v57 offset:28672\n"
    "v_mfma_f32_16x16x32_bf16 v[132:135], a[228:231], a[44:47], v[132:135]\n"
    "ds_read_b64_tr_b16 a[210:211], v57 offset:34816\n"
    "v_mfma_f32_16x16x32_bf16 v[128:131], a[228:231], a[36:39], v[128:131]\n"
    "buffer_load_dwordx4 a[4:7], v9, s[52:55], s88 offen offset:1024 sc1\n"
    "v_mfma_f32_16x16x32_bf16 v[128:131], a[224:227], a[32:35], v[128:131]\n"
    "ds_read_b64_tr_b16 a[212:213], v57 offset:40960\n"
    "v_mfma_f32_16x16x32_bf16 v[144:147], a[232:235], a[32:35], v[144:147]\n"
    "ds_read_b64_tr_b16 a[214:215], v57 offset:47104\n"
    "v_mfma_f32_16x16x32_bf16 v[144:147], a[236:239], a[36:39], v[144:147]\n"
    "v_mfma_f32_16x16x32_bf16 v[148:151], a[236:239], a[44:47], v[148:151]\n"
    "buffer_load_dwordx4 a[8:11], v9, s[52:55], s88 offen offset:2048 sc1\n"
    "v_mfma_f32_16x16x32_bf16 v[148:151], a[232:235], a[40:43], v[148:151]\n"
    "v_mfma_f32_16x16x32_bf16 v[152:155], a[232:235], a[48:51], v[152:155]\n"
    "v_mfma_f32_16x16x32_bf16 v[152:155], a[236:239], a[52:55], v[152:155]\n"
    "v_mfma_f32_16x16x32_bf16 v[156:159], a[236:239], a[60:63], v[156:159]\n"
    "s_waitcnt lgkmcnt(0)\n"
    "v_mfma_f32_16x16x32_bf16 v[156:159], a[232:235], a[56:59], v[156:159]\n"
    "buffer_load_dwordx4 a[12:15], v9, s[52:55], s88 offen offset:3072 sc1\n"
    "v_mfma_f32_16x16x32_bf16 v[160:163], a[192:195], a[32:35], v[160:163]\n"
    "ds_read_b64_tr_b16 a[216:217], v57 offset:28680\n"
    "v_mfma_f32_16x16x32_bf16 v[160:163], a[196:199], a[36:39], v[160:163]\n"
    "ds_read_b64_tr_b16 a[218:219], v57 offset:34824\n"
    "v_mfma_f32_16x16x32_bf16 v[164:167], a[196:199], a[44:47], v[164:167]\n"
    "v_mfma_f32_16x16x32_bf16 v[164:167], a[192:195], a[40:43], v[164:167]\n"
    "buffer_load_dwordx4 a[16:19], v9, s[52:55], s89 offen offset:0 sc1\n"
    "ds_read_b64_tr_b16 a[220:221], v57 offset:40968\n"
    "v_mfma_f32_16x16x32_bf16 v[168:171], a[192:195], a[48:51], v[168:171]\n"
    "ds_read_b64_tr_b16 a[222:223], v57 offset:47112\n"
    "v_mfma_f32_16x16x32_bf16 v[168:171], a[196:199], a[52:55], v[168:171]\n"
    "v_mfma_f32_16x16x32_bf16 v[172:175], a[196:199], a[60:63], v[172:175]\n"
    "ds_read_b64_tr_b16 a[224:225], v58 offset:28672\n"
    "v_mfma_f32_16x16x32_bf16 v[172:175], a[192:195], a[56:59], v[172:175]\n"
    "buffer_load_dwordx4 a[20:23], v9, s[52:55], s89 offen offset:1024 sc1\n"
    "ds_read_b64_tr_b16 a[226:227], v58 offset:34816\n"
    "v_mfma_f32_16x16x32_bf16 v[188:191], a[200:203], a[56:59], v[188:191]\n"
    "v_mfma_f32_16x16x32_bf16 v[188:191], a[204:207], a[60:63], v[188:191]\n"
    "ds_read_b64_tr_b16 a[228:229], v58 offset:40960\n"
    "v_mfma_f32_16x16x32_bf16 v[184:187], a[204:207], a[52:55], v[184:187]\n"
    "ds_read_b64_tr_b16 a[230:231], v58 offset:47104\n"
    "v_mfma_f32_16x16x32_bf16 v[184:187], a[200:203], a[48:51], v[184:187]\n"
    "buffer_load_dwordx4 a[24:27], v9, s[52:55], s89 offen offset:2048 sc1\n"
    "v_mfma_f32_16x16x32_bf16 v[180:183], a[200:203], a[40:43], v[180:183]\n"
    "ds_read_b64_tr_b16 a[232:233], v58 offset:28680\n"
    "v_mfma_f32_16x16x32_bf16 v[180:183], a[204:207], a[44:47], v[180:183]\n"
    "ds_read_b64_tr_b16 a[234:235], v58 offset:34824\n"
    "v_mfma_f32_16x16x32_bf16 v[176:179], a[204:207], a[36:39], v[176:179]\n"
    "v_mfma_f32_16x16x32_bf16 v[176:179], a[200:203], a[32:35], v[176:179]\n"
    "buffer_load_dwordx4 a[28:31], v9, s[52:55], s89 offen offset:3072 sc1\n"
    "ds_read_b64_tr_b16 a[236:237], v58 offset:40968\n"
    "v_mfma_f32_16x16x32_bf16 v[192:195], a[208:211], a[32:35], v[192:195]\n"
    "ds_read_b64_tr_b16 a[238:239], v58 offset:47112\n"
    "v_mfma_f32_16x16x32_bf16 v[192:195], a[212:215], a[36:39], v[192:195]\n"
    "v_mfma_f32_16x16x32_bf16 v[196:199], a[212:215], a[44:47], v[196:199]\n"
    "v_mfma_f32_16x16x32_bf16 v[196:199], a[208:211], a[40:43], v[196:199]\n"
    "s_add_u32 s88, s88, 8192\n"
    "v_mfma_f32_16x16x32_bf16 v[200:203], a[208:211], a[48:51], v[200:203]\n"
    "s_add_u32 s89, s89, 8192\n"
    "v_mfma_f32_16x16x32_bf16 v[200:203], a[212:215], a[52:55], v[200:203]\n"
    "s_add_u32 m0, s91, 139264\n"
    "v_mfma_f32_16x16x32_bf16 v[204:207], a[212:215], a[60:63], v[204:207]\n"
    "s_waitcnt vmcnt(35) lgkmcnt(0)\n"
    "s_barrier\n"
    "v_mfma_f32_16x16x32_bf16 v[204:207], a[208:211], a[56:59], v[204:207]\n"
    "v_add_u32 v57, 4294918144, v57\n"
    "v_add_u32 v58, 4294918144, v58\n"
    "v_mfma_f32_16x16x32_bf16 v[208:211], a[216:219], a[32:35], v[208:211]\n"
    "buffer_load_dwordx4 v11, s[48:51], s90 offen offset:0 lds\n"
    "ds_read_b64_tr_b16 a[192:193], v57 offset:0\n"
    "v_mfma_f32_16x16x32_bf16 v[208:211], a[220:223], a[36:39], v[208:211]\n"
    "buffer_load_dwordx4 v12, s[48:51], s90 offen offset:1024 lds\n"
    "ds_read_b64_tr_b16 a[194:195], v57 offset:6144\n"
    "v_mfma_f32_16x16x32_bf16 v[212:215], a[220:223], a[44:47], v[212:215]\n"
    "v_cmp_ne_u64 vcc, s[80:81], v[62:63]\n"
    "s_nop 1\n"
    "s_cbranch_vccnz L_slow1\n"
    "L_back1:\n"
    "v_mfma_f32_16x16x32_bf16 v[212:215], a[216:219], a[40:43], v[212:215]\n"
    "ds_read_b64_tr_b16 a[196:197], v57 offset:12288\n"
    "v_mfma_f32_16x16x32_bf16 v[216:219], a[216:219], a[48:51], v[216:219]\n"
    "buffer_load_dwordx4 v13, s[48:51], s90 offen offset:2048 lds\n"
    "ds_read_b64_tr_b16 a[198:199], v57 offset:18432\n"
    "v_mfma_f32_16x16x32_bf16 v[216:219], a[220:223], a[52:55], v[216:219]\n"
    "s_mov_b64 s[84:85], s[24:25]\n"
    "s_add_u32 s84, s84, 4194304\n"
    "s_addc_u32 s85, s85, 0\n"
    "s_mov_b32 s86, 2621440\n"
    "s_mov_b32 s87, 0x20000\n"
    "v_mfma_f32_16x16x32_bf16 v[220:223], a[220:223], a[60:63], v[220:223]\n"
    "ds_read_b64_tr_b16 a[200:201], v57 offset:8\n"
    "v_mfma_f32_16x16x32_bf16 v[220:223], a[216:219], a[56:59], v[220:223]\n"
    "ds_read_b64_tr_b16 a[202:203], v57 offset:6152\n"
    "v_mfma_f32_16x16x32_bf16 v[236:239], a[224:227], a[56:59], v[236:239]\n"
    "buffer_load_dwordx4 v14, s[48:51], s90 offen offset:3072 lds\n"
    "v_mfma_f32_16x16x32_bf16 v[236:239], a[228:231], a[60:63], v[236:239]\n"
    "ds_read_b64_tr_b16 a[204:205], v57 offset:12296\n"
    "v_mfma_f32_16x16x32_bf16 v[232:235], a[228:231], a[52:55], v[232:235]\n"
    "ds_read_b64_tr_b16 a[206:207], v57 offset:18440\n"
    "v_mfma_f32_16x16x32_bf16 v[232:235], a[224:227], a[48:51], v[232:235]\n"
    "v_mfma_f32_16x16x32_bf16 v[228:231], a[224:227], a[40:43], v[228:231]\n"
    "s_add_u32 m0, s91, 143360\n"
    "ds_read_b64_tr_b16 a[208:209], v58 offset:0\n"
    "v_mfma_f32_16x16x32_bf16 v[228:231], a[228:231], a[44:47], v[228:231]\n"
    "buffer_load_dwordx4 v15, s[48:51], s90 offen offset:0 lds\n"
    "ds_read_b64_tr_b16 a[210:211], v58 offset:6144\n"
    "v_mfma_f32_16x16x32_bf16 v[224:227], a[228:231], a[36:39], v[224:227]\n"
    "v_mfma_f32_16x16x32_bf16 v[224:227], a[224:227], a[32:35], v[224:227]\n"
    "ds_read_b64_tr_b16 a[212:213], v58 offset:12288\n"
    "v_mfma_f32_16x16x32_bf16 v[240:243], a[232:235], a[32:35], v[240:243]\n"
    "s_add_u32 s90, s90, 163840\n"
    "ds_read_b64_tr_b16 a[214:215], v58 offset:18432\n"
    "v_mfma_f32_16x16x32_bf16 v[240:243], a[236:239], a[36:39], v[240:243]\n"
    "v_mfma_f32_16x16x32_bf16 v[244:247], a[236:239], a[44:47], v[244:247]\n"
    "v_mfma_f32_16x16x32_bf16 v[244:247], a[232:235], a[40:43], v[244:247]\n"
    "v_mfma_f32_16x16x32_bf16 v[248:251], a[232:235], a[48:51], v[248:251]\n"
    "v_mfma_f32_16x16x32_bf16 v[248:251], a[236:239], a[52:55], v[248:251]\n"
    "v_mfma_f32_16x16x32_bf16 v[252:255], a[236:239], a[60:63], v[252:255]\n"
    "s_waitcnt lgkmcnt(0)\n"
    "v_mfma_f32_16x16x32_bf16 v[252:255], a[232:235], a[56:59], v[252:255]\n"
    "v_mfma_f32_16x16x32_bf16 v[64:67], a[192:195], a[64:67], v[64:67]\n"
    "ds_read_b64_tr_b16 a[216:217], v58 offset:8\n"
    "v_mfma_f32_16x16x32_bf16 v[64:67], a[196:199], a[68:71], v[64:67]\n"
    "s_add_u32 m0, s68, 73728\n"
    "ds_read_b64_tr_b16 a[218:219], v58 offset:6152\n"
    "v_mfma_f32_16x16x32_bf16 v[68:71], a[196:199], a[76:79], v[68:71]\n"
    "buffer_load_dwordx4 v59, s[44:47], s62 offen offset:0 lds\n"
    "v_mfma_f32_16x16x32_bf16 v[68:71], a[192:195], a[72:75], v[68:71]\n"
    "ds_read_b64_tr_b16 a[220:221], v58 offset:12296\n"
    "v_mfma_f32_16x16x32_bf16 v[72:75], a[192:195], a[80:83], v[72:75]\n"
    "ds_read_b64_tr_b16 a[222:223], v58 offset:18440\n"
    "v_mfma_f32_16x16x32_bf16 v[72:75], a[196:199], a[84:87], v[72:75]\n"
    "v_mfma_f32_16x16x32_bf16 v[76:79], a[196:199], a[92:95], v[76:79]\n"
    "ds_read_b64_tr_b16 a[224:225], v57 offset:2048\n"
    "v_mfma_f32_16x16x32_bf16 v[76:79], a[192:195], a[88:91], v[76:79]\n"
    "buffer_load_dwordx4 v59, s[44:47], s63 offen offset:1024 lds\n"
    "ds_read_b64_tr_b16 a[226:227], v57 offset:8192\n"
    "v_mfma_f32_16x16x32_bf16 v[92:95], a[200:203], a[88:91], v[92:95]\n"
    "v_mfma_f32_16x16x32_bf16 v[92:95], a[204:207], a[92:95], v[92:95]\n"
    "ds_read_b64_tr_b16 a[228:229], v57 offset:14336\n"
    "v_mfma_f32_16x16x32_bf16 v[88:91], a[204:207], a[84:87], v[88:91]\n"
    "ds_read_b64_tr_b16 a[230:231], v57 offset:20480\n"
    "v_mfma_f32_16x16x32_bf16 v[88:91], a[200:203], a[80:83], v[88:91]\n"
    "v_mfma_f32_16x16x32_bf16 v[84:87], a[200:203], a[72:75], v[84:87]\n"
    "ds_read_b64_tr_b16 a[232:233], v57 offset:2056\n"
    "v_mfma_f32_16x16x32_bf16 v[84:87], a[204:207], a[76:79], v[84:87]\n"
    "s_add_u32 m0, s68, 75776\n"
    "ds_read_b64_tr_b16 a[234:235], v57 offset:8200\n"
    "v_mfma_f32_16x16x32_bf16 v[80:83], a[204:207], a[68:71], v[80:83]\n"
    "buffer_load_dwordx4 v59, s[44:47], s64 offen offset:0 lds\n"
    "v_mfma_f32_16x16x32_bf16 v[80:83], a[200:203], a[64:67], v[80:83]\n"
    "ds_read_b64_tr_b16 a[236:237], v57 offset:14344\n"
    "v_mfma_f32_16x16x32_bf16 v[96:99], a[208:211], a[64:67], v[96:99]\n"
    "ds_read_b64_tr_b16 a[238:239], v57 offset:20488\n"
    "v_mfma_f32_16x16x32_bf16 v[96:99], a[212:215], a[68:71], v[96:99]\n"
    "v_mfma_f32_16x16x32_bf16 v[100:103], a[212:215], a[76:79], v[100:103]\n"
    "v_mfma_f32_16x16x32_bf16 v[100:103], a[208:211], a[72:75], v[100:103]\n"
    "buffer_load_dwordx4 v59, s[44:47], s65 offen offset:1024 lds\n"
    "v_mfma_f32_16x16x32_bf16 v[104:107], a[208:211], a[80:83], v[104:107]\n"
    "v_mfma_f32_16x16x32_bf16 v[104:107], a[212:215], a[84:87], v[104:107]\n"
    "v_mfma_f32_16x16x32_bf16 v[108:111], a[212:215], a[92:95], v[108:111]\n"
    "s_waitcnt lgkmcnt(0)\n"
    "v_mfma_f32_16x16x32_bf16 v[108:111], a[208:211], a[88:91], v[108:111]\n"
    "v_mfma_f32_16x16x32_bf16 v[112:115], a[216:219], a[64:67], v[112:115]\n"
    "ds_read_b64_tr_b16 a[192:193], v58 offset:2048\n"
    "v_mfma_f32_16x16x32_bf16 v[112:115], a[220:223], a[68:71], v[112:115]\n"
    "s_add_u32 m0, s68, 77824\n"
    "ds_read_b64_tr_b16 a[194:195], v58 offset:8192\n"
    "v_mfma_f32_16x16x32_bf16 v[116:119], a[220:223], a[76:79], v[116:119]\n"
    "buffer_load_dwordx4 v59, s[44:47], s66 offen offset:0 lds\n"
    "v_mfma_f32_16x16x32_bf16 v[116:119], a[216:219], a[72:75], v[116:119]\n"
    "ds_read_b64_tr_b16 a[196:197], v58 offset:14336\n"
    "v_mfma_f32_16x16x32_bf16 v[120:123], a[216:219], a[80:83], v[120:123]\n"
    "ds_read_b64_tr_b16 a[198:199], v58 offset:20480\n"
    "v_mfma_f32_16x16x32_bf16 v[120:123], a[220:223], a[84:87], v[120:123]\n"
    "v_mfma_f32_16x16x32_bf16 v[124:127], a[220:223], a[92:95], v[124:127]\n"
    "ds_read_b64_tr_b16 a[200:201], v58 offset:2056\n"
    "v_mfma_f32_16x16x32_bf16 v[124:127], a[216:219], a[88:91], v[124:127]\n"
    "buffer_load_dwordx4 v59, s[44:47], s67 offen offset:1024 lds\n"
    "ds_read_b64_tr_b16 a[202:203], v58 offset:8200\n"
    "v_mfma_f32_16x16x32_bf16 v[140:143], a[224:227], a[88:91], v[140:143]\n"
    "v_mfma_f32_16x16x32_bf16 v[140:143], a[228:231], a[92:95], v[140:143]\n"
    "ds_read_b64_tr_b16 a[204:205], v58 offset:14344\n"
    "v_mfma_f32_16x16x32_bf16 v[136:139], a[228:231], a[84:87], v[136:139]\n"
    "ds_read_b64_tr_b16 a[206:207], v58 offset:20488\n"
    "v_mfma_f32_16x16x32_bf16 v[136:139], a[224:227], a[80:83], v[136:139]\n"
    "v_mfma_f32_16x16x32_bf16 v[132:135], a[224:227], a[72:75], v[132:135]\n"
    "ds_read_b64_tr_b16 a[208:209], v57 offset:4096\n"
    "v_mfma_f32_16x16x32_bf16 v[132:135], a[228:231], a[76:79], v[132:135]\n"
    "ds_read_b64_tr_b16 a[210:211], v57 offset:10240\n"
    "v_add_u32 v59, 786432, v59\n"
    "v_mfma_f32_16x16x32_bf16 v[128:131], a[228:231], a[68:71], v[128:131]\n"
    "buffer_load_dwordx4 a[32:35], v9, s[52:55], s88 offen offset:0 sc1\n"
    "v_mfma_f32_16x16x32_bf16 v[128:131], a[224:227], a[64:67], v[128:131]\n"
    "ds_read_b64_tr_b16 a[212:213], v57 offset:16384\n"
    "v_mfma_f32_16x16x32_bf16 v[144:147], a[232:235], a[64:67], v[144:147]\n"
    "ds_read_b64_tr_b16 a[214:215], v57 offset:22528\n"
    "v_mfma_f32_16x16x32_bf16 v[144:147], a[236:239], a[68:71], v[144:147]\n"
    "v_mfma_f32_16x16x32_bf16 v[148:151], a[236:239], a[76:79], v[148:151]\n"
    "v_mfma_f32_16x16x32_bf16 v[148:151], a[232:235], a[72:75], v[148:151]\n"
    "buffer_load_dwordx4 a[36:39], v9, s[52:55], s88 offen offset:1024 sc1\n"
    "v_mfma_f32_16x16x32_bf16 v[152:155], a[232:235], a[80:83], v[152:155]\n"
    "v_mfma_f32_16x16x32_bf16 v[152:155], a[236:239], a[84:87], v[152:155]\n"
    "v_mfma_f32_16x16x32_bf16 v[156:159], a[236:239], a[92:95], v[156:159]\n"
    "s_waitcnt lgkmcnt(0)\n"
    "v_mfma_f32_16x16x32_bf16 v[156:159], a[232:235], a[88:91], v[156:159]\n"
    "v_mfma_f32_16x16x32_bf16 v[160:163], a[192:195], a[64:67], v[160:163]\n"
    "ds_read_b64_tr_b16 a[216:217], v57 offset:4104\n"
    "v_mfma_f32_16x16x32_bf16 v[160:163], a[196:199], a[68:71], v[160:163]\n"
    "ds_read_b64_tr_b16 a[218:219], v57 offset:10248\n"
    "v_mfma_f32_16x16x32_bf16 v[164:167], a[196:199], a[76:79], v[164:167]\n"
    "buffer_load_dwordx4 a[40:43], v9, s[52:55], s88 offen offset:2048 sc1\n"
    "v_mfma_f32_16x16x32_bf16 v[164:167], a[192:195], a[72:75], v[164:167]\n"
    "ds_read_b64_tr_b16 a[220:221], v57 offset:16392\n"
    "v_mfma_f32_16x16x32_bf16 v[168:171], a[192:195], a[80:83], v[168:171]\n"
    "ds_read_b64_tr_b16 a[222:223], v57 offset:22536\n"
    "v_mfma_f32_16x16x32_bf16 v[168:171], a[196:199], a[84:87], v[168:171]\n"
    "v_mfma_f32_16x16x32_bf16 v[172:175], a[196:199], a[92:95], v[172:175]\n"
    "ds_read_b64_tr_b16 a[224:225], v58 offset:4096\n"
    "v_mfma_f32_16x16x32_bf16 v[172:175], a[192:195], a[88:91], v[172:175]\n"
    "ds_read_b64_tr_b16 a[226:227], v58 offset:10240\n"
    "v_mfma_f32_16x16x32_bf16 v[188:191], a[200:203], a[88:91], v[188:191]\n"
    "buffer_load_dwordx4 a[44:47], v9, s[52:55], s88 offen offset:3072 sc1\n"
    "v_mfma_f32_16x16x32_bf16 v[188:191], a[204:207], a[92:95], v[188:191]\n"
    "ds_read_b64_tr_b16 a[228:229], v58 offset:16384\n"
    "v_mfma_f32_16x16x32_bf16 v[184:187], a[204:207], a[84:87], v[184:187]\n"
    "ds_read_b64_tr_b16 a[230:231], v58 offset:22528\n"
    "v_mfma_f32_16x16x32_bf16 v[184:187], a[200:203], a[80:83], v[184:187]\n"
    "v_mfma_f32_16x16x32_bf16 v[180:183], a[200:203], a[72:75], v[180:183]\n"
    "ds_read_b64_tr_b16 a[232:233], v58 offset:4104\n"
    "v_mfma_f32_16x16x32_bf16 v[180:183], a[204:207], a[76:79], v[180:183]\n"
    "ds_read_b64_tr_b16 a[234:235], v58 offset:10248\n"
    "v_mfma_f32_16x16x32_bf16 v[176:179], a[204:207], a[68:71], v[176:179]\n"
    "buffer_load_dwordx4 a[48:51], v9, s[52:55], s89 offen offset:0 sc1\n"
    "v_mfma_f32_16x16x32_bf16 v[176:179], a[200:203], a[64:67], v[176:179]\n"
    "ds_read_b64_tr_b16 a[236:237], v58 offset:16392\n"
    "v_mfma_f32_16x16x32_bf16 v[192:195], a[208:211], a[64:67], v[192:195]\n"
    "ds_read_b64_tr_b16 a[238:239], v58 offset:22536\n"
    "v_mfma_f32_16x16x32_bf16 v[192:195], a[212:215], a[68:71], v[192:195]\n"
    "v_mfma_f32_16x16x32_bf16 v[196:199], a[212:215], a[76:79], v[196:199]\n"
    "v_mfma_f32_16x16x32_bf16 v[196:199], a[208:211], a[72:75], v[196:199]\n"
    "v_mfma_f32_16x16x32_bf16 v[200:203], a[208:211], a[80:83], v[200:203]\n"
    "buffer_load_dwordx4 a[52:55], v9, s[52:55], s89 offen offset:1024 sc1\n"
    "v_mfma_f32_16x16x32_bf16 v[200:203], a[212:215], a[84:87], v[200:203]\n"
    "v_mfma_f32_16x16x32_bf16 v[204:207], a[212:215], a[92:95], v[204:207]\n"
    "s_waitcnt vmcnt(38) lgkmcnt(0)\n"
    "s_barrier\n"
    "v_mfma_f32_16x16x32_bf16 v[204:207], a[208:211], a[88:91], v[204:207]\n"
    "v_mfma_f32_16x16x32_bf16 v[208:211], a[216:219], a[64:67], v[208:211]\n"
    "ds_read_b64_tr_b16 a[192:193], v57 offset:24576\n"
    "v_mfma_f32_16x16x32_bf16 v[208:211], a[220:223], a[68:71], v[208:211]\n"
    "ds_read_b64_tr_b16 a[194:195], v57 offset:30720\n"
    "v_mfma_f32_16x16x32_bf16 v[212:215], a[220:223], a[76:79], v[212:215]\n"
    "buffer_load_dwordx4 a[56:59], v9, s[52:55], s89 offen offset:2048 sc1\n"
    "v_mfma_f32_16x16x32_bf16 v[212:215], a[216:219], a[72:75], v[212:215]\n"
    "ds_read_b64_tr_b16 a[196:197], v57 offset:36864\n"
    "v_mfma_f32_16x16x32_bf16 v[216:219], a[216:219], a[80:83], v[216:219]\n"
    "ds_read_b64_tr_b16 a[198:199], v57 offset:43008\n"
    "v_mfma_f32_16x16x32_bf16 v[216:219], a[220:223], a[84:87], v[216:219]\n"
    "v_mfma_f32_16x16x32_bf16 v[220:223], a[220:223], a[92:95], v[220:223]\n"
    "ds_read_b64_tr_b16 a[200:201], v57 offset:24584\n"
    "v_mfma_f32_16x16x32_bf16 v[220:223], a[216:219], a[88:91], v[220:223]\n"
    "ds_read_b64_tr_b16 a[202:203], v57 offset:30728\n"
    "v_mfma_f32_16x16x32_bf16 v[236:239], a[224:227], a[88:91], v[236:239]\n"
    "buffer_load_dwordx4 a[60:63], v9, s[52:55], s89 offen offset:3072 sc1\n"
    "v_mfma_f32_16x16x32_bf16 v[236:239], a[228:231], a[92:95], v[236:239]\n"
    "ds_read_b64_tr_b16 a[204:205], v57 offset:36872\n"
    "v_mfma_f32_16x16x32_bf16 v[232:235], a[228:231], a[84:87], v[232:235]\n"
    "ds_read_b64_tr_b16 a[206:207], v57 offset:43016\n"
    "v_mfma_f32_16x16x32_bf16 v[232:235], a[224:227], a[80:83], v[232:235]\n"
    "v_mfma_f32_16x16x32_bf16 v[228:231], a[224:227], a[72:75], v[228:231]\n"
    "ds_read_b64_tr_b16 a[208:209], v58 offset:24576\n"
    "v_mfma_f32_16x16x32_bf16 v[228:231], a[228:231], a[76:79], v[228:231]\n"
    "ds_read_b64_tr_b16 a[210:211], v58 offset:30720\n"
    "v_mfma_f32_16x16x32_bf16 v[224:227], a[228:231], a[68:71], v[224:227]\n"
    "s_add_u32 s88, s88, 8192\n"
    "v_mfma_f32_16x16x32_bf16 v[224:227], a[224:227], a[64:67], v[224:227]\n"
    "s_add_u32 s89, s89, 8192\n"
    "ds_read_b64_tr_b16 a[212:213], v58 offset:36864\n"
    "v_mfma_f32_16x16x32_bf16 v[240:243], a[232:235], a[64:67], v[240:243]\n"
    "ds_read_b64_tr_b16 a[214:215], v58 offset:43008\n"
    "v_mfma_f32_16x16x32_bf16 v[240:243], a[236:239], a[68:71], v[240:243]\n"
    "v_mfma_f32_16x16x32_bf16 v[244:247], a[236:239], a[76:79], v[244:247]\n"
    "v_mfma_f32_16x16x32_bf16 v[244:247], a[232:235], a[72:75], v[244:247]\n"
    "v_mfma_f32_16x16x32_bf16 v[248:251], a[232:235], a[80:83], v[248:251]\n"
    "v_mfma_f32_16x16x32_bf16 v[248:251], a[236:239], a[84:87], v[248:251]\n"
    "v_mfma_f32_16x16x32_bf16 v[252:255], a[236:239], a[92:95], v[252:255]\n"
    "s_waitcnt lgkmcnt(0)\n"
    "v_mfma_f32_16x16x32_bf16 v[252:255], a[232:235], a[88:91], v[252:255]\n"
    "v_mfma_f32_16x16x32_bf16 v[64:67], a[192:195], a[96:99], v[64:67]\n"
    "ds_read_b64_tr_b16 a[216:217], v58 offset:24584\n"
    "v_mfma_f32_16x16x32_bf16 v[64:67], a[196:199], a[100:103], v[64:67]\n"
    "s_lshl_b32 s73, s83, 4\n"
    "ds_read_b64_tr_b16 a[218:219], v58 offset:30728\n"
    "v_mfma_f32_16x16x32_bf16 v[68:71], a[196:199], a[108:111], v[68:71]\n"
    "s_sub_u32 s73, s73, 1\n"
    "v_bfe_u32 v16, v9, 4, 4\n"
    "v_add_u32 v16, s73, v16\n"
    "v_mfma_f32_16x16x32_bf16 v[68:71], a[192:195], a[104:107], v[68:71]\n"
    "ds_read_b64_tr_b16 a[220:221], v58 offset:36872\n"
    "v_lshrrev_b32 v17, 4, v16\n"
    "v_mfma_f32_16x16x32_bf16 v[72:75], a[192:195], a[112:115], v[72:75]\n"
    "ds_read_b64_tr_b16 a[222:223], v58 offset:43016\n"
    "v_min_u32 v17, 0x80, v17\n"
    "v_mfma_f32_16x16x32_bf16 v[72:75], a[196:199], a[116:119], v[72:75]\n"
    "v_mul_u32_u24 v17, 20480, v17\n"
    "v_add_u32 v17, 0, v17\n"
    "v_mfma_f32_16x16x32_bf16 v[76:79], a[196:199], a[124:127], v[76:79]\n"
    "ds_read_b64_tr_b16 a[224:225], v57 offset:26624\n"
    "v_and_b32 v16, 15, v16\n"
    "v_mfma_f32_16x16x32_bf16 v[76:79], a[192:195], a[120:123], v[76:79]\n"
    "ds_read_b64_tr_b16 a[226:227], v57 offset:32768\n"
    "v_lshrrev_b32 v18, 8, v9\n"
    "v_mfma_f32_16x16x32_bf16 v[92:95], a[200:203], a[120:123], v[92:95]\n"
    "v_lshl_add_u32 v16, v18, 4, v16\n"
    "v_lshl_add_u32 v20, v16, 4, v17\n"
    "v_mfma_f32_16x16x32_bf16 v[92:95], a[204:207], a[124:127], v[92:95]\n"
    "ds_read_b64_tr_b16 a[228:229], v57 offset:38912\n"
    "v_lshl_add_u32 v21, v16, 3, v17\n"
    "v_mfma_f32_16x16x32_bf16 v[88:91], a[204:207], a[116:119], v[88:91]\n"
    "ds_read_b64_tr_b16 a[230:231], v57 offset:45056\n"
    "v_add_u32 v21, 2048, v21\n"
    "v_mfma_f32_16x16x32_bf16 v[88:91], a[200:203], a[112:115], v[88:91]\n"
    "buffer_load_dwordx4 a[128:131], v20, s[84:87], 0 offen offset:0 sc1\n"
    "v_mfma_f32_16x16x32_bf16 v[84:87], a[200:203], a[104:107], v[84:87]\n"
    "buffer_load_dwordx4 a[132:135], v20, s[84:87], 0 offen offset:1024 sc1\n"
    "ds_read_b64_tr_b16 a[232:233], v57 offset:26632\n"
    "v_mfma_f32_16x16x32_bf16 v[84:87], a[204:207], a[108:111], v[84:87]\n"
    "buffer_load_dwordx4 a[140:143], v20, s[84:87], 0 offen offset:2560 sc1\n"
    "ds_read_b64_tr_b16 a[234:235], v57 offset:32776\n"
    "v_mfma_f32_16x16x32_bf16 v[80:83], a[204:207], a[100:103], v[80:83]\n"
    "v_mfma_f32_16x16x32_bf16 v[80:83], a[200:203], a[96:99], v[80:83]\n"
    "ds_read_b64_tr_b16 a[236:237], v57 offset:38920\n"
    "v_mfma_f32_16x16x32_bf16 v[96:99], a[208:211], a[96:99], v[96:99]\n"
    "buffer_load_dwordx4 a[144:147], v20, s[84:87], 0 offen offset:3584 sc1\n"
    "ds_read_b64_tr_b16 a[238:239], v57 offset:45064\n"
    "v_mfma_f32_16x16x32_bf16 v[96:99], a[212:215], a[100:103], v[96:99]\n"
    "v_mfma_f32_16x16x32_bf16 v[100:103], a[212:215], a[108:111], v[100:103]\n"
    "v_mfma_f32_16x16x32_bf16 v[100:103], a[208:211], a[104:107], v[100:103]\n"
    "v_mfma_f32_16x16x32_bf16 v[104:107], a[208:211], a[112:115], v[104:107]\n"
    "v_mfma_f32_16x16x32_bf16 v[104:107], a[212:215], a[116:119], v[104:107]\n"
    "buffer_load_dwordx2 a[136:137], v21, s[84:87], 0 offen offset:0 sc1\n"
    "v_mfma_f32_16x16x32_bf16 v[108:111], a[212:215], a[124:127], v[108:111]\n"
    "s_waitcnt lgkmcnt(0)\n"
    "v_mfma_f32_16x16x32_bf16 v[108:111], a[208:211], a[120:123], v[108:111]\n"
    "v_mfma_f32_16x16x32_bf16 v[112:115], a[216:219], a[96:99], v[112:115]\n"
    "ds_read_b64_tr_b16 a[192:193], v58 offset:26624\n"
    "v_mfma_f32_16x16x32_bf16 v[112:115], a[220:223], a[100:103], v[112:115]\n"
    "ds_read_b64_tr_b16 a[194:195], v58 offset:32768\n"
    "v_mfma_f32_16x16x32_bf16 v[116:119], a[220:223], a[108:111], v[116:119]\n"
    "buffer_load_dwordx2 a[138:139], v21, s[84:87], 0 offen offset:2560 sc1\n"
    "v_mfma_f32_16x16x32_bf16 v[116:119], a[216:219], a[104:107], v[116:119]\n"
    "ds_read_b64_tr_b16 a[196:197], v58 offset:38912\n"
    "v_mfma_f32_16x16x32_bf16 v[120:123], a[216:219], a[112:115], v[120:123]\n"
    "ds_read_b64_tr_b16 a[198:199], v58 offset:45056\n"
    "v_mfma_f32_16x16x32_bf16 v[120:123], a[220:223], a[116:119], v[120:123]\n"
    "v_mfma_f32_16x16x32_bf16 v[124:127], a[220:223], a[124:127], v[124:127]\n"
    "ds_read_b64_tr_b16 a[200:201], v58 offset:26632\n"
    "v_mfma_f32_16x16x32_bf16 v[124:127], a[216:219], a[120:123], v[124:127]\n"
    "s_lshl_b32 s73, s83, 4\n"
    "ds_read_b64_tr_b16 a[202:203], v58 offset:32776\n"
    "v_mfma_f32_16x16x32_bf16 v[140:143], a[224:227], a[120:123], v[140:143]\n"
    "s_sub_u32 s73, s73, 4294967295\n"
    "v_bfe_u32 v16, v9, 4, 4\n"
    "v_add_u32 v16, s73, v16\n"
    "v_mfma_f32_16x16x32_bf16 v[140:143], a[228:231], a[124:127], v[140:143]\n"
    "ds_read_b64_tr_b16 a[204:205], v58 offset:38920\n"
    "v_lshrrev_b32 v17, 4, v16\n"
    "v_mfma_f32_16x16x32_bf16 v[136:139], a[228:231], a[116:119], v[136:139]\n"
    "ds_read_b64_tr_b16 a[206:207], v58 offset:45064\n"
    "v_min_u32 v17, 0x80, v17\n"
    "v_mfma_f32_16x16x32_bf16 v[136:139], a[224:227], a[112:115], v[136:139]\n"
    "v_mul_u32_u24 v17, 20480, v17\n"
    "v_add_u32 v17, 5120, v17\n"
    "v_mfma_f32_16x16x32_bf16 v[132:135], a[224:227], a[104:107], v[132:135]\n"
    "ds_read_b64_tr_b16 a[208:209], v57 offset:28672\n"
    "v_and_b32 v16, 15, v16\n"
    "v_mfma_f32_16x16x32_bf16 v[132:135], a[228:231], a[108:111], v[132:135]\n"
    "ds_read_b64_tr_b16 a[210:211], v57 offset:34816\n"
    "v_lshrrev_b32 v18, 8, v9\n"
    "v_mfma_f32_16x16x32_bf16 v[128:131], a[228:231], a[100:103], v[128:131]\n"
    "v_lshl_add_u32 v16, v18, 4, v16\n"
    "v_lshl_add_u32 v20, v16, 4, v17\n"
    "v_mfma_f32_16x16x32_bf16 v[128:131], a[224:227], a[96:99], v[128:131]\n"
    "ds_read_b64_tr_b16 a[212:213], v57 offset:40960\n"
    "v_lshl_add_u32 v21, v16, 3, v17\n"
    "v_mfma_f32_16x16x32_bf16 v[144:147], a[232:235], a[96:99], v[144:147]\n"
    "ds_read_b64_tr_b16 a[214:215], v57 offset:47104\n"
    "v_add_u32 v21, 2048, v21\n"
    "v_mfma_f32_16x16x32_bf16 v[144:147], a[236:239], a[100:103], v[144:147]\n"
    "buffer_load_dwordx4 a[148:151], v20, s[84:87], 0 offen offset:0 sc1\n"
    "v_mfma_f32_16x16x32_bf16 v[148:151], a[236:239], a[108:111], v[148:151]\n"
    "buffer_load_dwordx4 a[152:155], v20, s[84:87], 0 offen offset:1024 sc1\n"
    "v_mfma_f32_16x16x32_bf16 v[148:151], a[232:235], a[104:107], v[148:151]\n"
    "buffer_load_dwordx4 a[160:163], v20, s[84:87], 0 offen offset:2560 sc1\n"
    "v_mfma_f32_16x16x32_bf16 v[152:155], a[232:235], a[112:115], v[152:155]\n"
    "v_mfma_f32_16x16x32_bf16 v[152:155], a[236:239], a[116:119], v[152:155]\n"
    "v_mfma_f32_16x16x32_bf16 v[156:159], a[236:239], a[124:127], v[156:159]\n"
    "s_waitcnt lgkmcnt(0)\n"
    "v_mfma_f32_16x16x32_bf16 v[156:159], a[232:235], a[120:123], v[156:159]\n"
    "buffer_load_dwordx4 a[164:167], v20, s[84:87], 0 offen offset:3584 sc1\n"
    "v_mfma_f32_16x16x32_bf16 v[160:163], a[192:195], a[96:99], v[160:163]\n"
    "ds_read_b64_tr_b16 a[216:217], v57 offset:28680\n"
    "v_mfma_f32_16x16x32_bf16 v[160:163], a[196:199], a[100:103], v[160:163]\n"
    "ds_read_b64_tr_b16 a[218:219], v57 offset:34824\n"
    "v_mfma_f32_16x16x32_bf16 v[164:167], a[196:199], a[108:111], v[164:167]\n"
    "v_mfma_f32_16x16x32_bf16 v[164:167], a[192:195], a[104:107], v[164:167]\n"
    "ds_read_b64_tr_b16 a[220:221], v57 offset:40968\n"
    "v_mfma_f32_16x16x32_bf16 v[168:171], a[192:195], a[112:115], v[168:171]\n"
    "buffer_load_dwordx2 a[156:157], v21, s[84:87], 0 offen offset:0 sc1\n"
    "ds_read_b64_tr_b16 a[222:223], v57 offset:47112\n"
    "v_mfma_f32_16x16x32_bf16 v[168:171], a[196:199], a[116:119], v[168:171]\n"
    "v_mfma_f32_16x16x32_bf16 v[172:175], a[196:199], a[124:127], v[172:175]\n"
    "ds_read_b64_tr_b16 a[224:225], v58 offset:28672\n"
    "v_mfma_f32_16x16x32_bf16 v[172:175], a[192:195], a[120:123], v[172:175]\n"
    "ds_read_b64_tr_b16 a[226:227], v58 offset:34816\n"
    "v_mfma_f32_16x16x32_bf16 v[188:191], a[200:203], a[120:123], v[188:191]\n"
    "v_mfma_f32_16x16x32_bf16 v[188:191], a[204:207], a[124:127], v[188:191]\n"
    "buffer_load_dwordx2 a[158:159], v21, s[84:87], 0 offen offset:2560 sc1\n"
    "ds_read_b64_tr_b16 a[228:229], v58 offset:40960\n"
    "v_mfma_f32_16x16x32_bf16 v[184:187], a[204:207], a[116:119], v[184:187]\n"
    "ds_read_b64_tr_b16 a[230:231], v58 offset:47104\n"
    "v_mfma_f32_16x16x32_bf16 v[184:187], a[200:203], a[112:115], v[184:187]\n"
    "v_mfma_f32_16x16x32_bf16 v[180:183], a[200:203], a[104:107], v[180:183]\n"
    "ds_read_b64_tr_b16 a[232:233], v58 offset:28680\n"
    "v_mfma_f32_16x16x32_bf16 v[180:183], a[204:207], a[108:111], v[180:183]\n"
    "ds_read_b64_tr_b16 a[234:235], v58 offset:34824\n"
    "v_mfma_f32_16x16x32_bf16 v[176:179], a[204:207], a[100:103], v[176:179]\n"
    "s_lshl_b32 s73, s83, 4\n"
    "v_mfma_f32_16x16x32_bf16 v[176:179], a[200:203], a[96:99], v[176:179]\n"
    "s_sub_u32 s73, s73, 2\n"
    "ds_read_b64_tr_b16 a[236:237], v58 offset:40968\n"
    "v_bfe_u32 v16, v9, 4, 4\n"
    "v_mfma_f32_16x16x32_bf16 v[192:195], a[208:211], a[96:99], v[192:195]\n"
    "ds_read_b64_tr_b16 a[238:239], v58 offset:47112\n"
    "v_add_u32 v16, s73, v16\n"
    "v_mfma_f32_16x16x32_bf16 v[192:195], a[212:215], a[100:103], v[192:195]\n"
    "v_lshrrev_b32 v17, 4, v16\n"
    "v_min_u32 v17, 0x80, v17\n"
    "v_mfma_f32_16x16x32_bf16 v[196:199], a[212:215], a[108:111], v[196:199]\n"
    "v_mul_u32_u24 v17, 20480, v17\n"
    "v_add_u32 v17, 10240, v17\n"
    "v_mfma_f32_16x16x32_bf16 v[196:199], a[208:211], a[104:107], v[196:199]\n"
    "v_and_b32 v16, 15, v16\n"
    "v_lshrrev_b32 v18, 8, v9\n"
    "v_mfma_f32_16x16x32_bf16 v[200:203], a[208:211], a[112:115], v[200:203]\n"
    "v_lshl_add_u32 v16, v18, 4, v16\n"
    "v_lshl_add_u32 v20, v16, 4, v17\n"
    "v_mfma_f32_16x16x32_bf16 v[200:203], a[212:215], a[116:119], v[200:203]\n"
    "v_lshl_add_u32 v21, v16, 3, v17\n"
    "v_add_u32 v21, 2048, v21\n"
    "v_mfma_f32_16x16x32_bf16 v[204:207], a[212:215], a[124:127], v[204:207]\n"
    "s_waitcnt vmcnt(31) lgkmcnt(0)\n"
    "s_barrier\n"
    "v_mfma_f32_16x16x32_bf16 v[204:207], a[208:211], a[120:123], v[204:207]\n"
    "v_add_u32 v57, 49152, v57\n"
    "v_add_u32 v58, 49152, v58\n"
    "v_mfma_f32_16x16x32_bf16 v[208:211], a[216:219], a[96:99], v[208:211]\n"
    "buffer_load_dwordx4 a[168:171], v20, s[84:87], 0 offen offset:0 sc1\n"
    "ds_read_b64_tr_b16 a[192:193], v57 offset:0\n"
    "v_mfma_f32_16x16x32_bf16 v[208:211], a[220:223], a[100:103], v[208:211]\n"
    "buffer_load_dwordx4 a[172:175], v20, s[84:87], 0 offen offset:1024 sc1\n"
    "ds_read_b64_tr_b16 a[194:195], v57 offset:6144\n"
    "v_mfma_f32_16x16x32_bf16 v[212:215], a[220:223], a[108:111], v[212:215]\n"
    "buffer_load_dwordx4 a[180:183], v20, s[84:87], 0 offen offset:2560 sc1\n"
    "v_mfma_f32_16x16x32_bf16 v[212:215], a[216:219], a[104:107], v[212:215]\n"
    "ds_read_b64_tr_b16 a[196:197], v57 offset:12288\n"
    "v_mfma_f32_16x16x32_bf16 v[216:219], a[216:219], a[112:115], v[216:219]\n"
    "ds_read_b64_tr_b16 a[198:199], v57 offset:18432\n"
    "v_mfma_f32_16x16x32_bf16 v[216:219], a[220:223], a[116:119], v[216:219]\n"
    "buffer_load_dwordx4 a[184:187], v20, s[84:87], 0 offen offset:3584 sc1\n"
    "v_mfma_f32_16x16x32_bf16 v[220:223], a[220:223], a[124:127], v[220:223]\n"
    "ds_read_b64_tr_b16 a[200:201], v57 offset:8\n"
    "v_mfma_f32_16x16x32_bf16 v[220:223], a[216:219], a[120:123], v[220:223]\n"
    "ds_read_b64_tr_b16 a[202:203], v57 offset:6152\n"
    "v_mfma_f32_16x16x32_bf16 v[236:239], a[224:227], a[120:123], v[236:239]\n"
    "v_mfma_f32_16x16x32_bf16 v[236:239], a[228:231], a[124:127], v[236:239]\n"
    "ds_read_b64_tr_b16 a[204:205], v57 offset:12296\n"
    "v_mfma_f32_16x16x32_bf16 v[232:235], a[228:231], a[116:119], v[232:235]\n"
    "buffer_load_dwordx2 a[176:177], v21, s[84:87], 0 offen offset:0 sc1\n"
    "ds_read_b64_tr_b16 a[206:207], v57 offset:18440\n"
    "v_mfma_f32_16x16x32_bf16 v[232:235], a[224:227], a[112:115], v[232:235]\n"
    "v_mfma_f32_16x16x32_bf16 v[228:231], a[224:227], a[104:107], v[228:231]\n"
    "ds_read_b64_tr_b16 a[208:209], v58 offset:0\n"
    "v_mfma_f32_16x16x32_bf16 v[228:231], a[228:231], a[108:111], v[228:231]\n"
    "ds_read_b64_tr_b16 a[210:211], v58 offset:6144\n"
    "v_mfma_f32_16x16x32_bf16 v[224:227], a[228:231], a[100:103], v[224:227]\n"
    "v_mfma_f32_16x16x32_bf16 v[224:227], a[224:227], a[96:99], v[224:227]\n"
    "buffer_load_dwordx2 a[178:179], v21, s[84:87], 0 offen offset:2560 sc1\n"
    "ds_read_b64_tr_b16 a[212:213], v58 offset:12288\n"
    "v_mfma_f32_16x16x32_bf16 v[240:243], a[232:235], a[96:99], v[240:243]\n"
    "ds_read_b64_tr_b16 a[214:215], v58 offset:18432\n"
    "v_mfma_f32_16x16x32_bf16 v[240:243], a[236:239], a[100:103], v[240:243]\n"
    "v_mfma_f32_16x16x32_bf16 v[244:247], a[236:239], a[108:111], v[244:247]\n"
    "v_mfma_f32_16x16x32_bf16 v[244:247], a[232:235], a[104:107], v[244:247]\n"
    "v_mfma_f32_16x16x32_bf16 v[248:251], a[232:235], a[112:115], v[248:251]\n"
    "v_mfma_f32_16x16x32_bf16 v[248:251], a[236:239], a[116:119], v[248:251]\n"
    "v_mfma_f32_16x16x32_bf16 v[252:255], a[236:239], a[124:127], v[252:255]\n"
    "s_waitcnt lgkmcnt(0)\n"
    "v_mfma_f32_16x16x32_bf16 v[252:255], a[232:235], a[120:123], v[252:255]\n"
    "v_mfma_f32_16x16x32_bf16 v[64:67], a[192:195], a[0:3], v[64:67]\n"
    "ds_read_b64_tr_b16 a[216:217], v58 offset:8\n"
    "v_mfma_f32_16x16x32_bf16 v[64:67], a[196:199], a[4:7], v[64:67]\n"
    "s_lshl_b32 s73, s83, 4\n"
    "ds_read_b64_tr_b16 a[218:219], v58 offset:6152\n"
    "v_mfma_f32_16x16x32_bf16 v[68:71], a[196:199], a[12:15], v[68:71]\n"
    "s_sub_u32 s73, s73, 4294967294\n"
    "v_bfe_u32 v16, v9, 4, 4\n"
    "v_add_u32 v16, s73, v16\n"
    "v_mfma_f32_16x16x32_bf16 v[68:71], a[192:195], a[8:11], v[68:71]\n"
    "ds_read_b64_tr_b16 a[220:221], v58 offset:12296\n"
    "v_lshrrev_b32 v17, 4, v16\n"
    "v_mfma_f32_16x16x32_bf16 v[72:75], a[192:195], a[16:19], v[72:75]\n"
    "ds_read_b64_tr_b16 a[222:223], v58 offset:18440\n"
    "v_min_u32 v17, 0x80, v17\n"
    "v_mfma_f32_16x16x32_bf16 v[72:75], a[196:199], a[20:23], v[72:75]\n"
    "v_mul_u32_u24 v17, 20480, v17\n"
    "v_add_u32 v17, 15360, v17\n"
    "v_mfma_f32_16x16x32_bf16 v[76:79], a[196:199], a[28:31], v[76:79]\n"
    "ds_read_b64_tr_b16 a[224:225], v57 offset:2048\n"
    "v_and_b32 v16, 15, v16\n"
    "v_mfma_f32_16x16x32_bf16 v[76:79], a[192:195], a[24:27], v[76:79]\n"
    "ds_read_b64_tr_b16 a[226:227], v57 offset:8192\n"
    "v_lshrrev_b32 v18, 8, v9\n"
    "v_mfma_f32_16x16x32_bf16 v[92:95], a[200:203], a[24:27], v[92:95]\n"
    "v_lshl_add_u32 v16, v18, 4, v16\n"
    "v_lshl_add_u32 v20, v16, 4, v17\n"
    "v_mfma_f32_16x16x32_bf16 v[92:95], a[204:207], a[28:31], v[92:95]\n"
    "ds_read_b64_tr_b16 a[228:229], v57 offset:14336\n"
    "v_lshl_add_u32 v21, v16, 3, v17\n"
    "v_mfma_f32_16x16x32_bf16 v[88:91], a[204:207], a[20:23], v[88:91]\n"
    "ds_read_b64_tr_b16 a[230:231], v57 offset:20480\n"
    "v_add_u32 v21, 2048, v21\n"
    "v_mfma_f32_16x16x32_bf16 v[88:91], a[200:203], a[16:19], v[88:91]\n"
    "buffer_load_dwordx4 a[188:191], v20, s[84:87], 0 offen offset:0 sc1\n"
    "v_mfma_f32_16x16x32_bf16 v[84:87], a[200:203], a[8:11], v[84:87]\n"
    "buffer_load_dwordx4 a[240:243], v20, s[84:87], 0 offen offset:1024 sc1\n"
    "ds_read_b64_tr_b16 a[232:233], v57 offset:2056\n"
    "v_mfma_f32_16x16x32_bf16 v[84:87], a[204:207], a[12:15], v[84:87]\n"
    "buffer_load_dwordx4 a[248:251], v20, s[84:87], 0 offen offset:2560 sc1\n"
    "ds_read_b64_tr_b16 a[234:235], v57 offset:8200\n"
    "v_mfma_f32_16x16x32_bf16 v[80:83], a[204:207], a[4:7], v[80:83]\n"
    "v_mfma_f32_16x16x32_bf16 v[80:83], a[200:203], a[0:3], v[80:83]\n"
    "ds_read_b64_tr_b16 a[236:237], v57 offset:14344\n"
    "v_mfma_f32_16x16x32_bf16 v[96:99], a[208:211], a[0:3], v[96:99]\n"
    "ds_read_b64_tr_b16 a[238:239], v57 offset:20488\n"
    "v_mfma_f32_16x16x32_bf16 v[96:99], a[212:215], a[4:7], v[96:99]\n"
    "v_mfma_f32_16x16x32_bf16 v[100:103], a[212:215], a[12:15], v[100:103]\n"
    "buffer_load_dwordx4 a[252:255], v20, s[84:87], 0 offen offset:3584 sc1\n"
    "v_mfma_f32_16x16x32_bf16 v[100:103], a[208:211], a[8:11], v[100:103]\n"
    "v_mfma_f32_16x16x32_bf16 v[104:107], a[208:211], a[16:19], v[104:107]\n"
    "v_mfma_f32_16x16x32_bf16 v[104:107], a[212:215], a[20:23], v[104:107]\n"
    "v_mfma_f32_16x16x32_bf16 v[108:111], a[212:215], a[28:31], v[108:111]\n"
    "s_waitcnt lgkmcnt(0)\n"
    "v_mfma_f32_16x16x32_bf16 v[108:111], a[208:211], a[24:27], v[108:111]\n"
    "v_mfma_f32_16x16x32_bf16 v[112:115], a[216:219], a[0:3], v[112:115]\n"
    "buffer_load_dwordx2 a[244:245], v21, s[84:87], 0 offen offset:0 sc1\n"
    "ds_read_b64_tr_b16 a[192:193], v58 offset:2048\n"
    "v_mfma_f32_16x16x32_bf16 v[112:115], a[220:223], a[4:7], v[112:115]\n"
    "ds_read_b64_tr_b16 a[194:195], v58 offset:8192\n"
    "v_mfma_f32_16x16x32_bf16 v[116:119], a[220:223], a[12:15], v[116:119]\n"
    "v_mfma_f32_16x16x32_bf16 v[116:119], a[216:219], a[8:11], v[116:119]\n"
    "ds_read_b64_tr_b16 a[196:197], v58 offset:14336\n"
    "v_mfma_f32_16x16x32_bf16 v[120:123], a[216:219], a[16:19], v[120:123]\n"
    "ds_read_b64_tr_b16 a[198:199], v58 offset:20480\n"
    "v_mfma_f32_16x16x32_bf16 v[120:123], a[220:223], a[20:23], v[120:123]\n"
    "buffer_load_dwordx2 a[246:247], v21, s[84:87], 0 offen offset:2560 sc1\n"
    "v_mfma_f32_16x16x32_bf16 v[124:127], a[220:223], a[28:31], v[124:127]\n"
    "ds_read_b64_tr_b16 a[200:201], v58 offset:2056\n"
    "v_mfma_f32_16x16x32_bf16 v[124:127], a[216:219], a[24:27], v[124:127]\n"
    "ds_read_b64_tr_b16 a[202:203], v58 offset:8200\n"
    "v_mfma_f32_16x16x32_bf16 v[140:143], a[224:227], a[24:27], v[140:143]\n"
    "v_mfma_f32_16x16x32_bf16 v[140:143], a[228:231], a[28:31], v[140:143]\n"
    "ds_read_b64_tr_b16 a[204:205], v58 offset:14344\n"
    "v_mfma_f32_16x16x32_bf16 v[136:139], a[228:231], a[20:23], v[136:139]\n"
    "ds_read_b64_tr_b16 a[206:207], v58 offset:20488\n"
    "v_mfma_f32_16x16x32_bf16 v[136:139], a[224:227], a[16:19], v[136:139]\n"
    "s_add_u32 m0, s91, 0\n"
    "v_mfma_f32_16x16x32_bf16 v[132:135], a[224:227], a[8:11], v[132:135]\n"
    "buffer_load_dwordx4 v11, s[48:51], s90 offen offset:0 lds\n"
    "ds_read_b64_tr_b16 a[208:209], v57 offset:4096\n"
    "v_mfma_f32_16x16x32_bf16 v[132:135], a[228:231], a[12:15], v[132:135]\n"
    "ds_read_b64_tr_b16 a[210:211], v57 offset:10240\n"
    "v_mfma_f32_16x16x32_bf16 v[128:131], a[228:231], a[4:7], v[128:131]\n"
    "v_mfma_f32_16x16x32_bf16 v[128:131], a[224:227], a[0:3], v[128:131]\n"
    "ds_read_b64_tr_b16 a[212:213], v57 offset:16384\n"
    "v_mfma_f32_16x16x32_bf16 v[144:147], a[232:235], a[0:3], v[144:147]\n"
    "ds_read_b64_tr_b16 a[214:215], v57 offset:22528\n"
    "v_mfma_f32_16x16x32_bf16 v[144:147], a[236:239], a[4:7], v[144:147]\n"
    "buffer_load_dwordx4 v12, s[48:51], s90 offen offset:1024 lds\n"
    "v_mfma_f32_16x16x32_bf16 v[148:151], a[236:239], a[12:15], v[148:151]\n"
    "v_mfma_f32_16x16x32_bf16 v[148:151], a[232:235], a[8:11], v[148:151]\n"
    "v_mfma_f32_16x16x32_bf16 v[152:155], a[232:235], a[16:19], v[152:155]\n"
    "v_mfma_f32_16x16x32_bf16 v[152:155], a[236:239], a[20:23], v[152:155]\n"
    "v_mfma_f32_16x16x32_bf16 v[156:159], a[236:239], a[28:31], v[156:159]\n"
    "s_waitcnt lgkmcnt(0)\n"
    "v_mfma_f32_16x16x32_bf16 v[156:159], a[232:235], a[24:27], v[156:159]\n"
    "buffer_load_dwordx4 v13, s[48:51], s90 offen offset:2048 lds\n"
    "v_mfma_f32_16x16x32_bf16 v[160:163], a[192:195], a[0:3], v[160:163]\n"
    "ds_read_b64_tr_b16 a[216:217], v57 offset:4104\n"
    "v_mfma_f32_16x16x32_bf16 v[160:163], a[196:199], a[4:7], v[160:163]\n"
    "ds_read_b64_tr_b16 a[218:219], v57 offset:10248\n"
    "v_mfma_f32_16x16x32_bf16 v[164:167], a[196:199], a[12:15], v[164:167]\n"
    "v_mfma_f32_16x16x32_bf16 v[164:167], a[192:195], a[8:11], v[164:167]\n"
    "ds_read_b64_tr_b16 a[220:221], v57 offset:16392\n"
    "v_mfma_f32_16x16x32_bf16 v[168:171], a[192:195], a[16:19], v[168:171]\n"
    "buffer_load_dwordx4 v14, s[48:51], s90 offen offset:3072 lds\n"
    "ds_read_b64_tr_b16 a[222:223], v57 offset:22536\n"
    "v_mfma_f32_16x16x32_bf16 v[168:171], a[196:199], a[20:23], v[168:171]\n"
    "v_mfma_f32_16x16x32_bf16 v[172:175], a[196:199], a[28:31], v[172:175]\n"
    "ds_read_b64_tr_b16 a[224:225], v58 offset:4096\n"
    "v_mfma_f32_16x16x32_bf16 v[172:175], a[192:195], a[24:27], v[172:175]\n"
    "ds_read_b64_tr_b16 a[226:227], v58 offset:10240\n"
    "v_mfma_f32_16x16x32_bf16 v[188:191], a[200:203], a[24:27], v[188:191]\n"
    "v_mfma_f32_16x16x32_bf16 v[188:191], a[204:207], a[28:31], v[188:191]\n"
    "ds_read_b64_tr_b16 a[228:229], v58 offset:16384\n"
    "v_mfma_f32_16x16x32_bf16 v[184:187], a[204:207], a[20:23], v[184:187]\n"
    "s_add_u32 m0, s91, 4096\n"
    "ds_read_b64_tr_b16 a[230:231], v58 offset:22528\n"
    "v_mfma_f32_16x16x32_bf16 v[184:187], a[200:203], a[16:19], v[184:187]\n"
    "buffer_load_dwordx4 v15, s[48:51], s90 offen offset:0 lds\n"
    "v_mfma_f32_16x16x32_bf16 v[180:183], a[200:203], a[8:11], v[180:183]\n"
    "ds_read_b64_tr_b16 a[232:233], v58 offset:4104\n"
    "v_mfma_f32_16x16x32_bf16 v[180:183], a[204:207], a[12:15], v[180:183]\n"
    "ds_read_b64_tr_b16 a[234:235], v58 offset:10248\n"
    "v_mfma_f32_16x16x32_bf16 v[176:179], a[204:207], a[4:7], v[176:179]\n"
    "v_mfma_f32_16x16x32_bf16 v[176:179], a[200:203], a[0:3], v[176:179]\n"
    "ds_read_b64_tr_b16 a[236:237], v58 offset:16392\n"
    "v_mfma_f32_16x16x32_bf16 v[192:195], a[208:211], a[0:3], v[192:195]\n"
    "s_add_u32 s90, s90, 163840\n"
    "ds_read_b64_tr_b16 a[238:239], v58 offset:22536\n"
    "v_mfma_f32_16x16x32_bf16 v[192:195], a[212:215], a[4:7], v[192:195]\n"
    "s_add_u32 m0, s91, 20480\n"
    "v_mfma_f32_16x16x32_bf16 v[196:199], a[212:215], a[12:15], v[196:199]\n"
    "buffer_load_dwordx4 v11, s[48:51], s90 offen offset:0 lds\n"
    "v_mfma_f32_16x16x32_bf16 v[196:199], a[208:211], a[8:11], v[196:199]\n"
    "v_mfma_f32_16x16x32_bf16 v[200:203], a[208:211], a[16:19], v[200:203]\n"
    "v_mfma_f32_16x16x32_bf16 v[200:203], a[212:215], a[20:23], v[200:203]\n"
    "v_mfma_f32_16x16x32_bf16 v[204:207], a[212:215], a[28:31], v[204:207]\n"
    "s_waitcnt vmcnt(30) lgkmcnt(0)\n"
    "s_barrier\n"
    "v_mfma_f32_16x16x32_bf16 v[204:207], a[208:211], a[24:27], v[204:207]\n"
    "buffer_load_dwordx4 v12, s[48:51], s90 offen offset:1024 lds\n"
    "v_mfma_f32_16x16x32_bf16 v[208:211], a[216:219], a[0:3], v[208:211]\n"
    "ds_read_b64_tr_b16 a[192:193], v57 offset:24576\n"
    "v_mfma_f32_16x16x32_bf16 v[208:211], a[220:223], a[4:7], v[208:211]\n"
    "ds_read_b64_tr_b16 a[194:195], v57 offset:30720\n"
    "v_mfma_f32_16x16x32_bf16 v[212:215], a[220:223], a[12:15], v[212:215]\n"
    "v_mfma_f32_16x16x32_bf16 v[212:215], a[216:219], a[8:11], v[212:215]\n"
    "buffer_load_dwordx4 v13, s[48:51], s90 offen offset:2048 lds\n"
    "ds_read_b64_tr_b16 a[196:197], v57 offset:36864\n"
    "v_mfma_f32_16x16x32_bf16 v[216:219], a[216:219], a[16:19], v[216:219]\n"
    "ds_read_b64_tr_b16 a[198:199], v57 offset:43008\n"
    "v_mfma_f32_16x16x32_bf16 v[216:219], a[220:223], a[20:23], v[216:219]\n"
    "v_mfma_f32_16x16x32_bf16 v[220:223], a[220:223], a[28:31], v[220:223]\n"
    "ds_read_b64_tr_b16 a[200:201], v57 offset:24584\n"
    "v_mfma_f32_16x16x32_bf16 v[220:223], a[216:219], a[24:27], v[220:223]\n"
    "ds_read_b64_tr_b16 a[202:203], v57 offset:30728\n"
    "v_mfma_f32_16x16x32_bf16 v[236:239], a[224:227], a[24:27], v[236:239]\n"
    "v_mfma_f32_16x16x32_bf16 v[236:239], a[228:231], a[28:31], v[236:239]\n"
    "buffer_load_dwordx4 v14, s[48:51], s90 offen offset:3072 lds\n"
    "ds_read_b64_tr_b16 a[204:205], v57 offset:36872\n"
    "v_mfma_f32_16x16x32_bf16 v[232:235], a[228:231], a[20:23], v[232:235]\n"
    "ds_read_b64_tr_b16 a[206:207], v57 offset:43016\n"
    "v_mfma_f32_16x16x32_bf16 v[232:235], a[224:227], a[16:19], v[232:235]\n"
    "v_mfma_f32_16x16x32_bf16 v[228:231], a[224:227], a[8:11], v[228:231]\n"
    "ds_read_b64_tr_b16 a[208:209], v58 offset:24576\n"
    "v_mfma_f32_16x16x32_bf16 v[228:231], a[228:231], a[12:15], v[228:231]\n"
    "ds_read_b64_tr_b16 a[210:211], v58 offset:30720\n"
    "v_mfma_f32_16x16x32_bf16 v[224:227], a[228:231], a[4:7], v[224:227]\n"
    "v_mfma_f32_16x16x32_bf16 v[224:227], a[224:227], a[0:3], v[224:227]\n"
    "s_add_u32 m0, s91, 24576\n"
    "ds_read_b64_tr_b16 a[212:213], v58 offset:36864\n"
    "v_mfma_f32_16x16x32_bf16 v[240:243], a[232:235], a[0:3], v[240:243]\n"
    "buffer_load_dwordx4 v15, s[48:51], s90 offen offset:0 lds\n"
    "ds_read_b64_tr_b16 a[214:215], v58 offset:43008\n"
    "v_mfma_f32_16x16x32_bf16 v[240:243], a[236:239], a[4:7], v[240:243]\n"
    "v_mfma_f32_16x16x32_bf16 v[244:247], a[236:239], a[12:15], v[244:247]\n"
    "v_mfma_f32_16x16x32_bf16 v[244:247], a[232:235], a[8:11], v[244:247]\n"
    "v_mfma_f32_16x16x32_bf16 v[248:251], a[232:235], a[16:19], v[248:251]\n"
    "v_mfma_f32_16x16x32_bf16 v[248:251], a[236:239], a[20:23], v[248:251]\n"
    "v_mfma_f32_16x16x32_bf16 v[252:255], a[236:239], a[28:31], v[252:255]\n"
    "s_waitcnt lgkmcnt(0)\n"
    "v_mfma_f32_16x16x32_bf16 v[252:255], a[232:235], a[24:27], v[252:255]\n"
    "v_mfma_f32_16x16x32_bf16 v[64:67], a[192:195], a[32:35], v[64:67]\n"
    "ds_read_b64_tr_b16 a[216:217], v58 offset:24584\n"
    "v_mfma_f32_16x16x32_bf16 v[64:67], a[196:199], a[36:39], v[64:67]\n"
    "s_add_u32 m0, s68, 49152\n"
    "ds_read_b64_tr_b16 a[218:219], v58 offset:30728\n"
    "v_mfma_f32_16x16x32_bf16 v[68:71], a[196:199], a[44:47], v[68:71]\n"
    "s_mul_i32 s74, s82, 61440\n"
    "s_mul_i32 s92, s78, 160\n"
    "s_add_u32 s74, s74, s92\n"
    "s_mov_b64 s[48:49], s[20:21]\n"
    "s_add_u32 s48, s48, s74\n"
    "s_addc_u32 s49, s49, 0\n"
    "s_mov_b32 s50, 60320\n"
    "s_mov_b32 s51, 0x20000\n"
    "buffer_load_dwordx4 v8, s[36:39], s56 offen offset:0 lds\n"
    "v_add_u32 v24, 0, %2\n"
    "v_mfma_f32_16x16x32_bf16 v[68:71], a[192:195], a[40:43], v[68:71]\n"
    "ds_read_b64_tr_b16 a[220:221], v58 offset:36872\n"
    "v_mul_u32_u24 v25, 6554, v24\n"
    "v_mfma_f32_16x16x32_bf16 v[72:75], a[192:195], a[48:51], v[72:75]\n"
    "ds_read_b64_tr_b16 a[222:223], v58 offset:43016\n"
    "v_lshrrev_b32 v25, 16, v25\n"
    "v_mfma_f32_16x16x32_bf16 v[72:75], a[196:199], a[52:55], v[72:75]\n"
    "v_mul_u32_u24 v25, 1120, v25\n"
    "v_lshl_add_u32 v25, v24, 4, v25\n"
    "v_mfma_f32_16x16x32_bf16 v[76:79], a[196:199], a[60:63], v[76:79]\n"
    "buffer_load_dwordx4 v[16:19], v25, s[48:51], 0 offen\n"
    "ds_read_b64_tr_b16 a[224:225], v57 offset:26624\n"
    "v_mfma_f32_16x16x32_bf16 v[76:79], a[192:195], a[56:59], v[76:79]\n"
    "buffer_load_dwordx4 v8, s[36:39], s57 offen offset:1024 lds\n"
    "ds_read_b64_tr_b16 a[226:227], v57 offset:32768\n"
    "v_mfma_f32_16x16x32_bf16 v[92:95], a[200:203], a[56:59], v[92:95]\n"
    "v_add_u32 v24, 256, %2\n"
    "v_mul_u32_u24 v25, 6554, v24\n"
    "v_mfma_f32_16x16x32_bf16 v[92:95], a[204:207], a[60:63], v[92:95]\n"
    "ds_read_b64_tr_b16 a[228:229], v57 offset:38912\n"
    "v_lshrrev_b32 v25, 16, v25\n"
    "v_mfma_f32_16x16x32_bf16 v[88:91], a[204:207], a[52:55], v[88:91]\n"
    "ds_read_b64_tr_b16 a[230:231], v57 offset:45056\n"
    "v_mul_u32_u24 v25, 1120, v25\n"
    "v_mfma_f32_16x16x32_bf16 v[88:91], a[200:203], a[48:51], v[88:91]\n"
    "v_lshl_add_u32 v25, v24, 4, v25\n"
    "v_mfma_f32_16x16x32_bf16 v[84:87], a[200:203], a[40:43], v[84:87]\n"
    "buffer_load_dwordx4 v[20:23], v25, s[48:51], 0 offen\n"
    "ds_read_b64_tr_b16 a[232:233], v57 offset:26632\n"
    "v_mfma_f32_16x16x32_bf16 v[84:87], a[204:207], a[44:47], v[84:87]\n"
    "s_getpc_b64 s[84:85]\n"
    "L_pc1:\n"
    "s_add_u32 s84, s84, L_g2-L_pc1\n"
    "s_addc_u32 s85, s85, 0\n"
    "s_mov_b32 s86, L_tail-L_g2\n"
    "s_mov_b32 s87, 0x20000\n"
    "s_lshr_b32 s95, %1, 3\n"
    "s_lshl_b32 s95, s95, 7\n"
    "ds_read_b64_tr_b16 a[234:235], v57 offset:32776\n"
    "v_lshl_add_u32 v26, %2, 12, s95\n"
    "v_mfma_f32_16x16x32_bf16 v[80:83], a[204:207], a[36:39], v[80:83]\n"
    "s_add_u32 m0, s68, 51200\n"
    "buffer_load_dword v51, v26, s[84:87], 0 offen\n"
    "v_mfma_f32_16x16x32_bf16 v[80:83], a[200:203], a[32:35], v[80:83]\n"
    "buffer_load_dwordx4 v8, s[36:39], s58 offen offset:0 lds\n"
    "ds_read_b64_tr_b16 a[236:237], v57 offset:38920\n"
    "v_mfma_f32_16x16x32_bf16 v[96:99], a[208:211], a[32:35], v[96:99]\n"
    "ds_read_b64_tr_b16 a[238:239], v57 offset:45064\n"
    "v_mfma_f32_16x16x32_bf16 v[96:99], a[212:215], a[36:39], v[96:99]\n"
    "v_mfma_f32_16x16x32_bf16 v[100:103], a[212:215], a[44:47], v[100:103]\n"
    "v_mfma_f32_16x16x32_bf16 v[100:103], a[208:211], a[40:43], v[100:103]\n"
    "v_mfma_f32_16x16x32_bf16 v[104:107], a[208:211], a[48:51], v[104:107]\n"
    "buffer_load_dwordx4 v8, s[36:39], s59 offen offset:1024 lds\n"
    "v_mfma_f32_16x16x32_bf16 v[104:107], a[212:215], a[52:55], v[104:107]\n"
    "v_mfma_f32_16x16x32_bf16 v[108:111], a[212:215], a[60:63], v[108:111]\n"
    "s_waitcnt lgkmcnt(0)\n"
    "v_mfma_f32_16x16x32_bf16 v[108:111], a[208:211], a[56:59], v[108:111]\n"
    "v_cvt_pk_bf16_f32 v64, v64, v65\n"
    "v_cvt_pk_bf16_f32 v65, v66, v67\n"
    "v_mfma_f32_16x16x32_bf16 v[112:115], a[216:219], a[32:35], v[112:115]\n"
    "ds_read_b64_tr_b16 a[192:193], v58 offset:26624\n"
    "v_cvt_pk_bf16_f32 v66, v80, v81\n"
    "v_mfma_f32_16x16x32_bf16 v[112:115], a[220:223], a[36:39], v[112:115]\n"
    "ds_read_b64_tr_b16 a[194:195], v58 offset:32768\n"
    "v_cvt_pk_bf16_f32 v67, v82, v83\n"
    "v_mfma_f32_16x16x32_bf16 v[116:119], a[220:223], a[44:47], v[116:119]\n"
    "v_cvt_pk_bf16_f32 v68, v68, v69\n"
    "v_cvt_pk_bf16_f32 v69, v70, v71\n"
    "v_mfma_f32_16x16x32_bf16 v[116:119], a[216:219], a[40:43], v[116:119]\n"
    "s_add_u32 m0, s68, 53248\n"
    "ds_read_b64_tr_b16 a[196:197], v58 offset:38912\n"
    "v_cvt_pk_bf16_f32 v70, v84, v85\n"
    "v_mfma_f32_16x16x32_bf16 v[120:123], a[216:219], a[48:51], v[120:123]\n"
    "buffer_load_dwordx4 v8, s[36:39], s60 offen offset:0 lds\n"
    "ds_read_b64_tr_b16 a[198:199], v58 offset:45056\n"
    "v_mfma_f32_16x16x32_bf16 v[120:123], a[220:223], a[52:55], v[120:123]\n"
    "v_cvt_pk_bf16_f32 v71, v86, v87\n"
    "v_cvt_pk_bf16_f32 v72, v72, v73\n"
    "v_mfma_f32_16x16x32_bf16 v[124:127], a[220:223], a[60:63], v[124:127]\n"
    "ds_read_b64_tr_b16 a[200:201], v58 offset:26632\n"
    "v_cvt_pk_bf16_f32 v73, v74, v75\n"
    "v_mfma_f32_16x16x32_bf16 v[124:127], a[216:219], a[56:59], v[124:127]\n"
    "ds_read_b64_tr_b16 a[202:203], v58 offset:32776\n"
    "v_cvt_pk_bf16_f32 v74, v88, v89\n"
    "v_mfma_f32_16x16x32_bf16 v[140:143], a[224:227], a[56:59], v[140:143]\n"
    "v_cvt_pk_bf16_f32 v75, v90, v91\n"
    "v_cvt_pk_bf16_f32 v76, v76, v77\n"
    "v_mfma_f32_16x16x32_bf16 v[140:143], a[228:231], a[60:63], v[140:143]\n"
    "buffer_load_dwordx4 v8, s[36:39], s61 offen offset:1024 lds\n"
    "ds_read_b64_tr_b16 a[204:205], v58 offset:38920\n"
    "v_mfma_f32_16x16x32_bf16 v[136:139], a[228:231], a[52:55], v[136:139]\n"
    "s_add_u32 s36, s36, 24576\n"
    "s_addc_u32 s37, s37, 0\n"
    "s_sub_u32 s38, s38, 24576\n"
    "ds_read_b64_tr_b16 a[206:207], v58 offset:45064\n"
    "v_cvt_pk_bf16_f32 v77, v78, v79\n"
    "v_mfma_f32_16x16x32_bf16 v[136:139], a[224:227], a[48:51], v[136:139]\n"
    "v_cvt_pk_bf16_f32 v78, v92, v93\n"
    "v_cvt_pk_bf16_f32 v79, v94, v95\n"
    "v_mfma_f32_16x16x32_bf16 v[132:135], a[224:227], a[40:43], v[132:135]\n"
    "ds_read_b64_tr_b16 a[208:209], v57 offset:28672\n"
    "v_mfma_f32_16x16x32_bf16 v[132:135], a[228:231], a[44:47], v[132:135]\n"
    "ds_read_b64_tr_b16 a[210:211], v57 offset:34816\n"
    "v_mfma_f32_16x16x32_bf16 v[128:131], a[228:231], a[36:39], v[128:131]\n"
    "v_mfma_f32_16x16x32_bf16 v[128:131], a[224:227], a[32:35], v[128:131]\n"
    "ds_read_b64_tr_b16 a[212:213], v57 offset:40960\n"
    "v_cvt_pk_bf16_f32 v96, v96, v97\n"
    "v_mfma_f32_16x16x32_bf16 v[144:147], a[232:235], a[32:35], v[144:147]\n"
    "ds_read_b64_tr_b16 a[214:215], v57 offset:47104\n"
    "v_cvt_pk_bf16_f32 v97, v98, v99\n"
    "v_mfma_f32_16x16x32_bf16 v[144:147], a[236:239], a[36:39], v[144:147]\n"
    "v_cvt_pk_bf16_f32 v98, v112, v113\n"
    "v_cvt_pk_bf16_f32 v99, v114, v115\n"
    "v_mfma_f32_16x16x32_bf16 v[148:151], a[236:239], a[44:47], v[148:151]\n"
    "v_cvt_pk_bf16_f32 v100, v100, v101\n"
    "v_cvt_pk_bf16_f32 v101, v102, v103\n"
    "v_mfma_f32_16x16x32_bf16 v[148:151], a[232:235], a[40:43], v[148:151]\n"
    "v_cvt_pk_bf16_f32 v102, v116, v117\n"
    "v_cvt_pk_bf16_f32 v103, v118, v119\n"
    "v_mfma_f32_16x16x32_bf16 v[152:155], a[232:235], a[48:51], v[152:155]\n"
    "v_cvt_pk_bf16_f32 v104, v104, v105\n"
    "v_cvt_pk_bf16_f32 v105, v106, v107\n"
    "v_mfma_f32_16x16x32_bf16 v[152:155], a[236:239], a[52:55], v[152:155]\n"
    "v_cvt_pk_bf16_f32 v106, v120, v121\n"
    "v_cvt_pk_bf16_f32 v107, v122, v123\n"
    "v_mfma_f32_16x16x32_bf16 v[156:159], a[236:239], a[60:63], v[156:159]\n"
    "s_waitcnt lgkmcnt(0)\n"
    "v_cvt_pk_bf16_f32 v108, v108, v109\n"
    "v_cvt_pk_bf16_f32 v109, v110, v111\n"
    "v_mfma_f32_16x16x32_bf16 v[156:159], a[232:235], a[56:59], v[156:159]\n"
    "v_cvt_pk_bf16_f32 v110, v124, v125\n"
    "v_cvt_pk_bf16_f32 v111, v126, v127\n"
    "v_mfma_f32_16x16x32_bf16 v[160:163], a[192:195], a[32:35], v[160:163]\n"
    "ds_read_b64_tr_b16 a[216:217], v57 offset:28680\n"
    "v_mfma_f32_16x16x32_bf16 v[160:163], a[196:199], a[36:39], v[160:163]\n"
    "ds_read_b64_tr_b16 a[218:219], v57 offset:34824\n"
    "v_mfma_f32_16x16x32_bf16 v[164:167], a[196:199], a[44:47], v[164:167]\n"
    "v_mfma_f32_16x16x32_bf16 v[164:167], a[192:195], a[40:43], v[164:167]\n"
    "ds_read_b64_tr_b16 a[220:221], v57 offset:40968\n"
    "v_mfma_f32_16x16x32_bf16 v[168:171], a[192:195], a[48:51], v[168:171]\n"
    "ds_read_b64_tr_b16 a[222:223], v57 offset:47112\n"
    "v_mfma_f32_16x16x32_bf16 v[168:171], a[196:199], a[52:55], v[168:171]\n"
    "v_mfma_f32_16x16x32_bf16 v[172:175], a[196:199], a[60:63], v[172:175]\n"
    "ds_read_b64_tr_b16 a[224:225], v58 offset:28672\n"
    "v_mfma_f32_16x16x32_bf16 v[172:175], a[192:195], a[56:59], v[172:175]\n"
    "ds_read_b64_tr_b16 a[226:227], v58 offset:34816\n"
    "v_cvt_pk_bf16_f32 v128, v128, v129\n"
    "v_mfma_f32_16x16x32_bf16 v[188:191], a[200:203], a[56:59], v[188:191]\n"
    "v_cvt_pk_bf16_f32 v129, v130, v131\n"
    "v_cvt_pk_bf16_f32 v130, v144, v145\n"
    "v_mfma_f32_16x16x32_bf16 v[188:191], a[204:207], a[60:63], v[188:191]\n"
    "ds_read_b64_tr_b16 a[228:229], v58 offset:40960\n"
    "v_cvt_pk_bf16_f32 v131, v146, v147\n"
    "v_mfma_f32_16x16x32_bf16 v[184:187], a[204:207], a[52:55], v[184:187]\n"
    "ds_read_b64_tr_b16 a[230:231], v58 offset:47104\n"
    "v_cvt_pk_bf16_f32 v132, v132, v133\n"
    "v_mfma_f32_16x16x32_bf16 v[184:187], a[200:203], a[48:51], v[184:187]\n"
    "v_cvt_pk_bf16_f32 v133, v134, v135\n"
    "v_cvt_pk_bf16_f32 v134, v148, v149\n"
    "v_mfma_f32_16x16x32_bf16 v[180:183], a[200:203], a[40:43], v[180:183]\n"
    "ds_read_b64_tr_b16 a[232:233], v58 offset:28680\n"
    "v_cvt_pk_bf16_f32 v135, v150, v151\n"
    "v_mfma_f32_16x16x32_bf16 v[180:183], a[204:207], a[44:47], v[180:183]\n"
    "ds_read_b64_tr_b16 a[234:235], v58 offset:34824\n"
    "v_cvt_pk_bf16_f32 v136, v136, v137\n"
    "v_mfma_f32_16x16x32_bf16 v[176:179], a[204:207], a[36:39], v[176:179]\n"
    "v_cvt_pk_bf16_f32 v137, v138, v139\n"
    "v_cvt_pk_bf16_f32 v138, v152, v153\n"
    "v_mfma_f32_16x16x32_bf16 v[176:179], a[200:203], a[32:35], v[176:179]\n"
    "ds_read_b64_tr_b16 a[236:237], v58 offset:40968\n"
    "v_cvt_pk_bf16_f32 v139, v154, v155\n"
    "v_mfma_f32_16x16x32_bf16 v[192:195], a[208:211], a[32:35], v[192:195]\n"
    "ds_read_b64_tr_b16 a[238:239], v58 offset:47112\n"
    "v_cvt_pk_bf16_f32 v140, v140, v141\n"
    "v_mfma_f32_16x16x32_bf16 v[192:195], a[212:215], a[36:39], v[192:195]\n"
    "v_cvt_pk_bf16_f32 v141, v142, v143\n"
    "v_cvt_pk_bf16_f32 v142, v156, v157\n"
    "v_mfma_f32_16x16x32_bf16 v[196:199], a[212:215], a[44:47], v[196:199]\n"
    "v_cvt_pk_bf16_f32 v143, v158, v159\n"
    "v_mfma_f32_16x16x32_bf16 v[196:199], a[208:211], a[40:43], v[196:199]\n"
    "v_mfma_f32_16x16x32_bf16 v[200:203], a[208:211], a[48:51], v[200:203]\n"
    "v_mfma_f32_16x16x32_bf16 v[200:203], a[212:215], a[52:55], v[200:203]\n"
    "v_mfma_f32_16x16x32_bf16 v[204:207], a[212:215], a[60:63], v[204:207]\n"
    "s_waitcnt vmcnt(19) lgkmcnt(0)\n"
    "s_barrier\n"
    "v_mfma_f32_16x16x32_bf16 v[204:207], a[208:211], a[56:59], v[204:207]\n"
    "v_cvt_pk_bf16_f32 v160, v160, v161\n"
    "v_cvt_pk_bf16_f32 v161, v162, v163\n"
    "v_mfma_f32_16x16x32_bf16 v[208:211], a[216:219], a[32:35], v[208:211]\n"
    "ds_read_b64_tr_b16 a[192:193], v60 offset:0\n"
    "v_cvt_pk_bf16_f32 v162, v176, v177\n"
    "v_mfma_f32_16x16x32_bf16 v[208:211], a[220:223], a[36:39], v[208:211]\n"
    "ds_read_b64_tr_b16 a[194:195], v60 offset:2560\n"
    "v_cvt_pk_bf16_f32 v163, v178, v179\n"
    "v_mfma_f32_16x16x32_bf16 v[212:215], a[220:223], a[44:47], v[212:215]\n"
    "v_cvt_pk_bf16_f32 v164, v164, v165\n"
    "v_cvt_pk_bf16_f32 v165, v166, v167\n"
    "v_mfma_f32_16x16x32_bf16 v[212:215], a[216:219], a[40:43], v[212:215]\n"
    "ds_read_b64_tr_b16 a[196:197], v60 offset:5120\n"
    "v_cvt_pk_bf16_f32 v166, v180, v181\n"
    "v_mfma_f32_16x16x32_bf16 v[216:219], a[216:219], a[48:51], v[216:219]\n"
    "ds_read_b64_tr_b16 a[198:199], v60 offset:7680\n"
    "v_cvt_pk_bf16_f32 v167, v182, v183\n"
    "v_mfma_f32_16x16x32_bf16 v[216:219], a[220:223], a[52:55], v[216:219]\n"
    "v_cvt_pk_bf16_f32 v168, v168, v169\n"
    "v_cvt_pk_bf16_f32 v169, v170, v171\n"
    "v_mfma_f32_16x16x32_bf16 v[220:223], a[220:223], a[60:63], v[220:223]\n"
    "ds_read_b64_tr_b16 a[200:201], v60 offset:32\n"
    "v_cvt_pk_bf16_f32 v170, v184, v185\n"
    "v_mfma_f32_16x16x32_bf16 v[220:223], a[216:219], a[56:59], v[220:223]\n"
    "ds_read_b64_tr_b16 a[202:203], v60 offset:2592\n"
    "v_cvt_pk_bf16_f32 v171, v186, v187\n"
    "v_mfma_f32_16x16x32_bf16 v[236:239], a[224:227], a[56:59], v[236:239]\n"
    "v_cvt_pk_bf16_f32 v172, v172, v173\n"
    "v_cvt_pk_bf16_f32 v173, v174, v175\n"
    "v_mfma_f32_16x16x32_bf16 v[236:239], a[228:231], a[60:63], v[236:239]\n"
    "ds_read_b64_tr_b16 a[204:205], v60 offset:5152\n"
    "v_cvt_pk_bf16_f32 v174, v188, v189\n"
    "v_mfma_f32_16x16x32_bf16 v[232:235], a[228:231], a[52:55], v[232:235]\n"
    "ds_read_b64_tr_b16 a[206:207], v60 offset:7712\n"
    "v_cvt_pk_bf16_f32 v175, v190, v191\n"
    "v_mfma_f32_16x16x32_bf16 v[232:235], a[224:227], a[48:51], v[232:235]\n"
    "v_mfma_f32_16x16x32_bf16 v[228:231], a[224:227], a[40:43], v[228:231]\n"
    "ds_read_b64_tr_b16 a[208:209], v60 offset:64\n"
    "v_mfma_f32_16x16x32_bf16 v[228:231], a[228:231], a[44:47], v[228:231]\n"
    "ds_read_b64_tr_b16 a[210:211], v60 offset:2624\n"
    "v_mfma_f32_16x16x32_bf16 v[224:227], a[228:231], a[36:39], v[224:227]\n"
    "v_mfma_f32_16x16x32_bf16 v[224:227], a[224:227], a[32:35], v[224:227]\n"
    "ds_read_b64_tr_b16 a[212:213], v60 offset:5184\n"
    "v_cvt_pk_bf16_f32 v192, v192, v193\n"
    "v_mfma_f32_16x16x32_bf16 v[240:243], a[232:235], a[32:35], v[240:243]\n"
    "ds_read_b64_tr_b16 a[214:215], v60 offset:7744\n"
    "v_cvt_pk_bf16_f32 v193, v194, v195\n"
    "v_mfma_f32_16x16x32_bf16 v[240:243], a[236:239], a[36:39], v[240:243]\n"
    "v_cvt_pk_bf16_f32 v194, v208, v209\n"
    "v_cvt_pk_bf16_f32 v195, v210, v211\n"
    "v_mfma_f32_16x16x32_bf16 v[244:247], a[236:239], a[44:47], v[244:247]\n"
    "v_cvt_pk_bf16_f32 v196, v196, v197\n"
    "v_cvt_pk_bf16_f32 v197, v198, v199\n"
    "v_mfma_f32_16x16x32_bf16 v[244:247], a[232:235], a[40:43], v[244:247]\n"
    "v_cvt_pk_bf16_f32 v198, v212, v213\n"
    "v_cvt_pk_bf16_f32 v199, v214, v215\n"
    "v_mfma_f32_16x16x32_bf16 v[248:251], a[232:235], a[48:51], v[248:251]\n"
    "v_cvt_pk_bf16_f32 v200, v200, v201\n"
    "v_cvt_pk_bf16_f32 v201, v202, v203\n"
    "v_mfma_f32_16x16x32_bf16 v[248:251], a[236:239], a[52:55], v[248:251]\n"
    "v_cvt_pk_bf16_f32 v202, v216, v217\n"
    "v_cvt_pk_bf16_f32 v203, v218, v219\n"
    "v_mfma_f32_16x16x32_bf16 v[252:255], a[236:239], a[60:63], v[252:255]\n"
    "v_cvt_pk_bf16_f32 v204, v204, v205\n"
    "v_cvt_pk_bf16_f32 v205, v206, v207\n"
    "v_mfma_f32_16x16x32_bf16 v[252:255], a[232:235], a[56:59], v[252:255]\n"
    "L_g2:\n"
    "s_waitcnt lgkmcnt(10)\n"
    "v_mfma_f32_16x16x32_bf16 v[28:31], a[192:195], a[128:131], v[28:31]\n"
    "s_waitcnt lgkmcnt(8)\n"
    "ds_read_b64_tr_b16 a[216:217], v60 offset:96\n"
    "ds_read_b64_tr_b16 a[218:219], v60 offset:2656\n"
    "v_mfma_f32_16x16x32_bf16 v[28:31], a[196:199], a[132:135], v[28:31]\n"
    "s_waitcnt lgkmcnt(8)\n"
    "ds_read_b64_tr_b16 a[220:221], v60 offset:5216\n"
    "ds_read_b64_tr_b16 a[222:223], v60 offset:7776\n"
    "v_mfma_f32_16x16x32_bf16 v[32:35], a[200:203], a[128:131], v[32:35]\n"
    "s_waitcnt lgkmcnt(8)\n"
    "ds_read_b64_tr_b16 a[224:225], v60 offset:128\n"
    "ds_read_b64_tr_b16 a[226:227], v60 offset:2688\n"
    "v_mfma_f32_16x16x32_bf16 v[32:35], a[204:207], a[132:135], v[32:35]\n"
    "s_waitcnt lgkmcnt(8)\n"
    "ds_read_b64_tr_b16 a[228:229], v60 offset:5248\n"
    "ds_read_b64_tr_b16 a[230:231], v60 offset:7808\n"
    "v_mfma_f32_16x16x32_bf16 v[36:39], a[208:211], a[128:131], v[36:39]\n"
    "s_waitcnt lgkmcnt(8)\n"
    "ds_read_b64_tr_b16 a[232:233], v60 offset:10240\n"
    "ds_read_b64_tr_b16 a[234:235], v60 offset:23040\n"
    "v_mfma_f32_16x16x32_bf16 v[36:39], a[212:215], a[132:135], v[36:39]\n"
    "s_waitcnt lgkmcnt(8)\n"
    "ds_read_b64_tr_b16 a[236:237], v60 offset:12800\n"
    "ds_read_b64_tr_b16 a[238:239], v60 offset:15360\n"
    "v_mfma_f32_16x16x32_bf16 v[40:43], a[216:219], a[128:131], v[40:43]\n"
    "s_waitcnt lgkmcnt(8)\n"
    "ds_read_b64_tr_b16 a[192:193], v60 offset:10272\n"
    "ds_read_b64_tr_b16 a[194:195], v60 offset:23072\n"
    "v_mfma_f32_16x16x32_bf16 v[40:43], a[220:223], a[132:135], v[40:43]\n"
    "s_waitcnt lgkmcnt(8)\n"
    "ds_read_b64_tr_b16 a[196:197], v60 offset:12832\n"
    "ds_read_b64_tr_b16 a[198:199], v60 offset:15392\n"
    "v_mfma_f32_16x16x32_bf16 v[44:47], a[224:227], a[128:131], v[44:47]\n"
    "s_waitcnt lgkmcnt(8)\n"
    "ds_read_b64_tr_b16 a[200:201], v60 offset:10304\n"
    "ds_read_b64_tr_b16 a[202:203], v60 offset:23104\n"
    "v_cvt_pk_bf16_f32 v206, v220, v221\n"
    "v_mfma_f32_16x16x32_bf16 v[44:47], a[228:231], a[132:135], v[44:47]\n"
    "s_waitcnt lgkmcnt(8)\n"
    "ds_read_b64_tr_b16 a[204:205], v60 offset:12864\n"
    "ds_read_b64_tr_b16 a[206:207], v60 offset:15424\n"
    "v_cvt_pk_bf16_f32 v207, v222, v223\n"
    "v_mfma_f32_16x16x32_bf16 v[28:31], a[232:235], a[136:139], v[28:31]\n"
    "s_waitcnt lgkmcnt(8)\n"
    "ds_read_b64_tr_b16 a[208:209], v60 offset:10336\n"
    "ds_read_b64_tr_b16 a[210:211], v60 offset:23136\n"
    "v_cvt_pk_bf16_f32 v224, v224, v225\n"
    "v_mfma_f32_16x16x32_bf16 v[28:31], a[236:239], a[140:143], v[28:31]\n"
    "s_waitcnt lgkmcnt(8)\n"
    "ds_read_b64_tr_b16 a[212:213], v60 offset:12896\n"
    "ds_read_b64_tr_b16 a[214:215], v60 offset:15456\n"
    "v_cvt_pk_bf16_f32 v225, v226, v227\n"
    "v_mfma_f32_16x16x32_bf16 v[32:35], a[192:195], a[136:139], v[32:35]\n"
    "s_waitcnt lgkmcnt(8)\n"
    "ds_read_b64_tr_b16 a[216:217], v60 offset:10368\n"
    "ds_read_b64_tr_b16 a[218:219], v60 offset:23168\n"
    "v_cvt_pk_bf16_f32 v226, v240, v241\n"
    "v_mfma_f32_16x16x32_bf16 v[32:35], a[196:199], a[140:143], v[32:35]\n"
    "s_waitcnt lgkmcnt(8)\n"
    "ds_read_b64_tr_b16 a[220:221], v60 offset:12928\n"
    "ds_read_b64_tr_b16 a[222:223], v60 offset:15488\n"
    "v_cvt_pk_bf16_f32 v227, v242, v243\n"
    "v_mfma_f32_16x16x32_bf16 v[36:39], a[200:203], a[136:139], v[36:39]\n"
    "s_waitcnt lgkmcnt(8)\n"
    "ds_read_b64_tr_b16 a[224:225], v60 offset:17920\n"
    "ds_read_b64_tr_b16 a[226:227], v60 offset:20480\n"
    "v_cvt_pk_bf16_f32 v228, v228, v229\n"
    "v_mfma_f32_16x16x32_bf16 v[36:39], a[204:207], a[140:143], v[36:39]\n"
    "s_waitcnt lgkmcnt(8)\n"
    "ds_read_b64_tr_b16 a[228:229], v60 offset:25600\n"
    "ds_read_b64_tr_b16 a[230:231], v60 offset:28160\n"
    "v_cvt_pk_bf16_f32 v229, v230, v231\n"
    "v_mfma_f32_16x16x32_bf16 v[40:43], a[208:211], a[136:139], v[40:43]\n"
    "s_waitcnt lgkmcnt(8)\n"
    "ds_read_b64_tr_b16 a[232:233], v60 offset:17952\n"
    "ds_read_b64_tr_b16 a[234:235], v60 offset:20512\n"
    "v_cvt_pk_bf16_f32 v230, v244, v245\n"
    "v_mfma_f32_16x16x32_bf16 v[40:43], a[212:215], a[140:143], v[40:43]\n"
    "s_waitcnt lgkmcnt(8)\n"
    "ds_read_b64_tr_b16 a[236:237], v60 offset:25632\n"
    "ds_read_b64_tr_b16 a[238:239], v60 offset:28192\n"
    "v_cvt_pk_bf16_f32 v231, v246, v247\n"
    "v_mfma_f32_16x16x32_bf16 v[44:47], a[216:219], a[136:139], v[44:47]\n"
    "s_waitcnt lgkmcnt(8)\n"
    "ds_read_b64_tr_b16 a[192:193], v60 offset:17984\n"
    "ds_read_b64_tr_b16 a[194:195], v60 offset:20544\n"
    "v_cvt_pk_bf16_f32 v232, v232, v233\n"
    "v_mfma_f32_16x16x32_bf16 v[44:47], a[220:223], a[140:143], v[44:47]\n"
    "s_waitcnt lgkmcnt(8)\n"
    "ds_read_b64_tr_b16 a[196:197], v60 offset:25664\n"
    "ds_read_b64_tr_b16 a[198:199], v60 offset:28224\n"
    "v_cvt_pk_bf16_f32 v233, v234, v235\n"
    "v_mfma_f32_16x16x32_bf16 v[28:31], a[224:227], a[144:147], v[28:31]\n"
    "s_waitcnt lgkmcnt(8)\n"
    "ds_read_b64_tr_b16 a[200:201], v60 offset:18016\n"
    "ds_read_b64_tr_b16 a[202:203], v60 offset:20576\n"
    "v_cvt_pk_bf16_f32 v234, v248, v249\n"
    "v_mfma_f32_16x16x32_bf16 v[28:31], a[228:231], a[148:151], v[28:31]\n"
    "s_waitcnt lgkmcnt(8)\n"
    "ds_read_b64_tr_b16 a[204:205], v60 offset:25696\n"
    "ds_read_b64_tr_b16 a[206:207], v60 offset:28256\n"
    "v_cvt_pk_bf16_f32 v235, v250, v251\n"
    "v_mfma_f32_16x16x32_bf16 v[32:35], a[232:235], a[144:147], v[32:35]\n"
    "s_waitcnt lgkmcnt(8)\n"
    "ds_read_b64_tr_b16 a[208:209], v60 offset:18048\n"
    "ds_read_b64_tr_b16 a[210:211], v60 offset:20608\n"
    "v_cvt_pk_bf16_f32 v236, v236, v237\n"
    "v_mfma_f32_16x16x32_bf16 v[32:35], a[236:239], a[148:151], v[32:35]\n"
    "s_waitcnt lgkmcnt(8)\n"
    "ds_read_b64_tr_b16 a[212:213], v60 offset:25728\n"
    "ds_read_b64_tr_b16 a[214:215], v60 offset:28288\n"
    "v_cvt_pk_bf16_f32 v237, v238, v239\n"
    "v_mfma_f32_16x16x32_bf16 v[36:39], a[192:195], a[144:147], v[36:39]\n"
    "s_waitcnt lgkmcnt(8)\n"
    "ds_read_b64_tr_b16 a[216:217], v60 offset:30720\n"
    "ds_read_b64_tr_b16 a[218:219], v60 offset:33280\n"
    "v_cvt_pk_bf16_f32 v238, v252, v253\n"
    "v_mfma_f32_16x16x32_bf16 v[36:39], a[196:199], a[148:151], v[36:39]\n"
    "s_waitcnt lgkmcnt(8)\n"
    "ds_read_b64_tr_b16 a[220:221], v60 offset:35840\n"
    "ds_read_b64_tr_b16 a[222:223], v60 offset:48640\n"
    "v_cvt_pk_bf16_f32 v239, v254, v255\n"
    "v_mfma_f32_16x16x32_bf16 v[40:43], a[200:203], a[144:147], v[40:43]\n"
    "s_waitcnt lgkmcnt(8)\n"
    "ds_read_b64_tr_b16 a[224:225], v60 offset:30752\n"
    "ds_read_b64_tr_b16 a[226:227], v60 offset:33312\n"
    "v_mfma_f32_16x16x32_bf16 v[40:43], a[204:207], a[148:151], v[40:43]\n"
    "s_waitcnt lgkmcnt(8)\n"
    "ds_read_b64_tr_b16 a[228:229], v60 offset:35872\n"
    "ds_read_b64_tr_b16 a[230:231], v60 offset:48672\n"
    "v_mfma_f32_16x16x32_bf16 v[44:47], a[208:211], a[144:147], v[44:47]\n"
    "s_waitcnt lgkmcnt(8)\n"
    "ds_read_b64_tr_b16 a[232:233], v60 offset:30784\n"
    "ds_read_b64_tr_b16 a[234:235], v60 offset:33344\n"
    "v_mfma_f32_16x16x32_bf16 v[44:47], a[212:215], a[148:151], v[44:47]\n"
    "s_waitcnt lgkmcnt(8)\n"
    "ds_read_b64_tr_b16 a[236:237], v60 offset:35904\n"
    "ds_read_b64_tr_b16 a[238:239], v60 offset:48704\n"
    "v_mfma_f32_16x16x32_bf16 v[28:31], a[216:219], a[152:155], v[28:31]\n"
    "s_waitcnt lgkmcnt(8)\n"
    "ds_read_b64_tr_b16 a[192:193], v60 offset:30816\n"
    "ds_read_b64_tr_b16 a[194:195], v60 offset:33376\n"
    "v_mfma_f32_16x16x32_bf16 v[28:31], a[220:223], a[156:159], v[28:31]\n"
    "s_waitcnt lgkmcnt(8)\n"
    "ds_read_b64_tr_b16 a[196:197], v60 offset:35936\n"
    "ds_read_b64_tr_b16 a[198:199], v60 offset:48736\n"
    "v_mfma_f32_16x16x32_bf16 v[32:35], a[224:227], a[152:155], v[32:35]\n"
    "s_waitcnt lgkmcnt(8)\n"
    "ds_read_b64_tr_b16 a[200:201], v60 offset:30848\n"
    "ds_read_b64_tr_b16 a[202:203], v60 offset:33408\n"
    "v_mfma_f32_16x16x32_bf16 v[32:35], a[228:231], a[156:159], v[32:35]\n"
    "s_waitcnt lgkmcnt(8)\n"
    "ds_read_b64_tr_b16 a[204:205], v60 offset:35968\n"
    "ds_read_b64_tr_b16 a[206:207], v60 offset:48768\n"
    "v_mfma_f32_16x16x32_bf16 v[36:39], a[232:235], a[152:155], v[36:39]\n"
    "s_waitcnt lgkmcnt(8)\n"
    "ds_read_b64_tr_b16 a[208:209], v60 offset:38400\n"
    "ds_read_b64_tr_b16 a[210:211], v60 offset:40960\n"
    "v_mfma_f32_16x16x32_bf16 v[36:39], a[236:239], a[156:159], v[36:39]\n"
    "s_waitcnt lgkmcnt(8)\n"
    "ds_read_b64_tr_b16 a[212:213], v60 offset:43520\n"
    "ds_read_b64_tr_b16 a[214:215], v60 offset:46080\n"
    "v_mfma_f32_16x16x32_bf16 v[40:43], a[192:195], a[152:155], v[40:43]\n"
    "s_waitcnt lgkmcnt(8)\n"
    "ds_read_b64_tr_b16 a[216:217], v60 offset:38432\n"
    "ds_read_b64_tr_b16 a[218:219], v60 offset:40992\n"
    "v_mfma_f32_16x16x32_bf16 v[40:43], a[196:199], a[156:159], v[40:43]\n"
    "s_waitcnt lgkmcnt(8)\n"
    "ds_read_b64_tr_b16 a[220:221], v60 offset:43552\n"
    "ds_read_b64_tr_b16 a[222:223], v60 offset:46112\n"
    "v_mfma_f32_16x16x32_bf16 v[44:47], a[200:203], a[152:155], v[44:47]\n"
    "s_waitcnt lgkmcnt(8)\n"
    "ds_read_b64_tr_b16 a[224:225], v60 offset:38464\n"
    "ds_read_b64_tr_b16 a[226:227], v60 offset:41024\n"
    "v_mfma_f32_16x16x32_bf16 v[44:47], a[204:207], a[156:159], v[44:47]\n"
    "s_waitcnt lgkmcnt(8)\n"
    "ds_read_b64_tr_b16 a[228:229], v60 offset:43584\n"
    "ds_read_b64_tr_b16 a[230:231], v60 offset:46144\n"
    "v_mfma_f32_16x16x32_bf16 v[28:31], a[208:211], a[160:163], v[28:31]\n"
    "s_waitcnt lgkmcnt(8)\n"
    "ds_read_b64_tr_b16 a[232:233], v60 offset:38496\n"
    "ds_read_b64_tr_b16 a[234:235], v60 offset:41056\n"
    "v_mfma_f32_16x16x32_bf16 v[28:31], a[212:215], a[164:167], v[28:31]\n"
    "s_waitcnt lgkmcnt(8)\n"
    "ds_read_b64_tr_b16 a[236:237], v60 offset:43616\n"
    "ds_read_b64_tr_b16 a[238:239], v60 offset:46176\n"
    "v_mfma_f32_16x16x32_bf16 v[32:35], a[216:219], a[160:163], v[32:35]\n"
    "s_waitcnt lgkmcnt(8)\n"
    "ds_read_b64_tr_b16 a[192:193], v60 offset:38528\n"
    "ds_read_b64_tr_b16 a[194:195], v60 offset:41088\n"
    "v_mfma_f32_16x16x32_bf16 v[32:35], a[220:223], a[164:167], v[32:35]\n"
    "s_waitcnt lgkmcnt(8)\n"
    "ds_read_b64_tr_b16 a[196:197], v60 offset:43648\n"
    "ds_read_b64_tr_b16 a[198:199], v60 offset:46208\n"
    "v_mfma_f32_16x16x32_bf16 v[36:39], a[224:227], a[160:163], v[36:39]\n"
    "s_waitcnt lgkmcnt(8)\n"
    "ds_read_b64_tr_b16 a[200:201], v60 offset:51200\n"
    "ds_read_b64_tr_b16 a[202:203], v60 offset:53760\n"
    "v_mfma_f32_16x16x32_bf16 v[36:39], a[228:231], a[164:167], v[36:39]\n"
    "s_waitcnt lgkmcnt(8)\n"
    "ds_read_b64_tr_b16 a[204:205], v60 offset:56320\n"
    "ds_read_b64_tr_b16 a[206:207], v60 offset:58880\n"
    "v_mfma_f32_16x16x32_bf16 v[40:43], a[232:235], a[160:163], v[40:43]\n"
    "s_waitcnt lgkmcnt(8)\n"
    "ds_read_b64_tr_b16 a[208:209], v60 offset:51232\n"
    "ds_read_b64_tr_b16 a[210:211], v60 offset:53792\n"
    "v_mfma_f32_16x16x32_bf16 v[40:43], a[236:239], a[164:167], v[40:43]\n"
    "s_waitcnt lgkmcnt(8)\n"
    "ds_read_b64_tr_b16 a[212:213], v60 offset:56352\n"
    "ds_read_b64_tr_b16 a[214:215], v60 offset:58912\n"
    "v_mfma_f32_16x16x32_bf16 v[44:47], a[192:195], a[160:163], v[44:47]\n"
    "s_waitcnt lgkmcnt(8)\n"
    "ds_read_b64_tr_b16 a[216:217], v60 offset:51264\n"
    "ds_read_b64_tr_b16 a[218:219], v60 offset:53824\n"
    "s_mul_i32 s74, s78, 245760\n"
    "s_mul_i32 s92, s82, 7680\n"
    "s_add_u32 s74, s74, s92\n"
    "s_add_u32 s74, s74, 65011712\n"
    "s_mov_b64 s[48:49], s[24:25]\n"
    "s_add_u32 s48, s48, s74\n"
    "s_addc_u32 s49, s49, 0\n"
    "s_mov_b32 s50, 7680\n"
    "s_mov_b32 s51, 0x20000\n"
    "v_lshlrev_b32 v24, 4, %2\n"
    "v_add_u32 v25, 4096, v24\n"
    "v_mfma_f32_16x16x32_bf16 v[44:47], a[196:199], a[164:167], v[44:47]\n"
    "s_waitcnt lgkmcnt(8)\n"
    "ds_read_b64_tr_b16 a[220:221], v60 offset:56384\n"
    "ds_read_b64_tr_b16 a[222:223], v60 offset:58944\n"
    "v_mfma_f32_16x16x32_bf16 v[28:31], a[200:203], a[168:171], v[28:31]\n"
    "s_waitcnt lgkmcnt(8)\n"
    "ds_read_b64_tr_b16 a[224:225], v60 offset:51296\n"
    "ds_read_b64_tr_b16 a[226:227], v60 offset:53856\n"
    "v_mfma_f32_16x16x32_bf16 v[28:31], a[204:207], a[172:175], v[28:31]\n"
    "s_waitcnt lgkmcnt(8)\n"
    "ds_read_b64_tr_b16 a[228:229], v60 offset:56416\n"
    "ds_read_b64_tr_b16 a[230:231], v60 offset:58976\n"
    "v_mfma_f32_16x16x32_bf16 v[32:35], a[208:211], a[168:171], v[32:35]\n"
    "s_waitcnt lgkmcnt(8)\n"
    "s_waitcnt vmcnt(0)\n"
    "s_barrier\n"
    "ds_read_b64_tr_b16 a[232:233], v60 offset:51328\n"
    "ds_read_b64_tr_b16 a[234:235], v60 offset:53888\n"
    "v_mfma_f32_16x16x32_bf16 v[32:35], a[212:215], a[172:175], v[32:35]\n"
    "s_waitcnt lgkmcnt(8)\n"
    "s_add_u32 m0, s68, 98304\n"
    "ds_read_b64_tr_b16 a[236:237], v60 offset:56448\n"
    "ds_read_b64_tr_b16 a[238:239], v60 offset:59008\n"
    "v_mfma_f32_16x16x32_bf16 v[36:39], a[216:219], a[168:171], v[36:39]\n"
    "s_waitcnt lgkmcnt(8)\n"
    "buffer_load_dwordx4 v8, s[36:39], s56 offen offset:0 lds\n"
    "ds_read_b64_tr_b16 a[192:193], v61 offset:0\n"
    "ds_read_b64_tr_b16 a[194:195], v61 offset:12800\n"
    "v_mfma_f32_16x16x32_bf16 v[36:39], a[220:223], a[172:175], v[36:39]\n"
    "s_waitcnt lgkmcnt(8)\n"
    "ds_read_b64_tr_b16 a[196:197], v61 offset:2560\n"
    "ds_read_b64_tr_b16 a[198:199], v61 offset:5120\n"
    "v_mfma_f32_16x16x32_bf16 v[40:43], a[224:227], a[168:171], v[40:43]\n"
    "s_waitcnt lgkmcnt(8)\n"
    "ds_read_b64_tr_b16 a[200:201], v61 offset:32\n"
    "ds_read_b64_tr_b16 a[202:203], v61 offset:12832\n"
    "v_mfma_f32_16x16x32_bf16 v[40:43], a[228:231], a[172:175], v[40:43]\n"
    "s_waitcnt lgkmcnt(8)\n"
    "ds_read_b64_tr_b16 a[204:205], v61 offset:2592\n"
    "ds_read_b64_tr_b16 a[206:207], v61 offset:5152\n"
    "v_mfma_f32_16x16x32_bf16 v[44:47], a[232:235], a[168:171], v[44:47]\n"
    "s_waitcnt lgkmcnt(8)\n"
    "buffer_load_dwordx4 v8, s[36:39], s57 offen offset:1024 lds\n"
    "ds_read_b64_tr_b16 a[208:209], v61 offset:64\n"
    "ds_read_b64_tr_b16 a[210:211], v61 offset:12864\n"
    "v_mfma_f32_16x16x32_bf16 v[44:47], a[236:239], a[172:175], v[44:47]\n"
    "s_waitcnt lgkmcnt(8)\n"
    "ds_read_b64_tr_b16 a[212:213], v61 offset:2624\n"
    "ds_read_b64_tr_b16 a[214:215], v61 offset:5184\n"
    "v_mfma_f32_16x16x32_bf16 v[28:31], a[192:195], a[176:179], v[28:31]\n"
    "s_waitcnt lgkmcnt(8)\n"
    "ds_read_b64_tr_b16 a[216:217], v61 offset:96\n"
    "ds_read_b64_tr_b16 a[218:219], v61 offset:12896\n"
    "v_mfma_f32_16x16x32_bf16 v[28:31], a[196:199], a[180:183], v[28:31]\n"
    "s_waitcnt lgkmcnt(8)\n"
    "s_add_u32 m0, s68, 100352\n"
    "ds_read_b64_tr_b16 a[220:221], v61 offset:2656\n"
    "ds_read_b64_tr_b16 a[222:223], v61 offset:5216\n"
    "v_mfma_f32_16x16x32_bf16 v[32:35], a[200:203], a[176:179], v[32:35]\n"
    "s_waitcnt lgkmcnt(8)\n"
    "buffer_load_dwordx4 v8, s[36:39], s58 offen offset:0 lds\n"
    "ds_read_b64_tr_b16 a[224:225], v61 offset:128\n"
    "ds_read_b64_tr_b16 a[226:227], v61 offset:12928\n"
    "v_mfma_f32_16x16x32_bf16 v[32:35], a[204:207], a[180:183], v[32:35]\n"
    "s_waitcnt lgkmcnt(8)\n"
    "ds_read_b64_tr_b16 a[228:229], v61 offset:2688\n"
    "ds_read_b64_tr_b16 a[230:231], v61 offset:5248\n"
    "v_mfma_f32_16x16x32_bf16 v[36:39], a[208:211], a[176:179], v[36:39]\n"
    "s_waitcnt lgkmcnt(8)\n"
    "ds_read_b64_tr_b16 a[232:233], v61 offset:7680\n"
    "ds_read_b64_tr_b16 a[234:235], v61 offset:10240\n"
    "v_mfma_f32_16x16x32_bf16 v[36:39], a[212:215], a[180:183], v[36:39]\n"
    "s_waitcnt lgkmcnt(8)\n"
    "ds_read_b64_tr_b16 a[236:237], v61 offset:15360\n"
    "ds_read_b64_tr_b16 a[238:239], v61 offset:17920\n"
    "v_mfma_f32_16x16x32_bf16 v[40:43], a[216:219], a[176:179], v[40:43]\n"
    "s_waitcnt lgkmcnt(8)\n"
    "buffer_load_dwordx4 v8, s[36:39], s59 offen offset:1024 lds\n"
    "ds_read_b64_tr_b16 a[192:193], v61 offset:7712\n"
    "ds_read_b64_tr_b16 a[194:195], v61 offset:10272\n"
    "v_mfma_f32_16x16x32_bf16 v[40:43], a[220:223], a[180:183], v[40:43]\n"
    "s_waitcnt lgkmcnt(8)\n"
    "ds_read_b64_tr_b16 a[196:197], v61 offset:15392\n"
    "ds_read_b64_tr_b16 a[198:199], v61 offset:17952\n"
    "v_mfma_f32_16x16x32_bf16 v[44:47], a[224:227], a[176:179], v[44:47]\n"
    "s_waitcnt lgkmcnt(8)\n"
    "ds_read_b64_tr_b16 a[200:201], v61 offset:7744\n"
    "ds_read_b64_tr_b16 a[202:203], v61 offset:10304\n"
    "v_mfma_f32_16x16x32_bf16 v[44:47], a[228:231], a[180:183], v[44:47]\n"
    "s_waitcnt lgkmcnt(8)\n"
    "s_add_u32 m0, s68, 102400\n"
    "ds_read_b64_tr_b16 a[204:205], v61 offset:15424\n"
    "ds_read_b64_tr_b16 a[206:207], v61 offset:17984\n"
    "v_mfma_f32_16x16x32_bf16 v[28:31], a[232:235], a[184:187], v[28:31]\n"
    "s_waitcnt lgkmcnt(8)\n"
    "buffer_load_dwordx4 v8, s[36:39], s60 offen offset:0 lds\n"
    "ds_read_b64_tr_b16 a[208:209], v61 offset:7776\n"
    "ds_read_b64_tr_b16 a[210:211], v61 offset:10336\n"
    "v_mfma_f32_16x16x32_bf16 v[28:31], a[236:239], a[188:191], v[28:31]\n"
    "s_waitcnt lgkmcnt(8)\n"
    "ds_read_b64_tr_b16 a[212:213], v61 offset:15456\n"
    "ds_read_b64_tr_b16 a[214:215], v61 offset:18016\n"
    "v_mfma_f32_16x16x32_bf16 v[32:35], a[192:195], a[184:187], v[32:35]\n"
    "s_waitcnt lgkmcnt(8)\n"
    "ds_read_b64_tr_b16 a[216:217], v61 offset:7808\n"
    "ds_read_b64_tr_b16 a[218:219], v61 offset:10368\n"
    "v_mfma_f32_16x16x32_bf16 v[32:35], a[196:199], a[188:191], v[32:35]\n"
    "s_waitcnt lgkmcnt(8)\n"
    "ds_read_b64_tr_b16 a[220:221], v61 offset:15488\n"
    "ds_read_b64_tr_b16 a[222:223], v61 offset:18048\n"
    "v_mfma_f32_16x16x32_bf16 v[36:39], a[200:203], a[184:187], v[36:39]\n"
    "s_waitcnt lgkmcnt(8)\n"
    "buffer_load_dwordx4 v8, s[36:39], s61 offen offset:1024 lds\n"
    "ds_read_b64_tr_b16 a[224:225], v61 offset:20480\n"
    "ds_read_b64_tr_b16 a[226:227], v61 offset:23040\n"
    "v_mfma_f32_16x16x32_bf16 v[36:39], a[204:207], a[188:191], v[36:39]\n"
    "s_waitcnt lgkmcnt(8)\n"
    "ds_read_b64_tr_b16 a[228:229], v61 offset:25600\n"
    "ds_read_b64_tr_b16 a[230:231], v61 offset:38400\n"
    "v_mfma_f32_16x16x32_bf16 v[40:43], a[208:211], a[184:187], v[40:43]\n"
    "s_waitcnt lgkmcnt(8)\n"
    "ds_read_b64_tr_b16 a[232:233], v61 offset:20512\n"
    "ds_read_b64_tr_b16 a[234:235], v61 offset:23072\n"
    "v_mfma_f32_16x16x32_bf16 v[40:43], a[212:215], a[188:191], v[40:43]\n"
    "s_waitcnt lgkmcnt(8)\n"
    "s_add_u32 m0, s68, 122880\n"
    "ds_read_b64_tr_b16 a[236:237], v61 offset:25632\n"
    "ds_read_b64_tr_b16 a[238:239], v61 offset:38432\n"
    "v_mfma_f32_16x16x32_bf16 v[44:47], a[216:219], a[184:187], v[44:47]\n"
    "s_waitcnt lgkmcnt(8)\n"
    "buffer_load_dwordx4 v8, s[40:43], s56 offen offset:0 lds\n"
    "ds_read_b64_tr_b16 a[192:193], v61 offset:20544\n"
    "ds_read_b64_tr_b16 a[194:195], v61 offset:23104\n"
    "v_mfma_f32_16x16x32_bf16 v[44:47], a[220:223], a[188:191], v[44:47]\n"
    "s_waitcnt lgkmcnt(8)\n"
    "ds_read_b64_tr_b16 a[196:197], v61 offset:25664\n"
    "ds_read_b64_tr_b16 a[198:199], v61 offset:38464\n"
    "v_mfma_f32_16x16x32_bf16 v[28:31], a[224:227], a[240:243], v[28:31]\n"
    "s_waitcnt lgkmcnt(8)\n"
    "ds_read_b64_tr_b16 a[200:201], v61 offset:20576\n"
    "ds_read_b64_tr_b16 a[202:203], v61 offset:23136\n"
    "v_mfma_f32_16x16x32_bf16 v[28:31], a[228:231], a[244:247], v[28:31]\n"
    "s_waitcnt lgkmcnt(8)\n"
    "ds_read_b64_tr_b16 a[204:205], v61 offset:25696\n"
    "ds_read_b64_tr_b16 a[206:207], v61 offset:38496\n"
    "v_mfma_f32_16x16x32_bf16 v[32:35], a[232:235], a[240:243], v[32:35]\n"
    "s_waitcnt lgkmcnt(8)\n"
    "buffer_load_dwordx4 v8, s[40:43], s57 offen offset:1024 lds\n"
    "ds_read_b64_tr_b16 a[208:209], v61 offset:20608\n"
    "ds_read_b64_tr_b16 a[210:211], v61 offset:23168\n"
    "v_mfma_f32_16x16x32_bf16 v[32:35], a[236:239], a[244:247], v[32:35]\n"
    "s_waitcnt lgkmcnt(8)\n"
    "ds_read_b64_tr_b16 a[212:213], v61 offset:25728\n"
    "ds_read_b64_tr_b16 a[214:215], v61 offset:38528\n"
    "v_mfma_f32_16x16x32_bf16 v[36:39], a[192:195], a[240:243], v[36:39]\n"
    "s_waitcnt lgkmcnt(8)\n"
    "ds_read_b64_tr_b16 a[216:217], v61 offset:28160\n"
    "ds_read_b64_tr_b16 a[218:219], v61 offset:30720\n"
    "v_mfma_f32_16x16x32_bf16 v[36:39], a[196:199], a[244:247], v[36:39]\n"
    "s_waitcnt lgkmcnt(8)\n"
    "s_add_u32 m0, s68, 124928\n"
    "ds_read_b64_tr_b16 a[220:221], v61 offset:33280\n"
    "ds_read_b64_tr_b16 a[222:223], v61 offset:35840\n"
    "v_mfma_f32_16x16x32_bf16 v[40:43], a[200:203], a[240:243], v[40:43]\n"
    "s_waitcnt lgkmcnt(8)\n"
    "buffer_load_dwordx4 v8, s[40:43], s58 offen offset:0 lds\n"
    "ds_read_b64_tr_b16 a[224:225], v61 offset:28192\n"
    "ds_read_b64_tr_b16 a[226:227], v61 offset:30752\n"
    "v_mfma_f32_16x16x32_bf16 v[40:43], a[204:207], a[244:247], v[40:43]\n"
    "s_waitcnt lgkmcnt(8)\n"
    "ds_read_b64_tr_b16 a[228:229], v61 offset:33312\n"
    "ds_read_b64_tr_b16 a[230:231], v61 offset:35872\n"
    "v_mfma_f32_16x16x32_bf16 v[44:47], a[208:211], a[240:243], v[44:47]\n"
    "s_waitcnt lgkmcnt(8)\n"
    "ds_read_b64_tr_b16 a[232:233], v61 offset:28224\n"
    "ds_read_b64_tr_b16 a[234:235], v61 offset:30784\n"
    "v_mfma_f32_16x16x32_bf16 v[44:47], a[212:215], a[244:247], v[44:47]\n"
    "s_waitcnt lgkmcnt(8)\n"
    "ds_read_b64_tr_b16 a[236:237], v61 offset:33344\n"
    "ds_read_b64_tr_b16 a[238:239], v61 offset:35904\n"
    "v_mfma_f32_16x16x32_bf16 v[28:31], a[216:219], a[248:251], v[28:31]\n"
    "s_waitcnt lgkmcnt(8)\n"
    "buffer_load_dwordx4 v8, s[40:43], s59 offen offset:1024 lds\n"
    "ds_read_b64_tr_b16 a[192:193], v61 offset:28256\n"
    "ds_read_b64_tr_b16 a[194:195], v61 offset:30816\n"
    "v_mfma_f32_16x16x32_bf16 v[28:31], a[220:223], a[252:255], v[28:31]\n"
    "s_waitcnt lgkmcnt(8)\n"
    "ds_read_b64_tr_b16 a[196:197], v61 offset:33376\n"
    "ds_read_b64_tr_b16 a[198:199], v61 offset:35936\n"
    "v_mfma_f32_16x16x32_bf16 v[32:35], a[224:227], a[248:251], v[32:35]\n"
    "s_waitcnt lgkmcnt(8)\n"
    "ds_read_b64_tr_b16 a[200:201], v61 offset:28288\n"
    "ds_read_b64_tr_b16 a[202:203], v61 offset:30848\n"
    "v_mfma_f32_16x16x32_bf16 v[32:35], a[228:231], a[252:255], v[32:35]\n"
    "s_waitcnt lgkmcnt(8)\n"
    "s_add_u32 m0, s68, 126976\n"
    "ds_read_b64_tr_b16 a[204:205], v61 offset:33408\n"
    "ds_read_b64_tr_b16 a[206:207], v61 offset:35968\n"
    "v_mfma_f32_16x16x32_bf16 v[36:39], a[232:235], a[248:251], v[36:39]\n"
    "s_waitcnt lgkmcnt(8)\n"
    "buffer_load_dwordx4 v8, s[40:43], s60 offen offset:0 lds\n"
    "v_mfma_f32_16x16x32_bf16 v[36:39], a[236:239], a[252:255], v[36:39]\n"
    "s_waitcnt lgkmcnt(6)\n"
    "v_mfma_f32_16x16x32_bf16 v[40:43], a[192:195], a[248:251], v[40:43]\n"
    "s_waitcnt lgkmcnt(4)\n"
    "v_mfma_f32_16x16x32_bf16 v[40:43], a[196:199], a[252:255], v[40:43]\n"
    "s_waitcnt lgkmcnt(2)\n"
    "v_mfma_f32_16x16x32_bf16 v[44:47], a[200:203], a[248:251], v[44:47]\n"
    "s_waitcnt lgkmcnt(0)\n"
    "buffer_load_dwordx4 v8, s[40:43], s61 offen offset:1024 lds\n"
    "v_mfma_f32_16x16x32_bf16 v[44:47], a[204:207], a[252:255], v[44:47]\n"
    "v_add_u32 v8, 24576, v8\n"
    "s_barrier\n"
    "ds_read_b128 a[192:195], v2 offset:0\n"
    "ds_read_b128 a[196:199], v3 offset:0\n"
    "ds_read_b128 a[200:203], v2 offset:2048\n"
    "ds_read_b128 a[204:207], v3 offset:2048\n"
    "ds_read_b128 a[208:211], v2 offset:4096\n"
    "ds_read_b128 a[212:215], v3 offset:4096\n"
    "buffer_store_dwordx4 v[16:19], v24, s[48:51], 0 offen sc1\n"
    "buffer_store_dwordx4 v[20:23], v25, s[48:51], 0 offen sc1\n"
    "s_waitcnt lgkmcnt(5)\n"
    "v_mfma_f32_16x16x32_bf16 v[80:83], a[192:195], v[64:67], 0\n"
    "s_waitcnt lgkmcnt(4)\n"
    "v_mfma_f32_16x16x32_bf16 v[80:83], a[196:199], v[96:99], v[80:83]\n"
    "s_waitcnt lgkmcnt(3)\n"
    "v_mfma_f32_16x16x32_bf16 v[80:83], a[200:203], v[128:131], v[80:83]\n"
    "s_waitcnt lgkmcnt(2)\n"
    "v_mfma_f32_16x16x32_bf16 v[80:83], a[204:207], v[160:163], v[80:83]\n"
    "s_waitcnt lgkmcnt(1)\n"
    "v_mfma_f32_16x16x32_bf16 v[80:83], a[208:211], v[192:195], v[80:83]\n"
    "s_waitcnt lgkmcnt(0)\n"
    "v_add_f32 v26, v28, v29\n"
    "v_mul_f32 v27, v28, v28\n"
    "v_mfma_f32_16x16x32_bf16 v[80:83], a[212:215], v[224:227], v[80:83]\n"
    "v_fmac_f32 v27, v29, v29\n"
    "v_add_f32 v26, v26, v30\n"
    "v_mfma_f32_16x16x32_bf16 v[84:87], a[212:215], v[228:231], 0\n"
    "v_fmac_f32 v27, v30, v30\n"
    "v_add_f32 v26, v26, v31\n"
    "v_mfma_f32_16x16x32_bf16 v[84:87], a[208:211], v[196:199], v[84:87]\n"
    "v_fmac_f32 v27, v31, v31\n"
    "v_add_f32 v26, v26, v32\n"
    "v_mfma_f32_16x16x32_bf16 v[84:87], a[204:207], v[164:167], v[84:87]\n"
    "v_fmac_f32 v27, v32, v32\n"
    "v_add_f32 v26, v26, v33\n"
    "v_mfma_f32_16x16x32_bf16 v[84:87], a[200:203], v[132:135], v[84:87]\n"
    "v_fmac_f32 v27, v33, v33\n"
    "v_add_f32 v26, v26, v34\n"
    "v_mfma_f32_16x16x32_bf16 v[84:87], a[196:199], v[100:103], v[84:87]\n"
    "v_fmac_f32 v27, v34, v34\n"
    "v_add_f32 v26, v26, v35\n"
    "v_mfma_f32_16x16x32_bf16 v[84:87], a[192:195], v[68:71], v[84:87]\n"
    "v_fmac_f32 v27, v35, v35\n"
    "v_add_f32 v26, v26, v36\n"
    "v_mfma_f32_16x16x32_bf16 v[88:91], a[192:195], v[72:75], 0\n"
    "v_fmac_f32 v27, v36, v36\n"
    "v_add_f32 v26, v26, v37\n"
    "v_mfma_f32_16x16x32_bf16 v[88:91], a[196:199], v[104:107], v[88:91]\n"
    "v_fmac_f32 v27, v37, v37\n"
    "v_add_f32 v26, v26, v38\n"
    "v_mfma_f32_16x16x32_bf16 v[88:91], a[200:203], v[136:139], v[88:91]\n"
    "v_fmac_f32 v27, v38, v38\n"
    "v_add_f32 v26, v26, v39\n"
    "v_mfma_f32_16x16x32_bf16 v[88:91], a[204:207], v[168:171], v[88:91]\n"
    "v_fmac_f32 v27, v39, v39\n"
    "v_add_f32 v26, v26, v40\n"
    "v_mfma_f32_16x16x32_bf16 v[88:91], a[208:211], v[200:203], v[88:91]\n"
    "v_fmac_f32 v27, v40, v40\n"
    "v_add_f32 v26, v26, v41\n"
    "v_mfma_f32_16x16x32_bf16 v[88:91], a[212:215], v[232:235], v[88:91]\n"
    "v_fmac_f32 v27, v41, v41\n"
    "v_add_f32 v26, v26, v42\n"
    "v_mfma_f32_16x16x32_bf16 v[92:95], a[212:215], v[236:239], 0\n"
    "v_fmac_f32 v27, v42, v42\n"
    "v_add_f32 v26, v26, v43\n"
    "v_mfma_f32_16x16x32_bf16 v[92:95], a[208:211], v[204:207], v[92:95]\n"
    "v_fmac_f32 v27, v43, v43\n"
    "v_add_f32 v26, v26, v44\n"
    "v_mfma_f32_16x16x32_bf16 v[92:95], a[204:207], v[172:175], v[92:95]\n"
    "v_fmac_f32 v27, v44, v44\n"
    "v_add_f32 v26, v26, v45\n"
    "v_mfma_f32_16x16x32_bf16 v[92:95], a[200:203], v[140:143], v[92:95]\n"
    "v_fmac_f32 v27, v45, v45\n"
    "v_add_f32 v26, v26, v46\n"
    "v_mfma_f32_16x16x32_bf16 v[92:95], a[196:199], v[108:111], v[92:95]\n"
    "v_fmac_f32 v27, v46, v46\n"
    "v_add_f32 v26, v26, v47\n"
    "v_mfma_f32_16x16x32_bf16 v[92:95], a[192:195], v[76:79], v[92:95]\n"
    "v_fmac_f32 v27, v47, v47\n"
    "v_mov_b32 v112, v26\n"
    "v_mov_b32 v113, v27\n"
    "s_nop 1\n"
    "v_permlane16_swap_b32 v26, v112\n"
    "v_permlane16_swap_b32 v27, v113\n"
    "v_add_f32 v26, v26, v112\n"
    "v_add_f32 v27, v27, v113\n"
    "v_mov_b32 v112, v26\n"
    "v_mov_b32 v113, v27\n"
    "s_nop 1\n"
    "v_permlane32_swap_b32 v26, v112\n"
    "v_permlane32_swap_b32 v27, v113\n"
    "v_add_f32 v26, v26, v112\n"
    "v_add_f32 v27, v27, v113\n"
    "v_lshrrev_b32 v112, 1, v9\n"
    "v_cmp_gt_u32 vcc, 0x80, v112\n"
    "v_bfrev_b32 v113, 1\n"
    "s_lshl_b32 s74, s83, 3\n"
    "s_add_u32 s74, s74, s78\n"
    "s_lshl_b32 s74, s74, 7\n"
    "s_add_u32 s74, s74, 10485760\n"
    "v_cndmask_b32 v112, v113, v112, vcc\n"
    "buffer_store_dwordx2 v[26:27], v112, s[52:55], s74 offen sc1\n"
    "v_max3_f32 v16, v80, v81, v82\n"
    "v_max_f32 v16, v16, v83\n"
    "v_max3_f32 v17, v84, v85, v86\n"
    "v_max_f32 v17, v17, v87\n"
    "v_max3_f32 v18, v88, v89, v90\n"
    "v_max_f32 v18, v18, v91\n"
    "v_max3_f32 v19, v92, v93, v94\n"
    "v_max_f32 v19, v19, v95\n"
    "L_setmax:\n"
    "v_mov_b32 v20, v16\n"
    "v_mov_b32 v21, v17\n"
    "v_mov_b32 v22, v18\n"
    "v_mov_b32 v23, v19\n"
    "s_nop 1\n"
    "v_permlane16_swap_b32 v16, v20\n"
    "v_permlane16_swap_b32 v17, v21\n"
    "v_permlane16_swap_b32 v18, v22\n"
    "v_permlane16_swap_b32 v19, v23\n"
    "v_max_f32 v16, v16, v20\n"
    "v_max_f32 v17, v17, v21\n"
    "v_max_f32 v18, v18, v22\n"
    "v_max_f32 v19, v19, v23\n"
    "v_mov_b32 v20, v16\n"
    "v_mov_b32 v21, v17\n"
    "v_mov_b32 v22, v18\n"
    "v_mov_b32 v23, v19\n"
    "s_nop 1\n"
    "v_permlane32_swap_b32 v16, v20\n"
    "v_permlane32_swap_b32 v17, v21\n"
    "v_permlane32_swap_b32 v18, v22\n"
    "v_permlane32_swap_b32 v19, v23\n"
    "v_max_f32 v16, v16, v20\n"
    "v_max_f32 v17, v17, v21\n"
    "v_max_f32 v18, v18, v22\n"
    "v_max_f32 v19, v19, v23\n"
    "v_sub_f32 v112, 0xc2000000, v16\n"
    "v_sub_f32 v113, 0xc2000000, v16\n"
    "v_sub_f32 v114, 0xc2000000, v16\n"
    "v_sub_f32 v115, 0xc2000000, v16\n"
    "v_sub_f32 v116, 0xc2000000, v17\n"
    "v_sub_f32 v117, 0xc2000000, v17\n"
    "v_sub_f32 v118, 0xc2000000, v17\n"
    "v_sub_f32 v119, 0xc2000000, v17\n"
    "v_sub_f32 v120, 0xc2000000, v18\n"
    "v_sub_f32 v121, 0xc2000000, v18\n"
    "v_sub_f32 v122, 0xc2000000, v18\n"
    "v_sub_f32 v123, 0xc2000000, v18\n"
    "v_sub_f32 v124, 0xc2000000, v19\n"
    "v_sub_f32 v125, 0xc2000000, v19\n"
    "v_sub_f32 v126, 0xc2000000, v19\n"
    "v_sub_f32 v127, 0xc2000000, v19\n"
    "v_mov_b32 v48, 0\n"
    "v_mov_b32 v49, 0\n"
    "v_mov_b32 v50, 0\n"
    "v_mov_b32 v51, 0\n"
    "s_mov_b32 s69, 98304\n"
    "s_add_u32 s70, s68, 49152\n"
    "v_mfma_f32_16x16x32_bf16 v[80:83], a[192:195], v[64:67], v[112:115]\n"
    "ds_read_b128 a[216:219], v2 offset:6144\n"
    "v_mfma_f32_16x16x32_bf16 v[80:83], a[196:199], v[96:99], v[80:83]\n"
    "s_add_u32 m0, s68, 0\n"
    "v_mfma_f32_16x16x32_bf16 v[80:83], a[200:203], v[128:131], v[80:83]\n"
    "buffer_load_dwordx4 v8, s[36:39], s56 offen offset:0 lds\n"
    "v_mfma_f32_16x16x32_bf16 v[80:83], a[204:207], v[160:163], v[80:83]\n"
    "ds_read_b128 a[220:223], v3 offset:6144\n"
    "v_mfma_f32_16x16x32_bf16 v[80:83], a[208:211], v[192:195], v[80:83]\n"
    "v_mfma_f32_16x16x32_bf16 v[80:83], a[212:215], v[224:227], v[80:83]\n"
    "s_nop 0\n"
    "v_mfma_f32_16x16x32_bf16 v[84:87], a[212:215], v[228:231], v[116:119]\n"
    "ds_read_b128 a[224:227], v2 offset:8192\n"
    "v_mfma_f32_16x16x32_bf16 v[84:87], a[208:211], v[196:199], v[84:87]\n"
    "v_mfma_f32_16x16x32_bf16 v[84:87], a[204:207], v[164:167], v[84:87]\n"
    "buffer_load_dwordx4 v8, s[36:39], s57 offen offset:1024 lds\n"
    "v_mfma_f32_16x16x32_bf16 v[84:87], a[200:203], v[132:135], v[84:87]\n"
    "ds_read_b128 a[228:231], v3 offset:8192\n"
    "v_mfma_f32_16x16x32_bf16 v[84:87], a[196:199], v[100:103], v[84:87]\n"
    "v_exp_f32 v80, v80\n"
    "v_mfma_f32_16x16x32_bf16 v[84:87], a[192:195], v[68:71], v[84:87]\n"
    "v_exp_f32 v81, v81\n"
    "v_mfma_f32_16x16x32_bf16 v[88:91], a[192:195], v[72:75], v[120:123]\n"
    "ds_read_b128 a[232:235], v2 offset:10240\n"
    "v_cvt_pk_bf16_f32 v144, v80, v81\n"
    "v_add_f32 v48, v48, v80\n"
    "v_add_f32 v48, v48, v81\n"
    "v_mfma_f32_16x16x32_bf16 v[88:91], a[196:199], v[104:107], v[88:91]\n"
    "v_exp_f32 v82, v82\n"
    "v_mfma_f32_16x16x32_bf16 v[88:91], a[200:203], v[136:139], v[88:91]\n"
    "v_exp_f32 v83, v83\n"
    "v_mfma_f32_16x16x32_bf16 v[88:91], a[204:207], v[168:171], v[88:91]\n"
    "ds_read_b128 a[236:239], v3 offset:10240\n"
    "v_cvt_pk_bf16_f32 v145, v82, v83\n"
    "v_add_f32 v48, v48, v82\n"
    "v_add_f32 v48, v48, v83\n"
    "v_mfma_f32_16x16x32_bf16 v[88:91], a[208:211], v[200:203], v[88:91]\n"
    "v_exp_f32 v84, v84\n"
    "v_mfma_f32_16x16x32_bf16 v[88:91], a[212:215], v[232:235], v[88:91]\n"
    "s_add_u32 m0, s68, 2048\n"
    "v_exp_f32 v85, v85\n"
    "v_mfma_f32_16x16x32_bf16 v[92:95], a[212:215], v[236:239], v[124:127]\n"
    "buffer_load_dwordx4 v8, s[36:39], s58 offen offset:0 lds\n"
    "v_cvt_pk_bf16_f32 v148, v84, v85\n"
    "v_add_f32 v49, v49, v84\n"
    "v_add_f32 v49, v49, v85\n"
    "v_mfma_f32_16x16x32_bf16 v[92:95], a[208:211], v[204:207], v[92:95]\n"
    "v_exp_f32 v86, v86\n"
    "v_mfma_f32_16x16x32_bf16 v[92:95], a[204:207], v[172:175], v[92:95]\n"
    "v_exp_f32 v87, v87\n"
    "v_mfma_f32_16x16x32_bf16 v[92:95], a[200:203], v[140:143], v[92:95]\n"
    "s_waitcnt lgkmcnt(0)\n"
    "v_cvt_pk_bf16_f32 v149, v86, v87\n"
    "v_add_f32 v49, v49, v86\n"
    "v_add_f32 v49, v49, v87\n"
    "v_mfma_f32_16x16x32_bf16 v[92:95], a[196:199], v[108:111], v[92:95]\n"
    "v_exp_f32 v88, v88\n"
    "v_mfma_f32_16x16x32_bf16 v[92:95], a[192:195], v[76:79], v[92:95]\n"
    "v_exp_f32 v89, v89\n"
    "v_mfma_f32_16x16x32_bf16 v[80:83], a[216:219], v[64:67], v[112:115]\n"
    "buffer_load_dwordx4 v8, s[36:39], s59 offen offset:1024 lds\n"
    "ds_read_b128 a[192:195], v2 offset:12288\n"
    "v_cvt_pk_bf16_f32 v152, v88, v89\n"
    "v_add_f32 v50, v50, v88\n"
    "v_add_f32 v50, v50, v89\n"
    "v_mfma_f32_16x16x32_bf16 v[80:83], a[220:223], v[96:99], v[80:83]\n"
    "v_exp_f32 v90, v90\n"
    "v_mfma_f32_16x16x32_bf16 v[80:83], a[224:227], v[128:131], v[80:83]\n"
    "v_exp_f32 v91, v91\n"
    "v_mfma_f32_16x16x32_bf16 v[80:83], a[228:231], v[160:163], v[80:83]\n"
    "ds_read_b128 a[196:199], v3 offset:12288\n"
    "v_cvt_pk_bf16_f32 v153, v90, v91\n"
    "v_add_f32 v50, v50, v90\n"
    "v_add_f32 v50, v50, v91\n"
    "v_mfma_f32_16x16x32_bf16 v[80:83], a[232:235], v[192:195], v[80:83]\n"
    "v_exp_f32 v92, v92\n"
    "v_mfma_f32_16x16x32_bf16 v[80:83], a[236:239], v[224:227], v[80:83]\n"
    "s_add_u32 m0, s68, 4096\n"
    "v_exp_f32 v93, v93\n"
    "v_mfma_f32_16x16x32_bf16 v[84:87], a[236:239], v[228:231], v[116:119]\n"
    "buffer_load_dwordx4 v8, s[36:39], s60 offen offset:0 lds\n"
    "ds_read_b128 a[200:203], v2 offset:14336\n"
    "v_cvt_pk_bf16_f32 v156, v92, v93\n"
    "v_add_f32 v51, v51, v92\n"
    "v_add_f32 v51, v51, v93\n"
    "v_mfma_f32_16x16x32_bf16 v[84:87], a[232:235], v[196:199], v[84:87]\n"
    "v_exp_f32 v94, v94\n"
    "v_mfma_f32_16x16x32_bf16 v[84:87], a[228:231], v[164:167], v[84:87]\n"
    "v_exp_f32 v95, v95\n"
    "v_mfma_f32_16x16x32_bf16 v[84:87], a[224:227], v[132:135], v[84:87]\n"
    "ds_read_b128 a[204:207], v3 offset:14336\n"
    "v_cvt_pk_bf16_f32 v157, v94, v95\n"
    "v_add_f32 v51, v51, v94\n"
    "v_add_f32 v51, v51, v95\n"
    "v_mfma_f32_16x16x32_bf16 v[84:87], a[220:223], v[100:103], v[84:87]\n"
    "v_exp_f32 v80, v80\n"
    "v_mfma_f32_16x16x32_bf16 v[84:87], a[216:219], v[68:71], v[84:87]\n"
    "v_exp_f32 v81, v81\n"
    "v_mfma_f32_16x16x32_bf16 v[88:91], a[216:219], v[72:75], v[120:123]\n"
    "ds_read_b128 a[208:211], v2 offset:16384\n"
    "v_cvt_pk_bf16_f32 v146, v80, v81\n"
    "v_add_f32 v48, v48, v80\n"
    "v_add_f32 v48, v48, v81\n"
    "v_mfma_f32_16x16x32_bf16 v[88:91], a[220:223], v[104:107], v[88:91]\n"
    "v_exp_f32 v82, v82\n"
    "v_mfma_f32_16x16x32_bf16 v[88:91], a[224:227], v[136:139], v[88:91]\n"
    "v_exp_f32 v83, v83\n"
    "v_mfma_f32_16x16x32_bf16 v[88:91], a[228:231], v[168:171], v[88:91]\n"
    "ds_read_b128 a[212:215], v3 offset:16384\n"
    "v_cvt_pk_bf16_f32 v147, v82, v83\n"
    "v_add_f32 v48, v48, v82\n"
    "v_add_f32 v48, v48, v83\n"
    "v_mfma_f32_16x16x32_bf16 v[88:91], a[232:235], v[200:203], v[88:91]\n"
    "v_exp_f32 v84, v84\n"
    "v_mfma_f32_16x16x32_bf16 v[88:91], a[236:239], v[232:235], v[88:91]\n"
    "v_exp_f32 v85, v85\n"
    "v_mfma_f32_16x16x32_bf16 v[92:95], a[236:239], v[236:239], v[124:127]\n"
    "buffer_load_dwordx4 v8, s[36:39], s61 offen offset:1024 lds\n"
    "v_cvt_pk_bf16_f32 v150, v84, v85\n"
    "v_add_f32 v49, v49, v84\n"
    "v_add_f32 v49, v49, v85\n"
    "v_mfma_f32_16x16x32_bf16 v[92:95], a[232:235], v[204:207], v[92:95]\n"
    "v_exp_f32 v86, v86\n"
    "v_mfma_f32_16x16x32_bf16 v[92:95], a[228:231], v[172:175], v[92:95]\n"
    "v_exp_f32 v87, v87\n"
    "v_mfma_f32_16x16x32_bf16 v[92:95], a[224:227], v[140:143], v[92:95]\n"
    "s_waitcnt lgkmcnt(0)\n"
    "v_cvt_pk_bf16_f32 v151, v86, v87\n"
    "v_add_f32 v49, v49, v86\n"
    "v_add_f32 v49, v49, v87\n"
    "v_mfma_f32_16x16x32_bf16 v[92:95], a[220:223], v[108:111], v[92:95]\n"
    "v_exp_f32 v88, v88\n"
    "v_mfma_f32_16x16x32_bf16 v[92:95], a[216:219], v[76:79], v[92:95]\n"
    "s_add_u32 m0, s68, 24576\n"
    "v_exp_f32 v89, v89\n"
    "v_mfma_f32_16x16x32_bf16 v[80:83], a[192:195], v[64:67], v[112:115]\n"
    "buffer_load_dwordx4 v8, s[40:43], s56 offen offset:0 lds\n"
    "ds_read_b128 a[216:219], v2 offset:18432\n"
    "v_cvt_pk_bf16_f32 v154, v88, v89\n"
    "v_add_f32 v50, v50, v88\n"
    "v_add_f32 v50, v50, v89\n"
    "v_mfma_f32_16x16x32_bf16 v[80:83], a[196:199], v[96:99], v[80:83]\n"
    "v_exp_f32 v90, v90\n"
    "v_mfma_f32_16x16x32_bf16 v[80:83], a[200:203], v[128:131], v[80:83]\n"
    "v_exp_f32 v91, v91\n"
    "v_mfma_f32_16x16x32_bf16 v[80:83], a[204:207], v[160:163], v[80:83]\n"
    "ds_read_b128 a[220:223], v3 offset:18432\n"
    "v_cvt_pk_bf16_f32 v155, v90, v91\n"
    "v_add_f32 v50, v50, v90\n"
    "v_add_f32 v50, v50, v91\n"
    "v_mfma_f32_16x16x32_bf16 v[80:83], a[208:211], v[192:195], v[80:83]\n"
    "v_exp_f32 v92, v92\n"
    "v_mfma_f32_16x16x32_bf16 v[80:83], a[212:215], v[224:227], v[80:83]\n"
    "v_exp_f32 v93, v93\n"
    "v_mfma_f32_16x16x32_bf16 v[84:87], a[212:215], v[228:231], v[116:119]\n"
    "buffer_load_dwordx4 v8, s[40:43], s57 offen offset:1024 lds\n"
    "ds_read_b128 a[224:227], v2 offset:20480\n"
    "v_cvt_pk_bf16_f32 v158, v92, v93\n"
    "v_add_f32 v51, v51, v92\n"
    "v_add_f32 v51, v51, v93\n"
    "v_mfma_f32_16x16x32_bf16 v[84:87], a[208:211], v[196:199], v[84:87]\n"
    "v_exp_f32 v94, v94\n"
    "v_mfma_f32_16x16x32_bf16 v[84:87], a[204:207], v[164:167], v[84:87]\n"
    "v_exp_f32 v95, v95\n"
    "v_mfma_f32_16x16x32_bf16 v[84:87], a[200:203], v[132:135], v[84:87]\n"
    "ds_read_b128 a[228:231], v3 offset:20480\n"
    "v_cvt_pk_bf16_f32 v159, v94, v95\n"
    "v_add_f32 v51, v51, v94\n"
    "v_add_f32 v51, v51, v95\n"
    "v_mfma_f32_16x16x32_bf16 v[84:87], a[196:199], v[100:103], v[84:87]\n"
    "v_exp_f32 v80, v80\n"
    "v_mfma_f32_16x16x32_bf16 v[84:87], a[192:195], v[68:71], v[84:87]\n"
    "v_exp_f32 v81, v81\n"
    "v_mfma_f32_16x16x32_bf16 v[88:91], a[192:195], v[72:75], v[120:123]\n"
    "ds_read_b128 a[232:235], v2 offset:22528\n"
    "v_cvt_pk_bf16_f32 v176, v80, v81\n"
    "v_add_f32 v48, v48, v80\n"
    "v_add_f32 v48, v48, v81\n"
    "v_mfma_f32_16x16x32_bf16 v[88:91], a[196:199], v[104:107], v[88:91]\n"
    "v_exp_f32 v82, v82\n"
    "v_mfma_f32_16x16x32_bf16 v[88:91], a[200:203], v[136:139], v[88:91]\n"
    "v_exp_f32 v83, v83\n"
    "v_mfma_f32_16x16x32_bf16 v[88:91], a[204:207], v[168:171], v[88:91]\n"
    "ds_read_b128 a[236:239], v3 offset:22528\n"
    "v_cvt_pk_bf16_f32 v177, v82, v83\n"
    "v_add_f32 v48, v48, v82\n"
    "v_add_f32 v48, v48, v83\n"
    "v_mfma_f32_16x16x32_bf16 v[88:91], a[208:211], v[200:203], v[88:91]\n"
    "v_exp_f32 v84, v84\n"
    "v_mfma_f32_16x16x32_bf16 v[88:91], a[212:215], v[232:235], v[88:91]\n"
    "s_add_u32 m0, s68, 26624\n"
    "v_exp_f32 v85, v85\n"
    "v_mfma_f32_16x16x32_bf16 v[92:95], a[212:215], v[236:239], v[124:127]\n"
    "buffer_load_dwordx4 v8, s[40:43], s58 offen offset:0 lds\n"
    "v_cvt_pk_bf16_f32 v180, v84, v85\n"
    "v_add_f32 v49, v49, v84\n"
    "v_add_f32 v49, v49, v85\n"
    "v_mfma_f32_16x16x32_bf16 v[92:95], a[208:211], v[204:207], v[92:95]\n"
    "v_exp_f32 v86, v86\n"
    "v_mfma_f32_16x16x32_bf16 v[92:95], a[204:207], v[172:175], v[92:95]\n"
    "v_exp_f32 v87, v87\n"
    "v_mfma_f32_16x16x32_bf16 v[92:95], a[200:203], v[140:143], v[92:95]\n"
    "s_waitcnt vmcnt(12) lgkmcnt(0)\n"
    "s_barrier\n"
    "v_cvt_pk_bf16_f32 v181, v86, v87\n"
    "v_add_f32 v49, v49, v86\n"
    "v_add_f32 v49, v49, v87\n"
    "v_add_u32 v2, 49152, v2\n"
    "v_add_u32 v3, 49152, v3\n"
    "v_mfma_f32_16x16x32_bf16 v[92:95], a[196:199], v[108:111], v[92:95]\n"
    "v_exp_f32 v88, v88\n"
    "v_mfma_f32_16x16x32_bf16 v[92:95], a[192:195], v[76:79], v[92:95]\n"
    "v_exp_f32 v89, v89\n"
    "v_mfma_f32_16x16x32_bf16 v[80:83], a[216:219], v[64:67], v[112:115]\n"
    "ds_read_b128 a[192:195], v2 offset:0\n"
    "v_cvt_pk_bf16_f32 v184, v88, v89\n"
    "v_add_f32 v50, v50, v88\n"
    "v_add_f32 v50, v50, v89\n"
    "v_mfma_f32_16x16x32_bf16 v[80:83], a[220:223], v[96:99], v[80:83]\n"
    "v_exp_f32 v90, v90\n"
    "v_mfma_f32_16x16x32_bf16 v[80:83], a[224:227], v[128:131], v[80:83]\n"
    "v_exp_f32 v91, v91\n"
    "v_mfma_f32_16x16x32_bf16 v[80:83], a[228:231], v[160:163], v[80:83]\n"
    "buffer_load_dwordx4 v8, s[40:43], s59 offen offset:1024 lds\n"
    "ds_read_b128 a[196:199], v3 offset:0\n"
    "v_cvt_pk_bf16_f32 v185, v90, v91\n"
    "v_add_f32 v50, v50, v90\n"
    "v_add_f32 v50, v50, v91\n"
    "v_mfma_f32_16x16x32_bf16 v[80:83], a[232:235], v[192:195], v[80:83]\n"
    "v_exp_f32 v92, v92\n"
    "v_mfma_f32_16x16x32_bf16 v[80:83], a[236:239], v[224:227], v[80:83]\n"
    "v_exp_f32 v93, v93\n"
    "v_mfma_f32_16x16x32_bf16 v[84:87], a[236:239], v[228:231], v[116:119]\n"
    "ds_read_b128 a[200:203], v2 offset:2048\n"
    "v_cvt_pk_bf16_f32 v188, v92, v93\n"
    "v_add_f32 v51, v51, v92\n"
    "v_add_f32 v51, v51, v93\n"
    "v_mfma_f32_16x16x32_bf16 v[84:87], a[232:235], v[196:199], v[84:87]\n"
    "v_exp_f32 v94, v94\n"
    "v_mfma_f32_16x16x32_bf16 v[84:87], a[228:231], v[164:167], v[84:87]\n"
    "s_add_u32 m0, s68, 28672\n"
    "v_exp_f32 v95, v95\n"
    "v_mfma_f32_16x16x32_bf16 v[84:87], a[224:227], v[132:135], v[84:87]\n"
    "buffer_load_dwordx4 v8, s[40:43], s60 offen offset:0 lds\n"
    "ds_read_b128 a[204:207], v3 offset:2048\n"
    "v_cvt_pk_bf16_f32 v189, v94, v95\n"
    "v_add_f32 v51, v51, v94\n"
    "v_add_f32 v51, v51, v95\n"
    "v_mfma_f32_16x16x32_bf16 v[84:87], a[220:223], v[100:103], v[84:87]\n"
    "v_exp_f32 v80, v80\n"
    "v_mfma_f32_16x16x32_bf16 v[84:87], a[216:219], v[68:71], v[84:87]\n"
    "v_exp_f32 v81, v81\n"
    "v_mfma_f32_16x16x32_bf16 v[88:91], a[216:219], v[72:75], v[120:123]\n"
    "ds_read_b128 a[208:211], v2 offset:4096\n"
    "v_cvt_pk_bf16_f32 v178, v80, v81\n"
    "v_add_f32 v48, v48, v80\n"
    "v_add_f32 v48, v48, v81\n"
    "v_mfma_f32_16x16x32_bf16 v[88:91], a[220:223], v[104:107], v[88:91]\n"
    "v_exp_f32 v82, v82\n"
    "v_mfma_f32_16x16x32_bf16 v[88:91], a[224:227], v[136:139], v[88:91]\n"
    "v_exp_f32 v83, v83\n"
    "v_mfma_f32_16x16x32_bf16 v[88:91], a[228:231], v[168:171], v[88:91]\n"
    "ds_read_b128 a[212:215], v3 offset:4096\n"
    "v_cvt_pk_bf16_f32 v179, v82, v83\n"
    "v_add_f32 v48, v48, v82\n"
    "v_add_f32 v48, v48, v83\n"
    "v_mfma_f32_16x16x32_bf16 v[88:91], a[232:235], v[200:203], v[88:91]\n"
    "v_exp_f32 v84, v84\n"
    "v_mfma_f32_16x16x32_bf16 v[88:91], a[236:239], v[232:235], v[88:91]\n"
    "v_exp_f32 v85, v85\n"
    "v_mfma_f32_16x16x32_bf16 v[92:95], a[236:239], v[236:239], v[124:127]\n"
    "buffer_load_dwordx4 v8, s[40:43], s61 offen offset:1024 lds\n"
    "v_cvt_pk_bf16_f32 v182, v84, v85\n"
    "v_add_f32 v49, v49, v84\n"
    "v_add_f32 v49, v49, v85\n"
    "v_mfma_f32_16x16x32_bf16 v[92:95], a[232:235], v[204:207], v[92:95]\n"
    "v_exp_f32 v86, v86\n"
    "v_mfma_f32_16x16x32_bf16 v[92:95], a[228:231], v[172:175], v[92:95]\n"
    "v_exp_f32 v87, v87\n"
    "v_mfma_f32_16x16x32_bf16 v[92:95], a[224:227], v[140:143], v[92:95]\n"
    "s_waitcnt lgkmcnt(0)\n"
    "v_cvt_pk_bf16_f32 v183, v86, v87\n"
    "v_add_f32 v49, v49, v86\n"
    "v_add_f32 v49, v49, v87\n"
    "v_add_u32 v8, 24576, v8\n"
    "v_mfma_f32_16x16x32_bf16 v[92:95], a[220:223], v[108:111], v[92:95]\n"
    "v_exp_f32 v88, v88\n"
    "v_mfma_f32_16x16x32_bf16 v[92:95], a[216:219], v[76:79], v[92:95]\n"
    "v_exp_f32 v89, v89\n"
    "v_mfma_f32_16x16x32_bf16 v[80:83], a[192:195], v[64:67], v[112:115]\n"
    "ds_read_b64_tr_b16 a[216:217], v4 offset:24576\n"
    "v_cvt_pk_bf16_f32 v186, v88, v89\n"
    "v_add_f32 v50, v50, v88\n"
    "v_add_f32 v50, v50, v89\n"
    "v_mfma_f32_16x16x32_bf16 v[80:83], a[196:199], v[96:99], v[80:83]\n"
    "v_exp_f32 v90, v90\n"
    "v_mfma_f32_16x16x32_bf16 v[80:83], a[200:203], v[128:131], v[80:83]\n"
    "v_exp_f32 v91, v91\n"
    "v_mfma_f32_16x16x32_bf16 v[80:83], a[204:207], v[160:163], v[80:83]\n"
    "ds_read_b64_tr_b16 a[218:219], v4 offset:30720\n"
    "v_cvt_pk_bf16_f32 v187, v90, v91\n"
    "v_add_f32 v50, v50, v90\n"
    "v_add_f32 v50, v50, v91\n"
    "v_mfma_f32_16x16x32_bf16 v[80:83], a[208:211], v[192:195], v[80:83]\n"
    "v_exp_f32 v92, v92\n"
    "v_mfma_f32_16x16x32_bf16 v[80:83], a[212:215], v[224:227], v[80:83]\n"
    "v_exp_f32 v93, v93\n"
    "v_mfma_f32_16x16x32_bf16 v[84:87], a[212:215], v[228:231], v[116:119]\n"
    "ds_read_b64_tr_b16 a[220:221], v4 offset:36864\n"
    "v_cvt_pk_bf16_f32 v190, v92, v93\n"
    "v_mfma_f32_16x16x32_bf16 v[84:87], a[208:211], v[196:199], v[84:87]\n"
    "v_exp_f32 v94, v94\n"
    "v_mfma_f32_16x16x32_bf16 v[84:87], a[204:207], v[164:167], v[84:87]\n"
    "v_exp_f32 v95, v95\n"
    "v_mfma_f32_16x16x32_bf16 v[84:87], a[200:203], v[132:135], v[84:87]\n"
    "ds_read_b64_tr_b16 a[222:223], v4 offset:43008\n"
    "v_cvt_pk_bf16_f32 v191, v94, v95\n"
    "v_add_f32 v51, v51, v92\n"
    "v_add_f32 v51, v51, v93\n"
    "v_add_f32 v51, v51, v94\n"
    "v_add_f32 v51, v51, v95\n"
    "v_mfma_f32_16x16x32_bf16 v[84:87], a[196:199], v[100:103], v[84:87]\n"
    "ds_read_b64_tr_b16 a[224:225], v5 offset:24576\n"
    "v_mfma_f32_16x16x32_bf16 v[84:87], a[192:195], v[68:71], v[84:87]\n"
    "v_exp_f32 v80, v80\n"
    "v_mfma_f32_16x16x32_bf16 v[88:91], a[192:195], v[72:75], v[120:123]\n"
    "ds_read_b64_tr_b16 a[226:227], v5 offset:30720\n"
    "v_add_f32 v48, v48, v80\n"
    "v_mfma_f32_16x16x32_bf16 v[88:91], a[196:199], v[104:107], v[88:91]\n"
    "ds_read_b64_tr_b16 a[228:229], v5 offset:36864\n"
    "v_mfma_f32_16x16x32_bf16 v[88:91], a[200:203], v[136:139], v[88:91]\n"
    "v_exp_f32 v81, v81\n"
    "v_mfma_f32_16x16x32_bf16 v[88:91], a[204:207], v[168:171], v[88:91]\n"
    "ds_read_b64_tr_b16 a[230:231], v5 offset:43008\n"
    "v_cvt_pk_bf16_f32 v208, v80, v81\n"
    "v_mfma_f32_16x16x32_bf16 v[88:91], a[208:211], v[200:203], v[88:91]\n"
    "ds_read_b64_tr_b16 a[232:233], v6 offset:24576\n"
    "v_add_f32 v48, v48, v81\n"
    "v_mfma_f32_16x16x32_bf16 v[88:91], a[212:215], v[232:235], v[88:91]\n"
    "v_exp_f32 v82, v82\n"
    "v_mfma_f32_16x16x32_bf16 v[92:95], a[212:215], v[236:239], v[124:127]\n"
    "ds_read_b64_tr_b16 a[234:235], v6 offset:30720\n"
    "v_add_f32 v48, v48, v82\n"
    "v_mfma_f32_16x16x32_bf16 v[92:95], a[208:211], v[204:207], v[92:95]\n"
    "ds_read_b64_tr_b16 a[236:237], v6 offset:36864\n"
    "v_mfma_f32_16x16x32_bf16 v[92:95], a[204:207], v[172:175], v[92:95]\n"
    "v_exp_f32 v83, v83\n"
    "v_mfma_f32_16x16x32_bf16 v[92:95], a[200:203], v[140:143], v[92:95]\n"
    "ds_read_b64_tr_b16 a[238:239], v6 offset:43008\n"
    "v_cvt_pk_bf16_f32 v209, v82, v83\n"
    "v_mfma_f32_16x16x32_bf16 v[92:95], a[196:199], v[108:111], v[92:95]\n"
    "s_waitcnt lgkmcnt(0)\n"
    "v_add_f32 v48, v48, v83\n"
    "v_mfma_f32_16x16x32_bf16 v[92:95], a[192:195], v[76:79], v[92:95]\n"
    "v_exp_f32 v84, v84\n"
    "v_mfma_f32_16x16x32_bf16 a[0:3], a[216:219], v[144:147], 0\n"
    "s_add_u32 m0, s70, 0\n"
    "ds_read_b128 a[192:195], v2 offset:6144\n"
    "v_add_f32 v49, v49, v84\n"
    "v_mfma_f32_16x16x32_bf16 a[0:3], a[220:223], v[176:179], a[0:3]\n"
    "buffer_load_dwordx4 v8, s[36:39], s56 offen offset:0 lds\n"
    "v_mfma_f32_16x16x32_bf16 a[4:7], a[220:223], v[180:183], 0\n"
    "v_exp_f32 v85, v85\n"
    "v_mfma_f32_16x16x32_bf16 a[4:7], a[216:219], v[148:151], a[4:7]\n"
    "ds_read_b128 a[196:199], v3 offset:6144\n"
    "v_cvt_pk_bf16_f32 v212, v84, v85\n"
    "v_mfma_f32_16x16x32_bf16 a[8:11], a[216:219], v[152:155], 0\n"
    "v_add_f32 v49, v49, v85\n"
    "v_mfma_f32_16x16x32_bf16 a[8:11], a[220:223], v[184:187], a[8:11]\n"
    "s_mov_b32 s72, 24576\n"
    "s_lshr_b32 s92, %1, 3\n"
    "s_lshl_b32 s74, s77, 5\n"
    "s_add_u32 s74, s74, s92\n"
    "s_lshl_b32 s74, s74, 1\n"
    "s_add_u32 s74, s74, s76\n"
    "s_mul_i32 s74, s74, 15360\n"
    "s_mov_b64 s[84:85], s[12:13]\n"
    "s_add_u32 s84, s84, s74\n"
    "s_addc_u32 s85, s85, 0\n"
    "s_mov_b32 s86, 15360\n"
    "s_mov_b32 s87, 0x20000\n"
    "v_exp_f32 v86, v86\n"
    "v_mfma_f32_16x16x32_bf16 a[12:15], a[220:223], v[188:191], 0\n"
    "ds_read_b128 a[200:203], v2 offset:8192\n"
    "v_add_f32 v49, v49, v86\n"
    "v_lshrrev_b32 v59, 4, v9\n"
    "v_mfma_f32_16x16x32_bf16 a[12:15], a[216:219], v[156:159], a[12:15]\n"
    "s_mul_i32 s74, %1, 7680\n"
    "s_mov_b64 s[94:95], s[18:19]\n"
    "s_add_u32 s94, s94, s74\n"
    "s_addc_u32 s95, s95, 0\n"
    "s_cmp_eq_u32 s76, 2\n"
    "s_cselect_b64 s[84:85], s[94:95], s[84:85]\n"
    "s_cselect_b32 s86, 7680, s86\n"
    "buffer_load_dwordx4 v8, s[36:39], s57 offen offset:1024 lds\n"
    "v_cmp_gt_u32 vcc, 12, v59\n"
    "v_mfma_f32_16x16x32_bf16 a[28:31], a[224:227], v[156:159], 0\n"
    "s_getpc_b64 s[94:95]\n"
    "L_pct:\n"
    "s_add_u32 s94, s94, L_tail-L_pct\n"
    "s_addc_u32 s95, s95, 0\n"
    "s_lshl_b32 s92, s92, 11\n"
    "s_add_u32 s94, s94, s92\n"
    "s_addc_u32 s95, s95, 0\n"
    "v_exp_f32 v87, v87\n"
    "v_mfma_f32_16x16x32_bf16 a[28:31], a[228:231], v[188:191], a[28:31]\n"
    "ds_read_b128 a[204:207], v3 offset:8192\n"
    "v_cvt_pk_bf16_f32 v213, v86, v87\n"
    "v_lshlrev_b32 v59, 7, v59\n"
    "v_mfma_f32_16x16x32_bf16 a[24:27], a[228:231], v[184:187], 0\n"
    "s_sub_u32 s74, L_end-L_tail, s92\n"
    "s_max_i32 s74, s74, 0\n"
    "s_min_i32 s74, s74, 2048\n"
    "s_cmp_eq_u32 s76, 3\n"
    "s_cselect_b64 s[84:85], s[94:95], s[84:85]\n"
    "s_cselect_b32 s86, s74, s86\n"
    "s_cselect_b32 s74, 35328, s72\n"
    "v_add_f32 v49, v49, v87\n"
    "v_bfrev_b32 v24, 2\n"
    "v_mfma_f32_16x16x32_bf16 a[24:27], a[224:227], v[152:155], a[24:27]\n"
    "v_exp_f32 v88, v88\n"
    "v_mfma_f32_16x16x32_bf16 a[20:23], a[224:227], v[148:151], 0\n"
    "s_add_u32 m0, s70, 2048\n"
    "ds_read_b128 a[208:211], v2 offset:10240\n"
    "v_add_f32 v50, v50, v88\n"
    "v_cndmask_b32 v59, v24, v59, vcc\n"
    "v_mfma_f32_16x16x32_bf16 a[20:23], a[228:231], v[180:183], a[20:23]\n"
    "buffer_load_dwordx4 v8, s[36:39], s58 offen offset:0 lds\n"
    "v_subrev_u32 v59, s74, v59\n"
    "v_mfma_f32_16x16x32_bf16 a[16:19], a[228:231], v[176:179], 0\n"
    "v_exp_f32 v89, v89\n"
    "v_mfma_f32_16x16x32_bf16 a[16:19], a[224:227], v[144:147], a[16:19]\n"
    "ds_read_b128 a[212:215], v3 offset:10240\n"
    "v_cvt_pk_bf16_f32 v216, v88, v89\n"
    "v_mfma_f32_16x16x32_bf16 a[32:35], a[232:235], v[144:147], 0\n"
    "v_add_f32 v50, v50, v89\n"
    "v_mfma_f32_16x16x32_bf16 a[32:35], a[236:239], v[176:179], a[32:35]\n"
    "v_exp_f32 v90, v90\n"
    "v_mfma_f32_16x16x32_bf16 a[36:39], a[236:239], v[180:183], 0\n"
    "v_add_f32 v50, v50, v90\n"
    "v_mfma_f32_16x16x32_bf16 a[36:39], a[232:235], v[148:151], a[36:39]\n"
    "buffer_load_dwordx4 v8, s[36:39], s59 offen offset:1024 lds\n"
    "v_mfma_f32_16x16x32_bf16 a[40:43], a[232:235], v[152:155], 0\n"
    "v_exp_f32 v91, v91\n"
    "v_mfma_f32_16x16x32_bf16 a[40:43], a[236:239], v[184:187], a[40:43]\n"
    "v_cvt_pk_bf16_f32 v217, v90, v91\n"
    "v_add_f32 v50, v50, v91\n"
    "v_mfma_f32_16x16x32_bf16 a[44:47], a[236:239], v[188:191], 0\n"
    "s_waitcnt lgkmcnt(0)\n"
    "v_mfma_f32_16x16x32_bf16 a[44:47], a[232:235], v[156:159], a[44:47]\n"
    "v_exp_f32 v92, v92\n"
    "v_mfma_f32_16x16x32_bf16 v[80:83], a[192:195], v[64:67], v[112:115]\n"
    "ds_read_b64_tr_b16 a[216:217], v7 offset:24576\n"
    "v_add_f32 v51, v51, v92\n"
    "v_mfma_f32_16x16x32_bf16 v[80:83], a[196:199], v[96:99], v[80:83]\n"
    "ds_read_b64_tr_b16 a[218:219], v7 offset:30720\n"
    "v_mfma_f32_16x16x32_bf16 v[80:83], a[200:203], v[128:131], v[80:83]\n"
    "v_exp_f32 v93, v93\n"
    "v_mfma_f32_16x16x32_bf16 v[80:83], a[204:207], v[160:163], v[80:83]\n"
    "ds_read_b64_tr_b16 a[220:221], v7 offset:36864\n"
    "v_cvt_pk_bf16_f32 v220, v92, v93\n"
    "v_mfma_f32_16x16x32_bf16 v[80:83], a[208:211], v[192:195], v[80:83]\n"
    "ds_read_b64_tr_b16 a[222:223], v7 offset:43008\n"
    "v_add_f32 v51, v51, v93\n"
    "v_mfma_f32_16x16x32_bf16 v[80:83], a[212:215], v[224:227], v[80:83]\n"
    "v_exp_f32 v94, v94\n"
    "v_mfma_f32_16x16x32_bf16 v[84:87], a[212:215], v[228:231], v[116:119]\n"
    "ds_read_b64_tr_b16 a[224:225], v4 offset:26624\n"
    "v_add_f32 v51, v51, v94\n"
    "v_mfma_f32_16x16x32_bf16 v[84:87], a[208:211], v[196:199], v[84:87]\n"
    "ds_read_b64_tr_b16 a[226:227], v4 offset:32768\n"
    "v_mfma_f32_16x16x32_bf16 v[84:87], a[204:207], v[164:167], v[84:87]\n"
    "v_exp_f32 v95, v95\n"
    "v_mfma_f32_16x16x32_bf16 v[84:87], a[200:203], v[132:135], v[84:87]\n"
    "ds_read_b64_tr_b16 a[228:229], v4 offset:38912\n"
    "v_cvt_pk_bf16_f32 v221, v94, v95\n"
    "v_mfma_f32_16x16x32_bf16 v[84:87], a[196:199], v[100:103], v[84:87]\n"
    "ds_read_b64_tr_b16 a[230:231], v4 offset:45056\n"
    "v_add_f32 v51, v51, v95\n"
    "v_mfma_f32_16x16x32_bf16 v[84:87], a[192:195], v[68:71], v[84:87]\n"
    "v_exp_f32 v80, v80\n"
    "v_mfma_f32_16x16x32_bf16 v[88:91], a[192:195], v[72:75], v[120:123]\n"
    "ds_read_b64_tr_b16 a[232:233], v5 offset:26624\n"
    "v_add_f32 v48, v48, v80\n"
    "v_mfma_f32_16x16x32_bf16 v[88:91], a[196:199], v[104:107], v[88:91]\n"
    "ds_read_b64_tr_b16 a[234:235], v5 offset:32768\n"
    "v_mfma_f32_16x16x32_bf16 v[88:91], a[200:203], v[136:139], v[88:91]\n"
    "v_exp_f32 v81, v81\n"
    "v_mfma_f32_16x16x32_bf16 v[88:91], a[204:207], v[168:171], v[88:91]\n"
    "ds_read_b64_tr_b16 a[236:237], v5 offset:38912\n"
    "v_cvt_pk_bf16_f32 v210, v80, v81\n"
    "v_mfma_f32_16x16x32_bf16 v[88:91], a[208:211], v[200:203], v[88:91]\n"
    "ds_read_b64_tr_b16 a[238:239], v5 offset:45056\n"
    "v_add_f32 v48, v48, v81\n"
    "v_mfma_f32_16x16x32_bf16 v[88:91], a[212:215], v[232:235], v[88:91]\n"
    "v_exp_f32 v82, v82\n"
    "v_mfma_f32_16x16x32_bf16 v[92:95], a[212:215], v[236:239], v[124:127]\n"
    "v_add_f32 v48, v48, v82\n"
    "v_mfma_f32_16x16x32_bf16 v[92:95], a[208:211], v[204:207], v[92:95]\n"
    "v_mfma_f32_16x16x32_bf16 v[92:95], a[204:207], v[172:175], v[92:95]\n"
    "v_exp_f32 v83, v83\n"
    "v_mfma_f32_16x16x32_bf16 v[92:95], a[200:203], v[140:143], v[92:95]\n"
    "v_cvt_pk_bf16_f32 v211, v82, v83\n"
    "v_add_f32 v48, v48, v83\n"
    "v_mfma_f32_16x16x32_bf16 v[92:95], a[196:199], v[108:111], v[92:95]\n"
    "s_waitcnt lgkmcnt(0)\n"
    "v_mfma_f32_16x16x32_bf16 v[92:95], a[192:195], v[76:79], v[92:95]\n"
    "v_exp_f32 v84, v84\n"
    "v_mfma_f32_16x16x32_bf16 a[48:51], a[216:219], v[144:147], 0\n"
    "s_add_u32 m0, s70, 4096\n"
    "ds_read_b128 a[192:195], v2 offset:12288\n"
    "v_add_f32 v49, v49, v84\n"
    "v_mfma_f32_16x16x32_bf16 a[48:51], a[220:223], v[176:179], a[48:51]\n"
    "buffer_load_dwordx4 v8, s[36:39], s60 offen offset:0 lds\n"
    "v_mfma_f32_16x16x32_bf16 a[52:55], a[220:223], v[180:183], 0\n"
    "v_exp_f32 v85, v85\n"
    "v_mfma_f32_16x16x32_bf16 a[52:55], a[216:219], v[148:151], a[52:55]\n"
    "ds_read_b128 a[196:199], v3 offset:12288\n"
    "v_cvt_pk_bf16_f32 v214, v84, v85\n"
    "v_mfma_f32_16x16x32_bf16 a[56:59], a[216:219], v[152:155], 0\n"
    "v_add_f32 v49, v49, v85\n"
    "v_mfma_f32_16x16x32_bf16 a[56:59], a[220:223], v[184:187], a[56:59]\n"
    "v_exp_f32 v86, v86\n"
    "v_mfma_f32_16x16x32_bf16 a[60:63], a[220:223], v[188:191], 0\n"
    "ds_read_b128 a[200:203], v2 offset:14336\n"
    "v_add_f32 v49, v49, v86\n"
    "v_mfma_f32_16x16x32_bf16 a[60:63], a[216:219], v[156:159], a[60:63]\n"
    "buffer_load_dwordx4 v8, s[36:39], s61 offen offset:1024 lds\n"
    "v_mfma_f32_16x16x32_bf16 a[76:79], a[224:227], v[156:159], 0\n"
    "v_exp_f32 v87, v87\n"
    "v_mfma_f32_16x16x32_bf16 a[76:79], a[228:231], v[188:191], a[76:79]\n"
    "ds_read_b128 a[204:207], v3 offset:14336\n"
    "v_cvt_pk_bf16_f32 v215, v86, v87\n"
    "v_mfma_f32_16x16x32_bf16 a[72:75], a[228:231], v[184:187], 0\n"
    "v_add_f32 v49, v49, v87\n"
    "v_mfma_f32_16x16x32_bf16 a[72:75], a[224:227], v[152:155], a[72:75]\n"
    "v_exp_f32 v88, v88\n"
    "v_mfma_f32_16x16x32_bf16 a[68:71], a[224:227], v[148:151], 0\n"
    "s_add_u32 m0, s70, 24576\n"
    "ds_read_b128 a[208:211], v2 offset:16384\n"
    "v_add_f32 v50, v50, v88\n"
    "v_mfma_f32_16x16x32_bf16 a[68:71], a[228:231], v[180:183], a[68:71]\n"
    "buffer_load_dwordx4 v8, s[40:43], s56 offen offset:0 lds\n"
    "v_mfma_f32_16x16x32_bf16 a[64:67], a[228:231], v[176:179], 0\n"
    "v_exp_f32 v89, v89\n"
    "v_mfma_f32_16x16x32_bf16 a[64:67], a[224:227], v[144:147], a[64:67]\n"
    "ds_read_b128 a[212:215], v3 offset:16384\n"
    "v_cvt_pk_bf16_f32 v218, v88, v89\n"
    "v_mfma_f32_16x16x32_bf16 a[80:83], a[232:235], v[144:147], 0\n"
    "v_add_f32 v50, v50, v89\n"
    "v_mfma_f32_16x16x32_bf16 a[80:83], a[236:239], v[176:179], a[80:83]\n"
    "v_exp_f32 v90, v90\n"
    "v_mfma_f32_16x16x32_bf16 a[84:87], a[236:239], v[180:183], 0\n"
    "v_add_f32 v50, v50, v90\n"
    "v_mfma_f32_16x16x32_bf16 a[84:87], a[232:235], v[148:151], a[84:87]\n"
    "buffer_load_dwordx4 v8, s[40:43], s57 offen offset:1024 lds\n"
    "v_mfma_f32_16x16x32_bf16 a[88:91], a[232:235], v[152:155], 0\n"
    "v_exp_f32 v91, v91\n"
    "v_mfma_f32_16x16x32_bf16 a[88:91], a[236:239], v[184:187], a[88:91]\n"
    "v_cvt_pk_bf16_f32 v219, v90, v91\n"
    "v_add_f32 v50, v50, v91\n"
    "v_mfma_f32_16x16x32_bf16 a[92:95], a[236:239], v[188:191], 0\n"
    "s_waitcnt lgkmcnt(0)\n"
    "v_mfma_f32_16x16x32_bf16 a[92:95], a[232:235], v[156:159], a[92:95]\n"
    "v_exp_f32 v92, v92\n"
    "v_mfma_f32_16x16x32_bf16 v[80:83], a[192:195], v[64:67], v[112:115]\n"
    "ds_read_b64_tr_b16 a[216:217], v6 offset:26624\n"
    "v_add_f32 v51, v51, v92\n"
    "v_mfma_f32_16x16x32_bf16 v[80:83], a[196:199], v[96:99], v[80:83]\n"
    "ds_read_b64_tr_b16 a[218:219], v6 offset:32768\n"
    "v_mfma_f32_16x16x32_bf16 v[80:83], a[200:203], v[128:131], v[80:83]\n"
    "v_exp_f32 v93, v93\n"
    "v_mfma_f32_16x16x32_bf16 v[80:83], a[204:207], v[160:163], v[80:83]\n"
    "ds_read_b64_tr_b16 a[220:221], v6 offset:38912\n"
    "v_cvt_pk_bf16_f32 v222, v92, v93\n"
    "v_mfma_f32_16x16x32_bf16 v[80:83], a[208:211], v[192:195], v[80:83]\n"
    "ds_read_b64_tr_b16 a[222:223], v6 offset:45056\n"
    "v_add_f32 v51, v51, v93\n"
    "v_mfma_f32_16x16x32_bf16 v[80:83], a[212:215], v[224:227], v[80:83]\n"
    "v_exp_f32 v94, v94\n"
    "v_mfma_f32_16x16x32_bf16 v[84:87], a[212:215], v[228:231], v[116:119]\n"
    "ds_read_b64_tr_b16 a[224:225], v7 offset:26624\n"
    "v_add_f32 v51, v51, v94\n"
    "v_mfma_f32_16x16x32_bf16 v[84:87], a[208:211], v[196:199], v[84:87]\n"
    "ds_read_b64_tr_b16 a[226:227], v7 offset:32768\n"
    "v_mfma_f32_16x16x32_bf16 v[84:87], a[204:207], v[164:167], v[84:87]\n"
    "v_exp_f32 v95, v95\n"
    "v_mfma_f32_16x16x32_bf16 v[84:87], a[200:203], v[132:135], v[84:87]\n"
    "ds_read_b64_tr_b16 a[228:229], v7 offset:38912\n"
    "v_cvt_pk_bf16_f32 v223, v94, v95\n"
    "v_mfma_f32_16x16x32_bf16 v[84:87], a[196:199], v[100:103], v[84:87]\n"
    "ds_read_b64_tr_b16 a[230:231], v7 offset:45056\n"
    "v_add_f32 v51, v51, v95\n"
    "v_mfma_f32_16x16x32_bf16 v[84:87], a[192:195], v[68:71], v[84:87]\n"
    "v_exp_f32 v80, v80\n"
    "v_mfma_f32_16x16x32_bf16 v[88:91], a[192:195], v[72:75], v[120:123]\n"
    "ds_read_b64_tr_b16 a[232:233], v4 offset:28672\n"
    "v_add_f32 v48, v48, v80\n"
    "v_mfma_f32_16x16x32_bf16 v[88:91], a[196:199], v[104:107], v[88:91]\n"
    "ds_read_b64_tr_b16 a[234:235], v4 offset:34816\n"
    "v_mfma_f32_16x16x32_bf16 v[88:91], a[200:203], v[136:139], v[88:91]\n"
    "v_exp_f32 v81, v81\n"
    "v_mfma_f32_16x16x32_bf16 v[88:91], a[204:207], v[168:171], v[88:91]\n"
    "ds_read_b64_tr_b16 a[236:237], v4 offset:40960\n"
    "v_cvt_pk_bf16_f32 v240, v80, v81\n"
    "v_mfma_f32_16x16x32_bf16 v[88:91], a[208:211], v[200:203], v[88:91]\n"
    "ds_read_b64_tr_b16 a[238:239], v4 offset:47104\n"
    "v_add_f32 v48, v48, v81\n"
    "v_mfma_f32_16x16x32_bf16 v[88:91], a[212:215], v[232:235], v[88:91]\n"
    "v_exp_f32 v82, v82\n"
    "v_mfma_f32_16x16x32_bf16 v[92:95], a[212:215], v[236:239], v[124:127]\n"
    "v_add_f32 v48, v48, v82\n"
    "v_mfma_f32_16x16x32_bf16 v[92:95], a[208:211], v[204:207], v[92:95]\n"
    "v_mfma_f32_16x16x32_bf16 v[92:95], a[204:207], v[172:175], v[92:95]\n"
    "v_exp_f32 v83, v83\n"
    "v_mfma_f32_16x16x32_bf16 v[92:95], a[200:203], v[140:143], v[92:95]\n"
    "v_cvt_pk_bf16_f32 v241, v82, v83\n"
    "v_add_f32 v48, v48, v83\n"
    "v_mfma_f32_16x16x32_bf16 v[92:95], a[196:199], v[108:111], v[92:95]\n"
    "s_waitcnt lgkmcnt(0)\n"
    "v_mfma_f32_16x16x32_bf16 v[92:95], a[192:195], v[76:79], v[92:95]\n"
    "v_exp_f32 v84, v84\n"
    "v_mfma_f32_16x16x32_bf16 a[96:99], a[216:219], v[144:147], 0\n"
    "s_add_u32 m0, s70, 26624\n"
    "ds_read_b128 a[192:195], v2 offset:18432\n"
    "v_add_f32 v49, v49, v84\n"
    "v_mfma_f32_16x16x32_bf16 a[96:99], a[220:223], v[176:179], a[96:99]\n"
    "buffer_load_dwordx4 v8, s[40:43], s58 offen offset:0 lds\n"
    "v_mfma_f32_16x16x32_bf16 a[100:103], a[220:223], v[180:183], 0\n"
    "v_exp_f32 v85, v85\n"
    "v_mfma_f32_16x16x32_bf16 a[100:103], a[216:219], v[148:151], a[100:103]\n"
    "ds_read_b128 a[196:199], v3 offset:18432\n"
    "v_cvt_pk_bf16_f32 v244, v84, v85\n"
    "v_mfma_f32_16x16x32_bf16 a[104:107], a[216:219], v[152:155], 0\n"
    "v_add_f32 v49, v49, v85\n"
    "v_mfma_f32_16x16x32_bf16 a[104:107], a[220:223], v[184:187], a[104:107]\n"
    "v_exp_f32 v86, v86\n"
    "v_mfma_f32_16x16x32_bf16 a[108:111], a[220:223], v[188:191], 0\n"
    "ds_read_b128 a[200:203], v2 offset:20480\n"
    "v_add_f32 v49, v49, v86\n"
    "v_mfma_f32_16x16x32_bf16 a[108:111], a[216:219], v[156:159], a[108:111]\n"
    "buffer_load_dwordx4 v8, s[40:43], s59 offen offset:1024 lds\n"
    "v_mfma_f32_16x16x32_bf16 a[124:127], a[224:227], v[156:159], 0\n"
    "v_exp_f32 v87, v87\n"
    "v_mfma_f32_16x16x32_bf16 a[124:127], a[228:231], v[188:191], a[124:127]\n"
    "ds_read_b128 a[204:207], v3 offset:20480\n"
    "v_cvt_pk_bf16_f32 v245, v86, v87\n"
    "v_mfma_f32_16x16x32_bf16 a[120:123], a[228:231], v[184:187], 0\n"
    "v_add_f32 v49, v49, v87\n"
    "v_mfma_f32_16x16x32_bf16 a[120:123], a[224:227], v[152:155], a[120:123]\n"
    "v_exp_f32 v88, v88\n"
    "v_mfma_f32_16x16x32_bf16 a[116:119], a[224:227], v[148:151], 0\n"
    "s_add_u32 m0, s70, 28672\n"
    "ds_read_b128 a[208:211], v2 offset:22528\n"
    "v_add_f32 v50, v50, v88\n"
    "v_mfma_f32_16x16x32_bf16 a[116:119], a[228:231], v[180:183], a[116:119]\n"
    "buffer_load_dwordx4 v8, s[40:43], s60 offen offset:0 lds\n"
    "v_mfma_f32_16x16x32_bf16 a[112:115], a[228:231], v[176:179], 0\n"
    "v_exp_f32 v89, v89\n"
    "v_mfma_f32_16x16x32_bf16 a[112:115], a[224:227], v[144:147], a[112:115]\n"
    "ds_read_b128 a[212:215], v3 offset:22528\n"
    "v_cvt_pk_bf16_f32 v248, v88, v89\n"
    "v_mfma_f32_16x16x32_bf16 a[128:131], a[232:235], v[144:147], 0\n"
    "v_add_f32 v50, v50, v89\n"
    "v_mfma_f32_16x16x32_bf16 a[128:131], a[236:239], v[176:179], a[128:131]\n"
    "v_exp_f32 v90, v90\n"
    "v_mfma_f32_16x16x32_bf16 a[132:135], a[236:239], v[180:183], 0\n"
    "v_add_f32 v50, v50, v90\n"
    "v_mfma_f32_16x16x32_bf16 a[132:135], a[232:235], v[148:151], a[132:135]\n"
    "buffer_load_dwordx4 v8, s[40:43], s61 offen offset:1024 lds\n"
    "v_mfma_f32_16x16x32_bf16 a[136:139], a[232:235], v[152:155], 0\n"
    "v_exp_f32 v91, v91\n"
    "v_mfma_f32_16x16x32_bf16 a[136:139], a[236:239], v[184:187], a[136:139]\n"
    "v_cvt_pk_bf16_f32 v249, v90, v91\n"
    "v_add_f32 v50, v50, v91\n"
    "v_mfma_f32_16x16x32_bf16 a[140:143], a[236:239], v[188:191], 0\n"
    "s_waitcnt lgkmcnt(0)\n"
    "v_mfma_f32_16x16x32_bf16 a[140:143], a[232:235], v[156:159], a[140:143]\n"
    "v_exp_f32 v92, v92\n"
    "v_mfma_f32_16x16x32_bf16 v[80:83], a[192:195], v[64:67], v[112:115]\n"
    "ds_read_b64_tr_b16 a[216:217], v5 offset:28672\n"
    "v_add_f32 v51, v51, v92\n"
    "v_mfma_f32_16x16x32_bf16 v[80:83], a[196:199], v[96:99], v[80:83]\n"
    "s_add_u32 s70, s69, s68\n"
    "ds_read_b64_tr_b16 a[218:219], v5 offset:34816\n"
    "v_mfma_f32_16x16x32_bf16 v[80:83], a[200:203], v[128:131], v[80:83]\n"
    "s_add_u32 s73, s69, 49152\n"
    "v_exp_f32 v93, v93\n"
    "v_mfma_f32_16x16x32_bf16 v[80:83], a[204:207], v[160:163], v[80:83]\n"
    "s_cmp_eq_u32 s73, 147456\n"
    "ds_read_b64_tr_b16 a[220:221], v5 offset:40960\n"
    "v_cvt_pk_bf16_f32 v252, v92, v93\n"
    "v_mfma_f32_16x16x32_bf16 v[80:83], a[208:211], v[192:195], v[80:83]\n"
    "s_cselect_b32 s73, 0, s73\n"
    "ds_read_b64_tr_b16 a[222:223], v5 offset:47104\n"
    "v_add_f32 v51, v51, v93\n"
    "v_mfma_f32_16x16x32_bf16 v[80:83], a[212:215], v[224:227], v[80:83]\n"
    "s_sub_u32 s71, s73, s69\n"
    "v_exp_f32 v94, v94\n"
    "v_mfma_f32_16x16x32_bf16 v[84:87], a[212:215], v[228:231], v[116:119]\n"
    "s_mov_b32 s69, s73\n"
    "ds_read_b64_tr_b16 a[224:225], v6 offset:28672\n"
    "v_add_f32 v51, v51, v94\n"
    "v_mfma_f32_16x16x32_bf16 v[84:87], a[208:211], v[196:199], v[84:87]\n"
    "ds_read_b64_tr_b16 a[226:227], v6 offset:34816\n"
    "v_add_u32 v8, 24576, v8\n"
    "v_mfma_f32_16x16x32_bf16 v[84:87], a[204:207], v[164:167], v[84:87]\n"
    "s_mov_b32 s72, 13\n"
    "v_exp_f32 v95, v95\n"
    "v_mfma_f32_16x16x32_bf16 v[84:87], a[200:203], v[132:135], v[84:87]\n"
    "ds_read_b64_tr_b16 a[228:229], v6 offset:40960\n"
    "v_cvt_pk_bf16_f32 v253, v94, v95\n"
    "v_mfma_f32_16x16x32_bf16 v[84:87], a[196:199], v[100:103], v[84:87]\n"
    "ds_read_b64_tr_b16 a[230:231], v6 offset:47104\n"
    "v_add_f32 v51, v51, v95\n"
    "v_mfma_f32_16x16x32_bf16 v[84:87], a[192:195], v[68:71], v[84:87]\n"
    "v_exp_f32 v80, v80\n"
    "v_mfma_f32_16x16x32_bf16 v[88:91], a[192:195], v[72:75], v[120:123]\n"
    "ds_read_b64_tr_b16 a[232:233], v7 offset:28672\n"
    "v_add_f32 v48, v48, v80\n"
    "v_mfma_f32_16x16x32_bf16 v[88:91], a[196:199], v[104:107], v[88:91]\n"
    "ds_read_b64_tr_b16 a[234:235], v7 offset:34816\n"
    "v_mfma_f32_16x16x32_bf16 v[88:91], a[200:203], v[136:139], v[88:91]\n"
    "v_exp_f32 v81, v81\n"
    "v_mfma_f32_16x16x32_bf16 v[88:91], a[204:207], v[168:171], v[88:91]\n"
    "ds_read_b64_tr_b16 a[236:237], v7 offset:40960\n"
    "v_cvt_pk_bf16_f32 v242, v80, v81\n"
    "v_mfma_f32_16x16x32_bf16 v[88:91], a[208:211], v[200:203], v[88:91]\n"
    "ds_read_b64_tr_b16 a[238:239], v7 offset:47104\n"
    "v_add_f32 v48, v48, v81\n"
    "v_mfma_f32_16x16x32_bf16 v[88:91], a[212:215], v[232:235], v[88:91]\n"
    "v_exp_f32 v82, v82\n"
    "v_mfma_f32_16x16x32_bf16 v[92:95], a[212:215], v[236:239], v[124:127]\n"
    "v_add_f32 v48, v48, v82\n"
    "v_add_u32 v2, s71, v2\n"
    "v_mfma_f32_16x16x32_bf16 v[92:95], a[208:211], v[204:207], v[92:95]\n"
    "v_add_u32 v3, s71, v3\n"
    "v_mfma_f32_16x16x32_bf16 v[92:95], a[204:207], v[172:175], v[92:95]\n"
    "v_exp_f32 v83, v83\n"
    "v_mfma_f32_16x16x32_bf16 v[92:95], a[200:203], v[140:143], v[92:95]\n"
    "v_cvt_pk_bf16_f32 v243, v82, v83\n"
    "v_add_f32 v48, v48, v83\n"
    "v_mfma_f32_16x16x32_bf16 v[92:95], a[196:199], v[108:111], v[92:95]\n"
    "s_waitcnt vmcnt(12) lgkmcnt(0)\n"
    "s_barrier\n"
    "v_mfma_f32_16x16x32_bf16 v[92:95], a[192:195], v[76:79], v[92:95]\n"
    "v_exp_f32 v84, v84\n"
    "v_mfma_f32_16x16x32_bf16 a[144:147], a[216:219], v[144:147], 0\n"
    "s_lshl_b32 s74, s82, 3\n"
    "ds_read_b128 a[192:195], v2 offset:0\n"
    "v_add_f32 v49, v49, v84\n"
    "v_mfma_f32_16x16x32_bf16 a[144:147], a[220:223], v[176:179], a[144:147]\n"
    "s_add_u32 s74, s74, s78\n"
    "v_add_u32 v4, s71, v4\n"
    "v_add_u32 v5, s71, v5\n"
    "v_mfma_f32_16x16x32_bf16 a[148:151], a[220:223], v[180:183], 0\n"
    "s_lshl_b32 s95, s74, 6\n"
    "v_exp_f32 v85, v85\n"
    "v_mfma_f32_16x16x32_bf16 a[148:151], a[216:219], v[148:151], a[148:151]\n"
    "s_add_u32 s95, s95, 36864\n"
    "ds_read_b128 a[196:199], v3 offset:0\n"
    "v_cvt_pk_bf16_f32 v246, v84, v85\n"
    "v_mfma_f32_16x16x32_bf16 a[152:155], a[216:219], v[152:155], 0\n"
    "v_add_f32 v49, v49, v85\n"
    "v_add_u32 v6, s71, v6\n"
    "v_mfma_f32_16x16x32_bf16 a[152:155], a[220:223], v[184:187], a[152:155]\n"
    "v_exp_f32 v86, v86\n"
    "v_mfma_f32_16x16x32_bf16 a[156:159], a[220:223], v[188:191], 0\n"
    "ds_read_b128 a[200:203], v2 offset:2048\n"
    "v_add_f32 v49, v49, v86\n"
    "v_mfma_f32_16x16x32_bf16 a[156:159], a[216:219], v[156:159], a[156:159]\n"
    "v_add_u32 v7, s71, v7\n"
    "v_mov_b32 v62, s80\n"
    "v_mfma_f32_16x16x32_bf16 a[172:175], a[224:227], v[156:159], 0\n"
    "v_exp_f32 v87, v87\n"
    "v_mfma_f32_16x16x32_bf16 a[172:175], a[228:231], v[188:191], a[172:175]\n"
    "ds_read_b128 a[204:207], v3 offset:2048\n"
    "v_cvt_pk_bf16_f32 v247, v86, v87\n"
    "v_mfma_f32_16x16x32_bf16 a[168:171], a[228:231], v[184:187], 0\n"
    "v_add_f32 v49, v49, v87\n"
    "v_mov_b32 v63, s81\n"
    "v_mfma_f32_16x16x32_bf16 a[168:171], a[224:227], v[152:155], a[168:171]\n"
    "v_exp_f32 v88, v88\n"
    "v_mfma_f32_16x16x32_bf16 a[164:167], a[224:227], v[148:151], 0\n"
    "ds_read_b128 a[208:211], v2 offset:4096\n"
    "v_add_f32 v50, v50, v88\n"
    "v_mfma_f32_16x16x32_bf16 a[164:167], a[228:231], v[180:183], a[164:167]\n"
    "buffer_store_dwordx2 v[62:63], v10, s[52:55], s95 offen sc1\n"
    "v_mfma_f32_16x16x32_bf16 a[160:163], a[228:231], v[176:179], 0\n"
    "v_exp_f32 v89, v89\n"
    "v_mfma_f32_16x16x32_bf16 a[160:163], a[224:227], v[144:147], a[160:163]\n"
    "ds_read_b128 a[212:215], v3 offset:4096\n"
    "v_cvt_pk_bf16_f32 v250, v88, v89\n"
    "v_mfma_f32_16x16x32_bf16 a[176:179], a[232:235], v[144:147], 0\n"
    "v_add_f32 v50, v50, v89\n"
    "v_mfma_f32_16x16x32_bf16 a[176:179], a[236:239], v[176:179], a[176:179]\n"
    "v_exp_f32 v90, v90\n"
    "v_mfma_f32_16x16x32_bf16 a[180:183], a[236:239], v[180:183], 0\n"
    "v_add_f32 v50, v50, v90\n"
    "v_mfma_f32_16x16x32_bf16 a[180:183], a[232:235], v[148:151], a[180:183]\n"
    "v_mfma_f32_16x16x32_bf16 a[184:187], a[232:235], v[152:155], 0\n"
    "v_exp_f32 v91, v91\n"
    "v_mfma_f32_16x16x32_bf16 a[184:187], a[236:239], v[184:187], a[184:187]\n"
    "v_cvt_pk_bf16_f32 v251, v90, v91\n"
    "v_add_f32 v50, v50, v91\n"
    "v_mfma_f32_16x16x32_bf16 a[188:191], a[236:239], v[188:191], 0\n"
    "s_waitcnt lgkmcnt(0)\n"
    "v_mfma_f32_16x16x32_bf16 a[188:191], a[232:235], v[156:159], a[188:191]\n"
    "v_exp_f32 v92, v92\n"
    ".p2align 6\n"
    "L_loop:\n"
    "v_mfma_f32_16x16x32_bf16 v[80:83], a[192:195], v[64:67], v[112:115]\n"
    "ds_read_b64_tr_b16 a[216:217], v4 offset:24576\n"
    "v_add_f32 v51, v51, v92\n"
    "v_mfma_f32_16x16x32_bf16 v[80:83], a[196:199], v[96:99], v[80:83]\n"
    "ds_read_b64_tr_b16 a[218:219], v4 offset:30720\n"
    "v_mfma_f32_16x16x32_bf16 v[80:83], a[200:203], v[128:131], v[80:83]\n"
    "v_exp_f32 v93, v93\n"
    "v_mfma_f32_16x16x32_bf16 v[80:83], a[204:207], v[160:163], v[80:83]\n"
    "ds_read_b64_tr_b16 a[220:221], v4 offset:36864\n"
    "v_cvt_pk_bf16_f32 v254, v92, v93\n"
    "v_mfma_f32_16x16x32_bf16 v[80:83], a[208:211], v[192:195], v[80:83]\n"
    "ds_read_b64_tr_b16 a[222:223], v4 offset:43008\n"
    "v_add_f32 v51, v51, v93\n"
    "v_mfma_f32_16x16x32_bf16 v[80:83], a[212:215], v[224:227], v[80:83]\n"
    "v_exp_f32 v94, v94\n"
    "v_mfma_f32_16x16x32_bf16 v[84:87], a[212:215], v[228:231], v[116:119]\n"
    "ds_read_b64_tr_b16 a[224:225], v5 offset:24576\n"
    "v_add_f32 v51, v51, v94\n"
    "v_mfma_f32_16x16x32_bf16 v[84:87], a[208:211], v[196:199], v[84:87]\n"
    "ds_read_b64_tr_b16 a[226:227], v5 offset:30720\n"
    "v_mfma_f32_16x16x32_bf16 v[84:87], a[204:207], v[164:167], v[84:87]\n"
    "v_exp_f32 v95, v95\n"
    "v_mfma_f32_16x16x32_bf16 v[84:87], a[200:203], v[132:135], v[84:87]\n"
    "ds_read_b64_tr_b16 a[228:229], v5 offset:36864\n"
    "v_cvt_pk_bf16_f32 v255, v94, v95\n"
    "v_mfma_f32_16x16x32_bf16 v[84:87], a[196:199], v[100:103], v[84:87]\n"
    "ds_read_b64_tr_b16 a[230:231], v5 offset:43008\n"
    "v_add_f32 v51, v51, v95\n"
    "v_mfma_f32_16x16x32_bf16 v[84:87], a[192:195], v[68:71], v[84:87]\n"
    "v_exp_f32 v80, v80\n"
    "v_mfma_f32_16x16x32_bf16 v[88:91], a[192:195], v[72:75], v[120:123]\n"
    "ds_read_b64_tr_b16 a[232:233], v6 offset:24576\n"
    "v_add_f32 v48, v48, v80\n"
    "v_mfma_f32_16x16x32_bf16 v[88:91], a[196:199], v[104:107], v[88:91]\n"
    "ds_read_b64_tr_b16 a[234:235], v6 offset:30720\n"
    "v_mfma_f32_16x16x32_bf16 v[88:91], a[200:203], v[136:139], v[88:91]\n"
    "v_exp_f32 v81, v81\n"
    "v_mfma_f32_16x16x32_bf16 v[88:91], a[204:207], v[168:171], v[88:91]\n"
    "ds_read_b64_tr_b16 a[236:237], v6 offset:36864\n"
    "v_cvt_pk_bf16_f32 v144, v80, v81\n"
    "v_mfma_f32_16x16x32_bf16 v[88:91], a[208:211], v[200:203], v[88:91]\n"
    "ds_read_b64_tr_b16 a[238:239], v6 offset:43008\n"
    "v_add_f32 v48, v48, v81\n"
    "v_mfma_f32_16x16x32_bf16 v[88:91], a[212:215], v[232:235], v[88:91]\n"
    "v_exp_f32 v82, v82\n"
    "v_mfma_f32_16x16x32_bf16 v[92:95], a[212:215], v[236:239], v[124:127]\n"
    "v_add_f32 v48, v48, v82\n"
    "v_mfma_f32_16x16x32_bf16 v[92:95], a[208:211], v[204:207], v[92:95]\n"
    "v_mfma_f32_16x16x32_bf16 v[92:95], a[204:207], v[172:175], v[92:95]\n"
    "v_exp_f32 v83, v83\n"
    "v_mfma_f32_16x16x32_bf16 v[92:95], a[200:203], v[140:143], v[92:95]\n"
    "v_cvt_pk_bf16_f32 v145, v82, v83\n"
    "v_add_f32 v48, v48, v83\n"
    "v_mfma_f32_16x16x32_bf16 v[92:95], a[196:199], v[108:111], v[92:95]\n"
    "s_waitcnt lgkmcnt(0)\n"
    "v_mfma_f32_16x16x32_bf16 v[92:95], a[192:195], v[76:79], v[92:95]\n"
    "v_exp_f32 v84, v84\n"
    "v_mfma_f32_16x16x32_bf16 a[0:3], a[216:219], v[208:211], a[0:3]\n"
    "s_add_u32 m0, s70, 0\n"
    "ds_read_b128 a[192:195], v2 offset:6144\n"
    "v_add_f32 v49, v49, v84\n"
    "v_mfma_f32_16x16x32_bf16 a[0:3], a[220:223], v[240:243], a[0:3]\n"
    "buffer_load_dwordx4 v8, s[36:39], s56 offen offset:0 lds\n"
    "v_mfma_f32_16x16x32_bf16 a[4:7], a[220:223], v[244:247], a[4:7]\n"
    "v_exp_f32 v85, v85\n"
    "v_mfma_f32_16x16x32_bf16 a[4:7], a[216:219], v[212:215], a[4:7]\n"
    "ds_read_b128 a[196:199], v3 offset:6144\n"
    "v_cvt_pk_bf16_f32 v148, v84, v85\n"
    "v_mfma_f32_16x16x32_bf16 a[8:11], a[216:219], v[216:219], a[8:11]\n"
    "v_add_f32 v49, v49, v85\n"
    "v_mfma_f32_16x16x32_bf16 a[8:11], a[220:223], v[248:251], a[8:11]\n"
    "v_exp_f32 v86, v86\n"
    "v_mfma_f32_16x16x32_bf16 a[12:15], a[220:223], v[252:255], a[12:15]\n"
    "ds_read_b128 a[200:203], v2 offset:8192\n"
    "v_add_f32 v49, v49, v86\n"
    "v_mfma_f32_16x16x32_bf16 a[12:15], a[216:219], v[220:223], a[12:15]\n"
    "buffer_load_dwordx4 v8, s[36:39], s57 offen offset:1024 lds\n"
    "v_mfma_f32_16x16x32_bf16 a[28:31], a[224:227], v[220:223], a[28:31]\n"
    "v_exp_f32 v87, v87\n"
    "v_mfma_f32_16x16x32_bf16 a[28:31], a[228:231], v[252:255], a[28:31]\n"
    "ds_read_b128 a[204:207], v3 offset:8192\n"
    "v_cvt_pk_bf16_f32 v149, v86, v87\n"
    "v_mfma_f32_16x16x32_bf16 a[24:27], a[228:231], v[248:251], a[24:27]\n"
    "v_add_f32 v49, v49, v87\n"
    "v_mfma_f32_16x16x32_bf16 a[24:27], a[224:227], v[216:219], a[24:27]\n"
    "v_exp_f32 v88, v88\n"
    "v_mfma_f32_16x16x32_bf16 a[20:23], a[224:227], v[212:215], a[20:23]\n"
    "s_add_u32 m0, s70, 2048\n"
    "ds_read_b128 a[208:211], v2 offset:10240\n"
    "v_add_f32 v50, v50, v88\n"
    "v_mfma_f32_16x16x32_bf16 a[20:23], a[228:231], v[244:247], a[20:23]\n"
    "buffer_load_dwordx4 v8, s[36:39], s58 offen offset:0 lds\n"
    "v_mfma_f32_16x16x32_bf16 a[16:19], a[228:231], v[240:243], a[16:19]\n"
    "v_exp_f32 v89, v89\n"
    "v_mfma_f32_16x16x32_bf16 a[16:19], a[224:227], v[208:211], a[16:19]\n"
    "ds_read_b128 a[212:215], v3 offset:10240\n"
    "v_cvt_pk_bf16_f32 v152, v88, v89\n"
    "v_mfma_f32_16x16x32_bf16 a[32:35], a[232:235], v[208:211], a[32:35]\n"
    "v_add_f32 v50, v50, v89\n"
    "v_mfma_f32_16x16x32_bf16 a[32:35], a[236:239], v[240:243], a[32:35]\n"
    "v_exp_f32 v90, v90\n"
    "v_mfma_f32_16x16x32_bf16 a[36:39], a[236:239], v[244:247], a[36:39]\n"
    "v_add_f32 v50, v50, v90\n"
    "v_mfma_f32_16x16x32_bf16 a[36:39], a[232:235], v[212:215], a[36:39]\n"
    "buffer_load_dwordx4 v8, s[36:39], s59 offen offset:1024 lds\n"
    "v_mfma_f32_16x16x32_bf16 a[40:43], a[232:235], v[216:219], a[40:43]\n"
    "v_exp_f32 v91, v91\n"
    "v_mfma_f32_16x16x32_bf16 a[40:43], a[236:239], v[248:251], a[40:43]\n"
    "v_cvt_pk_bf16_f32 v153, v90, v91\n"
    "v_add_f32 v50, v50, v91\n"
    "v_mfma_f32_16x16x32_bf16 a[44:47], a[236:239], v[252:255], a[44:47]\n"
    "s_waitcnt lgkmcnt(0)\n"
    "v_mfma_f32_16x16x32_bf16 a[44:47], a[232:235], v[220:223], a[44:47]\n"
    "v_exp_f32 v92, v92\n"
    "v_mfma_f32_16x16x32_bf16 v[80:83], a[192:195], v[64:67], v[112:115]\n"
    "ds_read_b64_tr_b16 a[216:217], v7 offset:24576\n"
    "v_add_f32 v51, v51, v92\n"
    "v_mfma_f32_16x16x32_bf16 v[80:83], a[196:199], v[96:99], v[80:83]\n"
    "ds_read_b64_tr_b16 a[218:219], v7 offset:30720\n"
    "v_mfma_f32_16x16x32_bf16 v[80:83], a[200:203], v[128:131], v[80:83]\n"
    "v_exp_f32 v93, v93\n"
    "v_mfma_f32_16x16x32_bf16 v[80:83], a[204:207], v[160:163], v[80:83]\n"
    "ds_read_b64_tr_b16 a[220:221], v7 offset:36864\n"
    "v_cvt_pk_bf16_f32 v156, v92, v93\n"
    "v_mfma_f32_16x16x32_bf16 v[80:83], a[208:211], v[192:195], v[80:83]\n"
    "ds_read_b64_tr_b16 a[222:223], v7 offset:43008\n"
    "v_add_f32 v51, v51, v93\n"
    "v_mfma_f32_16x16x32_bf16 v[80:83], a[212:215], v[224:227], v[80:83]\n"
    "v_exp_f32 v94, v94\n"
    "v_mfma_f32_16x16x32_bf16 v[84:87], a[212:215], v[228:231], v[116:119]\n"
    "ds_read_b64_tr_b16 a[224:225], v4 offset:26624\n"
    "v_add_f32 v51, v51, v94\n"
    "v_mfma_f32_16x16x32_bf16 v[84:87], a[208:211], v[196:199], v[84:87]\n"
    "ds_read_b64_tr_b16 a[226:227], v4 offset:32768\n"
    "v_mfma_f32_16x16x32_bf16 v[84:87], a[204:207], v[164:167], v[84:87]\n"
    "v_exp_f32 v95, v95\n"
    "v_mfma_f32_16x16x32_bf16 v[84:87], a[200:203], v[132:135], v[84:87]\n"
    "ds_read_b64_tr_b16 a[228:229], v4 offset:38912\n"
    "v_cvt_pk_bf16_f32 v157, v94, v95\n"
    "v_mfma_f32_16x16x32_bf16 v[84:87], a[196:199], v[100:103], v[84:87]\n"
    "ds_read_b64_tr_b16 a[230:231], v4 offset:45056\n"
    "v_add_f32 v51, v51, v95\n"
    "v_mfma_f32_16x16x32_bf16 v[84:87], a[192:195], v[68:71], v[84:87]\n"
    "v_exp_f32 v80, v80\n"
    "v_mfma_f32_16x16x32_bf16 v[88:91], a[192:195], v[72:75], v[120:123]\n"
    "ds_read_b64_tr_b16 a[232:233], v5 offset:26624\n"
    "v_add_f32 v48, v48, v80\n"
    "v_mfma_f32_16x16x32_bf16 v[88:91], a[196:199], v[104:107], v[88:91]\n"
    "ds_read_b64_tr_b16 a[234:235], v5 offset:32768\n"
    "v_mfma_f32_16x16x32_bf16 v[88:91], a[200:203], v[136:139], v[88:91]\n"
    "v_exp_f32 v81, v81\n"
    "v_mfma_f32_16x16x32_bf16 v[88:91], a[204:207], v[168:171], v[88:91]\n"
    "ds_read_b64_tr_b16 a[236:237], v5 offset:38912\n"
    "v_cvt_pk_bf16_f32 v146, v80, v81\n"
    "v_mfma_f32_16x16x32_bf16 v[88:91], a[208:211], v[200:203], v[88:91]\n"
    "ds_read_b64_tr_b16 a[238:239], v5 offset:45056\n"
    "v_add_f32 v48, v48, v81\n"
    "v_mfma_f32_16x16x32_bf16 v[88:91], a[212:215], v[232:235], v[88:91]\n"
    "v_exp_f32 v82, v82\n"
    "v_mfma_f32_16x16x32_bf16 v[92:95], a[212:215], v[236:239], v[124:127]\n"
    "v_add_f32 v48, v48, v82\n"
    "v_mfma_f32_16x16x32_bf16 v[92:95], a[208:211], v[204:207], v[92:95]\n"
    "v_mfma_f32_16x16x32_bf16 v[92:95], a[204:207], v[172:175], v[92:95]\n"
    "v_exp_f32 v83, v83\n"
    "v_mfma_f32_16x16x32_bf16 v[92:95], a[200:203], v[140:143], v[92:95]\n"
    "v_cvt_pk_bf16_f32 v147, v82, v83\n"
    "v_add_f32 v48, v48, v83\n"
    "v_mfma_f32_16x16x32_bf16 v[92:95], a[196:199], v[108:111], v[92:95]\n"
    "s_waitcnt lgkmcnt(0)\n"
    "v_mfma_f32_16x16x32_bf16 v[92:95], a[192:195], v[76:79], v[92:95]\n"
    "v_exp_f32 v84, v84\n"
    "v_mfma_f32_16x16x32_bf16 a[48:51], a[216:219], v[208:211], a[48:51]\n"
    "s_add_u32 m0, s70, 4096\n"
    "ds_read_b128 a[192:195], v2 offset:12288\n"
    "v_add_f32 v49, v49, v84\n"
    "v_mfma_f32_16x16x32_bf16 a[48:51], a[220:223], v[240:243], a[48:51]\n"
    "buffer_load_dwordx4 v8, s[36:39], s60 offen offset:0 lds\n"
    "v_mfma_f32_16x16x32_bf16 a[52:55], a[220:223], v[244:247], a[52:55]\n"
    "v_exp_f32 v85, v85\n"
    "v_mfma_f32_16x16x32_bf16 a[52:55], a[216:219], v[212:215], a[52:55]\n"
    "ds_read_b128 a[196:199], v3 offset:12288\n"
    "v_cvt_pk_bf16_f32 v150, v84, v85\n"
    "v_mfma_f32_16x16x32_bf16 a[56:59], a[216:219], v[216:219], a[56:59]\n"
    "v_add_f32 v49, v49, v85\n"
    "v_mfma_f32_16x16x32_bf16 a[56:59], a[220:223], v[248:251], a[56:59]\n"
    "v_exp_f32 v86, v86\n"
    "v_mfma_f32_16x16x32_bf16 a[60:63], a[220:223], v[252:255], a[60:63]\n"
    "ds_read_b128 a[200:203], v2 offset:14336\n"
    "v_add_f32 v49, v49, v86\n"
    "v_mfma_f32_16x16x32_bf16 a[60:63], a[216:219], v[220:223], a[60:63]\n"
    "buffer_load_dwordx4 v8, s[36:39], s61 offen offset:1024 lds\n"
    "v_mfma_f32_16x16x32_bf16 a[76:79], a[224:227], v[220:223], a[76:79]\n"
    "v_exp_f32 v87, v87\n"
    "v_mfma_f32_16x16x32_bf16 a[76:79], a[228:231], v[252:255], a[76:79]\n"
    "ds_read_b128 a[204:207], v3 offset:14336\n"
    "v_cvt_pk_bf16_f32 v151, v86, v87\n"
    "v_mfma_f32_16x16x32_bf16 a[72:75], a[228:231], v[248:251], a[72:75]\n"
    "v_add_f32 v49, v49, v87\n"
    "v_mfma_f32_16x16x32_bf16 a[72:75], a[224:227], v[216:219], a[72:75]\n"
    "v_exp_f32 v88, v88\n"
    "v_mfma_f32_16x16x32_bf16 a[68:71], a[224:227], v[212:215], a[68:71]\n"
    "s_add_u32 m0, s70, 24576\n"
    "ds_read_b128 a[208:211], v2 offset:16384\n"
    "v_add_f32 v50, v50, v88\n"
    "v_mfma_f32_16x16x32_bf16 a[68:71], a[228:231], v[244:247], a[68:71]\n"
    "buffer_load_dwordx4 v8, s[40:43], s56 offen offset:0 lds\n"
    "v_mfma_f32_16x16x32_bf16 a[64:67], a[228:231], v[240:243], a[64:67]\n"
    "v_exp_f32 v89, v89\n"
    "v_mfma_f32_16x16x32_bf16 a[64:67], a[224:227], v[208:211], a[64:67]\n"
    "ds_read_b128 a[212:215], v3 offset:16384\n"
    "v_cvt_pk_bf16_f32 v154, v88, v89\n"
    "v_mfma_f32_16x16x32_bf16 a[80:83], a[232:235], v[208:211], a[80:83]\n"
    "v_add_f32 v50, v50, v89\n"
    "v_mfma_f32_16x16x32_bf16 a[80:83], a[236:239], v[240:243], a[80:83]\n"
    "v_exp_f32 v90, v90\n"
    "v_mfma_f32_16x16x32_bf16 a[84:87], a[236:239], v[244:247], a[84:87]\n"
    "v_add_f32 v50, v50, v90\n"
    "v_mfma_f32_16x16x32_bf16 a[84:87], a[232:235], v[212:215], a[84:87]\n"
    "buffer_load_dwordx4 v8, s[40:43], s57 offen offset:1024 lds\n"
    "v_mfma_f32_16x16x32_bf16 a[88:91], a[232:235], v[216:219], a[88:91]\n"
    "v_exp_f32 v91, v91\n"
    "v_mfma_f32_16x16x32_bf16 a[88:91], a[236:239], v[248:251], a[88:91]\n"
    "v_cvt_pk_bf16_f32 v155, v90, v91\n"
    "v_add_f32 v50, v50, v91\n"
    "v_mfma_f32_16x16x32_bf16 a[92:95], a[236:239], v[252:255], a[92:95]\n"
    "s_waitcnt lgkmcnt(0)\n"
    "v_mfma_f32_16x16x32_bf16 a[92:95], a[232:235], v[220:223], a[92:95]\n"
    "v_exp_f32 v92, v92\n"
    "v_mfma_f32_16x16x32_bf16 v[80:83], a[192:195], v[64:67], v[112:115]\n"
    "ds_read_b64_tr_b16 a[216:217], v6 offset:26624\n"
    "v_add_f32 v51, v51, v92\n"
    "v_mfma_f32_16x16x32_bf16 v[80:83], a[196:199], v[96:99], v[80:83]\n"
    "ds_read_b64_tr_b16 a[218:219], v6 offset:32768\n"
    "v_mfma_f32_16x16x32_bf16 v[80:83], a[200:203], v[128:131], v[80:83]\n"
    "v_exp_f32 v93, v93\n"
    "v_mfma_f32_16x16x32_bf16 v[80:83], a[204:207], v[160:163], v[80:83]\n"
    "ds_read_b64_tr_b16 a[220:221], v6 offset:38912\n"
    "v_cvt_pk_bf16_f32 v158, v92, v93\n"
    "v_mfma_f32_16x16x32_bf16 v[80:83], a[208:211], v[192:195], v[80:83]\n"
    "ds_read_b64_tr_b16 a[222:223], v6 offset:45056\n"
    "v_add_f32 v51, v51, v93\n"
    "v_mfma_f32_16x16x32_bf16 v[80:83], a[212:215], v[224:227], v[80:83]\n"
    "v_exp_f32 v94, v94\n"
    "v_mfma_f32_16x16x32_bf16 v[84:87], a[212:215], v[228:231], v[116:119]\n"
    "ds_read_b64_tr_b16 a[224:225], v7 offset:26624\n"
    "v_add_f32 v51, v51, v94\n"
    "v_mfma_f32_16x16x32_bf16 v[84:87], a[208:211], v[196:199], v[84:87]\n"
    "ds_read_b64_tr_b16 a[226:227], v7 offset:32768\n"
    "v_mfma_f32_16x16x32_bf16 v[84:87], a[204:207], v[164:167], v[84:87]\n"
    "v_exp_f32 v95, v95\n"
    "v_mfma_f32_16x16x32_bf16 v[84:87], a[200:203], v[132:135], v[84:87]\n"
    "ds_read_b64_tr_b16 a[228:229], v7 offset:38912\n"
    "v_cvt_pk_bf16_f32 v159, v94, v95\n"
    "v_mfma_f32_16x16x32_bf16 v[84:87], a[196:199], v[100:103], v[84:87]\n"
    "ds_read_b64_tr_b16 a[230:231], v7 offset:45056\n"
    "v_add_f32 v51, v51, v95\n"
    "v_mfma_f32_16x16x32_bf16 v[84:87], a[192:195], v[68:71], v[84:87]\n"
    "v_exp_f32 v80, v80\n"
    "v_mfma_f32_16x16x32_bf16 v[88:91], a[192:195], v[72:75], v[120:123]\n"
    "ds_read_b64_tr_b16 a[232:233], v4 offset:28672\n"
    "v_add_f32 v48, v48, v80\n"
    "v_mfma_f32_16x16x32_bf16 v[88:91], a[196:199], v[104:107], v[88:91]\n"
    "ds_read_b64_tr_b16 a[234:235], v4 offset:34816\n"
    "v_mfma_f32_16x16x32_bf16 v[88:91], a[200:203], v[136:139], v[88:91]\n"
    "v_exp_f32 v81, v81\n"
    "v_mfma_f32_16x16x32_bf16 v[88:91], a[204:207], v[168:171], v[88:91]\n"
    "ds_read_b64_tr_b16 a[236:237], v4 offset:40960\n"
    "v_cvt_pk_bf16_f32 v176, v80, v81\n"
    "v_mfma_f32_16x16x32_bf16 v[88:91], a[208:211], v[200:203], v[88:91]\n"
    "ds_read_b64_tr_b16 a[238:239], v4 offset:47104\n"
    "v_add_f32 v48, v48, v81\n"
    "v_mfma_f32_16x16x32_bf16 v[88:91], a[212:215], v[232:235], v[88:91]\n"
    "v_exp_f32 v82, v82\n"
    "v_mfma_f32_16x16x32_bf16 v[92:95], a[212:215], v[236:239], v[124:127]\n"
    "v_add_f32 v48, v48, v82\n"
    "v_mfma_f32_16x16x32_bf16 v[92:95], a[208:211], v[204:207], v[92:95]\n"
    "v_mfma_f32_16x16x32_bf16 v[92:95], a[204:207], v[172:175], v[92:95]\n"
    "v_exp_f32 v83, v83\n"
    "v_mfma_f32_16x16x32_bf16 v[92:95], a[200:203], v[140:143], v[92:95]\n"
    "v_cvt_pk_bf16_f32 v177, v82, v83\n"
    "v_add_f32 v48, v48, v83\n"
    "v_mfma_f32_16x16x32_bf16 v[92:95], a[196:199], v[108:111], v[92:95]\n"
    "s_waitcnt lgkmcnt(0)\n"
    "v_mfma_f32_16x16x32_bf16 v[92:95], a[192:195], v[76:79], v[92:95]\n"
    "v_exp_f32 v84, v84\n"
    "v_mfma_f32_16x16x32_bf16 a[96:99], a[216:219], v[208:211], a[96:99]\n"
    "s_add_u32 m0, s70, 26624\n"
    "ds_read_b128 a[192:195], v2 offset:18432\n"
    "v_add_f32 v49, v49, v84\n"
    "v_mfma_f32_16x16x32_bf16 a[96:99], a[220:223], v[240:243], a[96:99]\n"
    "buffer_load_dwordx4 v8, s[40:43], s58 offen offset:0 lds\n"
    "v_mfma_f32_16x16x32_bf16 a[100:103], a[220:223], v[244:247], a[100:103]\n"
    "v_exp_f32 v85, v85\n"
    "v_mfma_f32_16x16x32_bf16 a[100:103], a[216:219], v[212:215], a[100:103]\n"
    "ds_read_b128 a[196:199], v3 offset:18432\n"
    "v_cvt_pk_bf16_f32 v180, v84, v85\n"
    "v_mfma_f32_16x16x32_bf16 a[104:107], a[216:219], v[216:219], a[104:107]\n"
    "v_add_f32 v49, v49, v85\n"
    "v_mfma_f32_16x16x32_bf16 a[104:107], a[220:223], v[248:251], a[104:107]\n"
    "v_exp_f32 v86, v86\n"
    "v_mfma_f32_16x16x32_bf16 a[108:111], a[220:223], v[252:255], a[108:111]\n"
    "ds_read_b128 a[200:203], v2 offset:20480\n"
    "v_add_f32 v49, v49, v86\n"
    "v_mfma_f32_16x16x32_bf16 a[108:111], a[216:219], v[220:223], a[108:111]\n"
    "buffer_load_dwordx4 v8, s[40:43], s59 offen offset:1024 lds\n"
    "v_mfma_f32_16x16x32_bf16 a[124:127], a[224:227], v[220:223], a[124:127]\n"
    "v_exp_f32 v87, v87\n"
    "v_mfma_f32_16x16x32_bf16 a[124:127], a[228:231], v[252:255], a[124:127]\n"
    "ds_read_b128 a[204:207], v3 offset:20480\n"
    "v_cvt_pk_bf16_f32 v181, v86, v87\n"
    "v_mfma_f32_16x16x32_bf16 a[120:123], a[228:231], v[248:251], a[120:123]\n"
    "v_add_f32 v49, v49, v87\n"
    "v_mfma_f32_16x16x32_bf16 a[120:123], a[224:227], v[216:219], a[120:123]\n"
    "v_exp_f32 v88, v88\n"
    "v_mfma_f32_16x16x32_bf16 a[116:119], a[224:227], v[212:215], a[116:119]\n"
    "s_add_u32 m0, s70, 28672\n"
    "ds_read_b128 a[208:211], v2 offset:22528\n"
    "v_add_f32 v50, v50, v88\n"
    "v_mfma_f32_16x16x32_bf16 a[116:119], a[228:231], v[244:247], a[116:119]\n"
    "buffer_load_dwordx4 v8, s[40:43], s60 offen offset:0 lds\n"
    "v_mfma_f32_16x16x32_bf16 a[112:115], a[228:231], v[240:243], a[112:115]\n"
    "v_exp_f32 v89, v89\n"
    "v_mfma_f32_16x16x32_bf16 a[112:115], a[224:227], v[208:211], a[112:115]\n"
    "ds_read_b128 a[212:215], v3 offset:22528\n"
    "v_cvt_pk_bf16_f32 v184, v88, v89\n"
    "v_mfma_f32_16x16x32_bf16 a[128:131], a[232:235], v[208:211], a[128:131]\n"
    "v_add_f32 v50, v50, v89\n"
    "v_mfma_f32_16x16x32_bf16 a[128:131], a[236:239], v[240:243], a[128:131]\n"
    "v_exp_f32 v90, v90\n"
    "v_mfma_f32_16x16x32_bf16 a[132:135], a[236:239], v[244:247], a[132:135]\n"
    "v_add_f32 v50, v50, v90\n"
    "v_mfma_f32_16x16x32_bf16 a[132:135], a[232:235], v[212:215], a[132:135]\n"
    "buffer_load_dwordx4 v8, s[40:43], s61 offen offset:1024 lds\n"
    "v_mfma_f32_16x16x32_bf16 a[136:139], a[232:235], v[216:219], a[136:139]\n"
    "v_exp_f32 v91, v91\n"
    "v_mfma_f32_16x16x32_bf16 a[136:139], a[236:239], v[248:251], a[136:139]\n"
    "v_cvt_pk_bf16_f32 v185, v90, v91\n"
    "v_add_f32 v50, v50, v91\n"
    "v_mfma_f32_16x16x32_bf16 a[140:143], a[236:239], v[252:255], a[140:143]\n"
    "s_waitcnt lgkmcnt(0)\n"
    "v_mfma_f32_16x16x32_bf16 a[140:143], a[232:235], v[220:223], a[140:143]\n"
    "v_exp_f32 v92, v92\n"
    "v_mfma_f32_16x16x32_bf16 v[80:83], a[192:195], v[64:67], v[112:115]\n"
    "ds_read_b64_tr_b16 a[216:217], v5 offset:28672\n"
    "v_add_f32 v51, v51, v92\n"
    "v_mfma_f32_16x16x32_bf16 v[80:83], a[196:199], v[96:99], v[80:83]\n"
    "s_add_u32 s70, s69, s68\n"
    "ds_read_b64_tr_b16 a[218:219], v5 offset:34816\n"
    "v_mfma_f32_16x16x32_bf16 v[80:83], a[200:203], v[128:131], v[80:83]\n"
    "s_add_u32 s73, s69, 49152\n"
    "v_exp_f32 v93, v93\n"
    "v_mfma_f32_16x16x32_bf16 v[80:83], a[204:207], v[160:163], v[80:83]\n"
    "s_cmp_eq_u32 s73, 147456\n"
    "ds_read_b64_tr_b16 a[220:221], v5 offset:40960\n"
    "v_cvt_pk_bf16_f32 v188, v92, v93\n"
    "v_mfma_f32_16x16x32_bf16 v[80:83], a[208:211], v[192:195], v[80:83]\n"
    "s_cselect_b32 s73, 0, s73\n"
    "ds_read_b64_tr_b16 a[222:223], v5 offset:47104\n"
    "v_add_f32 v51, v51, v93\n"
    "v_mfma_f32_16x16x32_bf16 v[80:83], a[212:215], v[224:227], v[80:83]\n"
    "s_sub_u32 s71, s73, s69\n"
    "v_exp_f32 v94, v94\n"
    "v_mfma_f32_16x16x32_bf16 v[84:87], a[212:215], v[228:231], v[116:119]\n"
    "s_mov_b32 s69, s73\n"
    "ds_read_b64_tr_b16 a[224:225], v6 offset:28672\n"
    "v_add_f32 v51, v51, v94\n"
    "v_mfma_f32_16x16x32_bf16 v[84:87], a[208:211], v[196:199], v[84:87]\n"
    "ds_read_b64_tr_b16 a[226:227], v6 offset:34816\n"
    "v_add_u32 v8, 24576, v8\n"
    "v_mfma_f32_16x16x32_bf16 v[84:87], a[204:207], v[164:167], v[84:87]\n"
    "v_exp_f32 v95, v95\n"
    "v_mfma_f32_16x16x32_bf16 v[84:87], a[200:203], v[132:135], v[84:87]\n"
    "ds_read_b64_tr_b16 a[228:229], v6 offset:40960\n"
    "v_cvt_pk_bf16_f32 v189, v94, v95\n"
    "v_mfma_f32_16x16x32_bf16 v[84:87], a[196:199], v[100:103], v[84:87]\n"
    "ds_read_b64_tr_b16 a[230:231], v6 offset:47104\n"
    "v_add_f32 v51, v51, v95\n"
    "v_mfma_f32_16x16x32_bf16 v[84:87], a[192:195], v[68:71], v[84:87]\n"
    "v_exp_f32 v80, v80\n"
    "v_mfma_f32_16x16x32_bf16 v[88:91], a[192:195], v[72:75], v[120:123]\n"
    "ds_read_b64_tr_b16 a[232:233], v7 offset:28672\n"
    "v_add_f32 v48, v48, v80\n"
    "v_mfma_f32_16x16x32_bf16 v[88:91], a[196:199], v[104:107], v[88:91]\n"
    "ds_read_b64_tr_b16 a[234:235], v7 offset:34816\n"
    "v_mfma_f32_16x16x32_bf16 v[88:91], a[200:203], v[136:139], v[88:91]\n"
    "v_exp_f32 v81, v81\n"
    "v_mfma_f32_16x16x32_bf16 v[88:91], a[204:207], v[168:171], v[88:91]\n"
    "ds_read_b64_tr_b16 a[236:237], v7 offset:40960\n"
    "v_cvt_pk_bf16_f32 v178, v80, v81\n"
    "v_mfma_f32_16x16x32_bf16 v[88:91], a[208:211], v[200:203], v[88:91]\n"
    "ds_read_b64_tr_b16 a[238:239], v7 offset:47104\n"
    "v_add_f32 v48, v48, v81\n"
    "v_mfma_f32_16x16x32_bf16 v[88:91], a[212:215], v[232:235], v[88:91]\n"
    "v_exp_f32 v82, v82\n"
    "v_mfma_f32_16x16x32_bf16 v[92:95], a[212:215], v[236:239], v[124:127]\n"
    "v_add_f32 v48, v48, v82\n"
    "v_add_u32 v2, s71, v2\n"
    "v_mfma_f32_16x16x32_bf16 v[92:95], a[208:211], v[204:207], v[92:95]\n"
    "buffer_load_dword v55, v59, s[84:87], 0 offen\n"
    "v_add_u32 v3, s71, v3\n"
    "v_mfma_f32_16x16x32_bf16 v[92:95], a[204:207], v[172:175], v[92:95]\n"
    "v_exp_f32 v83, v83\n"
    "v_mfma_f32_16x16x32_bf16 v[92:95], a[200:203], v[140:143], v[92:95]\n"
    "v_cvt_pk_bf16_f32 v179, v82, v83\n"
    "v_add_f32 v48, v48, v83\n"
    "v_mfma_f32_16x16x32_bf16 v[92:95], a[196:199], v[108:111], v[92:95]\n"
    "s_waitcnt vmcnt(13) lgkmcnt(0)\n"
    "s_barrier\n"
    "v_mfma_f32_16x16x32_bf16 v[92:95], a[192:195], v[76:79], v[92:95]\n"
    "v_exp_f32 v84, v84\n"
    "v_mfma_f32_16x16x32_bf16 a[144:147], a[216:219], v[208:211], a[144:147]\n"
    "ds_read_b128 a[192:195], v2 offset:0\n"
    "v_add_f32 v49, v49, v84\n"
    "v_mfma_f32_16x16x32_bf16 a[144:147], a[220:223], v[240:243], a[144:147]\n"
    "v_add_u32 v59, 1536, v59\n"
    "v_add_u32 v4, s71, v4\n"
    "v_mfma_f32_16x16x32_bf16 a[148:151], a[220:223], v[244:247], a[148:151]\n"
    "v_exp_f32 v85, v85\n"
    "v_mfma_f32_16x16x32_bf16 a[148:151], a[216:219], v[212:215], a[148:151]\n"
    "ds_read_b128 a[196:199], v3 offset:0\n"
    "v_cvt_pk_bf16_f32 v182, v84, v85\n"
    "v_mfma_f32_16x16x32_bf16 a[152:155], a[216:219], v[216:219], a[152:155]\n"
    "v_add_f32 v49, v49, v85\n"
    "v_add_u32 v5, s71, v5\n"
    "v_mfma_f32_16x16x32_bf16 a[152:155], a[220:223], v[248:251], a[152:155]\n"
    "v_exp_f32 v86, v86\n"
    "v_mfma_f32_16x16x32_bf16 a[156:159], a[220:223], v[252:255], a[156:159]\n"
    "ds_read_b128 a[200:203], v2 offset:2048\n"
    "v_add_f32 v49, v49, v86\n"
    "v_mfma_f32_16x16x32_bf16 a[156:159], a[216:219], v[220:223], a[156:159]\n"
    "v_add_u32 v6, s71, v6\n"
    "v_add_u32 v7, s71, v7\n"
    "v_mfma_f32_16x16x32_bf16 a[172:175], a[224:227], v[220:223], a[172:175]\n"
    "v_exp_f32 v87, v87\n"
    "v_mfma_f32_16x16x32_bf16 a[172:175], a[228:231], v[252:255], a[172:175]\n"
    "ds_read_b128 a[204:207], v3 offset:2048\n"
    "v_cvt_pk_bf16_f32 v183, v86, v87\n"
    "v_mfma_f32_16x16x32_bf16 a[168:171], a[228:231], v[248:251], a[168:171]\n"
    "v_add_f32 v49, v49, v87\n"
    "v_mfma_f32_16x16x32_bf16 a[168:171], a[224:227], v[216:219], a[168:171]\n"
    "v_exp_f32 v88, v88\n"
    "v_mfma_f32_16x16x32_bf16 a[164:167], a[224:227], v[212:215], a[164:167]\n"
    "ds_read_b128 a[208:211], v2 offset:4096\n"
    "v_add_f32 v50, v50, v88\n"
    "v_mfma_f32_16x16x32_bf16 a[164:167], a[228:231], v[244:247], a[164:167]\n"
    "v_mfma_f32_16x16x32_bf16 a[160:163], a[228:231], v[240:243], a[160:163]\n"
    "v_exp_f32 v89, v89\n"
    "v_mfma_f32_16x16x32_bf16 a[160:163], a[224:227], v[208:211], a[160:163]\n"
    "ds_read_b128 a[212:215], v3 offset:4096\n"
    "v_cvt_pk_bf16_f32 v186, v88, v89\n"
    "v_mfma_f32_16x16x32_bf16 a[176:179], a[232:235], v[208:211], a[176:179]\n"
    "v_add_f32 v50, v50, v89\n"
    "v_mfma_f32_16x16x32_bf16 a[176:179], a[236:239], v[240:243], a[176:179]\n"
    "v_exp_f32 v90, v90\n"
    "v_mfma_f32_16x16x32_bf16 a[180:183], a[236:239], v[244:247], a[180:183]\n"
    "v_add_f32 v50, v50, v90\n"
    "v_mfma_f32_16x16x32_bf16 a[180:183], a[232:235], v[212:215], a[180:183]\n"
    "v_mfma_f32_16x16x32_bf16 a[184:187], a[232:235], v[216:219], a[184:187]\n"
    "v_exp_f32 v91, v91\n"
    "v_mfma_f32_16x16x32_bf16 a[184:187], a[236:239], v[248:251], a[184:187]\n"
    "v_cvt_pk_bf16_f32 v187, v90, v91\n"
    "v_add_f32 v50, v50, v91\n"
    "v_mfma_f32_16x16x32_bf16 a[188:191], a[236:239], v[252:255], a[188:191]\n"
    "s_waitcnt lgkmcnt(0)\n"
    "v_mfma_f32_16x16x32_bf16 a[188:191], a[232:235], v[220:223], a[188:191]\n"
    "v_exp_f32 v92, v92\n"
    "v_mfma_f32_16x16x32_bf16 v[80:83], a[192:195], v[64:67], v[112:115]\n"
    "ds_read_b64_tr_b16 a[216:217], v4 offset:24576\n"
    "v_add_f32 v51, v51, v92\n"
    "v_mfma_f32_16x16x32_bf16 v[80:83], a[196:199], v[96:99], v[80:83]\n"
    "ds_read_b64_tr_b16 a[218:219], v4 offset:30720\n"
    "v_mfma_f32_16x16x32_bf16 v[80:83], a[200:203], v[128:131], v[80:83]\n"
    "v_exp_f32 v93, v93\n"
    "v_mfma_f32_16x16x32_bf16 v[80:83], a[204:207], v[160:163], v[80:83]\n"
    "ds_read_b64_tr_b16 a[220:221], v4 offset:36864\n"
    "v_cvt_pk_bf16_f32 v190, v92, v93\n"
    "v_mfma_f32_16x16x32_bf16 v[80:83], a[208:211], v[192:195], v[80:83]\n"
    "ds_read_b64_tr_b16 a[222:223], v4 offset:43008\n"
    "v_add_f32 v51, v51, v93\n"
    "v_mfma_f32_16x16x32_bf16 v[80:83], a[212:215], v[224:227], v[80:83]\n"
    "v_exp_f32 v94, v94\n"
    "v_mfma_f32_16x16x32_bf16 v[84:87], a[212:215], v[228:231], v[116:119]\n"
    "ds_read_b64_tr_b16 a[224:225], v5 offset:24576\n"
    "v_add_f32 v51, v51, v94\n"
    "v_mfma_f32_16x16x32_bf16 v[84:87], a[208:211], v[196:199], v[84:87]\n"
    "ds_read_b64_tr_b16 a[226:227], v5 offset:30720\n"
    "v_mfma_f32_16x16x32_bf16 v[84:87], a[204:207], v[164:167], v[84:87]\n"
    "v_exp_f32 v95, v95\n"
    "v_mfma_f32_16x16x32_bf16 v[84:87], a[200:203], v[132:135], v[84:87]\n"
    "ds_read_b64_tr_b16 a[228:229], v5 offset:36864\n"
    "v_cvt_pk_bf16_f32 v191, v94, v95\n"
    "v_mfma_f32_16x16x32_bf16 v[84:87], a[196:199], v[100:103], v[84:87]\n"
    "ds_read_b64_tr_b16 a[230:231], v5 offset:43008\n"
    "v_add_f32 v51, v51, v95\n"
    "v_mfma_f32_16x16x32_bf16 v[84:87], a[192:195], v[68:71], v[84:87]\n"
    "v_exp_f32 v80, v80\n"
    "v_mfma_f32_16x16x32_bf16 v[88:91], a[192:195], v[72:75], v[120:123]\n"
    "ds_read_b64_tr_b16 a[232:233], v6 offset:24576\n"
    "v_add_f32 v48, v48, v80\n"
    "v_mfma_f32_16x16x32_bf16 v[88:91], a[196:199], v[104:107], v[88:91]\n"
    "ds_read_b64_tr_b16 a[234:235], v6 offset:30720\n"
    "v_mfma_f32_16x16x32_bf16 v[88:91], a[200:203], v[136:139], v[88:91]\n"
    "v_exp_f32 v81, v81\n"
    "v_mfma_f32_16x16x32_bf16 v[88:91], a[204:207], v[168:171], v[88:91]\n"
    "ds_read_b64_tr_b16 a[236:237], v6 offset:36864\n"
    "v_cvt_pk_bf16_f32 v208, v80, v81\n"
    "v_mfma_f32_16x16x32_bf16 v[88:91], a[208:211], v[200:203], v[88:91]\n"
    "ds_read_b64_tr_b16 a[238:239], v6 offset:43008\n"
    "v_add_f32 v48, v48, v81\n"
    "v_mfma_f32_16x16x32_bf16 v[88:91], a[212:215], v[232:235], v[88:91]\n"
    "v_exp_f32 v82, v82\n"
    "v_mfma_f32_16x16x32_bf16 v[92:95], a[212:215], v[236:239], v[124:127]\n"
    "v_add_f32 v48, v48, v82\n"
    "v_mfma_f32_16x16x32_bf16 v[92:95], a[208:211], v[204:207], v[92:95]\n"
    "v_mfma_f32_16x16x32_bf16 v[92:95], a[204:207], v[172:175], v[92:95]\n"
    "v_exp_f32 v83, v83\n"
    "v_mfma_f32_16x16x32_bf16 v[92:95], a[200:203], v[140:143], v[92:95]\n"
    "v_cvt_pk_bf16_f32 v209, v82, v83\n"
    "v_add_f32 v48, v48, v83\n"
    "v_mfma_f32_16x16x32_bf16 v[92:95], a[196:199], v[108:111], v[92:95]\n"
    "s_waitcnt lgkmcnt(0)\n"
    "v_mfma_f32_16x16x32_bf16 v[92:95], a[192:195], v[76:79], v[92:95]\n"
    "v_exp_f32 v84, v84\n"
    "v_mfma_f32_16x16x32_bf16 a[0:3], a[216:219], v[144:147], a[0:3]\n"
    "s_add_u32 m0, s70, 0\n"
    "ds_read_b128 a[192:195], v2 offset:6144\n"
    "v_add_f32 v49, v49, v84\n"
    "v_mfma_f32_16x16x32_bf16 a[0:3], a[220:223], v[176:179], a[0:3]\n"
    "buffer_load_dwordx4 v8, s[36:39], s56 offen offset:0 lds\n"
    "v_mfma_f32_16x16x32_bf16 a[4:7], a[220:223], v[180:183], a[4:7]\n"
    "v_exp_f32 v85, v85\n"
    "v_mfma_f32_16x16x32_bf16 a[4:7], a[216:219], v[148:151], a[4:7]\n"
    "ds_read_b128 a[196:199], v3 offset:6144\n"
    "v_cvt_pk_bf16_f32 v212, v84, v85\n"
    "v_mfma_f32_16x16x32_bf16 a[8:11], a[216:219], v[152:155], a[8:11]\n"
    "v_add_f32 v49, v49, v85\n"
    "v_mfma_f32_16x16x32_bf16 a[8:11], a[220:223], v[184:187], a[8:11]\n"
    "v_exp_f32 v86, v86\n"
    "v_mfma_f32_16x16x32_bf16 a[12:15], a[220:223], v[188:191], a[12:15]\n"
    "ds_read_b128 a[200:203], v2 offset:8192\n"
    "v_add_f32 v49, v49, v86\n"
    "v_mfma_f32_16x16x32_bf16 a[12:15], a[216:219], v[156:159], a[12:15]\n"
    "buffer_load_dwordx4 v8, s[36:39], s57 offen offset:1024 lds\n"
    "v_mfma_f32_16x16x32_bf16 a[28:31], a[224:227], v[156:159], a[28:31]\n"
    "v_exp_f32 v87, v87\n"
    "v_mfma_f32_16x16x32_bf16 a[28:31], a[228:231], v[188:191], a[28:31]\n"
    "ds_read_b128 a[204:207], v3 offset:8192\n"
    "v_cvt_pk_bf16_f32 v213, v86, v87\n"
    "v_mfma_f32_16x16x32_bf16 a[24:27], a[228:231], v[184:187], a[24:27]\n"
    "v_add_f32 v49, v49, v87\n"
    "v_mfma_f32_16x16x32_bf16 a[24:27], a[224:227], v[152:155], a[24:27]\n"
    "v_exp_f32 v88, v88\n"
    "v_mfma_f32_16x16x32_bf16 a[20:23], a[224:227], v[148:151], a[20:23]\n"
    "s_add_u32 m0, s70, 2048\n"
    "ds_read_b128 a[208:211], v2 offset:10240\n"
    "v_add_f32 v50, v50, v88\n"
    "v_mfma_f32_16x16x32_bf16 a[20:23], a[228:231], v[180:183], a[20:23]\n"
    "buffer_load_dwordx4 v8, s[36:39], s58 offen offset:0 lds\n"
    "v_mfma_f32_16x16x32_bf16 a[16:19], a[228:231], v[176:179], a[16:19]\n"
    "v_exp_f32 v89, v89\n"
    "v_mfma_f32_16x16x32_bf16 a[16:19], a[224:227], v[144:147], a[16:19]\n"
    "ds_read_b128 a[212:215], v3 offset:10240\n"
    "v_cvt_pk_bf16_f32 v216, v88, v89\n"
    "v_mfma_f32_16x16x32_bf16 a[32:35], a[232:235], v[144:147], a[32:35]\n"
    "v_add_f32 v50, v50, v89\n"
    "v_mfma_f32_16x16x32_bf16 a[32:35], a[236:239], v[176:179], a[32:35]\n"
    "v_exp_f32 v90, v90\n"
    "v_mfma_f32_16x16x32_bf16 a[36:39], a[236:239], v[180:183], a[36:39]\n"
    "v_add_f32 v50, v50, v90\n"
    "v_mfma_f32_16x16x32_bf16 a[36:39], a[232:235], v[148:151], a[36:39]\n"
    "buffer_load_dwordx4 v8, s[36:39], s59 offen offset:1024 lds\n"
    "v_mfma_f32_16x16x32_bf16 a[40:43], a[232:235], v[152:155], a[40:43]\n"
    "v_exp_f32 v91, v91\n"
    "v_mfma_f32_16x16x32_bf16 a[40:43], a[236:239], v[184:187], a[40:43]\n"
    "v_cvt_pk_bf16_f32 v217, v90, v91\n"
    "v_add_f32 v50, v50, v91\n"
    "v_mfma_f32_16x16x32_bf16 a[44:47], a[236:239], v[188:191], a[44:47]\n"
    "s_waitcnt lgkmcnt(0)\n"
    "v_mfma_f32_16x16x32_bf16 a[44:47], a[232:235], v[156:159], a[44:47]\n"
    "v_exp_f32 v92, v92\n"
    "v_mfma_f32_16x16x32_bf16 v[80:83], a[192:195], v[64:67], v[112:115]\n"
    "ds_read_b64_tr_b16 a[216:217], v7 offset:24576\n"
    "v_add_f32 v51, v51, v92\n"
    "v_mfma_f32_16x16x32_bf16 v[80:83], a[196:199], v[96:99], v[80:83]\n"
    "ds_read_b64_tr_b16 a[218:219], v7 offset:30720\n"
    "v_mfma_f32_16x16x32_bf16 v[80:83], a[200:203], v[128:131], v[80:83]\n"
    "v_exp_f32 v93, v93\n"
    "v_mfma_f32_16x16x32_bf16 v[80:83], a[204:207], v[160:163], v[80:83]\n"
    "ds_read_b64_tr_b16 a[220:221], v7 offset:36864\n"
    "v_cvt_pk_bf16_f32 v220, v92, v93\n"
    "v_mfma_f32_16x16x32_bf16 v[80:83], a[208:211], v[192:195], v[80:83]\n"
    "ds_read_b64_tr_b16 a[222:223], v7 offset:43008\n"
    "v_add_f32 v51, v51, v93\n"
    "v_mfma_f32_16x16x32_bf16 v[80:83], a[212:215], v[224:227], v[80:83]\n"
    "v_exp_f32 v94, v94\n"
    "v_mfma_f32_16x16x32_bf16 v[84:87], a[212:215], v[228:231], v[116:119]\n"
    "ds_read_b64_tr_b16 a[224:225], v4 offset:26624\n"
    "v_add_f32 v51, v51, v94\n"
    "v_mfma_f32_16x16x32_bf16 v[84:87], a[208:211], v[196:199], v[84:87]\n"
    "ds_read_b64_tr_b16 a[226:227], v4 offset:32768\n"
    "v_mfma_f32_16x16x32_bf16 v[84:87], a[204:207], v[164:167], v[84:87]\n"
    "v_exp_f32 v95, v95\n"
    "v_mfma_f32_16x16x32_bf16 v[84:87], a[200:203], v[132:135], v[84:87]\n"
    "ds_read_b64_tr_b16 a[228:229], v4 offset:38912\n"
    "v_cvt_pk_bf16_f32 v221, v94, v95\n"
    "v_mfma_f32_16x16x32_bf16 v[84:87], a[196:199], v[100:103], v[84:87]\n"
    "ds_read_b64_tr_b16 a[230:231], v4 offset:45056\n"
    "v_add_f32 v51, v51, v95\n"
    "v_mfma_f32_16x16x32_bf16 v[84:87], a[192:195], v[68:71], v[84:87]\n"
    "v_exp_f32 v80, v80\n"
    "v_mfma_f32_16x16x32_bf16 v[88:91], a[192:195], v[72:75], v[120:123]\n"
    "ds_read_b64_tr_b16 a[232:233], v5 offset:26624\n"
    "v_add_f32 v48, v48, v80\n"
    "v_mfma_f32_16x16x32_bf16 v[88:91], a[196:199], v[104:107], v[88:91]\n"
    "ds_read_b64_tr_b16 a[234:235], v5 offset:32768\n"
    "v_mfma_f32_16x16x32_bf16 v[88:91], a[200:203], v[136:139], v[88:91]\n"
    "v_exp_f32 v81, v81\n"
    "v_mfma_f32_16x16x32_bf16 v[88:91], a[204:207], v[168:171], v[88:91]\n"
    "ds_read_b64_tr_b16 a[236:237], v5 offset:38912\n"
    "v_cvt_pk_bf16_f32 v210, v80, v81\n"
    "v_mfma_f32_16x16x32_bf16 v[88:91], a[208:211], v[200:203], v[88:91]\n"
    "ds_read_b64_tr_b16 a[238:239], v5 offset:45056\n"
    "v_add_f32 v48, v48, v81\n"
    "v_mfma_f32_16x16x32_bf16 v[88:91], a[212:215], v[232:235], v[88:91]\n"
    "v_exp_f32 v82, v82\n"
    "v_mfma_f32_16x16x32_bf16 v[92:95], a[212:215], v[236:239], v[124:127]\n"
    "v_add_f32 v48, v48, v82\n"
    "v_mfma_f32_16x16x32_bf16 v[92:95], a[208:211], v[204:207], v[92:95]\n"
    "v_mfma_f32_16x16x32_bf16 v[92:95], a[204:207], v[172:175], v[92:95]\n"
    "v_exp_f32 v83, v83\n"
    "v_mfma_f32_16x16x32_bf16 v[92:95], a[200:203], v[140:143], v[92:95]\n"
    "v_cvt_pk_bf16_f32 v211, v82, v83\n"
    "v_add_f32 v48, v48, v83\n"
    "v_mfma_f32_16x16x32_bf16 v[92:95], a[196:199], v[108:111], v[92:95]\n"
    "s_waitcnt lgkmcnt(0)\n"
    "v_mfma_f32_16x16x32_bf16 v[92:95], a[192:195], v[76:79], v[92:95]\n"
    "v_exp_f32 v84, v84\n"
    "v_mfma_f32_16x16x32_bf16 a[48:51], a[216:219], v[144:147], a[48:51]\n"
    "s_add_u32 m0, s70, 4096\n"
    "ds_read_b128 a[192:195], v2 offset:12288\n"
    "v_add_f32 v49, v49, v84\n"
    "v_mfma_f32_16x16x32_bf16 a[48:51], a[220:223], v[176:179], a[48:51]\n"
    "buffer_load_dwordx4 v8, s[36:39], s60 offen offset:0 lds\n"
    "v_mfma_f32_16x16x32_bf16 a[52:55], a[220:223], v[180:183], a[52:55]\n"
    "v_exp_f32 v85, v85\n"
    "v_mfma_f32_16x16x32_bf16 a[52:55], a[216:219], v[148:151], a[52:55]\n"
    "ds_read_b128 a[196:199], v3 offset:12288\n"
    "v_cvt_pk_bf16_f32 v214, v84, v85\n"
    "v_mfma_f32_16x16x32_bf16 a[56:59], a[216:219], v[152:155], a[56:59]\n"
    "v_add_f32 v49, v49, v85\n"
    "v_mfma_f32_16x16x32_bf16 a[56:59], a[220:223], v[184:187], a[56:59]\n"
    "v_exp_f32 v86, v86\n"
    "v_mfma_f32_16x16x32_bf16 a[60:63], a[220:223], v[188:191], a[60:63]\n"
    "ds_read_b128 a[200:203], v2 offset:14336\n"
    "v_add_f32 v49, v49, v86\n"
    "v_mfma_f32_16x16x32_bf16 a[60:63], a[216:219], v[156:159], a[60:63]\n"
    "buffer_load_dwordx4 v8, s[36:39], s61 offen offset:1024 lds\n"
    "v_mfma_f32_16x16x32_bf16 a[76:79], a[224:227], v[156:159], a[76:79]\n"
    "v_exp_f32 v87, v87\n"
    "v_mfma_f32_16x16x32_bf16 a[76:79], a[228:231], v[188:191], a[76:79]\n"
    "ds_read_b128 a[204:207], v3 offset:14336\n"
    "v_cvt_pk_bf16_f32 v215, v86, v87\n"
    "v_mfma_f32_16x16x32_bf16 a[72:75], a[228:231], v[184:187], a[72:75]\n"
    "v_add_f32 v49, v49, v87\n"
    "v_mfma_f32_16x16x32_bf16 a[72:75], a[224:227], v[152:155], a[72:75]\n"
    "v_exp_f32 v88, v88\n"
    "v_mfma_f32_16x16x32_bf16 a[68:71], a[224:227], v[148:151], a[68:71]\n"
    "s_add_u32 m0, s70, 24576\n"
    "ds_read_b128 a[208:211], v2 offset:16384\n"
    "v_add_f32 v50, v50, v88\n"
    "v_mfma_f32_16x16x32_bf16 a[68:71], a[228:231], v[180:183], a[68:71]\n"
    "buffer_load_dwordx4 v8, s[40:43], s56 offen offset:0 lds\n"
    "v_mfma_f32_16x16x32_bf16 a[64:67], a[228:231], v[176:179], a[64:67]\n"
    "v_exp_f32 v89, v89\n"
    "v_mfma_f32_16x16x32_bf16 a[64:67], a[224:227], v[144:147], a[64:67]\n"
    "ds_read_b128 a[212:215], v3 offset:16384\n"
    "v_cvt_pk_bf16_f32 v218, v88, v89\n"
    "v_mfma_f32_16x16x32_bf16 a[80:83], a[232:235], v[144:147], a[80:83]\n"
    "v_add_f32 v50, v50, v89\n"
    "v_mfma_f32_16x16x32_bf16 a[80:83], a[236:239], v[176:179], a[80:83]\n"
    "v_exp_f32 v90, v90\n"
    "v_mfma_f32_16x16x32_bf16 a[84:87], a[236:239], v[180:183], a[84:87]\n"
    "v_add_f32 v50, v50, v90\n"
    "v_mfma_f32_16x16x32_bf16 a[84:87], a[232:235], v[148:151], a[84:87]\n"
    "buffer_load_dwordx4 v8, s[40:43], s57 offen offset:1024 lds\n"
    "v_mfma_f32_16x16x32_bf16 a[88:91], a[232:235], v[152:155], a[88:91]\n"
    "v_exp_f32 v91, v91\n"
    "v_mfma_f32_16x16x32_bf16 a[88:91], a[236:239], v[184:187], a[88:91]\n"
    "v_cvt_pk_bf16_f32 v219, v90, v91\n"
    "v_add_f32 v50, v50, v91\n"
    "v_mfma_f32_16x16x32_bf16 a[92:95], a[236:239], v[188:191], a[92:95]\n"
    "s_waitcnt lgkmcnt(0)\n"
    "v_mfma_f32_16x16x32_bf16 a[92:95], a[232:235], v[156:159], a[92:95]\n"
    "v_exp_f32 v92, v92\n"
    "v_mfma_f32_16x16x32_bf16 v[80:83], a[192:195], v[64:67], v[112:115]\n"
    "ds_read_b64_tr_b16 a[216:217], v6 offset:26624\n"
    "v_add_f32 v51, v51, v92\n"
    "v_mfma_f32_16x16x32_bf16 v[80:83], a[196:199], v[96:99], v[80:83]\n"
    "ds_read_b64_tr_b16 a[218:219], v6 offset:32768\n"
    "v_mfma_f32_16x16x32_bf16 v[80:83], a[200:203], v[128:131], v[80:83]\n"
    "v_exp_f32 v93, v93\n"
    "v_mfma_f32_16x16x32_bf16 v[80:83], a[204:207], v[160:163], v[80:83]\n"
    "ds_read_b64_tr_b16 a[220:221], v6 offset:38912\n"
    "v_cvt_pk_bf16_f32 v222, v92, v93\n"
    "v_mfma_f32_16x16x32_bf16 v[80:83], a[208:211], v[192:195], v[80:83]\n"
    "ds_read_b64_tr_b16 a[222:223], v6 offset:45056\n"
    "v_add_f32 v51, v51, v93\n"
    "v_mfma_f32_16x16x32_bf16 v[80:83], a[212:215], v[224:227], v[80:83]\n"
    "v_exp_f32 v94, v94\n"
    "v_mfma_f32_16x16x32_bf16 v[84:87], a[212:215], v[228:231], v[116:119]\n"
    "ds_read_b64_tr_b16 a[224:225], v7 offset:26624\n"
    "v_add_f32 v51, v51, v94\n"
    "v_mfma_f32_16x16x32_bf16 v[84:87], a[208:211], v[196:199], v[84:87]\n"
    "ds_read_b64_tr_b16 a[226:227], v7 offset:32768\n"
    "v_mfma_f32_16x16x32_bf16 v[84:87], a[204:207], v[164:167], v[84:87]\n"
    "v_exp_f32 v95, v95\n"
    "v_mfma_f32_16x16x32_bf16 v[84:87], a[200:203], v[132:135], v[84:87]\n"
    "ds_read_b64_tr_b16 a[228:229], v7 offset:38912\n"
    "v_cvt_pk_bf16_f32 v223, v94, v95\n"
    "v_mfma_f32_16x16x32_bf16 v[84:87], a[196:199], v[100:103], v[84:87]\n"
    "ds_read_b64_tr_b16 a[230:231], v7 offset:45056\n"
    "v_add_f32 v51, v51, v95\n"
    "v_mfma_f32_16x16x32_bf16 v[84:87], a[192:195], v[68:71], v[84:87]\n"
    "v_exp_f32 v80, v80\n"
    "v_mfma_f32_16x16x32_bf16 v[88:91], a[192:195], v[72:75], v[120:123]\n"
    "ds_read_b64_tr_b16 a[232:233], v4 offset:28672\n"
    "v_add_f32 v48, v48, v80\n"
    "v_mfma_f32_16x16x32_bf16 v[88:91], a[196:199], v[104:107], v[88:91]\n"
    "ds_read_b64_tr_b16 a[234:235], v4 offset:34816\n"
    "v_mfma_f32_16x16x32_bf16 v[88:91], a[200:203], v[136:139], v[88:91]\n"
    "v_exp_f32 v81, v81\n"
    "v_mfma_f32_16x16x32_bf16 v[88:91], a[204:207], v[168:171], v[88:91]\n"
    "ds_read_b64_tr_b16 a[236:237], v4 offset:40960\n"
    "v_cvt_pk_bf16_f32 v240, v80, v81\n"
    "v_mfma_f32_16x16x32_bf16 v[88:91], a[208:211], v[200:203], v[88:91]\n"
    "ds_read_b64_tr_b16 a[238:239], v4 offset:47104\n"
    "v_add_f32 v48, v48, v81\n"
    "v_mfma_f32_16x16x32_bf16 v[88:91], a[212:215], v[232:235], v[88:91]\n"
    "v_exp_f32 v82, v82\n"
    "v_mfma_f32_16x16x32_bf16 v[92:95], a[212:215], v[236:239], v[124:127]\n"
    "v_add_f32 v48, v48, v82\n"
    "v_mfma_f32_16x16x32_bf16 v[92:95], a[208:211], v[204:207], v[92:95]\n"
    "v_mfma_f32_16x16x32_bf16 v[92:95], a[204:207], v[172:175], v[92:95]\n"
    "v_exp_f32 v83, v83\n"
    "v_mfma_f32_16x16x32_bf16 v[92:95], a[200:203], v[140:143], v[92:95]\n"
    "v_cvt_pk_bf16_f32 v241, v82, v83\n"
    "v_add_f32 v48, v48, v83\n"
    "v_mfma_f32_16x16x32_bf16 v[92:95], a[196:199], v[108:111], v[92:95]\n"
    "s_waitcnt lgkmcnt(0)\n"
    "v_mfma_f32_16x16x32_bf16 v[92:95], a[192:195], v[76:79], v[92:95]\n"
    "v_exp_f32 v84, v84\n"
    "v_mfma_f32_16x16x32_bf16 a[96:99], a[216:219], v[144:147], a[96:99]\n"
    "s_add_u32 m0, s70, 26624\n"
    "ds_read_b128 a[192:195], v2 offset:18432\n"
    "v_add_f32 v49, v49, v84\n"
    "v_mfma_f32_16x16x32_bf16 a[96:99], a[220:223], v[176:179], a[96:99]\n"
    "buffer_load_dwordx4 v8, s[40:43], s58 offen offset:0 lds\n"
    "v_mfma_f32_16x16x32_bf16 a[100:103], a[220:223], v[180:183], a[100:103]\n"
    "v_exp_f32 v85, v85\n"
    "v_mfma_f32_16x16x32_bf16 a[100:103], a[216:219], v[148:151], a[100:103]\n"
    "ds_read_b128 a[196:199], v3 offset:18432\n"
    "v_cvt_pk_bf16_f32 v244, v84, v85\n"
    "v_mfma_f32_16x16x32_bf16 a[104:107], a[216:219], v[152:155], a[104:107]\n"
    "v_add_f32 v49, v49, v85\n"
    "v_mfma_f32_16x16x32_bf16 a[104:107], a[220:223], v[184:187], a[104:107]\n"
    "v_exp_f32 v86, v86\n"
    "v_mfma_f32_16x16x32_bf16 a[108:111], a[220:223], v[188:191], a[108:111]\n"
    "ds_read_b128 a[200:203], v2 offset:20480\n"
    "v_add_f32 v49, v49, v86\n"
    "v_mfma_f32_16x16x32_bf16 a[108:111], a[216:219], v[156:159], a[108:111]\n"
    "buffer_load_dwordx4 v8, s[40:43], s59 offen offset:1024 lds\n"
    "v_mfma_f32_16x16x32_bf16 a[124:127], a[224:227], v[156:159], a[124:127]\n"
    "v_exp_f32 v87, v87\n"
    "v_mfma_f32_16x16x32_bf16 a[124:127], a[228:231], v[188:191], a[124:127]\n"
    "ds_read_b128 a[204:207], v3 offset:20480\n"
    "v_cvt_pk_bf16_f32 v245, v86, v87\n"
    "v_mfma_f32_16x16x32_bf16 a[120:123], a[228:231], v[184:187], a[120:123]\n"
    "v_add_f32 v49, v49, v87\n"
    "v_mfma_f32_16x16x32_bf16 a[120:123], a[224:227], v[152:155], a[120:123]\n"
    "v_exp_f32 v88, v88\n"
    "v_mfma_f32_16x16x32_bf16 a[116:119], a[224:227], v[148:151], a[116:119]\n"
    "s_add_u32 m0, s70, 28672\n"
    "ds_read_b128 a[208:211], v2 offset:22528\n"
    "v_add_f32 v50, v50, v88\n"
    "v_mfma_f32_16x16x32_bf16 a[116:119], a[228:231], v[180:183], a[116:119]\n"
    "buffer_load_dwordx4 v8, s[40:43], s60 offen offset:0 lds\n"
    "v_mfma_f32_16x16x32_bf16 a[112:115], a[228:231], v[176:179], a[112:115]\n"
    "v_exp_f32 v89, v89\n"
    "v_mfma_f32_16x16x32_bf16 a[112:115], a[224:227], v[144:147], a[112:115]\n"
    "ds_read_b128 a[212:215], v3 offset:22528\n"
    "v_cvt_pk_bf16_f32 v248, v88, v89\n"
    "v_mfma_f32_16x16x32_bf16 a[128:131], a[232:235], v[144:147], a[128:131]\n"
    "v_add_f32 v50, v50, v89\n"
    "v_mfma_f32_16x16x32_bf16 a[128:131], a[236:239], v[176:179], a[128:131]\n"
    "v_exp_f32 v90, v90\n"
    "v_mfma_f32_16x16x32_bf16 a[132:135], a[236:239], v[180:183], a[132:135]\n"
    "v_add_f32 v50, v50, v90\n"
    "v_mfma_f32_16x16x32_bf16 a[132:135], a[232:235], v[148:151], a[132:135]\n"
    "buffer_load_dwordx4 v8, s[40:43], s61 offen offset:1024 lds\n"
    "v_mfma_f32_16x16x32_bf16 a[136:139], a[232:235], v[152:155], a[136:139]\n"
    "v_exp_f32 v91, v91\n"
    "v_mfma_f32_16x16x32_bf16 a[136:139], a[236:239], v[184:187], a[136:139]\n"
    "v_cvt_pk_bf16_f32 v249, v90, v91\n"
    "v_add_f32 v50, v50, v91\n"
    "v_mfma_f32_16x16x32_bf16 a[140:143], a[236:239], v[188:191], a[140:143]\n"
    "s_waitcnt lgkmcnt(0)\n"
    "v_mfma_f32_16x16x32_bf16 a[140:143], a[232:235], v[156:159], a[140:143]\n"
    "v_exp_f32 v92, v92\n"
    "v_mfma_f32_16x16x32_bf16 v[80:83], a[192:195], v[64:67], v[112:115]\n"
    "ds_read_b64_tr_b16 a[216:217], v5 offset:28672\n"
    "v_add_f32 v51, v51, v92\n"
    "v_mfma_f32_16x16x32_bf16 v[80:83], a[196:199], v[96:99], v[80:83]\n"
    "s_add_u32 s70, s69, s68\n"
    "ds_read_b64_tr_b16 a[218:219], v5 offset:34816\n"
    "v_mfma_f32_16x16x32_bf16 v[80:83], a[200:203], v[128:131], v[80:83]\n"
    "s_add_u32 s73, s69, 49152\n"
    "v_exp_f32 v93, v93\n"
    "v_mfma_f32_16x16x32_bf16 v[80:83], a[204:207], v[160:163], v[80:83]\n"
    "s_cmp_eq_u32 s73, 147456\n"
    "ds_read_b64_tr_b16 a[220:221], v5 offset:40960\n"
    "v_cvt_pk_bf16_f32 v252, v92, v93\n"
    "v_mfma_f32_16x16x32_bf16 v[80:83], a[208:211], v[192:195], v[80:83]\n"
    "s_cselect_b32 s73, 0, s73\n"
    "ds_read_b64_tr_b16 a[222:223], v5 offset:47104\n"
    "v_add_f32 v51, v51, v93\n"
    "v_mfma_f32_16x16x32_bf16 v[80:83], a[212:215], v[224:227], v[80:83]\n"
    "s_sub_u32 s71, s73, s69\n"
    "v_exp_f32 v94, v94\n"
    "v_mfma_f32_16x16x32_bf16 v[84:87], a[212:215], v[228:231], v[116:119]\n"
    "s_mov_b32 s69, s73\n"
    "ds_read_b64_tr_b16 a[224:225], v6 offset:28672\n"
    "v_add_f32 v51, v51, v94\n"
    "v_mfma_f32_16x16x32_bf16 v[84:87], a[208:211], v[196:199], v[84:87]\n"
    "ds_read_b64_tr_b16 a[226:227], v6 offset:34816\n"
    "v_add_u32 v8, 24576, v8\n"
    "v_mfma_f32_16x16x32_bf16 v[84:87], a[204:207], v[164:167], v[84:87]\n"
    "v_exp_f32 v95, v95\n"
    "v_mfma_f32_16x16x32_bf16 v[84:87], a[200:203], v[132:135], v[84:87]\n"
    "ds_read_b64_tr_b16 a[228:229], v6 offset:40960\n"
    "v_cvt_pk_bf16_f32 v253, v94, v95\n"
    "v_mfma_f32_16x16x32_bf16 v[84:87], a[196:199], v[100:103], v[84:87]\n"
    "ds_read_b64_tr_b16 a[230:231], v6 offset:47104\n"
    "v_add_f32 v51, v51, v95\n"
    "v_mfma_f32_16x16x32_bf16 v[84:87], a[192:195], v[68:71], v[84:87]\n"
    "v_exp_f32 v80, v80\n"
    "v_mfma_f32_16x16x32_bf16 v[88:91], a[192:195], v[72:75], v[120:123]\n"
    "ds_read_b64_tr_b16 a[232:233], v7 offset:28672\n"
    "v_add_f32 v48, v48, v80\n"
    "v_mfma_f32_16x16x32_bf16 v[88:91], a[196:199], v[104:107], v[88:91]\n"
    "ds_read_b64_tr_b16 a[234:235], v7 offset:34816\n"
    "v_mfma_f32_16x16x32_bf16 v[88:91], a[200:203], v[136:139], v[88:91]\n"
    "v_exp_f32 v81, v81\n"
    "v_mfma_f32_16x16x32_bf16 v[88:91], a[204:207], v[168:171], v[88:91]\n"
    "ds_read_b64_tr_b16 a[236:237], v7 offset:40960\n"
    "v_cvt_pk_bf16_f32 v242, v80, v81\n"
    "v_mfma_f32_16x16x32_bf16 v[88:91], a[208:211], v[200:203], v[88:91]\n"
    "ds_read_b64_tr_b16 a[238:239], v7 offset:47104\n"
    "v_add_f32 v48, v48, v81\n"
    "v_mfma_f32_16x16x32_bf16 v[88:91], a[212:215], v[232:235], v[88:91]\n"
    "v_exp_f32 v82, v82\n"
    "v_mfma_f32_16x16x32_bf16 v[92:95], a[212:215], v[236:239], v[124:127]\n"
    "v_add_f32 v48, v48, v82\n"
    "v_add_u32 v2, s71, v2\n"
    "v_mfma_f32_16x16x32_bf16 v[92:95], a[208:211], v[204:207], v[92:95]\n"
    "buffer_load_dword v55, v59, s[84:87], 0 offen\n"
    "v_add_u32 v3, s71, v3\n"
    "v_mfma_f32_16x16x32_bf16 v[92:95], a[204:207], v[172:175], v[92:95]\n"
    "v_exp_f32 v83, v83\n"
    "v_mfma_f32_16x16x32_bf16 v[92:95], a[200:203], v[140:143], v[92:95]\n"
    "v_cvt_pk_bf16_f32 v243, v82, v83\n"
    "v_add_f32 v48, v48, v83\n"
    "v_mfma_f32_16x16x32_bf16 v[92:95], a[196:199], v[108:111], v[92:95]\n"
    "s_waitcnt vmcnt(13) lgkmcnt(0)\n"
    "s_barrier\n"
    "v_mfma_f32_16x16x32_bf16 v[92:95], a[192:195], v[76:79], v[92:95]\n"
    "v_exp_f32 v84, v84\n"
    "v_mfma_f32_16x16x32_bf16 a[144:147], a[216:219], v[144:147], a[144:147]\n"
    "ds_read_b128 a[192:195], v2 offset:0\n"
    "v_add_f32 v49, v49, v84\n"
    "v_mfma_f32_16x16x32_bf16 a[144:147], a[220:223], v[176:179], a[144:147]\n"
    "v_add_u32 v59, 1536, v59\n"
    "v_add_u32 v4, s71, v4\n"
    "v_mfma_f32_16x16x32_bf16 a[148:151], a[220:223], v[180:183], a[148:151]\n"
    "v_exp_f32 v85, v85\n"
    "v_mfma_f32_16x16x32_bf16 a[148:151], a[216:219], v[148:151], a[148:151]\n"
    "ds_read_b128 a[196:199], v3 offset:0\n"
    "v_cvt_pk_bf16_f32 v246, v84, v85\n"
    "v_mfma_f32_16x16x32_bf16 a[152:155], a[216:219], v[152:155], a[152:155]\n"
    "v_add_f32 v49, v49, v85\n"
    "v_add_u32 v5, s71, v5\n"
    "v_mfma_f32_16x16x32_bf16 a[152:155], a[220:223], v[184:187], a[152:155]\n"
    "v_exp_f32 v86, v86\n"
    "v_mfma_f32_16x16x32_bf16 a[156:159], a[220:223], v[188:191], a[156:159]\n"
    "ds_read_b128 a[200:203], v2 offset:2048\n"
    "v_add_f32 v49, v49, v86\n"
    "v_mfma_f32_16x16x32_bf16 a[156:159], a[216:219], v[156:159], a[156:159]\n"
    "v_add_u32 v6, s71, v6\n"
    "v_add_u32 v7, s71, v7\n"
    "v_mfma_f32_16x16x32_bf16 a[172:175], a[224:227], v[156:159], a[172:175]\n"
    "v_exp_f32 v87, v87\n"
    "v_mfma_f32_16x16x32_bf16 a[172:175], a[228:231], v[188:191], a[172:175]\n"
    "ds_read_b128 a[204:207], v3 offset:2048\n"
    "v_cvt_pk_bf16_f32 v247, v86, v87\n"
    "v_mfma_f32_16x16x32_bf16 a[168:171], a[228:231], v[184:187], a[168:171]\n"
    "v_add_f32 v49, v49, v87\n"
    "v_mfma_f32_16x16x32_bf16 a[168:171], a[224:227], v[152:155], a[168:171]\n"
    "v_exp_f32 v88, v88\n"
    "v_mfma_f32_16x16x32_bf16 a[164:167], a[224:227], v[148:151], a[164:167]\n"
    "ds_read_b128 a[208:211], v2 offset:4096\n"
    "v_add_f32 v50, v50, v88\n"
    "v_mfma_f32_16x16x32_bf16 a[164:167], a[228:231], v[180:183], a[164:167]\n"
    "v_mfma_f32_16x16x32_bf16 a[160:163], a[228:231], v[176:179], a[160:163]\n"
    "v_exp_f32 v89, v89\n"
    "v_mfma_f32_16x16x32_bf16 a[160:163], a[224:227], v[144:147], a[160:163]\n"
    "ds_read_b128 a[212:215], v3 offset:4096\n"
    "v_cvt_pk_bf16_f32 v250, v88, v89\n"
    "v_mfma_f32_16x16x32_bf16 a[176:179], a[232:235], v[144:147], a[176:179]\n"
    "v_add_f32 v50, v50, v89\n"
    "v_mfma_f32_16x16x32_bf16 a[176:179], a[236:239], v[176:179], a[176:179]\n"
    "v_exp_f32 v90, v90\n"
    "v_mfma_f32_16x16x32_bf16 a[180:183], a[236:239], v[180:183], a[180:183]\n"
    "s_sub_u32 s72, s72, 1\n"
    "v_add_f32 v50, v50, v90\n"
    "v_mfma_f32_16x16x32_bf16 a[180:183], a[232:235], v[148:151], a[180:183]\n"
    "s_cmp_lg_u32 s72, 0\n"
    "v_mfma_f32_16x16x32_bf16 a[184:187], a[232:235], v[152:155], a[184:187]\n"
    "v_exp_f32 v91, v91\n"
    "v_mfma_f32_16x16x32_bf16 a[184:187], a[236:239], v[184:187], a[184:187]\n"
    "v_cvt_pk_bf16_f32 v251, v90, v91\n"
    "v_add_f32 v50, v50, v91\n"
    "v_mfma_f32_16x16x32_bf16 a[188:191], a[236:239], v[188:191], a[188:191]\n"
    "s_waitcnt lgkmcnt(0)\n"
    "v_mfma_f32_16x16x32_bf16 a[188:191], a[232:235], v[156:159], a[188:191]\n"
    "v_exp_f32 v92, v92\n"
    "s_cbranch_scc1 L_loop\n"
    "L_tail:\n"
    "v_mfma_f32_16x16x32_bf16 v[80:83], a[192:195], v[64:67], v[112:115]\n"
    "ds_read_b64_tr_b16 a[216:217], v4 offset:24576\n"
    "v_add_f32 v51, v51, v92\n"
    "v_mfma_f32_16x16x32_bf16 v[80:83], a[196:199], v[96:99], v[80:83]\n"
    "s_lshl_b32 s74, s82, 3\n"
    "ds_read_b64_tr_b16 a[218:219], v4 offset:30720\n"
    "v_lshrrev_b32 v56, 4, v9\n"
    "v_mfma_f32_16x16x32_bf16 v[80:83], a[200:203], v[128:131], v[80:83]\n"
    "v_exp_f32 v93, v93\n"
    "v_mfma_f32_16x16x32_bf16 v[80:83], a[204:207], v[160:163], v[80:83]\n"
    "ds_read_b64_tr_b16 a[220:221], v4 offset:36864\n"
    "v_cvt_pk_bf16_f32 v254, v92, v93\n"
    "v_mfma_f32_16x16x32_bf16 v[80:83], a[208:211], v[192:195], v[80:83]\n"
    "ds_read_b64_tr_b16 a[222:223], v4 offset:43008\n"
    "v_add_f32 v51, v51, v93\n"
    "v_mfma_f32_16x16x32_bf16 v[80:83], a[212:215], v[224:227], v[80:83]\n"
    "v_exp_f32 v94, v94\n"
    "v_mfma_f32_16x16x32_bf16 v[84:87], a[212:215], v[228:231], v[116:119]\n"
    "ds_read_b64_tr_b16 a[224:225], v5 offset:24576\n"
    "v_add_f32 v51, v51, v94\n"
    "v_mfma_f32_16x16x32_bf16 v[84:87], a[208:211], v[196:199], v[84:87]\n"
    "ds_read_b64_tr_b16 a[226:227], v5 offset:30720\n"
    "v_min_u32 v56, 7, v56\n"
    "v_mfma_f32_16x16x32_bf16 v[84:87], a[204:207], v[164:167], v[84:87]\n"
    "v_exp_f32 v95, v95\n"
    "v_mfma_f32_16x16x32_bf16 v[84:87], a[200:203], v[132:135], v[84:87]\n"
    "ds_read_b64_tr_b16 a[228:229], v5 offset:36864\n"
    "v_cvt_pk_bf16_f32 v255, v94, v95\n"
    "v_mfma_f32_16x16x32_bf16 v[84:87], a[196:199], v[100:103], v[84:87]\n"
    "ds_read_b64_tr_b16 a[230:231], v5 offset:43008\n"
    "v_add_f32 v51, v51, v95\n"
    "v_mfma_f32_16x16x32_bf16 v[84:87], a[192:195], v[68:71], v[84:87]\n"
    "v_exp_f32 v80, v80\n"
    "v_mfma_f32_16x16x32_bf16 v[88:91], a[192:195], v[72:75], v[120:123]\n"
    "ds_read_b64_tr_b16 a[232:233], v6 offset:24576\n"
    "v_add_f32 v48, v48, v80\n"
    "v_mfma_f32_16x16x32_bf16 v[88:91], a[196:199], v[104:107], v[88:91]\n"
    "ds_read_b64_tr_b16 a[234:235], v6 offset:30720\n"
    "v_add_u32 v56, s74, v56\n"
    "v_mfma_f32_16x16x32_bf16 v[88:91], a[200:203], v[136:139], v[88:91]\n"
    "v_exp_f32 v81, v81\n"
    "v_mfma_f32_16x16x32_bf16 v[88:91], a[204:207], v[168:171], v[88:91]\n"
    "ds_read_b64_tr_b16 a[236:237], v6 offset:36864\n"
    "v_cvt_pk_bf16_f32 v144, v80, v81\n"
    "v_mfma_f32_16x16x32_bf16 v[88:91], a[208:211], v[200:203], v[88:91]\n"
    "ds_read_b64_tr_b16 a[238:239], v6 offset:43008\n"
    "v_add_f32 v48, v48, v81\n"
    "v_mfma_f32_16x16x32_bf16 v[88:91], a[212:215], v[232:235], v[88:91]\n"
    "v_exp_f32 v82, v82\n"
    "v_mfma_f32_16x16x32_bf16 v[92:95], a[212:215], v[236:239], v[124:127]\n"
    "v_add_f32 v48, v48, v82\n"
    "v_lshlrev_b32 v56, 6, v56\n"
    "v_mfma_f32_16x16x32_bf16 v[92:95], a[208:211], v[204:207], v[92:95]\n"
    "v_add_u32 v56, 36864, v56\n"
    "v_mfma_f32_16x16x32_bf16 v[92:95], a[204:207], v[172:175], v[92:95]\n"
    "v_exp_f32 v83, v83\n"
    "v_mfma_f32_16x16x32_bf16 v[92:95], a[200:203], v[140:143], v[92:95]\n"
    "v_cvt_pk_bf16_f32 v145, v82, v83\n"
    "v_add_f32 v48, v48, v83\n"
    "v_mfma_f32_16x16x32_bf16 v[92:95], a[196:199], v[108:111], v[92:95]\n"
    "s_waitcnt lgkmcnt(0)\n"
    "buffer_load_dwordx2 v[62:63], v56, s[52:55], 0 offen sc1\n"
    "v_mfma_f32_16x16x32_bf16 v[92:95], a[192:195], v[76:79], v[92:95]\n"
    "v_exp_f32 v84, v84\n"
    "v_mfma_f32_16x16x32_bf16 a[0:3], a[216:219], v[208:211], a[0:3]\n"
    "s_add_u32 m0, s70, 0\n"
    "ds_read_b128 a[192:195], v2 offset:6144\n"
    "v_add_f32 v49, v49, v84\n"
    "v_mfma_f32_16x16x32_bf16 a[0:3], a[220:223], v[240:243], a[0:3]\n"
    "buffer_load_dwordx4 v8, s[36:39], s56 offen offset:0 lds\n"
    "v_mfma_f32_16x16x32_bf16 a[4:7], a[220:223], v[244:247], a[4:7]\n"
    "v_exp_f32 v85, v85\n"
    "v_mfma_f32_16x16x32_bf16 a[4:7], a[216:219], v[212:215], a[4:7]\n"
    "ds_read_b128 a[196:199], v3 offset:6144\n"
    "v_cvt_pk_bf16_f32 v148, v84, v85\n"
    "v_mfma_f32_16x16x32_bf16 a[8:11], a[216:219], v[216:219], a[8:11]\n"
    "v_add_f32 v49, v49, v85\n"
    "v_mfma_f32_16x16x32_bf16 a[8:11], a[220:223], v[248:251], a[8:11]\n"
    "v_exp_f32 v86, v86\n"
    "v_mfma_f32_16x16x32_bf16 a[12:15], a[220:223], v[252:255], a[12:15]\n"
    "ds_read_b128 a[200:203], v2 offset:8192\n"
    "v_add_f32 v49, v49, v86\n"
    "v_mfma_f32_16x16x32_bf16 a[12:15], a[216:219], v[220:223], a[12:15]\n"
    "buffer_load_dwordx4 v8, s[36:39], s57 offen offset:1024 lds\n"
    "v_mfma_f32_16x16x32_bf16 a[28:31], a[224:227], v[220:223], a[28:31]\n"
    "v_exp_f32 v87, v87\n"
    "v_mfma_f32_16x16x32_bf16 a[28:31], a[228:231], v[252:255], a[28:31]\n"
    "ds_read_b128 a[204:207], v3 offset:8192\n"
    "v_cvt_pk_bf16_f32 v149, v86, v87\n"
    "v_mfma_f32_16x16x32_bf16 a[24:27], a[228:231], v[248:251], a[24:27]\n"
    "v_add_f32 v49, v49, v87\n"
    "v_mfma_f32_16x16x32_bf16 a[24:27], a[224:227], v[216:219], a[24:27]\n"
    "v_exp_f32 v88, v88\n"
    "v_mfma_f32_16x16x32_bf16 a[20:23], a[224:227], v[212:215], a[20:23]\n"
    "s_add_u32 m0, s70, 2048\n"
    "ds_read_b128 a[208:211], v2 offset:10240\n"
    "v_add_f32 v50, v50, v88\n"
    "v_mfma_f32_16x16x32_bf16 a[20:23], a[228:231], v[244:247], a[20:23]\n"
    "buffer_load_dwordx4 v8, s[36:39], s58 offen offset:0 lds\n"
    "v_mfma_f32_16x16x32_bf16 a[16:19], a[228:231], v[240:243], a[16:19]\n"
    "v_exp_f32 v89, v89\n"
    "v_mfma_f32_16x16x32_bf16 a[16:19], a[224:227], v[208:211], a[16:19]\n"
    "ds_read_b128 a[212:215], v3 offset:10240\n"
    "v_cvt_pk_bf16_f32 v152, v88, v89\n"
    "v_mfma_f32_16x16x32_bf16 a[32:35], a[232:235], v[208:211], a[32:35]\n"
    "v_add_f32 v50, v50, v89\n"
    "v_mfma_f32_16x16x32_bf16 a[32:35], a[236:239], v[240:243], a[32:35]\n"
    "v_exp_f32 v90, v90\n"
    "v_mfma_f32_16x16x32_bf16 a[36:39], a[236:239], v[244:247], a[36:39]\n"
    "v_add_f32 v50, v50, v90\n"
    "v_mfma_f32_16x16x32_bf16 a[36:39], a[232:235], v[212:215], a[36:39]\n"
    "buffer_load_dwordx4 v8, s[36:39], s59 offen offset:1024 lds\n"
    "v_mfma_f32_16x16x32_bf16 a[40:43], a[232:235], v[216:219], a[40:43]\n"
    "v_exp_f32 v91, v91\n"
    "v_mfma_f32_16x16x32_bf16 a[40:43], a[236:239], v[248:251], a[40:43]\n"
    "v_cvt_pk_bf16_f32 v153, v90, v91\n"
    "v_add_f32 v50, v50, v91\n"
    "v_mfma_f32_16x16x32_bf16 a[44:47], a[236:239], v[252:255], a[44:47]\n"
    "s_waitcnt lgkmcnt(0)\n"
    "v_mfma_f32_16x16x32_bf16 a[44:47], a[232:235], v[220:223], a[44:47]\n"
    "v_exp_f32 v92, v92\n"
    "v_mfma_f32_16x16x32_bf16 v[80:83], a[192:195], v[64:67], v[112:115]\n"
    "ds_read_b64_tr_b16 a[216:217], v7 offset:24576\n"
    "v_add_f32 v51, v51, v92\n"
    "v_mfma_f32_16x16x32_bf16 v[80:83], a[196:199], v[96:99], v[80:83]\n"
    "ds_read_b64_tr_b16 a[218:219], v7 offset:30720\n"
    "v_mfma_f32_16x16x32_bf16 v[80:83], a[200:203], v[128:131], v[80:83]\n"
    "v_exp_f32 v93, v93\n"
    "v_mfma_f32_16x16x32_bf16 v[80:83], a[204:207], v[160:163], v[80:83]\n"
    "ds_read_b64_tr_b16 a[220:221], v7 offset:36864\n"
    "v_cvt_pk_bf16_f32 v156, v92, v93\n"
    "v_mfma_f32_16x16x32_bf16 v[80:83], a[208:211], v[192:195], v[80:83]\n"
    "ds_read_b64_tr_b16 a[222:223], v7 offset:43008\n"
    "v_add_f32 v51, v51, v93\n"
    "v_mfma_f32_16x16x32_bf16 v[80:83], a[212:215], v[224:227], v[80:83]\n"
    "v_exp_f32 v94, v94\n"
    "v_mfma_f32_16x16x32_bf16 v[84:87], a[212:215], v[228:231], v[116:119]\n"
    "ds_read_b64_tr_b16 a[224:225], v4 offset:26624\n"
    "v_add_f32 v51, v51, v94\n"
    "v_mfma_f32_16x16x32_bf16 v[84:87], a[208:211], v[196:199], v[84:87]\n"
    "ds_read_b64_tr_b16 a[226:227], v4 offset:32768\n"
    "v_mfma_f32_16x16x32_bf16 v[84:87], a[204:207], v[164:167], v[84:87]\n"
    "v_exp_f32 v95, v95\n"
    "v_mfma_f32_16x16x32_bf16 v[84:87], a[200:203], v[132:135], v[84:87]\n"
    "ds_read_b64_tr_b16 a[228:229], v4 offset:38912\n"
    "v_cvt_pk_bf16_f32 v157, v94, v95\n"
    "v_mfma_f32_16x16x32_bf16 v[84:87], a[196:199], v[100:103], v[84:87]\n"
    "ds_read_b64_tr_b16 a[230:231], v4 offset:45056\n"
    "v_add_f32 v51, v51, v95\n"
    "v_mfma_f32_16x16x32_bf16 v[84:87], a[192:195], v[68:71], v[84:87]\n"
    "v_exp_f32 v80, v80\n"
    "v_mfma_f32_16x16x32_bf16 v[88:91], a[192:195], v[72:75], v[120:123]\n"
    "ds_read_b64_tr_b16 a[232:233], v5 offset:26624\n"
    "v_add_f32 v48, v48, v80\n"
    "v_mfma_f32_16x16x32_bf16 v[88:91], a[196:199], v[104:107], v[88:91]\n"
    "ds_read_b64_tr_b16 a[234:235], v5 offset:32768\n"
    "v_mfma_f32_16x16x32_bf16 v[88:91], a[200:203], v[136:139], v[88:91]\n"
    "v_exp_f32 v81, v81\n"
    "v_mfma_f32_16x16x32_bf16 v[88:91], a[204:207], v[168:171], v[88:91]\n"
    "ds_read_b64_tr_b16 a[236:237], v5 offset:38912\n"
    "v_cvt_pk_bf16_f32 v146, v80, v81\n"
    "v_mfma_f32_16x16x32_bf16 v[88:91], a[208:211], v[200:203], v[88:91]\n"
    "ds_read_b64_tr_b16 a[238:239], v5 offset:45056\n"
    "v_add_f32 v48, v48, v81\n"
    "v_mfma_f32_16x16x32_bf16 v[88:91], a[212:215], v[232:235], v[88:91]\n"
    "v_exp_f32 v82, v82\n"
    "v_mfma_f32_16x16x32_bf16 v[92:95], a[212:215], v[236:239], v[124:127]\n"
    "v_add_f32 v48, v48, v82\n"
    "v_mfma_f32_16x16x32_bf16 v[92:95], a[208:211], v[204:207], v[92:95]\n"
    "v_mfma_f32_16x16x32_bf16 v[92:95], a[204:207], v[172:175], v[92:95]\n"
    "v_exp_f32 v83, v83\n"
    "v_mfma_f32_16x16x32_bf16 v[92:95], a[200:203], v[140:143], v[92:95]\n"
    "v_cvt_pk_bf16_f32 v147, v82, v83\n"
    "v_add_f32 v48, v48, v83\n"
    "v_mfma_f32_16x16x32_bf16 v[92:95], a[196:199], v[108:111], v[92:95]\n"
    "s_waitcnt lgkmcnt(0)\n"
    "v_mfma_f32_16x16x32_bf16 v[92:95], a[192:195], v[76:79], v[92:95]\n"
    "v_exp_f32 v84, v84\n"
    "v_mfma_f32_16x16x32_bf16 a[48:51], a[216:219], v[208:211], a[48:51]\n"
    "s_add_u32 m0, s70, 4096\n"
    "ds_read_b128 a[192:195], v2 offset:12288\n"
    "v_add_f32 v49, v49, v84\n"
    "v_mfma_f32_16x16x32_bf16 a[48:51], a[220:223], v[240:243], a[48:51]\n"
    "buffer_load_dwordx4 v8, s[36:39], s60 offen offset:0 lds\n"
    "v_mfma_f32_16x16x32_bf16 a[52:55], a[220:223], v[244:247], a[52:55]\n"
    "v_exp_f32 v85, v85\n"
    "v_mfma_f32_16x16x32_bf16 a[52:55], a[216:219], v[212:215], a[52:55]\n"
    "ds_read_b128 a[196:199], v3 offset:12288\n"
    "v_cvt_pk_bf16_f32 v150, v84, v85\n"
    "v_mfma_f32_16x16x32_bf16 a[56:59], a[216:219], v[216:219], a[56:59]\n"
    "v_add_f32 v49, v49, v85\n"
    "v_mfma_f32_16x16x32_bf16 a[56:59], a[220:223], v[248:251], a[56:59]\n"
    "v_exp_f32 v86, v86\n"
    "v_mfma_f32_16x16x32_bf16 a[60:63], a[220:223], v[252:255], a[60:63]\n"
    "ds_read_b128 a[200:203], v2 offset:14336\n"
    "v_add_f32 v49, v49, v86\n"
    "v_mfma_f32_16x16x32_bf16 a[60:63], a[216:219], v[220:223], a[60:63]\n"
    "buffer_load_dwordx4 v8, s[36:39], s61 offen offset:1024 lds\n"
    "v_mfma_f32_16x16x32_bf16 a[76:79], a[224:227], v[220:223], a[76:79]\n"
    "v_exp_f32 v87, v87\n"
    "v_mfma_f32_16x16x32_bf16 a[76:79], a[228:231], v[252:255], a[76:79]\n"
    "ds_read_b128 a[204:207], v3 offset:14336\n"
    "v_cvt_pk_bf16_f32 v151, v86, v87\n"
    "v_mfma_f32_16x16x32_bf16 a[72:75], a[228:231], v[248:251], a[72:75]\n"
    "v_add_f32 v49, v49, v87\n"
    "v_mfma_f32_16x16x32_bf16 a[72:75], a[224:227], v[216:219], a[72:75]\n"
    "v_exp_f32 v88, v88\n"
    "v_mfma_f32_16x16x32_bf16 a[68:71], a[224:227], v[212:215], a[68:71]\n"
    "s_add_u32 m0, s70, 24576\n"
    "ds_read_b128 a[208:211], v2 offset:16384\n"
    "v_add_f32 v50, v50, v88\n"
    "v_mfma_f32_16x16x32_bf16 a[68:71], a[228:231], v[244:247], a[68:71]\n"
    "buffer_load_dwordx4 v8, s[40:43], s56 offen offset:0 lds\n"
    "v_mfma_f32_16x16x32_bf16 a[64:67], a[228:231], v[240:243], a[64:67]\n"
    "v_exp_f32 v89, v89\n"
    "v_mfma_f32_16x16x32_bf16 a[64:67], a[224:227], v[208:211], a[64:67]\n"
    "ds_read_b128 a[212:215], v3 offset:16384\n"
    "v_cvt_pk_bf16_f32 v154, v88, v89\n"
    "v_mfma_f32_16x16x32_bf16 a[80:83], a[232:235], v[208:211], a[80:83]\n"
    "v_add_f32 v50, v50, v89\n"
    "v_mfma_f32_16x16x32_bf16 a[80:83], a[236:239], v[240:243], a[80:83]\n"
    "v_exp_f32 v90, v90\n"
    "v_mfma_f32_16x16x32_bf16 a[84:87], a[236:239], v[244:247], a[84:87]\n"
    "v_add_f32 v50, v50, v90\n"
    "v_mfma_f32_16x16x32_bf16 a[84:87], a[232:235], v[212:215], a[84:87]\n"
    "buffer_load_dwordx4 v8, s[40:43], s57 offen offset:1024 lds\n"
    "v_mfma_f32_16x16x32_bf16 a[88:91], a[232:235], v[216:219], a[88:91]\n"
    "v_exp_f32 v91, v91\n"
    "v_mfma_f32_16x16x32_bf16 a[88:91], a[236:239], v[248:251], a[88:91]\n"
    "v_cvt_pk_bf16_f32 v155, v90, v91\n"
    "v_add_f32 v50, v50, v91\n"
    "v_mfma_f32_16x16x32_bf16 a[92:95], a[236:239], v[252:255], a[92:95]\n"
    "s_waitcnt lgkmcnt(0)\n"
    "v_mfma_f32_16x16x32_bf16 a[92:95], a[232:235], v[220:223], a[92:95]\n"
    "v_exp_f32 v92, v92\n"
    "v_mfma_f32_16x16x32_bf16 v[80:83], a[192:195], v[64:67], v[112:115]\n"
    "ds_read_b64_tr_b16 a[216:217], v6 offset:26624\n"
    "v_add_f32 v51, v51, v92\n"
    "v_mfma_f32_16x16x32_bf16 v[80:83], a[196:199], v[96:99], v[80:83]\n"
    "ds_read_b64_tr_b16 a[218:219], v6 offset:32768\n"
    "v_mfma_f32_16x16x32_bf16 v[80:83], a[200:203], v[128:131], v[80:83]\n"
    "v_exp_f32 v93, v93\n"
    "v_mfma_f32_16x16x32_bf16 v[80:83], a[204:207], v[160:163], v[80:83]\n"
    "ds_read_b64_tr_b16 a[220:221], v6 offset:38912\n"
    "v_cvt_pk_bf16_f32 v158, v92, v93\n"
    "v_mfma_f32_16x16x32_bf16 v[80:83], a[208:211], v[192:195], v[80:83]\n"
    "ds_read_b64_tr_b16 a[222:223], v6 offset:45056\n"
    "v_add_f32 v51, v51, v93\n"
    "v_mfma_f32_16x16x32_bf16 v[80:83], a[212:215], v[224:227], v[80:83]\n"
    "v_exp_f32 v94, v94\n"
    "v_mfma_f32_16x16x32_bf16 v[84:87], a[212:215], v[228:231], v[116:119]\n"
    "ds_read_b64_tr_b16 a[224:225], v7 offset:26624\n"
    "v_add_f32 v51, v51, v94\n"
    "v_mfma_f32_16x16x32_bf16 v[84:87], a[208:211], v[196:199], v[84:87]\n"
    "ds_read_b64_tr_b16 a[226:227], v7 offset:32768\n"
    "v_mfma_f32_16x16x32_bf16 v[84:87], a[204:207], v[164:167], v[84:87]\n"
    "v_exp_f32 v95, v95\n"
    "v_mfma_f32_16x16x32_bf16 v[84:87], a[200:203], v[132:135], v[84:87]\n"
    "ds_read_b64_tr_b16 a[228:229], v7 offset:38912\n"
    "v_cvt_pk_bf16_f32 v159, v94, v95\n"
    "v_mfma_f32_16x16x32_bf16 v[84:87], a[196:199], v[100:103], v[84:87]\n"
    "ds_read_b64_tr_b16 a[230:231], v7 offset:45056\n"
    "v_add_f32 v51, v51, v95\n"
    "v_mfma_f32_16x16x32_bf16 v[84:87], a[192:195], v[68:71], v[84:87]\n"
    "v_exp_f32 v80, v80\n"
    "v_mfma_f32_16x16x32_bf16 v[88:91], a[192:195], v[72:75], v[120:123]\n"
    "ds_read_b64_tr_b16 a[232:233], v4 offset:28672\n"
    "v_add_f32 v48, v48, v80\n"
    "v_mfma_f32_16x16x32_bf16 v[88:91], a[196:199], v[104:107], v[88:91]\n"
    "ds_read_b64_tr_b16 a[234:235], v4 offset:34816\n"
    "v_mfma_f32_16x16x32_bf16 v[88:91], a[200:203], v[136:139], v[88:91]\n"
    "v_exp_f32 v81, v81\n"
    "v_mfma_f32_16x16x32_bf16 v[88:91], a[204:207], v[168:171], v[88:91]\n"
    "ds_read_b64_tr_b16 a[236:237], v4 offset:40960\n"
    "v_cvt_pk_bf16_f32 v176, v80, v81\n"
    "v_mfma_f32_16x16x32_bf16 v[88:91], a[208:211], v[200:203], v[88:91]\n"
    "ds_read_b64_tr_b16 a[238:239], v4 offset:47104\n"
    "v_add_f32 v48, v48, v81\n"
    "v_mfma_f32_16x16x32_bf16 v[88:91], a[212:215], v[232:235], v[88:91]\n"
    "v_exp_f32 v82, v82\n"
    "v_mfma_f32_16x16x32_bf16 v[92:95], a[212:215], v[236:239], v[124:127]\n"
    "s_waitcnt vmcnt(8)\n"
    "v_cmp_ne_u64 vcc, s[80:81], v[62:63]\n"
    "s_nop 1\n"
    "s_cbranch_vccnz L_slow2\n"
    "L_back2:\n"
    "v_add_f32 v48, v48, v82\n"
    "v_mfma_f32_16x16x32_bf16 v[92:95], a[208:211], v[204:207], v[92:95]\n"
    "v_bfe_u32 v20, v9, 4, 4\n"
    "v_and_b32 v21, 0x300, v9\n"
    "v_mfma_f32_16x16x32_bf16 v[92:95], a[204:207], v[172:175], v[92:95]\n"
    "v_exp_f32 v83, v83\n"
    "v_mfma_f32_16x16x32_bf16 v[92:95], a[200:203], v[140:143], v[92:95]\n"
    "v_cvt_pk_bf16_f32 v177, v82, v83\n"
    "v_add_f32 v48, v48, v83\n"
    "v_mfma_f32_16x16x32_bf16 v[92:95], a[196:199], v[108:111], v[92:95]\n"
    "s_waitcnt lgkmcnt(0)\n"
    "v_lshl_add_u32 v20, v20, 3, v21\n"
    "v_mfma_f32_16x16x32_bf16 v[92:95], a[192:195], v[76:79], v[92:95]\n"
    "s_lshl_b32 s74, s83, 10\n"
    "v_exp_f32 v84, v84\n"
    "v_mfma_f32_16x16x32_bf16 a[96:99], a[216:219], v[208:211], a[96:99]\n"
    "s_add_u32 m0, s70, 26624\n"
    "ds_read_b128 a[192:195], v2 offset:18432\n"
    "v_add_f32 v49, v49, v84\n"
    "v_mfma_f32_16x16x32_bf16 a[96:99], a[220:223], v[240:243], a[96:99]\n"
    "s_add_u32 s74, s74, 10485760\n"
    "buffer_load_dwordx4 v8, s[40:43], s58 offen offset:0 lds\n"
    "v_mfma_f32_16x16x32_bf16 a[100:103], a[220:223], v[244:247], a[100:103]\n"
    "v_exp_f32 v85, v85\n"
    "v_mfma_f32_16x16x32_bf16 a[100:103], a[216:219], v[212:215], a[100:103]\n"
    "ds_read_b128 a[196:199], v3 offset:18432\n"
    "v_cvt_pk_bf16_f32 v180, v84, v85\n"
    "v_mfma_f32_16x16x32_bf16 a[104:107], a[216:219], v[216:219], a[104:107]\n"
    "buffer_load_dwordx2 v[16:17], v20, s[52:55], s74 offen offset:0 sc1\n"
    "v_add_f32 v49, v49, v85\n"
    "v_mfma_f32_16x16x32_bf16 a[104:107], a[220:223], v[248:251], a[104:107]\n"
    "v_exp_f32 v86, v86\n"
    "v_mfma_f32_16x16x32_bf16 a[108:111], a[220:223], v[252:255], a[108:111]\n"
    "ds_read_b128 a[200:203], v2 offset:20480\n"
    "v_add_f32 v49, v49, v86\n"
    "v_mfma_f32_16x16x32_bf16 a[108:111], a[216:219], v[220:223], a[108:111]\n"
    "buffer_load_dwordx4 v8, s[40:43], s59 offen offset:1024 lds\n"
    "v_mfma_f32_16x16x32_bf16 a[124:127], a[224:227], v[220:223], a[124:127]\n"
    "v_exp_f32 v87, v87\n"
    "v_mfma_f32_16x16x32_bf16 a[124:127], a[228:231], v[252:255], a[124:127]\n"
    "ds_read_b128 a[204:207], v3 offset:20480\n"
    "v_cvt_pk_bf16_f32 v181, v86, v87\n"
    "v_mfma_f32_16x16x32_bf16 a[120:123], a[228:231], v[248:251], a[120:123]\n"
    "buffer_load_dwordx2 v[18:19], v20, s[52:55], s74 offen offset:128 sc1\n"
    "v_add_f32 v49, v49, v87\n"
    "v_mfma_f32_16x16x32_bf16 a[120:123], a[224:227], v[216:219], a[120:123]\n"
    "v_exp_f32 v88, v88\n"
    "v_mfma_f32_16x16x32_bf16 a[116:119], a[224:227], v[212:215], a[116:119]\n"
    "s_add_u32 m0, s70, 28672\n"
    "ds_read_b128 a[208:211], v2 offset:22528\n"
    "v_add_f32 v50, v50, v88\n"
    "v_mfma_f32_16x16x32_bf16 a[116:119], a[228:231], v[244:247], a[116:119]\n"
    "buffer_load_dwordx4 v8, s[40:43], s60 offen offset:0 lds\n"
    "v_mfma_f32_16x16x32_bf16 a[112:115], a[228:231], v[240:243], a[112:115]\n"
    "v_exp_f32 v89, v89\n"
    "v_mfma_f32_16x16x32_bf16 a[112:115], a[224:227], v[208:211], a[112:115]\n"
    "ds_read_b128 a[212:215], v3 offset:22528\n"
    "v_cvt_pk_bf16_f32 v184, v88, v89\n"
    "v_mfma_f32_16x16x32_bf16 a[128:131], a[232:235], v[208:211], a[128:131]\n"
    "v_add_f32 v50, v50, v89\n"
    "v_mfma_f32_16x16x32_bf16 a[128:131], a[236:239], v[240:243], a[128:131]\n"
    "v_exp_f32 v90, v90\n"
    "v_mfma_f32_16x16x32_bf16 a[132:135], a[236:239], v[244:247], a[132:135]\n"
    "v_add_f32 v50, v50, v90\n"
    "v_mfma_f32_16x16x32_bf16 a[132:135], a[232:235], v[212:215], a[132:135]\n"
    "buffer_load_dwordx4 v8, s[40:43], s61 offen offset:1024 lds\n"
    "v_mfma_f32_16x16x32_bf16 a[136:139], a[232:235], v[216:219], a[136:139]\n"
    "v_exp_f32 v91, v91\n"
    "v_mfma_f32_16x16x32_bf16 a[136:139], a[236:239], v[248:251], a[136:139]\n"
    "v_cvt_pk_bf16_f32 v185, v90, v91\n"
    "v_add_f32 v50, v50, v91\n"
    "v_mfma_f32_16x16x32_bf16 a[140:143], a[236:239], v[252:255], a[140:143]\n"
    "s_waitcnt lgkmcnt(0)\n"
    "v_mfma_f32_16x16x32_bf16 a[140:143], a[232:235], v[220:223], a[140:143]\n"
    "v_exp_f32 v92, v92\n"
    "v_mfma_f32_16x16x32_bf16 v[80:83], a[192:195], v[64:67], v[112:115]\n"
    "ds_read_b64_tr_b16 a[216:217], v5 offset:28672\n"
    "v_add_f32 v51, v51, v92\n"
    "v_mfma_f32_16x16x32_bf16 v[80:83], a[196:199], v[96:99], v[80:83]\n"
    "s_add_u32 s70, s69, s68\n"
    "ds_read_b64_tr_b16 a[218:219], v5 offset:34816\n"
    "v_mfma_f32_16x16x32_bf16 v[80:83], a[200:203], v[128:131], v[80:83]\n"
    "s_add_u32 s73, s69, 49152\n"
    "v_exp_f32 v93, v93\n"
    "v_mfma_f32_16x16x32_bf16 v[80:83], a[204:207], v[160:163], v[80:83]\n"
    "s_cmp_eq_u32 s73, 147456\n"
    "ds_read_b64_tr_b16 a[220:221], v5 offset:40960\n"
    "v_cvt_pk_bf16_f32 v188, v92, v93\n"
    "v_mfma_f32_16x16x32_bf16 v[80:83], a[208:211], v[192:195], v[80:83]\n"
    "s_cselect_b32 s73, 0, s73\n"
    "ds_read_b64_tr_b16 a[222:223], v5 offset:47104\n"
    "v_add_f32 v51, v51, v93\n"
    "v_mfma_f32_16x16x32_bf16 v[80:83], a[212:215], v[224:227], v[80:83]\n"
    "s_sub_u32 s71, s73, s69\n"
    "v_exp_f32 v94, v94\n"
    "v_mfma_f32_16x16x32_bf16 v[84:87], a[212:215], v[228:231], v[116:119]\n"
    "s_mov_b32 s69, s73\n"
    "ds_read_b64_tr_b16 a[224:225], v6 offset:28672\n"
    "v_add_f32 v51, v51, v94\n"
    "v_mfma_f32_16x16x32_bf16 v[84:87], a[208:211], v[196:199], v[84:87]\n"
    "ds_read_b64_tr_b16 a[226:227], v6 offset:34816\n"
    "v_add_u32 v8, 24576, v8\n"
    "v_mfma_f32_16x16x32_bf16 v[84:87], a[204:207], v[164:167], v[84:87]\n"
    "v_exp_f32 v95, v95\n"
    "v_mfma_f32_16x16x32_bf16 v[84:87], a[200:203], v[132:135], v[84:87]\n"
    "ds_read_b64_tr_b16 a[228:229], v6 offset:40960\n"
    "v_cvt_pk_bf16_f32 v189, v94, v95\n"
    "v_mfma_f32_16x16x32_bf16 v[84:87], a[196:199], v[100:103], v[84:87]\n"
    "ds_read_b64_tr_b16 a[230:231], v6 offset:47104\n"
    "v_add_f32 v51, v51, v95\n"
    "v_mfma_f32_16x16x32_bf16 v[84:87], a[192:195], v[68:71], v[84:87]\n"
    "v_exp_f32 v80, v80\n"
    "v_mfma_f32_16x16x32_bf16 v[88:91], a[192:195], v[72:75], v[120:123]\n"
    "ds_read_b64_tr_b16 a[232:233], v7 offset:28672\n"
    "v_add_f32 v48, v48, v80\n"
    "v_mfma_f32_16x16x32_bf16 v[88:91], a[196:199], v[104:107], v[88:91]\n"
    "ds_read_b64_tr_b16 a[234:235], v7 offset:34816\n"
    "v_mfma_f32_16x16x32_bf16 v[88:91], a[200:203], v[136:139], v[88:91]\n"
    "v_exp_f32 v81, v81\n"
    "v_mfma_f32_16x16x32_bf16 v[88:91], a[204:207], v[168:171], v[88:91]\n"
    "ds_read_b64_tr_b16 a[236:237], v7 offset:40960\n"
    "v_cvt_pk_bf16_f32 v178, v80, v81\n"
    "v_mfma_f32_16x16x32_bf16 v[88:91], a[208:211], v[200:203], v[88:91]\n"
    "ds_read_b64_tr_b16 a[238:239], v7 offset:47104\n"
    "v_add_f32 v48, v48, v81\n"
    "v_mfma_f32_16x16x32_bf16 v[88:91], a[212:215], v[232:235], v[88:91]\n"
    "v_exp_f32 v82, v82\n"
    "v_mfma_f32_16x16x32_bf16 v[92:95], a[212:215], v[236:239], v[124:127]\n"
    "v_add_f32 v48, v48, v82\n"
    "v_add_u32 v2, s71, v2\n"
    "v_mfma_f32_16x16x32_bf16 v[92:95], a[208:211], v[204:207], v[92:95]\n"
    "v_add_u32 v3, s71, v3\n"
    "v_mfma_f32_16x16x32_bf16 v[92:95], a[204:207], v[172:175], v[92:95]\n"
    "v_exp_f32 v83, v83\n"
    "v_mfma_f32_16x16x32_bf16 v[92:95], a[200:203], v[140:143], v[92:95]\n"
    "v_cvt_pk_bf16_f32 v179, v82, v83\n"
    "v_add_f32 v48, v48, v83\n"
    "v_mfma_f32_16x16x32_bf16 v[92:95], a[196:199], v[108:111], v[92:95]\n"
    "s_waitcnt lgkmcnt(0)\n"
    "s_barrier\n"
    "v_mfma_f32_16x16x32_bf16 v[92:95], a[192:195], v[76:79], v[92:95]\n"
    "v_exp_f32 v84, v84\n"
    "v_mfma_f32_16x16x32_bf16 a[144:147], a[216:219], v[208:211], a[144:147]\n"
    "s_mov_b32 s74, s78\n"
    "ds_read_b128 a[192:195], v2 offset:0\n"
    "v_add_f32 v49, v49, v84\n"
    "v_mfma_f32_16x16x32_bf16 a[144:147], a[220:223], v[240:243], a[144:147]\n"
    "v_add_u32 v4, s71, v4\n"
    "v_add_u32 v5, s71, v5\n"
    "v_mfma_f32_16x16x32_bf16 a[148:151], a[220:223], v[244:247], a[148:151]\n"
    "v_exp_f32 v85, v85\n"
    "v_mfma_f32_16x16x32_bf16 a[148:151], a[216:219], v[212:215], a[148:151]\n"
    "ds_read_b128 a[196:199], v3 offset:0\n"
    "v_cvt_pk_bf16_f32 v182, v84, v85\n"
    "v_mfma_f32_16x16x32_bf16 a[152:155], a[216:219], v[216:219], a[152:155]\n"
    "v_add_f32 v49, v49, v85\n"
    "v_add_u32 v6, s71, v6\n"
    "v_mfma_f32_16x16x32_bf16 a[152:155], a[220:223], v[248:251], a[152:155]\n"
    "v_exp_f32 v86, v86\n"
    "v_mfma_f32_16x16x32_bf16 a[156:159], a[220:223], v[252:255], a[156:159]\n"
    "ds_read_b128 a[200:203], v2 offset:2048\n"
    "v_add_f32 v49, v49, v86\n"
    "v_mfma_f32_16x16x32_bf16 a[156:159], a[216:219], v[220:223], a[156:159]\n"
    "v_add_u32 v7, s71, v7\n"
    "v_lshrrev_b32 v56, 4, v9\n"
    "v_mfma_f32_16x16x32_bf16 a[172:175], a[224:227], v[220:223], a[172:175]\n"
    "v_exp_f32 v87, v87\n"
    "v_mfma_f32_16x16x32_bf16 a[172:175], a[228:231], v[252:255], a[172:175]\n"
    "ds_read_b128 a[204:207], v3 offset:2048\n"
    "v_cvt_pk_bf16_f32 v183, v86, v87\n"
    "v_mfma_f32_16x16x32_bf16 a[168:171], a[228:231], v[248:251], a[168:171]\n"
    "v_add_f32 v49, v49, v87\n"
    "v_min_u32 v56, 31, v56\n"
    "v_mfma_f32_16x16x32_bf16 a[168:171], a[224:227], v[216:219], a[168:171]\n"
    "v_exp_f32 v88, v88\n"
    "v_mfma_f32_16x16x32_bf16 a[164:167], a[224:227], v[212:215], a[164:167]\n"
    "ds_read_b128 a[208:211], v2 offset:4096\n"
    "v_add_f32 v50, v50, v88\n"
    "v_mfma_f32_16x16x32_bf16 a[164:167], a[228:231], v[244:247], a[164:167]\n"
    "v_mul_u32_u24 v56, 8, v56\n"
    "v_add_u32 v56, s74, v56\n"
    "v_mfma_f32_16x16x32_bf16 a[160:163], a[228:231], v[240:243], a[160:163]\n"
    "v_exp_f32 v89, v89\n"
    "v_mfma_f32_16x16x32_bf16 a[160:163], a[224:227], v[208:211], a[160:163]\n"
    "ds_read_b128 a[212:215], v3 offset:4096\n"
    "v_cvt_pk_bf16_f32 v186, v88, v89\n"
    "v_mfma_f32_16x16x32_bf16 a[176:179], a[232:235], v[208:211], a[176:179]\n"
    "v_add_f32 v50, v50, v89\n"
    "v_lshlrev_b32 v56, 6, v56\n"
    "v_mfma_f32_16x16x32_bf16 a[176:179], a[236:239], v[240:243], a[176:179]\n"
    "v_exp_f32 v90, v90\n"
    "v_mfma_f32_16x16x32_bf16 a[180:183], a[236:239], v[244:247], a[180:183]\n"
    "v_add_f32 v50, v50, v90\n"
    "v_add_u32 v56, 36864, v56\n"
    "v_mfma_f32_16x16x32_bf16 a[180:183], a[232:235], v[212:215], a[180:183]\n"
    "buffer_load_dwordx2 v[62:63], v56, s[52:55], 0 offen sc1\n"
    "v_mfma_f32_16x16x32_bf16 a[184:187], a[232:235], v[216:219], a[184:187]\n"
    "v_exp_f32 v91, v91\n"
    "v_mfma_f32_16x16x32_bf16 a[184:187], a[236:239], v[248:251], a[184:187]\n"
    "v_cvt_pk_bf16_f32 v187, v90, v91\n"
    "v_add_f32 v50, v50, v91\n"
    "v_mfma_f32_16x16x32_bf16 a[188:191], a[236:239], v[252:255], a[188:191]\n"
    "s_waitcnt lgkmcnt(0)\n"
    "v_mfma_f32_16x16x32_bf16 a[188:191], a[232:235], v[220:223], a[188:191]\n"
    "v_exp_f32 v92, v92\n"
    "v_mfma_f32_16x16x32_bf16 v[80:83], a[192:195], v[64:67], v[112:115]\n"
    "ds_read_b64_tr_b16 a[216:217], v4 offset:24576\n"
    "v_add_f32 v51, v51, v92\n"
    "v_mfma_f32_16x16x32_bf16 v[80:83], a[196:199], v[96:99], v[80:83]\n"
    "ds_read_b64_tr_b16 a[218:219], v4 offset:30720\n"
    "v_mfma_f32_16x16x32_bf16 v[80:83], a[200:203], v[128:131], v[80:83]\n"
    "v_exp_f32 v93, v93\n"
    "v_mfma_f32_16x16x32_bf16 v[80:83], a[204:207], v[160:163], v[80:83]\n"
    "ds_read_b64_tr_b16 a[220:221], v4 offset:36864\n"
    "v_cvt_pk_bf16_f32 v190, v92, v93\n"
    "v_mfma_f32_16x16x32_bf16 v[80:83], a[208:211], v[192:195], v[80:83]\n"
    "ds_read_b64_tr_b16 a[222:223], v4 offset:43008\n"
    "v_add_f32 v51, v51, v93\n"
    "v_mfma_f32_16x16x32_bf16 v[80:83], a[212:215], v[224:227], v[80:83]\n"
    "v_exp_f32 v94, v94\n"
    "v_mfma_f32_16x16x32_bf16 v[84:87], a[212:215], v[228:231], v[116:119]\n"
    "ds_read_b64_tr_b16 a[224:225], v5 offset:24576\n"
    "v_add_f32 v51, v51, v94\n"
    "v_mfma_f32_16x16x32_bf16 v[84:87], a[208:211], v[196:199], v[84:87]\n"
    "ds_read_b64_tr_b16 a[226:227], v5 offset:30720\n"
    "v_mfma_f32_16x16x32_bf16 v[84:87], a[204:207], v[164:167], v[84:87]\n"
    "v_exp_f32 v95, v95\n"
    "v_mfma_f32_16x16x32_bf16 v[84:87], a[200:203], v[132:135], v[84:87]\n"
    "ds_read_b64_tr_b16 a[228:229], v5 offset:36864\n"
    "v_cvt_pk_bf16_f32 v191, v94, v95\n"
    "v_mfma_f32_16x16x32_bf16 v[84:87], a[196:199], v[100:103], v[84:87]\n"
    "ds_read_b64_tr_b16 a[230:231], v5 offset:43008\n"
    "v_add_f32 v51, v51, v95\n"
    "v_mfma_f32_16x16x32_bf16 v[84:87], a[192:195], v[68:71], v[84:87]\n"
    "v_exp_f32 v80, v80\n"
    "v_mfma_f32_16x16x32_bf16 v[88:91], a[192:195], v[72:75], v[120:123]\n"
    "ds_read_b64_tr_b16 a[232:233], v6 offset:24576\n"
    "v_add_f32 v48, v48, v80\n"
    "v_mfma_f32_16x16x32_bf16 v[88:91], a[196:199], v[104:107], v[88:91]\n"
    "ds_read_b64_tr_b16 a[234:235], v6 offset:30720\n"
    "v_mfma_f32_16x16x32_bf16 v[88:91], a[200:203], v[136:139], v[88:91]\n"
    "v_exp_f32 v81, v81\n"
    "v_mfma_f32_16x16x32_bf16 v[88:91], a[204:207], v[168:171], v[88:91]\n"
    "ds_read_b64_tr_b16 a[236:237], v6 offset:36864\n"
    "v_cvt_pk_bf16_f32 v208, v80, v81\n"
    "v_mfma_f32_16x16x32_bf16 v[88:91], a[208:211], v[200:203], v[88:91]\n"
    "ds_read_b64_tr_b16 a[238:239], v6 offset:43008\n"
    "v_add_f32 v48, v48, v81\n"
    "v_mfma_f32_16x16x32_bf16 v[88:91], a[212:215], v[232:235], v[88:91]\n"
    "v_exp_f32 v82, v82\n"
    "v_mfma_f32_16x16x32_bf16 v[92:95], a[212:215], v[236:239], v[124:127]\n"
    "v_add_f32 v48, v48, v82\n"
    "v_mfma_f32_16x16x32_bf16 v[92:95], a[208:211], v[204:207], v[92:95]\n"
    "v_mfma_f32_16x16x32_bf16 v[92:95], a[204:207], v[172:175], v[92:95]\n"
    "v_exp_f32 v83, v83\n"
    "v_mfma_f32_16x16x32_bf16 v[92:95], a[200:203], v[140:143], v[92:95]\n"
    "v_cvt_pk_bf16_f32 v209, v82, v83\n"
    "v_add_f32 v48, v48, v83\n"
    "v_mfma_f32_16x16x32_bf16 v[92:95], a[196:199], v[108:111], v[92:95]\n"
    "s_waitcnt lgkmcnt(0)\n"
    "v_mfma_f32_16x16x32_bf16 v[92:95], a[192:195], v[76:79], v[92:95]\n"
    "v_exp_f32 v84, v84\n"
    "v_mfma_f32_16x16x32_bf16 a[0:3], a[216:219], v[144:147], a[0:3]\n"
    "s_add_u32 m0, s70, 0\n"
    "ds_read_b128 a[192:195], v2 offset:6144\n"
    "v_add_f32 v49, v49, v84\n"
    "v_mfma_f32_16x16x32_bf16 a[0:3], a[220:223], v[176:179], a[0:3]\n"
    "buffer_load_dwordx4 v8, s[36:39], s56 offen offset:0 lds\n"
    "v_mfma_f32_16x16x32_bf16 a[4:7], a[220:223], v[180:183], a[4:7]\n"
    "v_exp_f32 v85, v85\n"
    "v_mfma_f32_16x16x32_bf16 a[4:7], a[216:219], v[148:151], a[4:7]\n"
    "ds_read_b128 a[196:199], v3 offset:6144\n"
    "v_cvt_pk_bf16_f32 v212, v84, v85\n"
    "v_mfma_f32_16x16x32_bf16 a[8:11], a[216:219], v[152:155], a[8:11]\n"
    "v_add_f32 v49, v49, v85\n"
    "v_mfma_f32_16x16x32_bf16 a[8:11], a[220:223], v[184:187], a[8:11]\n"
    "v_exp_f32 v86, v86\n"
    "v_mfma_f32_16x16x32_bf16 a[12:15], a[220:223], v[188:191], a[12:15]\n"
    "ds_read_b128 a[200:203], v2 offset:8192\n"
    "v_add_f32 v49, v49, v86\n"
    "v_mfma_f32_16x16x32_bf16 a[12:15], a[216:219], v[156:159], a[12:15]\n"
    "buffer_load_dwordx4 v8, s[36:39], s57 offen offset:1024 lds\n"
    "v_mfma_f32_16x16x32_bf16 a[28:31], a[224:227], v[156:159], a[28:31]\n"
    "v_exp_f32 v87, v87\n"
    "v_mfma_f32_16x16x32_bf16 a[28:31], a[228:231], v[188:191], a[28:31]\n"
    "ds_read_b128 a[204:207], v3 offset:8192\n"
    "v_cvt_pk_bf16_f32 v213, v86, v87\n"
    "v_mfma_f32_16x16x32_bf16 a[24:27], a[228:231], v[184:187], a[24:27]\n"
    "v_add_f32 v49, v49, v87\n"
    "v_mfma_f32_16x16x32_bf16 a[24:27], a[224:227], v[152:155], a[24:27]\n"
    "v_exp_f32 v88, v88\n"
    "v_mfma_f32_16x16x32_bf16 a[20:23], a[224:227], v[148:151], a[20:23]\n"
    "s_add_u32 m0, s70, 2048\n"
    "ds_read_b128 a[208:211], v2 offset:10240\n"
    "v_add_f32 v50, v50, v88\n"
    "v_mfma_f32_16x16x32_bf16 a[20:23], a[228:231], v[180:183], a[20:23]\n"
    "buffer_load_dwordx4 v8, s[36:39], s58 offen offset:0 lds\n"
    "v_mfma_f32_16x16x32_bf16 a[16:19], a[228:231], v[176:179], a[16:19]\n"
    "v_exp_f32 v89, v89\n"
    "v_mfma_f32_16x16x32_bf16 a[16:19], a[224:227], v[144:147], a[16:19]\n"
    "ds_read_b128 a[212:215], v3 offset:10240\n"
    "v_cvt_pk_bf16_f32 v216, v88, v89\n"
    "v_mfma_f32_16x16x32_bf16 a[32:35], a[232:235], v[144:147], a[32:35]\n"
    "v_add_f32 v50, v50, v89\n"
    "v_mfma_f32_16x16x32_bf16 a[32:35], a[236:239], v[176:179], a[32:35]\n"
    "v_exp_f32 v90, v90\n"
    "v_mfma_f32_16x16x32_bf16 a[36:39], a[236:239], v[180:183], a[36:39]\n"
    "v_add_f32 v50, v50, v90\n"
    "v_mfma_f32_16x16x32_bf16 a[36:39], a[232:235], v[148:151], a[36:39]\n"
    "buffer_load_dwordx4 v8, s[36:39], s59 offen offset:1024 lds\n"
    "v_mfma_f32_16x16x32_bf16 a[40:43], a[232:235], v[152:155], a[40:43]\n"
    "s_waitcnt vmcnt(7)\n"
    "v_exp_f32 v91, v91\n"
    "v_mfma_f32_16x16x32_bf16 a[40:43], a[236:239], v[184:187], a[40:43]\n"
    "v_cvt_pk_bf16_f32 v217, v90, v91\n"
    "v_add_f32 v50, v50, v91\n"
    "v_mfma_f32_16x16x32_bf16 a[44:47], a[236:239], v[188:191], a[44:47]\n"
    "s_waitcnt lgkmcnt(0)\n"
    "v_add_f32 v16, v16, v18\n"
    "v_add_f32 v17, v17, v19\n"
    "v_mfma_f32_16x16x32_bf16 a[44:47], a[232:235], v[156:159], a[44:47]\n"
    "v_exp_f32 v92, v92\n"
    "v_mfma_f32_16x16x32_bf16 v[80:83], a[192:195], v[64:67], v[112:115]\n"
    "ds_read_b64_tr_b16 a[216:217], v7 offset:24576\n"
    "v_add_f32 v51, v51, v92\n"
    "v_mov_b32 v18, v16\n"
    "v_mfma_f32_16x16x32_bf16 v[80:83], a[196:199], v[96:99], v[80:83]\n"
    "ds_read_b64_tr_b16 a[218:219], v7 offset:30720\n"
    "v_mov_b32 v19, v17\n"
    "v_mfma_f32_16x16x32_bf16 v[80:83], a[200:203], v[128:131], v[80:83]\n"
    "s_nop 1\n"
    "v_exp_f32 v93, v93\n"
    "v_mfma_f32_16x16x32_bf16 v[80:83], a[204:207], v[160:163], v[80:83]\n"
    "ds_read_b64_tr_b16 a[220:221], v7 offset:36864\n"
    "v_cvt_pk_bf16_f32 v220, v92, v93\n"
    "v_permlane16_swap_b32 v16, v18\n"
    "v_mfma_f32_16x16x32_bf16 v[80:83], a[208:211], v[192:195], v[80:83]\n"
    "ds_read_b64_tr_b16 a[222:223], v7 offset:43008\n"
    "v_add_f32 v51, v51, v93\n"
    "v_permlane16_swap_b32 v17, v19\n"
    "v_mfma_f32_16x16x32_bf16 v[80:83], a[212:215], v[224:227], v[80:83]\n"
    "v_exp_f32 v94, v94\n"
    "v_mfma_f32_16x16x32_bf16 v[84:87], a[212:215], v[228:231], v[116:119]\n"
    "ds_read_b64_tr_b16 a[224:225], v4 offset:26624\n"
    "v_add_f32 v51, v51, v94\n"
    "v_add_f32 v16, v16, v18\n"
    "v_mfma_f32_16x16x32_bf16 v[84:87], a[208:211], v[196:199], v[84:87]\n"
    "ds_read_b64_tr_b16 a[226:227], v4 offset:32768\n"
    "v_add_f32 v17, v17, v19\n"
    "v_mov_b32 v18, v16\n"
    "v_mfma_f32_16x16x32_bf16 v[84:87], a[204:207], v[164:167], v[84:87]\n"
    "v_exp_f32 v95, v95\n"
    "v_mfma_f32_16x16x32_bf16 v[84:87], a[200:203], v[132:135], v[84:87]\n"
    "ds_read_b64_tr_b16 a[228:229], v4 offset:38912\n"
    "v_cvt_pk_bf16_f32 v221, v94, v95\n"
    "v_mov_b32 v19, v17\n"
    "v_mfma_f32_16x16x32_bf16 v[84:87], a[196:199], v[100:103], v[84:87]\n"
    "s_nop 1\n"
    "ds_read_b64_tr_b16 a[230:231], v4 offset:45056\n"
    "v_add_f32 v51, v51, v95\n"
    "v_permlane32_swap_b32 v16, v18\n"
    "v_mfma_f32_16x16x32_bf16 v[84:87], a[192:195], v[68:71], v[84:87]\n"
    "v_exp_f32 v80, v80\n"
    "v_mfma_f32_16x16x32_bf16 v[88:91], a[192:195], v[72:75], v[120:123]\n"
    "ds_read_b64_tr_b16 a[232:233], v5 offset:26624\n"
    "v_add_f32 v48, v48, v80\n"
    "v_permlane32_swap_b32 v17, v19\n"
    "v_mfma_f32_16x16x32_bf16 v[88:91], a[196:199], v[104:107], v[88:91]\n"
    "ds_read_b64_tr_b16 a[234:235], v5 offset:32768\n"
    "v_add_f32 v16, v16, v18\n"
    "v_add_f32 v17, v17, v19\n"
    "v_mfma_f32_16x16x32_bf16 v[88:91], a[200:203], v[136:139], v[88:91]\n"
    "v_exp_f32 v81, v81\n"
    "v_mfma_f32_16x16x32_bf16 v[88:91], a[204:207], v[168:171], v[88:91]\n"
    "ds_read_b64_tr_b16 a[236:237], v5 offset:38912\n"
    "v_cvt_pk_bf16_f32 v210, v80, v81\n"
    "v_mul_f32 v52, 0x3acccccd, v16\n"
    "v_mfma_f32_16x16x32_bf16 v[88:91], a[208:211], v[200:203], v[88:91]\n"
    "ds_read_b64_tr_b16 a[238:239], v5 offset:45056\n"
    "v_add_f32 v48, v48, v81\n"
    "v_mul_f32 v17, 0x3acccccd, v17\n"
    "v_mfma_f32_16x16x32_bf16 v[88:91], a[212:215], v[232:235], v[88:91]\n"
    "v_exp_f32 v82, v82\n"
    "v_mfma_f32_16x16x32_bf16 v[92:95], a[212:215], v[236:239], v[124:127]\n"
    "v_add_f32 v48, v48, v82\n"
    "v_fma_f32 v17, -v52, v52, v17\n"
    "v_mfma_f32_16x16x32_bf16 v[92:95], a[208:211], v[204:207], v[92:95]\n"
    "v_max_f32 v17, 0, v17\n"
    "v_add_f32 v17, 0x3727c5ac, v17\n"
    "v_mfma_f32_16x16x32_bf16 v[92:95], a[204:207], v[172:175], v[92:95]\n"
    "v_rsq_f32 v53, v17\n"
    "v_exp_f32 v83, v83\n"
    "v_mfma_f32_16x16x32_bf16 v[92:95], a[200:203], v[140:143], v[92:95]\n"
    "s_lshl_b32 s74, s83, 3\n"
    "v_cvt_pk_bf16_f32 v211, v82, v83\n"
    "v_add_f32 v48, v48, v83\n"
    "v_mfma_f32_16x16x32_bf16 v[92:95], a[196:199], v[108:111], v[92:95]\n"
    "s_waitcnt lgkmcnt(0)\n"
    "v_mfma_f32_16x16x32_bf16 v[92:95], a[192:195], v[76:79], v[92:95]\n"
    "s_add_u32 s74, s74, s78\n"
    "v_exp_f32 v84, v84\n"
    "v_mfma_f32_16x16x32_bf16 a[48:51], a[216:219], v[144:147], a[48:51]\n"
    "s_add_u32 m0, s70, 4096\n"
    "ds_read_b128 a[192:195], v2 offset:12288\n"
    "v_add_f32 v49, v49, v84\n"
    "v_mfma_f32_16x16x32_bf16 a[48:51], a[220:223], v[176:179], a[48:51]\n"
    "s_mul_i32 s74, s74, 2560\n"
    "buffer_load_dwordx4 v8, s[36:39], s60 offen offset:0 lds\n"
    "v_mfma_f32_16x16x32_bf16 a[52:55], a[220:223], v[180:183], a[52:55]\n"
    "s_add_u32 s74, s74, 7340032\n"
    "v_exp_f32 v85, v85\n"
    "v_mfma_f32_16x16x32_bf16 a[52:55], a[216:219], v[148:151], a[52:55]\n"
    "ds_read_b128 a[196:199], v3 offset:12288\n"
    "v_cvt_pk_bf16_f32 v214, v84, v85\n"
    "v_mul_f32 v54, v52, v53\n"
    "v_mfma_f32_16x16x32_bf16 a[56:59], a[216:219], v[152:155], a[56:59]\n"
    "v_add_f32 v49, v49, v85\n"
    "v_fma_f32 v26, v28, v53, -v54\n"
    "v_mfma_f32_16x16x32_bf16 a[56:59], a[220:223], v[184:187], a[56:59]\n"
    "v_exp_f32 v86, v86\n"
    "v_mfma_f32_16x16x32_bf16 a[60:63], a[220:223], v[188:191], a[60:63]\n"
    "ds_read_b128 a[200:203], v2 offset:14336\n"
    "v_add_f32 v49, v49, v86\n"
    "v_fma_f32 v27, v29, v53, -v54\n"
    "v_mfma_f32_16x16x32_bf16 a[60:63], a[216:219], v[156:159], a[60:63]\n"
    "buffer_load_dwordx4 v8, s[36:39], s61 offen offset:1024 lds\n"
    "v_cvt_pk_bf16_f32 v16, v26, v27\n"
    "v_fma_f32 v26, v30, v53, -v54\n"
    "v_mfma_f32_16x16x32_bf16 a[76:79], a[224:227], v[156:159], a[76:79]\n"
    "v_exp_f32 v87, v87\n"
    "v_mfma_f32_16x16x32_bf16 a[76:79], a[228:231], v[188:191], a[76:79]\n"
    "ds_read_b128 a[204:207], v3 offset:14336\n"
    "v_cvt_pk_bf16_f32 v215, v86, v87\n"
    "v_fma_f32 v27, v31, v53, -v54\n"
    "v_mfma_f32_16x16x32_bf16 a[72:75], a[228:231], v[184:187], a[72:75]\n"
    "v_add_f32 v49, v49, v87\n"
    "v_cvt_pk_bf16_f32 v17, v26, v27\n"
    "v_mfma_f32_16x16x32_bf16 a[72:75], a[224:227], v[152:155], a[72:75]\n"
    "v_exp_f32 v88, v88\n"
    "v_mfma_f32_16x16x32_bf16 a[68:71], a[224:227], v[148:151], a[68:71]\n"
    "s_add_u32 m0, s70, 24576\n"
    "ds_read_b128 a[208:211], v2 offset:16384\n"
    "v_add_f32 v50, v50, v88\n"
    "v_fma_f32 v26, v32, v53, -v54\n"
    "v_mfma_f32_16x16x32_bf16 a[68:71], a[228:231], v[180:183], a[68:71]\n"
    "buffer_load_dwordx4 v8, s[40:43], s56 offen offset:0 lds\n"
    "v_fma_f32 v27, v33, v53, -v54\n"
    "v_cvt_pk_bf16_f32 v18, v26, v27\n"
    "v_mfma_f32_16x16x32_bf16 a[64:67], a[228:231], v[176:179], a[64:67]\n"
    "v_exp_f32 v89, v89\n"
    "v_mfma_f32_16x16x32_bf16 a[64:67], a[224:227], v[144:147], a[64:67]\n"
    "ds_read_b128 a[212:215], v3 offset:16384\n"
    "v_cvt_pk_bf16_f32 v218, v88, v89\n"
    "v_fma_f32 v26, v34, v53, -v54\n"
    "v_mfma_f32_16x16x32_bf16 a[80:83], a[232:235], v[144:147], a[80:83]\n"
    "v_add_f32 v50, v50, v89\n"
    "v_fma_f32 v27, v35, v53, -v54\n"
    "v_mfma_f32_16x16x32_bf16 a[80:83], a[236:239], v[176:179], a[80:83]\n"
    "v_exp_f32 v90, v90\n"
    "v_mfma_f32_16x16x32_bf16 a[84:87], a[236:239], v[180:183], a[84:87]\n"
    "v_add_f32 v50, v50, v90\n"
    "v_cvt_pk_bf16_f32 v19, v26, v27\n"
    "v_mfma_f32_16x16x32_bf16 a[84:87], a[232:235], v[148:151], a[84:87]\n"
    "buffer_load_dwordx4 v8, s[40:43], s57 offen offset:1024 lds\n"
    "v_fma_f32 v26, v36, v53, -v54\n"
    "v_fma_f32 v27, v37, v53, -v54\n"
    "v_mfma_f32_16x16x32_bf16 a[88:91], a[232:235], v[152:155], a[88:91]\n"
    "v_exp_f32 v91, v91\n"
    "v_mfma_f32_16x16x32_bf16 a[88:91], a[236:239], v[184:187], a[88:91]\n"
    "v_cvt_pk_bf16_f32 v219, v90, v91\n"
    "v_add_f32 v50, v50, v91\n"
    "v_mfma_f32_16x16x32_bf16 a[92:95], a[236:239], v[188:191], a[92:95]\n"
    "s_waitcnt lgkmcnt(0)\n"
    "v_cvt_pk_bf16_f32 v20, v26, v27\n"
    "v_fma_f32 v26, v38, v53, -v54\n"
    "v_mfma_f32_16x16x32_bf16 a[92:95], a[232:235], v[156:159], a[92:95]\n"
    "v_exp_f32 v92, v92\n"
    "v_mfma_f32_16x16x32_bf16 v[80:83], a[192:195], v[64:67], v[112:115]\n"
    "ds_read_b64_tr_b16 a[216:217], v6 offset:26624\n"
    "v_add_f32 v51, v51, v92\n"
    "v_fma_f32 v27, v39, v53, -v54\n"
    "v_mfma_f32_16x16x32_bf16 v[80:83], a[196:199], v[96:99], v[80:83]\n"
    "ds_read_b64_tr_b16 a[218:219], v6 offset:32768\n"
    "v_cvt_pk_bf16_f32 v21, v26, v27\n"
    "v_fma_f32 v26, v40, v53, -v54\n"
    "v_mfma_f32_16x16x32_bf16 v[80:83], a[200:203], v[128:131], v[80:83]\n"
    "v_exp_f32 v93, v93\n"
    "v_mfma_f32_16x16x32_bf16 v[80:83], a[204:207], v[160:163], v[80:83]\n"
    "ds_read_b64_tr_b16 a[220:221], v6 offset:38912\n"
    "v_cvt_pk_bf16_f32 v222, v92, v93\n"
    "v_fma_f32 v27, v41, v53, -v54\n"
    "v_mfma_f32_16x16x32_bf16 v[80:83], a[208:211], v[192:195], v[80:83]\n"
    "ds_read_b64_tr_b16 a[222:223], v6 offset:45056\n"
    "v_add_f32 v51, v51, v93\n"
    "v_cvt_pk_bf16_f32 v22, v26, v27\n"
    "v_mfma_f32_16x16x32_bf16 v[80:83], a[212:215], v[224:227], v[80:83]\n"
    "v_exp_f32 v94, v94\n"
    "v_mfma_f32_16x16x32_bf16 v[84:87], a[212:215], v[228:231], v[116:119]\n"
    "ds_read_b64_tr_b16 a[224:225], v7 offset:26624\n"
    "v_add_f32 v51, v51, v94\n"
    "v_fma_f32 v26, v42, v53, -v54\n"
    "v_mfma_f32_16x16x32_bf16 v[84:87], a[208:211], v[196:199], v[84:87]\n"
    "ds_read_b64_tr_b16 a[226:227], v7 offset:32768\n"
    "v_fma_f32 v27, v43, v53, -v54\n"
    "v_cvt_pk_bf16_f32 v23, v26, v27\n"
    "v_mfma_f32_16x16x32_bf16 v[84:87], a[204:207], v[164:167], v[84:87]\n"
    "v_exp_f32 v95, v95\n"
    "v_mfma_f32_16x16x32_bf16 v[84:87], a[200:203], v[132:135], v[84:87]\n"
    "ds_read_b64_tr_b16 a[228:229], v7 offset:38912\n"
    "v_cvt_pk_bf16_f32 v223, v94, v95\n"
    "v_fma_f32 v26, v44, v53, -v54\n"
    "v_mfma_f32_16x16x32_bf16 v[84:87], a[196:199], v[100:103], v[84:87]\n"
    "ds_read_b64_tr_b16 a[230:231], v7 offset:45056\n"
    "v_add_f32 v51, v51, v95\n"
    "v_fma_f32 v27, v45, v53, -v54\n"
    "v_mfma_f32_16x16x32_bf16 v[84:87], a[192:195], v[68:71], v[84:87]\n"
    "v_exp_f32 v80, v80\n"
    "v_mfma_f32_16x16x32_bf16 v[88:91], a[192:195], v[72:75], v[120:123]\n"
    "ds_read_b64_tr_b16 a[232:233], v4 offset:28672\n"
    "v_add_f32 v48, v48, v80\n"
    "v_cvt_pk_bf16_f32 v24, v26, v27\n"
    "v_mfma_f32_16x16x32_bf16 v[88:91], a[196:199], v[104:107], v[88:91]\n"
    "ds_read_b64_tr_b16 a[234:235], v4 offset:34816\n"
    "v_fma_f32 v26, v46, v53, -v54\n"
    "v_fma_f32 v27, v47, v53, -v54\n"
    "v_mfma_f32_16x16x32_bf16 v[88:91], a[200:203], v[136:139], v[88:91]\n"
    "v_exp_f32 v81, v81\n"
    "v_mfma_f32_16x16x32_bf16 v[88:91], a[204:207], v[168:171], v[88:91]\n"
    "ds_read_b64_tr_b16 a[236:237], v4 offset:40960\n"
    "v_cvt_pk_bf16_f32 v240, v80, v81\n"
    "v_cvt_pk_bf16_f32 v25, v26, v27\n"
    "v_mfma_f32_16x16x32_bf16 v[88:91], a[208:211], v[200:203], v[88:91]\n"
    "ds_read_b64_tr_b16 a[238:239], v4 offset:47104\n"
    "v_add_f32 v48, v48, v81\n"
    "v_lshrrev_b32 v26, 1, v9\n"
    "v_mfma_f32_16x16x32_bf16 v[88:91], a[212:215], v[232:235], v[88:91]\n"
    "v_exp_f32 v82, v82\n"
    "v_mfma_f32_16x16x32_bf16 v[92:95], a[212:215], v[236:239], v[124:127]\n"
    "buffer_store_dwordx4 v[16:19], v9, s[52:55], s74 offen sc1\n"
    "v_add_f32 v48, v48, v82\n"
    "v_mfma_f32_16x16x32_bf16 v[92:95], a[208:211], v[204:207], v[92:95]\n"
    "buffer_store_dwordx4 v[20:23], v9, s[52:55], s74 offen offset:1024 sc1\n"
    "v_mfma_f32_16x16x32_bf16 v[92:95], a[204:207], v[172:175], v[92:95]\n"
    "v_exp_f32 v83, v83\n"
    "v_mfma_f32_16x16x32_bf16 v[92:95], a[200:203], v[140:143], v[92:95]\n"
    "buffer_store_dwordx2 v[24:25], v26, s[52:55], s74 offen offset:2048 sc1\n"
    "v_cvt_pk_bf16_f32 v241, v82, v83\n"
    "v_add_f32 v48, v48, v83\n"
    "v_mfma_f32_16x16x32_bf16 v[92:95], a[196:199], v[108:111], v[92:95]\n"
    "s_waitcnt lgkmcnt(0)\n"
    "v_mfma_f32_16x16x32_bf16 v[92:95], a[192:195], v[76:79], v[92:95]\n"
    "v_exp_f32 v84, v84\n"
    "v_mfma_f32_16x16x32_bf16 a[96:99], a[216:219], v[144:147], a[96:99]\n"
    "s_add_u32 m0, s70, 26624\n"
    "ds_read_b128 a[192:195], v2 offset:18432\n"
    "v_add_f32 v49, v49, v84\n"
    "v_mfma_f32_16x16x32_bf16 a[96:99], a[220:223], v[176:179], a[96:99]\n"
    "buffer_load_dwordx4 v8, s[40:43], s58 offen offset:0 lds\n"
    "v_mfma_f32_16x16x32_bf16 a[100:103], a[220:223], v[180:183], a[100:103]\n"
    "v_exp_f32 v85, v85\n"
    "v_mfma_f32_16x16x32_bf16 a[100:103], a[216:219], v[148:151], a[100:103]\n"
    "ds_read_b128 a[196:199], v3 offset:18432\n"
    "v_cvt_pk_bf16_f32 v244, v84, v85\n"
    "v_mfma_f32_16x16x32_bf16 a[104:107], a[216:219], v[152:155], a[104:107]\n"
    "v_add_f32 v49, v49, v85\n"
    "v_mfma_f32_16x16x32_bf16 a[104:107], a[220:223], v[184:187], a[104:107]\n"
    "v_exp_f32 v86, v86\n"
    "v_mfma_f32_16x16x32_bf16 a[108:111], a[220:223], v[188:191], a[108:111]\n"
    "ds_read_b128 a[200:203], v2 offset:20480\n"
    "v_add_f32 v49, v49, v86\n"
    "v_mfma_f32_16x16x32_bf16 a[108:111], a[216:219], v[156:159], a[108:111]\n"
    "buffer_load_dwordx4 v8, s[40:43], s59 offen offset:1024 lds\n"
    "v_mfma_f32_16x16x32_bf16 a[124:127], a[224:227], v[156:159], a[124:127]\n"
    "v_exp_f32 v87, v87\n"
    "v_mfma_f32_16x16x32_bf16 a[124:127], a[228:231], v[188:191], a[124:127]\n"
    "ds_read_b128 a[204:207], v3 offset:20480\n"
    "v_cvt_pk_bf16_f32 v245, v86, v87\n"
    "v_mfma_f32_16x16x32_bf16 a[120:123], a[228:231], v[184:187], a[120:123]\n"
    "v_add_f32 v49, v49, v87\n"
    "v_mfma_f32_16x16x32_bf16 a[120:123], a[224:227], v[152:155], a[120:123]\n"
    "v_exp_f32 v88, v88\n"
    "v_mfma_f32_16x16x32_bf16 a[116:119], a[224:227], v[148:151], a[116:119]\n"
    "s_add_u32 m0, s70, 28672\n"
    "ds_read_b128 a[208:211], v2 offset:22528\n"
    "v_add_f32 v50, v50, v88\n"
    "v_mfma_f32_16x16x32_bf16 a[116:119], a[228:231], v[180:183], a[116:119]\n"
    "buffer_load_dwordx4 v8, s[40:43], s60 offen offset:0 lds\n"
    "v_mfma_f32_16x16x32_bf16 a[112:115], a[228:231], v[176:179], a[112:115]\n"
    "v_exp_f32 v89, v89\n"
    "v_mfma_f32_16x16x32_bf16 a[112:115], a[224:227], v[144:147], a[112:115]\n"
    "ds_read_b128 a[212:215], v3 offset:22528\n"
    "v_cvt_pk_bf16_f32 v248, v88, v89\n"
    "v_mfma_f32_16x16x32_bf16 a[128:131], a[232:235], v[144:147], a[128:131]\n"
    "v_add_f32 v50, v50, v89\n"
    "v_mfma_f32_16x16x32_bf16 a[128:131], a[236:239], v[176:179], a[128:131]\n"
    "v_exp_f32 v90, v90\n"
    "v_mfma_f32_16x16x32_bf16 a[132:135], a[236:239], v[180:183], a[132:135]\n"
    "v_add_f32 v50, v50, v90\n"
    "v_mfma_f32_16x16x32_bf16 a[132:135], a[232:235], v[148:151], a[132:135]\n"
    "buffer_load_dwordx4 v8, s[40:43], s61 offen offset:1024 lds\n"
    "v_mfma_f32_16x16x32_bf16 a[136:139], a[232:235], v[152:155], a[136:139]\n"
    "v_exp_f32 v91, v91\n"
    "v_mfma_f32_16x16x32_bf16 a[136:139], a[236:239], v[184:187], a[136:139]\n"
    "v_cvt_pk_bf16_f32 v249, v90, v91\n"
    "v_add_f32 v50, v50, v91\n"
    "v_mfma_f32_16x16x32_bf16 a[140:143], a[236:239], v[188:191], a[140:143]\n"
    "s_waitcnt lgkmcnt(0)\n"
    "v_mfma_f32_16x16x32_bf16 a[140:143], a[232:235], v[156:159], a[140:143]\n"
    "v_exp_f32 v92, v92\n"
    "v_mfma_f32_16x16x32_bf16 v[80:83], a[192:195], v[64:67], v[112:115]\n"
    "ds_read_b64_tr_b16 a[216:217], v5 offset:28672\n"
    "v_add_f32 v51, v51, v92\n"
    "v_mfma_f32_16x16x32_bf16 v[80:83], a[196:199], v[96:99], v[80:83]\n"
    "s_add_u32 s70, s69, s68\n"
    "ds_read_b64_tr_b16 a[218:219], v5 offset:34816\n"
    "v_mfma_f32_16x16x32_bf16 v[80:83], a[200:203], v[128:131], v[80:83]\n"
    "s_add_u32 s73, s69, 49152\n"
    "v_exp_f32 v93, v93\n"
    "v_mfma_f32_16x16x32_bf16 v[80:83], a[204:207], v[160:163], v[80:83]\n"
    "s_cmp_eq_u32 s73, 147456\n"
    "ds_read_b64_tr_b16 a[220:221], v5 offset:40960\n"
    "v_cvt_pk_bf16_f32 v252, v92, v93\n"
    "v_mfma_f32_16x16x32_bf16 v[80:83], a[208:211], v[192:195], v[80:83]\n"
    "s_cselect_b32 s73, 0, s73\n"
    "ds_read_b64_tr_b16 a[222:223], v5 offset:47104\n"
    "v_add_f32 v51, v51, v93\n"
    "v_mfma_f32_16x16x32_bf16 v[80:83], a[212:215], v[224:227], v[80:83]\n"
    "s_sub_u32 s71, s73, s69\n"
    "v_exp_f32 v94, v94\n"
    "v_mfma_f32_16x16x32_bf16 v[84:87], a[212:215], v[228:231], v[116:119]\n"
    "s_mov_b32 s69, s73\n"
    "ds_read_b64_tr_b16 a[224:225], v6 offset:28672\n"
    "v_add_f32 v51, v51, v94\n"
    "v_mfma_f32_16x16x32_bf16 v[84:87], a[208:211], v[196:199], v[84:87]\n"
    "ds_read_b64_tr_b16 a[226:227], v6 offset:34816\n"
    "v_add_u32 v8, 24576, v8\n"
    "v_mfma_f32_16x16x32_bf16 v[84:87], a[204:207], v[164:167], v[84:87]\n"
    "v_exp_f32 v95, v95\n"
    "v_mfma_f32_16x16x32_bf16 v[84:87], a[200:203], v[132:135], v[84:87]\n"
    "ds_read_b64_tr_b16 a[228:229], v6 offset:40960\n"
    "v_cvt_pk_bf16_f32 v253, v94, v95\n"
    "v_mfma_f32_16x16x32_bf16 v[84:87], a[196:199], v[100:103], v[84:87]\n"
    "ds_read_b64_tr_b16 a[230:231], v6 offset:47104\n"
    "v_add_f32 v51, v51, v95\n"
    "v_mfma_f32_16x16x32_bf16 v[84:87], a[192:195], v[68:71], v[84:87]\n"
    "v_exp_f32 v80, v80\n"
    "v_mfma_f32_16x16x32_bf16 v[88:91], a[192:195], v[72:75], v[120:123]\n"
    "ds_read_b64_tr_b16 a[232:233], v7 offset:28672\n"
    "v_add_f32 v48, v48, v80\n"
    "v_mfma_f32_16x16x32_bf16 v[88:91], a[196:199], v[104:107], v[88:91]\n"
    "ds_read_b64_tr_b16 a[234:235], v7 offset:34816\n"
    "v_mfma_f32_16x16x32_bf16 v[88:91], a[200:203], v[136:139], v[88:91]\n"
    "v_exp_f32 v81, v81\n"
    "v_mfma_f32_16x16x32_bf16 v[88:91], a[204:207], v[168:171], v[88:91]\n"
    "ds_read_b64_tr_b16 a[236:237], v7 offset:40960\n"
    "v_cvt_pk_bf16_f32 v242, v80, v81\n"
    "v_mfma_f32_16x16x32_bf16 v[88:91], a[208:211], v[200:203], v[88:91]\n"
    "ds_read_b64_tr_b16 a[238:239], v7 offset:47104\n"
    "v_add_f32 v48, v48, v81\n"
    "v_mfma_f32_16x16x32_bf16 v[88:91], a[212:215], v[232:235], v[88:91]\n"
    "v_exp_f32 v82, v82\n"
    "v_mfma_f32_16x16x32_bf16 v[92:95], a[212:215], v[236:239], v[124:127]\n"
    "v_add_f32 v48, v48, v82\n"
    "v_add_u32 v2, s71, v2\n"
    "v_mfma_f32_16x16x32_bf16 v[92:95], a[208:211], v[204:207], v[92:95]\n"
    "v_add_u32 v3, s71, v3\n"
    "v_mfma_f32_16x16x32_bf16 v[92:95], a[204:207], v[172:175], v[92:95]\n"
    "v_exp_f32 v83, v83\n"
    "v_mfma_f32_16x16x32_bf16 v[92:95], a[200:203], v[140:143], v[92:95]\n"
    "v_cvt_pk_bf16_f32 v243, v82, v83\n"
    "v_add_f32 v48, v48, v83\n"
    "v_mfma_f32_16x16x32_bf16 v[92:95], a[196:199], v[108:111], v[92:95]\n"
    "s_waitcnt vmcnt(15) lgkmcnt(0)\n"
    "s_barrier\n"
    "v_mfma_f32_16x16x32_bf16 v[92:95], a[192:195], v[76:79], v[92:95]\n"
    "v_exp_f32 v84, v84\n"
    "v_mfma_f32_16x16x32_bf16 a[144:147], a[216:219], v[144:147], a[144:147]\n"
    "ds_read_b128 a[192:195], v2 offset:0\n"
    "v_add_f32 v49, v49, v84\n"
    "v_mfma_f32_16x16x32_bf16 a[144:147], a[220:223], v[176:179], a[144:147]\n"
    "v_cmp_ne_u64 vcc, s[80:81], v[62:63]\n"
    "s_nop 1\n"
    "s_cbranch_vccnz L_slow3\n"
    "L_back3:\n"
    "v_add_u32 v4, s71, v4\n"
    "v_add_u32 v5, s71, v5\n"
    "v_mfma_f32_16x16x32_bf16 a[148:151], a[220:223], v[180:183], a[148:151]\n"
    "v_exp_f32 v85, v85\n"
    "v_mfma_f32_16x16x32_bf16 a[148:151], a[216:219], v[148:151], a[148:151]\n"
    "ds_read_b128 a[196:199], v3 offset:0\n"
    "v_cvt_pk_bf16_f32 v246, v84, v85\n"
    "v_mfma_f32_16x16x32_bf16 a[152:155], a[216:219], v[152:155], a[152:155]\n"
    "v_add_f32 v49, v49, v85\n"
    "v_add_u32 v6, s71, v6\n"
    "v_mfma_f32_16x16x32_bf16 a[152:155], a[220:223], v[184:187], a[152:155]\n"
    "v_exp_f32 v86, v86\n"
    "v_mfma_f32_16x16x32_bf16 a[156:159], a[220:223], v[188:191], a[156:159]\n"
    "ds_read_b128 a[200:203], v2 offset:2048\n"
    "v_add_f32 v49, v49, v86\n"
    "v_mfma_f32_16x16x32_bf16 a[156:159], a[216:219], v[156:159], a[156:159]\n"
    "v_add_u32 v7, s71, v7\n"
    "v_mfma_f32_16x16x32_bf16 a[172:175], a[224:227], v[156:159], a[172:175]\n"
    "v_exp_f32 v87, v87\n"
    "v_mfma_f32_16x16x32_bf16 a[172:175], a[228:231], v[188:191], a[172:175]\n"
    "ds_read_b128 a[204:207], v3 offset:2048\n"
    "v_cvt_pk_bf16_f32 v247, v86, v87\n"
    "v_mfma_f32_16x16x32_bf16 a[168:171], a[228:231], v[184:187], a[168:171]\n"
    "v_add_f32 v49, v49, v87\n"
    "v_mfma_f32_16x16x32_bf16 a[168:171], a[224:227], v[152:155], a[168:171]\n"
    "v_exp_f32 v88, v88\n"
    "v_mfma_f32_16x16x32_bf16 a[164:167], a[224:227], v[148:151], a[164:167]\n"
    "ds_read_b128 a[208:211], v2 offset:4096\n"
    "v_add_f32 v50, v50, v88\n"
    "v_mfma_f32_16x16x32_bf16 a[164:167], a[228:231], v[180:183], a[164:167]\n"
    "v_mfma_f32_16x16x32_bf16 a[160:163], a[228:231], v[176:179], a[160:163]\n"
    "v_exp_f32 v89, v89\n"
    "v_mfma_f32_16x16x32_bf16 a[160:163], a[224:227], v[144:147], a[160:163]\n"
    "ds_read_b128 a[212:215], v3 offset:4096\n"
    "v_cvt_pk_bf16_f32 v250, v88, v89\n"
    "v_mfma_f32_16x16x32_bf16 a[176:179], a[232:235], v[144:147], a[176:179]\n"
    "v_add_f32 v50, v50, v89\n"
    "v_mfma_f32_16x16x32_bf16 a[176:179], a[236:239], v[176:179], a[176:179]\n"
    "v_exp_f32 v90, v90\n"
    "v_mfma_f32_16x16x32_bf16 a[180:183], a[236:239], v[180:183], a[180:183]\n"
    "v_add_f32 v50, v50, v90\n"
    "v_mfma_f32_16x16x32_bf16 a[180:183], a[232:235], v[148:151], a[180:183]\n"
    "v_mfma_f32_16x16x32_bf16 a[184:187], a[232:235], v[152:155], a[184:187]\n"
    "v_exp_f32 v91, v91\n"
    "v_mfma_f32_16x16x32_bf16 a[184:187], a[236:239], v[184:187], a[184:187]\n"
    "v_cvt_pk_bf16_f32 v251, v90, v91\n"
    "v_add_f32 v50, v50, v91\n"
    "v_mfma_f32_16x16x32_bf16 a[188:191], a[236:239], v[188:191], a[188:191]\n"
    "s_waitcnt lgkmcnt(0)\n"
    "v_mfma_f32_16x16x32_bf16 a[188:191], a[232:235], v[156:159], a[188:191]\n"
    "v_exp_f32 v92, v92\n"
    "L_loopend:\n"
    "v_mfma_f32_16x16x32_bf16 v[80:83], a[192:195], v[64:67], v[112:115]\n"
    "ds_read_b64_tr_b16 a[216:217], v4 offset:24576\n"
    "v_add_f32 v51, v51, v92\n"
    "v_mfma_f32_16x16x32_bf16 v[80:83], a[196:199], v[96:99], v[80:83]\n"
    "s_and_b32 s62, s79, 1\n"
    "s_lshl_b32 s74, s78, 1\n"
    "s_add_u32 s62, s62, s74\n"
    "s_lshr_b32 s63, s79, 1\n"
    "s_lshl_b32 s63, s63, 2\n"
    "s_lshl_b32 s74, s77, 3\n"
    "s_add_u32 s63, s63, s74\n"
    "s_add_u32 s63, s63, s76\n"
    "s_lshl_b32 s63, s63, 1\n"
    "s_mul_i32 s74, s62, 192\n"
    "s_mov_b64 s[44:45], s[18:19]\n"
    "s_add_u32 s44, s44, s74\n"
    "s_addc_u32 s45, s45, 0\n"
    "s_mov_b32 s46, 1966080\n"
    "s_mov_b32 s47, 0x20000\n"
    "s_sub_u32 s46, s46, s74\n"
    "s_mul_i32 s90, s76, 49152\n"
    "s_mul_i32 s64, s76, 3072\n"
    "ds_read_b64_tr_b16 a[218:219], v4 offset:30720\n"
    "v_lshrrev_b32 v16, 4, v9\n"
    "v_add_u32 v16, 0, v16\n"
    "v_mfma_f32_16x16x32_bf16 v[80:83], a[200:203], v[128:131], v[80:83]\n"
    "v_exp_f32 v93, v93\n"
    "v_mfma_f32_16x16x32_bf16 v[80:83], a[204:207], v[160:163], v[80:83]\n"
    "ds_read_b64_tr_b16 a[220:221], v4 offset:36864\n"
    "v_cvt_pk_bf16_f32 v254, v92, v93\n"
    "v_mul_u32_u24 v17, 5462, v16\n"
    "v_mfma_f32_16x16x32_bf16 v[80:83], a[208:211], v[192:195], v[80:83]\n"
    "ds_read_b64_tr_b16 a[222:223], v4 offset:43008\n"
    "v_add_f32 v51, v51, v93\n"
    "v_lshrrev_b32 v17, 16, v17\n"
    "v_mfma_f32_16x16x32_bf16 v[80:83], a[212:215], v[224:227], v[80:83]\n"
    "v_exp_f32 v94, v94\n"
    "v_mfma_f32_16x16x32_bf16 v[84:87], a[212:215], v[228:231], v[116:119]\n"
    "ds_read_b64_tr_b16 a[224:225], v5 offset:24576\n"
    "v_add_f32 v51, v51, v94\n"
    "v_mul_u32_u24 v18, 12, v17\n"
    "v_mfma_f32_16x16x32_bf16 v[84:87], a[208:211], v[196:199], v[84:87]\n"
    "ds_read_b64_tr_b16 a[226:227], v5 offset:30720\n"
    "v_sub_u32 v16, v16, v18\n"
    "v_lshrrev_b32 v18, 1, v17\n"
    "v_mfma_f32_16x16x32_bf16 v[84:87], a[204:207], v[164:167], v[84:87]\n"
    "v_exp_f32 v95, v95\n"
    "v_mfma_f32_16x16x32_bf16 v[84:87], a[200:203], v[132:135], v[84:87]\n"
    "ds_read_b64_tr_b16 a[228:229], v5 offset:36864\n"
    "v_cvt_pk_bf16_f32 v255, v94, v95\n"
    "v_and_b32 v18, 2, v18\n"
    "v_mfma_f32_16x16x32_bf16 v[84:87], a[196:199], v[100:103], v[84:87]\n"
    "ds_read_b64_tr_b16 a[230:231], v5 offset:43008\n"
    "v_add_f32 v51, v51, v95\n"
    "v_xor_b32 v16, v16, v18\n"
    "v_mfma_f32_16x16x32_bf16 v[84:87], a[192:195], v[68:71], v[84:87]\n"
    "v_exp_f32 v80, v80\n"
    "v_mfma_f32_16x16x32_bf16 v[88:91], a[192:195], v[72:75], v[120:123]\n"
    "ds_read_b64_tr_b16 a[232:233], v6 offset:24576\n"
    "v_add_f32 v48, v48, v80\n"
    "v_mul_u32_u24 v17, 3072, v17\n"
    "v_mfma_f32_16x16x32_bf16 v[88:91], a[196:199], v[104:107], v[88:91]\n"
    "ds_read_b64_tr_b16 a[234:235], v6 offset:30720\n"
    "v_lshl_add_u32 v58, v16, 4, v17\n"
    "v_lshrrev_b32 v16, 4, v9\n"
    "v_mfma_f32_16x16x32_bf16 v[88:91], a[200:203], v[136:139], v[88:91]\n"
    "v_exp_f32 v81, v81\n"
    "v_mfma_f32_16x16x32_bf16 v[88:91], a[204:207], v[168:171], v[88:91]\n"
    "ds_read_b64_tr_b16 a[236:237], v6 offset:36864\n"
    "v_cvt_pk_bf16_f32 v144, v80, v81\n"
    "v_add_u32 v16, 64, v16\n"
    "v_mfma_f32_16x16x32_bf16 v[88:91], a[208:211], v[200:203], v[88:91]\n"
    "ds_read_b64_tr_b16 a[238:239], v6 offset:43008\n"
    "v_add_f32 v48, v48, v81\n"
    "v_mul_u32_u24 v17, 5462, v16\n"
    "v_mfma_f32_16x16x32_bf16 v[88:91], a[212:215], v[232:235], v[88:91]\n"
    "v_exp_f32 v82, v82\n"
    "v_mfma_f32_16x16x32_bf16 v[92:95], a[212:215], v[236:239], v[124:127]\n"
    "v_add_f32 v48, v48, v82\n"
    "v_lshrrev_b32 v17, 16, v17\n"
    "v_mfma_f32_16x16x32_bf16 v[92:95], a[208:211], v[204:207], v[92:95]\n"
    "v_mul_u32_u24 v18, 12, v17\n"
    "v_sub_u32 v16, v16, v18\n"
    "v_mfma_f32_16x16x32_bf16 v[92:95], a[204:207], v[172:175], v[92:95]\n"
    "v_exp_f32 v83, v83\n"
    "v_mfma_f32_16x16x32_bf16 v[92:95], a[200:203], v[140:143], v[92:95]\n"
    "v_cvt_pk_bf16_f32 v145, v82, v83\n"
    "v_add_f32 v48, v48, v83\n"
    "v_mfma_f32_16x16x32_bf16 v[92:95], a[196:199], v[108:111], v[92:95]\n"
    "s_waitcnt lgkmcnt(0)\n"
    "v_lshrrev_b32 v18, 1, v17\n"
    "v_and_b32 v18, 2, v18\n"
    "v_mfma_f32_16x16x32_bf16 v[92:95], a[192:195], v[76:79], v[92:95]\n"
    "v_exp_f32 v84, v84\n"
    "v_mfma_f32_16x16x32_bf16 a[0:3], a[216:219], v[208:211], a[0:3]\n"
    "s_add_u32 m0, s70, 24576\n"
    "ds_read_b128 a[192:195], v2 offset:6144\n"
    "v_add_f32 v49, v49, v84\n"
    "v_xor_b32 v16, v16, v18\n"
    "v_mfma_f32_16x16x32_bf16 a[0:3], a[220:223], v[240:243], a[0:3]\n"
    "buffer_load_dwordx4 v8, s[40:43], s56 offen offset:0 lds\n"
    "v_mul_u32_u24 v17, 3072, v17\n"
    "v_lshl_add_u32 v60, v16, 4, v17\n"
    "v_mfma_f32_16x16x32_bf16 a[4:7], a[220:223], v[244:247], a[4:7]\n"
    "v_exp_f32 v85, v85\n"
    "v_mfma_f32_16x16x32_bf16 a[4:7], a[216:219], v[212:215], a[4:7]\n"
    "ds_read_b128 a[196:199], v3 offset:6144\n"
    "v_cvt_pk_bf16_f32 v148, v84, v85\n"
    "v_subrev_u32 v60, 1024, v60\n"
    "v_mfma_f32_16x16x32_bf16 a[8:11], a[216:219], v[216:219], a[8:11]\n"
    "v_add_f32 v49, v49, v85\n"
    "v_lshrrev_b32 v16, 4, v9\n"
    "v_mfma_f32_16x16x32_bf16 a[8:11], a[220:223], v[248:251], a[8:11]\n"
    "v_exp_f32 v86, v86\n"
    "v_mfma_f32_16x16x32_bf16 a[12:15], a[220:223], v[252:255], a[12:15]\n"
    "ds_read_b128 a[200:203], v2 offset:8192\n"
    "v_add_f32 v49, v49, v86\n"
    "v_add_u32 v16, 128, v16\n"
    "v_mfma_f32_16x16x32_bf16 a[12:15], a[216:219], v[220:223], a[12:15]\n"
    "buffer_load_dwordx4 v8, s[40:43], s57 offen offset:1024 lds\n"
    "v_mul_u32_u24 v17, 5462, v16\n"
    "v_lshrrev_b32 v17, 16, v17\n"
    "v_mfma_f32_16x16x32_bf16 a[28:31], a[224:227], v[220:223], a[28:31]\n"
    "v_exp_f32 v87, v87\n"
    "v_mfma_f32_16x16x32_bf16 a[28:31], a[228:231], v[252:255], a[28:31]\n"
    "ds_read_b128 a[204:207], v3 offset:8192\n"
    "v_cvt_pk_bf16_f32 v149, v86, v87\n"
    "v_mul_u32_u24 v18, 12, v17\n"
    "v_mfma_f32_16x16x32_bf16 a[24:27], a[228:231], v[248:251], a[24:27]\n"
    "v_add_f32 v49, v49, v87\n"
    "v_sub_u32 v16, v16, v18\n"
    "v_mfma_f32_16x16x32_bf16 a[24:27], a[224:227], v[216:219], a[24:27]\n"
    "v_exp_f32 v88, v88\n"
    "v_mfma_f32_16x16x32_bf16 a[20:23], a[224:227], v[212:215], a[20:23]\n"
    "s_add_u32 m0, s70, 26624\n"
    "ds_read_b128 a[208:211], v2 offset:10240\n"
    "v_add_f32 v50, v50, v88\n"
    "v_lshrrev_b32 v18, 1, v17\n"
    "v_mfma_f32_16x16x32_bf16 a[20:23], a[228:231], v[244:247], a[20:23]\n"
    "buffer_load_dwordx4 v8, s[40:43], s58 offen offset:0 lds\n"
    "v_and_b32 v18, 2, v18\n"
    "v_xor_b32 v16, v16, v18\n"
    "v_mfma_f32_16x16x32_bf16 a[16:19], a[228:231], v[240:243], a[16:19]\n"
    "v_exp_f32 v89, v89\n"
    "v_mfma_f32_16x16x32_bf16 a[16:19], a[224:227], v[208:211], a[16:19]\n"
    "ds_read_b128 a[212:215], v3 offset:10240\n"
    "v_cvt_pk_bf16_f32 v152, v88, v89\n"
    "v_mul_u32_u24 v17, 3072, v17\n"
    "v_mfma_f32_16x16x32_bf16 a[32:35], a[232:235], v[208:211], a[32:35]\n"
    "v_add_f32 v50, v50, v89\n"
    "v_lshl_add_u32 v61, v16, 4, v17\n"
    "v_mfma_f32_16x16x32_bf16 a[32:35], a[236:239], v[240:243], a[32:35]\n"
    "v_exp_f32 v90, v90\n"
    "v_mfma_f32_16x16x32_bf16 a[36:39], a[236:239], v[244:247], a[36:39]\n"
    "v_add_f32 v50, v50, v90\n"
    "v_subrev_u32 v61, 2048, v61\n"
    "v_mfma_f32_16x16x32_bf16 a[36:39], a[232:235], v[212:215], a[36:39]\n"
    "buffer_load_dwordx4 v8, s[40:43], s59 offen offset:1024 lds\n"
    "v_mfma_f32_16x16x32_bf16 a[40:43], a[232:235], v[216:219], a[40:43]\n"
    "v_exp_f32 v91, v91\n"
    "v_mfma_f32_16x16x32_bf16 a[40:43], a[236:239], v[248:251], a[40:43]\n"
    "v_cvt_pk_bf16_f32 v153, v90, v91\n"
    "v_add_f32 v50, v50, v91\n"
    "v_mfma_f32_16x16x32_bf16 a[44:47], a[236:239], v[252:255], a[44:47]\n"
    "s_waitcnt lgkmcnt(0)\n"
    "v_mfma_f32_16x16x32_bf16 a[44:47], a[232:235], v[220:223], a[44:47]\n"
    "v_exp_f32 v92, v92\n"
    "v_mfma_f32_16x16x32_bf16 v[80:83], a[192:195], v[64:67], v[112:115]\n"
    "ds_read_b64_tr_b16 a[216:217], v7 offset:24576\n"
    "v_add_f32 v51, v51, v92\n"
    "v_mfma_f32_16x16x32_bf16 v[80:83], a[196:199], v[96:99], v[80:83]\n"
    "ds_read_b64_tr_b16 a[218:219], v7 offset:30720\n"
    "v_mfma_f32_16x16x32_bf16 v[80:83], a[200:203], v[128:131], v[80:83]\n"
    "v_exp_f32 v93, v93\n"
    "v_mfma_f32_16x16x32_bf16 v[80:83], a[204:207], v[160:163], v[80:83]\n"
    "ds_read_b64_tr_b16 a[220:221], v7 offset:36864\n"
    "v_cvt_pk_bf16_f32 v156, v92, v93\n"
    "v_mfma_f32_16x16x32_bf16 v[80:83], a[208:211], v[192:195], v[80:83]\n"
    "ds_read_b64_tr_b16 a[222:223], v7 offset:43008\n"
    "v_add_f32 v51, v51, v93\n"
    "v_mfma_f32_16x16x32_bf16 v[80:83], a[212:215], v[224:227], v[80:83]\n"
    "v_exp_f32 v94, v94\n"
    "v_mfma_f32_16x16x32_bf16 v[84:87], a[212:215], v[228:231], v[116:119]\n"
    "ds_read_b64_tr_b16 a[224:225], v4 offset:26624\n"
    "v_add_f32 v51, v51, v94\n"
    "v_mfma_f32_16x16x32_bf16 v[84:87], a[208:211], v[196:199], v[84:87]\n"
    "ds_read_b64_tr_b16 a[226:227], v4 offset:32768\n"
    "v_mfma_f32_16x16x32_bf16 v[84:87], a[204:207], v[164:167], v[84:87]\n"
    "v_exp_f32 v95, v95\n"
    "v_mfma_f32_16x16x32_bf16 v[84:87], a[200:203], v[132:135], v[84:87]\n"
    "ds_read_b64_tr_b16 a[228:229], v4 offset:38912\n"
    "v_cvt_pk_bf16_f32 v157, v94, v95\n"
    "v_mfma_f32_16x16x32_bf16 v[84:87], a[196:199], v[100:103], v[84:87]\n"
    "ds_read_b64_tr_b16 a[230:231], v4 offset:45056\n"
    "v_add_f32 v51, v51, v95\n"
    "v_mfma_f32_16x16x32_bf16 v[84:87], a[192:195], v[68:71], v[84:87]\n"
    "v_exp_f32 v80, v80\n"
    "v_mfma_f32_16x16x32_bf16 v[88:91], a[192:195], v[72:75], v[120:123]\n"
    "ds_read_b64_tr_b16 a[232:233], v5 offset:26624\n"
    "v_add_f32 v48, v48, v80\n"
    "v_mfma_f32_16x16x32_bf16 v[88:91], a[196:199], v[104:107], v[88:91]\n"
    "ds_read_b64_tr_b16 a[234:235], v5 offset:32768\n"
    "v_mfma_f32_16x16x32_bf16 v[88:91], a[200:203], v[136:139], v[88:91]\n"
    "v_exp_f32 v81, v81\n"
    "v_mfma_f32_16x16x32_bf16 v[88:91], a[204:207], v[168:171], v[88:91]\n"
    "ds_read_b64_tr_b16 a[236:237], v5 offset:38912\n"
    "v_cvt_pk_bf16_f32 v146, v80, v81\n"
    "v_mfma_f32_16x16x32_bf16 v[88:91], a[208:211], v[200:203], v[88:91]\n"
    "ds_read_b64_tr_b16 a[238:239], v5 offset:45056\n"
    "v_add_f32 v48, v48, v81\n"
    "v_mfma_f32_16x16x32_bf16 v[88:91], a[212:215], v[232:235], v[88:91]\n"
    "v_exp_f32 v82, v82\n"
    "v_mfma_f32_16x16x32_bf16 v[92:95], a[212:215], v[236:239], v[124:127]\n"
    "v_add_f32 v48, v48, v82\n"
    "v_mfma_f32_16x16x32_bf16 v[92:95], a[208:211], v[204:207], v[92:95]\n"
    "v_mfma_f32_16x16x32_bf16 v[92:95], a[204:207], v[172:175], v[92:95]\n"
    "v_exp_f32 v83, v83\n"
    "v_mfma_f32_16x16x32_bf16 v[92:95], a[200:203], v[140:143], v[92:95]\n"
    "v_cvt_pk_bf16_f32 v147, v82, v83\n"
    "v_add_f32 v48, v48, v83\n"
    "v_mfma_f32_16x16x32_bf16 v[92:95], a[196:199], v[108:111], v[92:95]\n"
    "s_waitcnt lgkmcnt(0)\n"
    "v_mfma_f32_16x16x32_bf16 v[92:95], a[192:195], v[76:79], v[92:95]\n"
    "v_exp_f32 v84, v84\n"
    "v_mfma_f32_16x16x32_bf16 a[48:51], a[216:219], v[208:211], a[48:51]\n"
    "s_add_u32 m0, s70, 28672\n"
    "ds_read_b128 a[192:195], v2 offset:12288\n"
    "v_add_f32 v49, v49, v84\n"
    "v_mfma_f32_16x16x32_bf16 a[48:51], a[220:223], v[240:243], a[48:51]\n"
    "buffer_load_dwordx4 v8, s[40:43], s60 offen offset:0 lds\n"
    "v_mfma_f32_16x16x32_bf16 a[52:55], a[220:223], v[244:247], a[52:55]\n"
    "v_exp_f32 v85, v85\n"
    "v_mfma_f32_16x16x32_bf16 a[52:55], a[216:219], v[212:215], a[52:55]\n"
    "ds_read_b128 a[196:199], v3 offset:12288\n"
    "v_cvt_pk_bf16_f32 v150, v84, v85\n"
    "v_mfma_f32_16x16x32_bf16 a[56:59], a[216:219], v[216:219], a[56:59]\n"
    "v_add_f32 v49, v49, v85\n"
    "v_mfma_f32_16x16x32_bf16 a[56:59], a[220:223], v[248:251], a[56:59]\n"
    "v_exp_f32 v86, v86\n"
    "v_mfma_f32_16x16x32_bf16 a[60:63], a[220:223], v[252:255], a[60:63]\n"
    "ds_read_b128 a[200:203], v2 offset:14336\n"
    "v_add_f32 v49, v49, v86\n"
    "v_mfma_f32_16x16x32_bf16 a[60:63], a[216:219], v[220:223], a[60:63]\n"
    "buffer_load_dwordx4 v8, s[40:43], s61 offen offset:1024 lds\n"
    "v_mfma_f32_16x16x32_bf16 a[76:79], a[224:227], v[220:223], a[76:79]\n"
    "v_exp_f32 v87, v87\n"
    "v_mfma_f32_16x16x32_bf16 a[76:79], a[228:231], v[252:255], a[76:79]\n"
    "ds_read_b128 a[204:207], v3 offset:14336\n"
    "v_cvt_pk_bf16_f32 v151, v86, v87\n"
    "v_mfma_f32_16x16x32_bf16 a[72:75], a[228:231], v[248:251], a[72:75]\n"
    "v_add_f32 v49, v49, v87\n"
    "v_mfma_f32_16x16x32_bf16 a[72:75], a[224:227], v[216:219], a[72:75]\n"
    "v_exp_f32 v88, v88\n"
    "v_mfma_f32_16x16x32_bf16 a[68:71], a[224:227], v[212:215], a[68:71]\n"
    "s_add_u32 m0, s64, 0\n"
    "ds_read_b128 a[208:211], v2 offset:16384\n"
    "v_add_f32 v50, v50, v88\n"
    "v_mfma_f32_16x16x32_bf16 a[68:71], a[228:231], v[244:247], a[68:71]\n"
    "buffer_load_dwordx4 v58, s[44:47], s90 offen offset:0 lds\n"
    "v_mfma_f32_16x16x32_bf16 a[64:67], a[228:231], v[240:243], a[64:67]\n"
    "v_exp_f32 v89, v89\n"
    "v_mfma_f32_16x16x32_bf16 a[64:67], a[224:227], v[208:211], a[64:67]\n"
    "ds_read_b128 a[212:215], v3 offset:16384\n"
    "v_cvt_pk_bf16_f32 v154, v88, v89\n"
    "v_mfma_f32_16x16x32_bf16 a[80:83], a[232:235], v[208:211], a[80:83]\n"
    "v_add_f32 v50, v50, v89\n"
    "v_mfma_f32_16x16x32_bf16 a[80:83], a[236:239], v[240:243], a[80:83]\n"
    "v_exp_f32 v90, v90\n"
    "v_mfma_f32_16x16x32_bf16 a[84:87], a[236:239], v[244:247], a[84:87]\n"
    "v_add_f32 v50, v50, v90\n"
    "v_mfma_f32_16x16x32_bf16 a[84:87], a[232:235], v[212:215], a[84:87]\n"
    "buffer_load_dwordx4 v60, s[44:47], s90 offen offset:1024 lds\n"
    "v_mfma_f32_16x16x32_bf16 a[88:91], a[232:235], v[216:219], a[88:91]\n"
    "v_exp_f32 v91, v91\n"
    "v_mfma_f32_16x16x32_bf16 a[88:91], a[236:239], v[248:251], a[88:91]\n"
    "v_cvt_pk_bf16_f32 v155, v90, v91\n"
    "v_add_f32 v50, v50, v91\n"
    "v_mfma_f32_16x16x32_bf16 a[92:95], a[236:239], v[252:255], a[92:95]\n"
    "s_waitcnt lgkmcnt(0)\n"
    "v_mfma_f32_16x16x32_bf16 a[92:95], a[232:235], v[220:223], a[92:95]\n"
    "v_exp_f32 v92, v92\n"
    "v_mfma_f32_16x16x32_bf16 v[80:83], a[192:195], v[64:67], v[112:115]\n"
    "ds_read_b64_tr_b16 a[216:217], v6 offset:26624\n"
    "v_add_f32 v51, v51, v92\n"
    "v_mfma_f32_16x16x32_bf16 v[80:83], a[196:199], v[96:99], v[80:83]\n"
    "ds_read_b64_tr_b16 a[218:219], v6 offset:32768\n"
    "v_mfma_f32_16x16x32_bf16 v[80:83], a[200:203], v[128:131], v[80:83]\n"
    "v_exp_f32 v93, v93\n"
    "v_mfma_f32_16x16x32_bf16 v[80:83], a[204:207], v[160:163], v[80:83]\n"
    "ds_read_b64_tr_b16 a[220:221], v6 offset:38912\n"
    "v_cvt_pk_bf16_f32 v158, v92, v93\n"
    "v_mfma_f32_16x16x32_bf16 v[80:83], a[208:211], v[192:195], v[80:83]\n"
    "ds_read_b64_tr_b16 a[222:223], v6 offset:45056\n"
    "v_add_f32 v51, v51, v93\n"
    "v_mfma_f32_16x16x32_bf16 v[80:83], a[212:215], v[224:227], v[80:83]\n"
    "v_exp_f32 v94, v94\n"
    "v_mfma_f32_16x16x32_bf16 v[84:87], a[212:215], v[228:231], v[116:119]\n"
    "ds_read_b64_tr_b16 a[224:225], v7 offset:26624\n"
    "v_add_f32 v51, v51, v94\n"
    "v_mfma_f32_16x16x32_bf16 v[84:87], a[208:211], v[196:199], v[84:87]\n"
    "ds_read_b64_tr_b16 a[226:227], v7 offset:32768\n"
    "v_mfma_f32_16x16x32_bf16 v[84:87], a[204:207], v[164:167], v[84:87]\n"
    "v_exp_f32 v95, v95\n"
    "v_mfma_f32_16x16x32_bf16 v[84:87], a[200:203], v[132:135], v[84:87]\n"
    "ds_read_b64_tr_b16 a[228:229], v7 offset:38912\n"
    "v_cvt_pk_bf16_f32 v159, v94, v95\n"
    "v_mfma_f32_16x16x32_bf16 v[84:87], a[196:199], v[100:103], v[84:87]\n"
    "ds_read_b64_tr_b16 a[230:231], v7 offset:45056\n"
    "v_add_f32 v51, v51, v95\n"
    "v_mfma_f32_16x16x32_bf16 v[84:87], a[192:195], v[68:71], v[84:87]\n"
    "v_exp_f32 v80, v80\n"
    "v_mfma_f32_16x16x32_bf16 v[88:91], a[192:195], v[72:75], v[120:123]\n"
    "ds_read_b64_tr_b16 a[232:233], v4 offset:28672\n"
    "v_add_f32 v48, v48, v80\n"
    "v_mfma_f32_16x16x32_bf16 v[88:91], a[196:199], v[104:107], v[88:91]\n"
    "ds_read_b64_tr_b16 a[234:235], v4 offset:34816\n"
    "v_mfma_f32_16x16x32_bf16 v[88:91], a[200:203], v[136:139], v[88:91]\n"
    "v_exp_f32 v81, v81\n"
    "v_mfma_f32_16x16x32_bf16 v[88:91], a[204:207], v[168:171], v[88:91]\n"
    "ds_read_b64_tr_b16 a[236:237], v4 offset:40960\n"
    "v_cvt_pk_bf16_f32 v176, v80, v81\n"
    "v_mfma_f32_16x16x32_bf16 v[88:91], a[208:211], v[200:203], v[88:91]\n"
    "ds_read_b64_tr_b16 a[238:239], v4 offset:47104\n"
    "v_add_f32 v48, v48, v81\n"
    "v_mfma_f32_16x16x32_bf16 v[88:91], a[212:215], v[232:235], v[88:91]\n"
    "v_exp_f32 v82, v82\n"
    "v_mfma_f32_16x16x32_bf16 v[92:95], a[212:215], v[236:239], v[124:127]\n"
    "v_add_f32 v48, v48, v82\n"
    "v_mfma_f32_16x16x32_bf16 v[92:95], a[208:211], v[204:207], v[92:95]\n"
    "v_mfma_f32_16x16x32_bf16 v[92:95], a[204:207], v[172:175], v[92:95]\n"
    "v_exp_f32 v83, v83\n"
    "v_mfma_f32_16x16x32_bf16 v[92:95], a[200:203], v[140:143], v[92:95]\n"
    "v_cvt_pk_bf16_f32 v177, v82, v83\n"
    "v_add_f32 v48, v48, v83\n"
    "v_mfma_f32_16x16x32_bf16 v[92:95], a[196:199], v[108:111], v[92:95]\n"
    "s_waitcnt lgkmcnt(0)\n"
    "v_mfma_f32_16x16x32_bf16 v[92:95], a[192:195], v[76:79], v[92:95]\n"
    "v_exp_f32 v84, v84\n"
    "v_mfma_f32_16x16x32_bf16 a[96:99], a[216:219], v[208:211], a[96:99]\n"
    "ds_read_b128 a[192:195], v2 offset:18432\n"
    "v_add_f32 v49, v49, v84\n"
    "v_mfma_f32_16x16x32_bf16 a[96:99], a[220:223], v[240:243], a[96:99]\n"
    "buffer_load_dwordx4 v61, s[44:47], s90 offen offset:2048 lds\n"
    "v_mfma_f32_16x16x32_bf16 a[100:103], a[220:223], v[244:247], a[100:103]\n"
    "v_exp_f32 v85, v85\n"
    "v_mfma_f32_16x16x32_bf16 a[100:103], a[216:219], v[212:215], a[100:103]\n"
    "ds_read_b128 a[196:199], v3 offset:18432\n"
    "v_cvt_pk_bf16_f32 v180, v84, v85\n"
    "v_mfma_f32_16x16x32_bf16 a[104:107], a[216:219], v[216:219], a[104:107]\n"
    "v_add_f32 v49, v49, v85\n"
    "v_mfma_f32_16x16x32_bf16 a[104:107], a[220:223], v[248:251], a[104:107]\n"
    "s_add_u32 s90, s90, 196608\n"
    "v_exp_f32 v86, v86\n"
    "v_mfma_f32_16x16x32_bf16 a[108:111], a[220:223], v[252:255], a[108:111]\n"
    "s_add_u32 m0, s64, 12288\n"
    "ds_read_b128 a[200:203], v2 offset:20480\n"
    "v_add_f32 v49, v49, v86\n"
    "v_mfma_f32_16x16x32_bf16 a[108:111], a[216:219], v[220:223], a[108:111]\n"
    "buffer_load_dwordx4 v58, s[44:47], s90 offen offset:0 lds\n"
    "v_mfma_f32_16x16x32_bf16 a[124:127], a[224:227], v[220:223], a[124:127]\n"
    "v_exp_f32 v87, v87\n"
    "v_mfma_f32_16x16x32_bf16 a[124:127], a[228:231], v[252:255], a[124:127]\n"
    "ds_read_b128 a[204:207], v3 offset:20480\n"
    "v_cvt_pk_bf16_f32 v181, v86, v87\n"
    "v_mfma_f32_16x16x32_bf16 a[120:123], a[228:231], v[248:251], a[120:123]\n"
    "v_add_f32 v49, v49, v87\n"
    "v_mfma_f32_16x16x32_bf16 a[120:123], a[224:227], v[216:219], a[120:123]\n"
    "v_exp_f32 v88, v88\n"
    "v_mfma_f32_16x16x32_bf16 a[116:119], a[224:227], v[212:215], a[116:119]\n"
    "ds_read_b128 a[208:211], v2 offset:22528\n"
    "v_add_f32 v50, v50, v88\n"
    "v_mfma_f32_16x16x32_bf16 a[116:119], a[228:231], v[244:247], a[116:119]\n"
    "buffer_load_dwordx4 v60, s[44:47], s90 offen offset:1024 lds\n"
    "v_mfma_f32_16x16x32_bf16 a[112:115], a[228:231], v[240:243], a[112:115]\n"
    "v_exp_f32 v89, v89\n"
    "v_mfma_f32_16x16x32_bf16 a[112:115], a[224:227], v[208:211], a[112:115]\n"
    "ds_read_b128 a[212:215], v3 offset:22528\n"
    "v_cvt_pk_bf16_f32 v184, v88, v89\n"
    "v_mfma_f32_16x16x32_bf16 a[128:131], a[232:235], v[208:211], a[128:131]\n"
    "v_add_f32 v50, v50, v89\n"
    "v_mfma_f32_16x16x32_bf16 a[128:131], a[236:239], v[240:243], a[128:131]\n"
    "v_exp_f32 v90, v90\n"
    "v_mfma_f32_16x16x32_bf16 a[132:135], a[236:239], v[244:247], a[132:135]\n"
    "v_add_f32 v50, v50, v90\n"
    "v_mfma_f32_16x16x32_bf16 a[132:135], a[232:235], v[212:215], a[132:135]\n"
    "buffer_load_dwordx4 v61, s[44:47], s90 offen offset:2048 lds\n"
    "v_mfma_f32_16x16x32_bf16 a[136:139], a[232:235], v[216:219], a[136:139]\n"
    "v_exp_f32 v91, v91\n"
    "v_mfma_f32_16x16x32_bf16 a[136:139], a[236:239], v[248:251], a[136:139]\n"
    "v_cvt_pk_bf16_f32 v185, v90, v91\n"
    "v_add_f32 v50, v50, v91\n"
    "v_mfma_f32_16x16x32_bf16 a[140:143], a[236:239], v[252:255], a[140:143]\n"
    "s_waitcnt lgkmcnt(0)\n"
    "v_mfma_f32_16x16x32_bf16 a[140:143], a[232:235], v[220:223], a[140:143]\n"
    "v_exp_f32 v92, v92\n"
    "v_mfma_f32_16x16x32_bf16 v[80:83], a[192:195], v[64:67], v[112:115]\n"
    "ds_read_b64_tr_b16 a[216:217], v5 offset:28672\n"
    "v_add_f32 v51, v51, v92\n"
    "v_mfma_f32_16x16x32_bf16 v[80:83], a[196:199], v[96:99], v[80:83]\n"
    "s_add_u32 s70, s69, s68\n"
    "ds_read_b64_tr_b16 a[218:219], v5 offset:34816\n"
    "v_mfma_f32_16x16x32_bf16 v[80:83], a[200:203], v[128:131], v[80:83]\n"
    "s_add_u32 s73, s69, 49152\n"
    "v_exp_f32 v93, v93\n"
    "v_mfma_f32_16x16x32_bf16 v[80:83], a[204:207], v[160:163], v[80:83]\n"
    "s_cmp_eq_u32 s73, 147456\n"
    "ds_read_b64_tr_b16 a[220:221], v5 offset:40960\n"
    "v_cvt_pk_bf16_f32 v188, v92, v93\n"
    "v_mfma_f32_16x16x32_bf16 v[80:83], a[208:211], v[192:195], v[80:83]\n"
    "s_cselect_b32 s73, 0, s73\n"
    "ds_read_b64_tr_b16 a[222:223], v5 offset:47104\n"
    "v_add_f32 v51, v51, v93\n"
    "v_mfma_f32_16x16x32_bf16 v[80:83], a[212:215], v[224:227], v[80:83]\n"
    "s_sub_u32 s71, s73, s69\n"
    "v_exp_f32 v94, v94\n"
    "v_mfma_f32_16x16x32_bf16 v[84:87], a[212:215], v[228:231], v[116:119]\n"
    "s_mov_b32 s69, s73\n"
    "ds_read_b64_tr_b16 a[224:225], v6 offset:28672\n"
    "v_add_f32 v51, v51, v94\n"
    "v_mfma_f32_16x16x32_bf16 v[84:87], a[208:211], v[196:199], v[84:87]\n"
    "ds_read_b64_tr_b16 a[226:227], v6 offset:34816\n"
    "v_add_u32 v8, 24576, v8\n"
    "v_mfma_f32_16x16x32_bf16 v[84:87], a[204:207], v[164:167], v[84:87]\n"
    "v_exp_f32 v95, v95\n"
    "v_mfma_f32_16x16x32_bf16 v[84:87], a[200:203], v[132:135], v[84:87]\n"
    "ds_read_b64_tr_b16 a[228:229], v6 offset:40960\n"
    "v_cvt_pk_bf16_f32 v189, v94, v95\n"
    "v_mfma_f32_16x16x32_bf16 v[84:87], a[196:199], v[100:103], v[84:87]\n"
    "ds_read_b64_tr_b16 a[230:231], v6 offset:47104\n"
    "v_add_f32 v51, v51, v95\n"
    "v_mfma_f32_16x16x32_bf16 v[84:87], a[192:195], v[68:71], v[84:87]\n"
    "v_exp_f32 v80, v80\n"
    "v_mfma_f32_16x16x32_bf16 v[88:91], a[192:195], v[72:75], v[120:123]\n"
    "ds_read_b64_tr_b16 a[232:233], v7 offset:28672\n"
    "v_add_f32 v48, v48, v80\n"
    "v_mfma_f32_16x16x32_bf16 v[88:91], a[196:199], v[104:107], v[88:91]\n"
    "ds_read_b64_tr_b16 a[234:235], v7 offset:34816\n"
    "v_mfma_f32_16x16x32_bf16 v[88:91], a[200:203], v[136:139], v[88:91]\n"
    "v_exp_f32 v81, v81\n"
    "v_mfma_f32_16x16x32_bf16 v[88:91], a[204:207], v[168:171], v[88:91]\n"
    "ds_read_b64_tr_b16 a[236:237], v7 offset:40960\n"
    "v_cvt_pk_bf16_f32 v178, v80, v81\n"
    "v_mfma_f32_16x16x32_bf16 v[88:91], a[208:211], v[200:203], v[88:91]\n"
    "ds_read_b64_tr_b16 a[238:239], v7 offset:47104\n"
    "v_add_f32 v48, v48, v81\n"
    "v_mfma_f32_16x16x32_bf16 v[88:91], a[212:215], v[232:235], v[88:91]\n"
    "v_exp_f32 v82, v82\n"
    "v_mfma_f32_16x16x32_bf16 v[92:95], a[212:215], v[236:239], v[124:127]\n"
    "v_add_f32 v48, v48, v82\n"
    "v_add_u32 v2, s71, v2\n"
    "v_mfma_f32_16x16x32_bf16 v[92:95], a[208:211], v[204:207], v[92:95]\n"
    "v_add_u32 v3, s71, v3\n"
    "v_mfma_f32_16x16x32_bf16 v[92:95], a[204:207], v[172:175], v[92:95]\n"
    "v_exp_f32 v83, v83\n"
    "v_mfma_f32_16x16x32_bf16 v[92:95], a[200:203], v[140:143], v[92:95]\n"
    "v_cvt_pk_bf16_f32 v179, v82, v83\n"
    "v_add_f32 v48, v48, v83\n"
    "v_mfma_f32_16x16x32_bf16 v[92:95], a[196:199], v[108:111], v[92:95]\n"
    "s_waitcnt vmcnt(12) lgkmcnt(0)\n"
    "s_barrier\n"
    "v_mfma_f32_16x16x32_bf16 v[92:95], a[192:195], v[76:79], v[92:95]\n"
    "v_exp_f32 v84, v84\n"
    "v_mfma_f32_16x16x32_bf16 a[144:147], a[216:219], v[208:211], a[144:147]\n"
    "s_lshl_b32 s74, s82, 3\n"
    "ds_read_b128 a[192:195], v2 offset:0\n"
    "v_add_f32 v49, v49, v84\n"
    "v_mfma_f32_16x16x32_bf16 a[144:147], a[220:223], v[240:243], a[144:147]\n"
    "s_add_u32 s74, s74, s78\n"
    "v_add_u32 v4, s71, v4\n"
    "v_add_u32 v5, s71, v5\n"
    "v_mfma_f32_16x16x32_bf16 a[148:151], a[220:223], v[244:247], a[148:151]\n"
    "s_lshl_b32 s95, s74, 6\n"
    "v_exp_f32 v85, v85\n"
    "v_mfma_f32_16x16x32_bf16 a[148:151], a[216:219], v[212:215], a[148:151]\n"
    "s_add_u32 s95, s95, 53248\n"
    "ds_read_b128 a[196:199], v3 offset:0\n"
    "v_cvt_pk_bf16_f32 v182, v84, v85\n"
    "v_mfma_f32_16x16x32_bf16 a[152:155], a[216:219], v[216:219], a[152:155]\n"
    "v_add_f32 v49, v49, v85\n"
    "v_add_u32 v6, s71, v6\n"
    "v_mfma_f32_16x16x32_bf16 a[152:155], a[220:223], v[248:251], a[152:155]\n"
    "v_exp_f32 v86, v86\n"
    "v_mfma_f32_16x16x32_bf16 a[156:159], a[220:223], v[252:255], a[156:159]\n"
    "ds_read_b128 a[200:203], v2 offset:2048\n"
    "v_add_f32 v49, v49, v86\n"
    "v_mfma_f32_16x16x32_bf16 a[156:159], a[216:219], v[220:223], a[156:159]\n"
    "v_add_u32 v7, s71, v7\n"
    "v_mov_b32 v62, s80\n"
    "v_mfma_f32_16x16x32_bf16 a[172:175], a[224:227], v[220:223], a[172:175]\n"
    "v_exp_f32 v87, v87\n"
    "v_mfma_f32_16x16x32_bf16 a[172:175], a[228:231], v[252:255], a[172:175]\n"
    "ds_read_b128 a[204:207], v3 offset:2048\n"
    "v_cvt_pk_bf16_f32 v183, v86, v87\n"
    "v_mfma_f32_16x16x32_bf16 a[168:171], a[228:231], v[248:251], a[168:171]\n"
    "v_add_f32 v49, v49, v87\n"
    "v_mov_b32 v63, s81\n"
    "v_mfma_f32_16x16x32_bf16 a[168:171], a[224:227], v[216:219], a[168:171]\n"
    "v_exp_f32 v88, v88\n"
    "v_mfma_f32_16x16x32_bf16 a[164:167], a[224:227], v[212:215], a[164:167]\n"
    "ds_read_b128 a[208:211], v2 offset:4096\n"
    "v_add_f32 v50, v50, v88\n"
    "v_mfma_f32_16x16x32_bf16 a[164:167], a[228:231], v[244:247], a[164:167]\n"
    "buffer_store_dwordx2 v[62:63], v10, s[52:55], s95 offen sc1\n"
    "v_mfma_f32_16x16x32_bf16 a[160:163], a[228:231], v[240:243], a[160:163]\n"
    "v_exp_f32 v89, v89\n"
    "v_mfma_f32_16x16x32_bf16 a[160:163], a[224:227], v[208:211], a[160:163]\n"
    "ds_read_b128 a[212:215], v3 offset:4096\n"
    "v_cvt_pk_bf16_f32 v186, v88, v89\n"
    "v_mfma_f32_16x16x32_bf16 a[176:179], a[232:235], v[208:211], a[176:179]\n"
    "v_add_f32 v50, v50, v89\n"
    "v_mfma_f32_16x16x32_bf16 a[176:179], a[236:239], v[240:243], a[176:179]\n"
    "v_exp_f32 v90, v90\n"
    "v_mfma_f32_16x16x32_bf16 a[180:183], a[236:239], v[244:247], a[180:183]\n"
    "v_add_f32 v50, v50, v90\n"
    "v_mfma_f32_16x16x32_bf16 a[180:183], a[232:235], v[212:215], a[180:183]\n"
    "v_mfma_f32_16x16x32_bf16 a[184:187], a[232:235], v[216:219], a[184:187]\n"
    "v_exp_f32 v91, v91\n"
    "v_mfma_f32_16x16x32_bf16 a[184:187], a[236:239], v[248:251], a[184:187]\n"
    "v_cvt_pk_bf16_f32 v187, v90, v91\n"
    "v_add_f32 v50, v50, v91\n"
    "v_mfma_f32_16x16x32_bf16 a[188:191], a[236:239], v[252:255], a[188:191]\n"
    "s_waitcnt lgkmcnt(0)\n"
    "v_mfma_f32_16x16x32_bf16 a[188:191], a[232:235], v[220:223], a[188:191]\n"
    "v_exp_f32 v92, v92\n"
    "v_mfma_f32_16x16x32_bf16 v[80:83], a[192:195], v[64:67], v[112:115]\n"
    "ds_read_b64_tr_b16 a[216:217], v4 offset:24576\n"
    "v_add_f32 v51, v51, v92\n"
    "v_mfma_f32_16x16x32_bf16 v[80:83], a[196:199], v[96:99], v[80:83]\n"
    "ds_read_b64_tr_b16 a[218:219], v4 offset:30720\n"
    "v_mfma_f32_16x16x32_bf16 v[80:83], a[200:203], v[128:131], v[80:83]\n"
    "s_mul_i32 s92, s77, 983040\n"
    "s_mul_i32 s74, s79, 320\n"
    "s_add_u32 s92, s92, s74\n"
    "s_mov_b64 s[48:49], s[12:13]\n"
    "s_add_u32 s48, s48, s92\n"
    "s_addc_u32 s49, s49, 0\n"
    "s_mov_b32 s50, 983040\n"
    "s_mov_b32 s51, 0x20000\n"
    "s_sub_u32 s50, s50, s74\n"
    "s_mul_i32 s74, s76, 20480\n"
    "v_exp_f32 v93, v93\n"
    "v_mfma_f32_16x16x32_bf16 v[80:83], a[204:207], v[160:163], v[80:83]\n"
    "ds_read_b64_tr_b16 a[220:221], v4 offset:36864\n"
    "v_cvt_pk_bf16_f32 v190, v92, v93\n"
    "v_mfma_f32_16x16x32_bf16 v[80:83], a[208:211], v[192:195], v[80:83]\n"
    "ds_read_b64_tr_b16 a[222:223], v4 offset:43008\n"
    "v_add_f32 v51, v51, v93\n"
    "v_mfma_f32_16x16x32_bf16 v[80:83], a[212:215], v[224:227], v[80:83]\n"
    "v_exp_f32 v94, v94\n"
    "v_mfma_f32_16x16x32_bf16 v[84:87], a[212:215], v[228:231], v[116:119]\n"
    "ds_read_b64_tr_b16 a[224:225], v5 offset:24576\n"
    "v_add_f32 v51, v51, v94\n"
    "v_mfma_f32_16x16x32_bf16 v[84:87], a[208:211], v[196:199], v[84:87]\n"
    "ds_read_b64_tr_b16 a[226:227], v5 offset:30720\n"
    "v_lshrrev_b32 v16, 4, v9\n"
    "v_mfma_f32_16x16x32_bf16 v[84:87], a[204:207], v[164:167], v[84:87]\n"
    "v_exp_f32 v95, v95\n"
    "v_mfma_f32_16x16x32_bf16 v[84:87], a[200:203], v[132:135], v[84:87]\n"
    "ds_read_b64_tr_b16 a[228:229], v5 offset:36864\n"
    "v_cvt_pk_bf16_f32 v191, v94, v95\n"
    "v_mfma_f32_16x16x32_bf16 v[84:87], a[196:199], v[100:103], v[84:87]\n"
    "ds_read_b64_tr_b16 a[230:231], v5 offset:43008\n"
    "v_add_f32 v51, v51, v95\n"
    "v_mfma_f32_16x16x32_bf16 v[84:87], a[192:195], v[68:71], v[84:87]\n"
    "v_exp_f32 v80, v80\n"
    "v_mfma_f32_16x16x32_bf16 v[88:91], a[192:195], v[72:75], v[120:123]\n"
    "ds_read_b64_tr_b16 a[232:233], v6 offset:24576\n"
    "v_add_f32 v48, v48, v80\n"
    "v_mfma_f32_16x16x32_bf16 v[88:91], a[196:199], v[104:107], v[88:91]\n"
    "ds_read_b64_tr_b16 a[234:235], v6 offset:30720\n"
    "v_add_u32 v16, 0, v16\n"
    "v_mfma_f32_16x16x32_bf16 v[88:91], a[200:203], v[136:139], v[88:91]\n"
    "v_exp_f32 v81, v81\n"
    "v_mfma_f32_16x16x32_bf16 v[88:91], a[204:207], v[168:171], v[88:91]\n"
    "ds_read_b64_tr_b16 a[236:237], v6 offset:36864\n"
    "v_cvt_pk_bf16_f32 v208, v80, v81\n"
    "v_mfma_f32_16x16x32_bf16 v[88:91], a[208:211], v[200:203], v[88:91]\n"
    "ds_read_b64_tr_b16 a[238:239], v6 offset:43008\n"
    "v_add_f32 v48, v48, v81\n"
    "v_mfma_f32_16x16x32_bf16 v[88:91], a[212:215], v[232:235], v[88:91]\n"
    "s_add_u32 s90, s90, 196608\n"
    "v_exp_f32 v82, v82\n"
    "v_mfma_f32_16x16x32_bf16 v[92:95], a[212:215], v[236:239], v[124:127]\n"
    "s_add_u32 m0, s64, 49152\n"
    "v_add_f32 v48, v48, v82\n"
    "v_mul_u32_u24 v17, 3277, v16\n"
    "v_mfma_f32_16x16x32_bf16 v[92:95], a[208:211], v[204:207], v[92:95]\n"
    "buffer_load_dwordx4 v58, s[44:47], s90 offen offset:0 lds\n"
    "v_lshrrev_b32 v17, 16, v17\n"
    "v_mfma_f32_16x16x32_bf16 v[92:95], a[204:207], v[172:175], v[92:95]\n"
    "v_exp_f32 v83, v83\n"
    "v_mfma_f32_16x16x32_bf16 v[92:95], a[200:203], v[140:143], v[92:95]\n"
    "v_cvt_pk_bf16_f32 v209, v82, v83\n"
    "v_add_f32 v48, v48, v83\n"
    "v_mfma_f32_16x16x32_bf16 v[92:95], a[196:199], v[108:111], v[92:95]\n"
    "s_waitcnt lgkmcnt(0)\n"
    "v_mul_u32_u24 v18, 20, v17\n"
    "v_sub_u32 v16, v16, v18\n"
    "v_mfma_f32_16x16x32_bf16 v[92:95], a[192:195], v[76:79], v[92:95]\n"
    "v_exp_f32 v84, v84\n"
    "v_mfma_f32_16x16x32_bf16 a[0:3], a[216:219], v[144:147], a[0:3]\n"
    "ds_read_b128 a[192:195], v2 offset:6144\n"
    "v_add_f32 v49, v49, v84\n"
    "v_mfma_f32_16x16x32_bf16 a[0:3], a[220:223], v[176:179], a[0:3]\n"
    "buffer_load_dwordx4 v60, s[44:47], s90 offen offset:1024 lds\n"
    "v_lshrrev_b32 v18, 1, v17\n"
    "v_mfma_f32_16x16x32_bf16 a[4:7], a[220:223], v[180:183], a[4:7]\n"
    "v_exp_f32 v85, v85\n"
    "v_mfma_f32_16x16x32_bf16 a[4:7], a[216:219], v[148:151], a[4:7]\n"
    "ds_read_b128 a[196:199], v3 offset:6144\n"
    "v_cvt_pk_bf16_f32 v212, v84, v85\n"
    "v_mfma_f32_16x16x32_bf16 a[8:11], a[216:219], v[152:155], a[8:11]\n"
    "buffer_load_dwordx4 v61, s[44:47], s90 offen offset:2048 lds\n"
    "v_add_f32 v49, v49, v85\n"
    "v_mfma_f32_16x16x32_bf16 a[8:11], a[220:223], v[184:187], a[8:11]\n"
    "s_add_u32 s90, s90, 196608\n"
    "v_exp_f32 v86, v86\n"
    "v_mfma_f32_16x16x32_bf16 a[12:15], a[220:223], v[188:191], a[12:15]\n"
    "s_add_u32 m0, s64, 61440\n"
    "ds_read_b128 a[200:203], v2 offset:8192\n"
    "v_add_f32 v49, v49, v86\n"
    "v_mfma_f32_16x16x32_bf16 a[12:15], a[216:219], v[156:159], a[12:15]\n"
    "buffer_load_dwordx4 v58, s[44:47], s90 offen offset:0 lds\n"
    "v_and_b32 v18, 2, v18\n"
    "v_mfma_f32_16x16x32_bf16 a[28:31], a[224:227], v[156:159], a[28:31]\n"
    "v_exp_f32 v87, v87\n"
    "v_mfma_f32_16x16x32_bf16 a[28:31], a[228:231], v[188:191], a[28:31]\n"
    "ds_read_b128 a[204:207], v3 offset:8192\n"
    "v_cvt_pk_bf16_f32 v213, v86, v87\n"
    "v_mfma_f32_16x16x32_bf16 a[24:27], a[228:231], v[184:187], a[24:27]\n"
    "v_add_f32 v49, v49, v87\n"
    "v_xor_b32 v16, v16, v18\n"
    "v_mfma_f32_16x16x32_bf16 a[24:27], a[224:227], v[152:155], a[24:27]\n"
    "v_exp_f32 v88, v88\n"
    "v_mfma_f32_16x16x32_bf16 a[20:23], a[224:227], v[148:151], a[20:23]\n"
    "ds_read_b128 a[208:211], v2 offset:10240\n"
    "v_add_f32 v50, v50, v88\n"
    "v_mfma_f32_16x16x32_bf16 a[20:23], a[228:231], v[180:183], a[20:23]\n"
    "buffer_load_dwordx4 v60, s[44:47], s90 offen offset:1024 lds\n"
    "v_mul_u32_u24 v17, 1280, v17\n"
    "v_mfma_f32_16x16x32_bf16 a[16:19], a[228:231], v[176:179], a[16:19]\n"
    "v_exp_f32 v89, v89\n"
    "v_mfma_f32_16x16x32_bf16 a[16:19], a[224:227], v[144:147], a[16:19]\n"
    "ds_read_b128 a[212:215], v3 offset:10240\n"
    "v_cvt_pk_bf16_f32 v216, v88, v89\n"
    "v_mfma_f32_16x16x32_bf16 a[32:35], a[232:235], v[144:147], a[32:35]\n"
    "v_add_f32 v50, v50, v89\n"
    "v_lshl_add_u32 v11, v16, 4, v17\n"
    "v_mfma_f32_16x16x32_bf16 a[32:35], a[236:239], v[176:179], a[32:35]\n"
    "v_exp_f32 v90, v90\n"
    "v_mfma_f32_16x16x32_bf16 a[36:39], a[236:239], v[180:183], a[36:39]\n"
    "v_add_f32 v50, v50, v90\n"
    "v_add_u32 v11, s74, v11\n"
    "v_mfma_f32_16x16x32_bf16 a[36:39], a[232:235], v[148:151], a[36:39]\n"
    "buffer_load_dwordx4 v61, s[44:47], s90 offen offset:2048 lds\n"
    "v_lshrrev_b32 v16, 4, v9\n"
    "v_mfma_f32_16x16x32_bf16 a[40:43], a[232:235], v[152:155], a[40:43]\n"
    "v_exp_f32 v91, v91\n"
    "v_mfma_f32_16x16x32_bf16 a[40:43], a[236:239], v[184:187], a[40:43]\n"
    "v_cvt_pk_bf16_f32 v217, v90, v91\n"
    "v_add_f32 v50, v50, v91\n"
    "v_mfma_f32_16x16x32_bf16 a[44:47], a[236:239], v[188:191], a[44:47]\n"
    "s_waitcnt lgkmcnt(0)\n"
    "v_add_u32 v16, 64, v16\n"
    "v_mul_u32_u24 v17, 3277, v16\n"
    "v_mfma_f32_16x16x32_bf16 a[44:47], a[232:235], v[156:159], a[44:47]\n"
    "v_exp_f32 v92, v92\n"
    "v_mfma_f32_16x16x32_bf16 v[80:83], a[192:195], v[64:67], v[112:115]\n"
    "ds_read_b64_tr_b16 a[216:217], v7 offset:24576\n"
    "v_add_f32 v51, v51, v92\n"
    "v_mfma_f32_16x16x32_bf16 v[80:83], a[196:199], v[96:99], v[80:83]\n"
    "ds_read_b64_tr_b16 a[218:219], v7 offset:30720\n"
    "v_lshrrev_b32 v17, 16, v17\n"
    "v_mfma_f32_16x16x32_bf16 v[80:83], a[200:203], v[128:131], v[80:83]\n"
    "v_exp_f32 v93, v93\n"
    "v_mfma_f32_16x16x32_bf16 v[80:83], a[204:207], v[160:163], v[80:83]\n"
    "ds_read_b64_tr_b16 a[220:221], v7 offset:36864\n"
    "v_cvt_pk_bf16_f32 v220, v92, v93\n"
    "v_mfma_f32_16x16x32_bf16 v[80:83], a[208:211], v[192:195], v[80:83]\n"
    "ds_read_b64_tr_b16 a[222:223], v7 offset:43008\n"
    "v_add_f32 v51, v51, v93\n"
    "v_mfma_f32_16x16x32_bf16 v[80:83], a[212:215], v[224:227], v[80:83]\n"
    "v_exp_f32 v94, v94\n"
    "v_mfma_f32_16x16x32_bf16 v[84:87], a[212:215], v[228:231], v[116:119]\n"
    "ds_read_b64_tr_b16 a[224:225], v4 offset:26624\n"
    "v_add_f32 v51, v51, v94\n"
    "v_mfma_f32_16x16x32_bf16 v[84:87], a[208:211], v[196:199], v[84:87]\n"
    "ds_read_b64_tr_b16 a[226:227], v4 offset:32768\n"
    "v_mul_u32_u24 v18, 20, v17\n"
    "v_mfma_f32_16x16x32_bf16 v[84:87], a[204:207], v[164:167], v[84:87]\n"
    "v_exp_f32 v95, v95\n"
    "v_mfma_f32_16x16x32_bf16 v[84:87], a[200:203], v[132:135], v[84:87]\n"
    "ds_read_b64_tr_b16 a[228:229], v4 offset:38912\n"
    "v_cvt_pk_bf16_f32 v221, v94, v95\n"
    "v_mfma_f32_16x16x32_bf16 v[84:87], a[196:199], v[100:103], v[84:87]\n"
    "ds_read_b64_tr_b16 a[230:231], v4 offset:45056\n"
    "v_add_f32 v51, v51, v95\n"
    "v_mfma_f32_16x16x32_bf16 v[84:87], a[192:195], v[68:71], v[84:87]\n"
    "v_exp_f32 v80, v80\n"
    "v_mfma_f32_16x16x32_bf16 v[88:91], a[192:195], v[72:75], v[120:123]\n"
    "ds_read_b64_tr_b16 a[232:233], v5 offset:26624\n"
    "v_add_f32 v48, v48, v80\n"
    "v_mfma_f32_16x16x32_bf16 v[88:91], a[196:199], v[104:107], v[88:91]\n"
    "ds_read_b64_tr_b16 a[234:235], v5 offset:32768\n"
    "v_sub_u32 v16, v16, v18\n"
    "v_mfma_f32_16x16x32_bf16 v[88:91], a[200:203], v[136:139], v[88:91]\n"
    "v_exp_f32 v81, v81\n"
    "v_mfma_f32_16x16x32_bf16 v[88:91], a[204:207], v[168:171], v[88:91]\n"
    "ds_read_b64_tr_b16 a[236:237], v5 offset:38912\n"
    "v_cvt_pk_bf16_f32 v210, v80, v81\n"
    "v_mfma_f32_16x16x32_bf16 v[88:91], a[208:211], v[200:203], v[88:91]\n"
    "ds_read_b64_tr_b16 a[238:239], v5 offset:45056\n"
    "v_add_f32 v48, v48, v81\n"
    "v_mfma_f32_16x16x32_bf16 v[88:91], a[212:215], v[232:235], v[88:91]\n"
    "s_add_u32 s90, s90, 196608\n"
    "v_exp_f32 v82, v82\n"
    "v_mfma_f32_16x16x32_bf16 v[92:95], a[212:215], v[236:239], v[124:127]\n"
    "s_add_u32 m0, s64, 73728\n"
    "v_add_f32 v48, v48, v82\n"
    "v_lshrrev_b32 v18, 1, v17\n"
    "v_mfma_f32_16x16x32_bf16 v[92:95], a[208:211], v[204:207], v[92:95]\n"
    "buffer_load_dwordx4 v58, s[44:47], s90 offen offset:0 lds\n"
    "v_and_b32 v18, 2, v18\n"
    "v_mfma_f32_16x16x32_bf16 v[92:95], a[204:207], v[172:175], v[92:95]\n"
    "v_exp_f32 v83, v83\n"
    "v_mfma_f32_16x16x32_bf16 v[92:95], a[200:203], v[140:143], v[92:95]\n"
    "v_cvt_pk_bf16_f32 v211, v82, v83\n"
    "v_add_f32 v48, v48, v83\n"
    "v_mfma_f32_16x16x32_bf16 v[92:95], a[196:199], v[108:111], v[92:95]\n"
    "s_waitcnt lgkmcnt(0)\n"
    "v_xor_b32 v16, v16, v18\n"
    "v_mul_u32_u24 v17, 1280, v17\n"
    "v_mfma_f32_16x16x32_bf16 v[92:95], a[192:195], v[76:79], v[92:95]\n"
    "v_exp_f32 v84, v84\n"
    "v_mfma_f32_16x16x32_bf16 a[48:51], a[216:219], v[144:147], a[48:51]\n"
    "ds_read_b128 a[192:195], v2 offset:12288\n"
    "v_add_f32 v49, v49, v84\n"
    "v_mfma_f32_16x16x32_bf16 a[48:51], a[220:223], v[176:179], a[48:51]\n"
    "buffer_load_dwordx4 v60, s[44:47], s90 offen offset:1024 lds\n"
    "v_lshl_add_u32 v12, v16, 4, v17\n"
    "v_mfma_f32_16x16x32_bf16 a[52:55], a[220:223], v[180:183], a[52:55]\n"
    "v_exp_f32 v85, v85\n"
    "v_mfma_f32_16x16x32_bf16 a[52:55], a[216:219], v[148:151], a[52:55]\n"
    "ds_read_b128 a[196:199], v3 offset:12288\n"
    "v_cvt_pk_bf16_f32 v214, v84, v85\n"
    "v_mfma_f32_16x16x32_bf16 a[56:59], a[216:219], v[152:155], a[56:59]\n"
    "buffer_load_dwordx4 v61, s[44:47], s90 offen offset:2048 lds\n"
    "v_add_f32 v49, v49, v85\n"
    "v_mfma_f32_16x16x32_bf16 a[56:59], a[220:223], v[184:187], a[56:59]\n"
    "s_add_u32 s90, s90, 196608\n"
    "v_exp_f32 v86, v86\n"
    "v_mfma_f32_16x16x32_bf16 a[60:63], a[220:223], v[188:191], a[60:63]\n"
    "s_add_u32 m0, s64, 86016\n"
    "ds_read_b128 a[200:203], v2 offset:14336\n"
    "v_add_f32 v49, v49, v86\n"
    "v_mfma_f32_16x16x32_bf16 a[60:63], a[216:219], v[156:159], a[60:63]\n"
    "buffer_load_dwordx4 v58, s[44:47], s90 offen offset:0 lds\n"
    "v_subrev_u32 v12, 1024, v12\n"
    "v_mfma_f32_16x16x32_bf16 a[76:79], a[224:227], v[156:159], a[76:79]\n"
    "v_exp_f32 v87, v87\n"
    "v_mfma_f32_16x16x32_bf16 a[76:79], a[228:231], v[188:191], a[76:79]\n"
    "ds_read_b128 a[204:207], v3 offset:14336\n"
    "v_cvt_pk_bf16_f32 v215, v86, v87\n"
    "v_mfma_f32_16x16x32_bf16 a[72:75], a[228:231], v[184:187], a[72:75]\n"
    "v_add_f32 v49, v49, v87\n"
    "v_add_u32 v12, s74, v12\n"
    "v_mfma_f32_16x16x32_bf16 a[72:75], a[224:227], v[152:155], a[72:75]\n"
    "v_exp_f32 v88, v88\n"
    "v_mfma_f32_16x16x32_bf16 a[68:71], a[224:227], v[148:151], a[68:71]\n"
    "ds_read_b128 a[208:211], v2 offset:16384\n"
    "v_add_f32 v50, v50, v88\n"
    "v_mfma_f32_16x16x32_bf16 a[68:71], a[228:231], v[180:183], a[68:71]\n"
    "buffer_load_dwordx4 v60, s[44:47], s90 offen offset:1024 lds\n"
    "v_lshrrev_b32 v16, 4, v9\n"
    "v_mfma_f32_16x16x32_bf16 a[64:67], a[228:231], v[176:179], a[64:67]\n"
    "v_exp_f32 v89, v89\n"
    "v_mfma_f32_16x16x32_bf16 a[64:67], a[224:227], v[144:147], a[64:67]\n"
    "ds_read_b128 a[212:215], v3 offset:16384\n"
    "v_cvt_pk_bf16_f32 v218, v88, v89\n"
    "v_mfma_f32_16x16x32_bf16 a[80:83], a[232:235], v[144:147], a[80:83]\n"
    "v_add_f32 v50, v50, v89\n"
    "v_add_u32 v16, 128, v16\n"
    "v_mfma_f32_16x16x32_bf16 a[80:83], a[236:239], v[176:179], a[80:83]\n"
    "v_exp_f32 v90, v90\n"
    "v_mfma_f32_16x16x32_bf16 a[84:87], a[236:239], v[180:183], a[84:87]\n"
    "v_add_f32 v50, v50, v90\n"
    "v_mul_u32_u24 v17, 3277, v16\n"
    "v_mfma_f32_16x16x32_bf16 a[84:87], a[232:235], v[148:151], a[84:87]\n"
    "buffer_load_dwordx4 v61, s[44:47], s90 offen offset:2048 lds\n"
    "v_lshrrev_b32 v17, 16, v17\n"
    "v_mfma_f32_16x16x32_bf16 a[88:91], a[232:235], v[152:155], a[88:91]\n"
    "v_exp_f32 v91, v91\n"
    "v_mfma_f32_16x16x32_bf16 a[88:91], a[236:239], v[184:187], a[88:91]\n"
    "v_cvt_pk_bf16_f32 v219, v90, v91\n"
    "v_add_f32 v50, v50, v91\n"
    "v_mfma_f32_16x16x32_bf16 a[92:95], a[236:239], v[188:191], a[92:95]\n"
    "s_waitcnt lgkmcnt(0)\n"
    "v_mul_u32_u24 v18, 20, v17\n"
    "v_sub_u32 v16, v16, v18\n"
    "v_mfma_f32_16x16x32_bf16 a[92:95], a[232:235], v[156:159], a[92:95]\n"
    "v_exp_f32 v92, v92\n"
    "v_mfma_f32_16x16x32_bf16 v[80:83], a[192:195], v[64:67], v[112:115]\n"
    "ds_read_b64_tr_b16 a[216:217], v6 offset:26624\n"
    "v_add_f32 v51, v51, v92\n"
    "v_mfma_f32_16x16x32_bf16 v[80:83], a[196:199], v[96:99], v[80:83]\n"
    "ds_read_b64_tr_b16 a[218:219], v6 offset:32768\n"
    "v_lshrrev_b32 v18, 1, v17\n"
    "v_and_b32 v18, 2, v18\n"
    "v_mfma_f32_16x16x32_bf16 v[80:83], a[200:203], v[128:131], v[80:83]\n"
    "v_exp_f32 v93, v93\n"
    "v_mfma_f32_16x16x32_bf16 v[80:83], a[204:207], v[160:163], v[80:83]\n"
    "ds_read_b64_tr_b16 a[220:221], v6 offset:38912\n"
    "v_cvt_pk_bf16_f32 v222, v92, v93\n"
    "v_xor_b32 v16, v16, v18\n"
    "v_mfma_f32_16x16x32_bf16 v[80:83], a[208:211], v[192:195], v[80:83]\n"
    "ds_read_b64_tr_b16 a[222:223], v6 offset:45056\n"
    "v_add_f32 v51, v51, v93\n"
    "v_mul_u32_u24 v17, 1280, v17\n"
    "v_mfma_f32_16x16x32_bf16 v[80:83], a[212:215], v[224:227], v[80:83]\n"
    "v_exp_f32 v94, v94\n"
    "v_mfma_f32_16x16x32_bf16 v[84:87], a[212:215], v[228:231], v[116:119]\n"
    "ds_read_b64_tr_b16 a[224:225], v7 offset:26624\n"
    "v_add_f32 v51, v51, v94\n"
    "v_lshl_add_u32 v13, v16, 4, v17\n"
    "v_mfma_f32_16x16x32_bf16 v[84:87], a[208:211], v[196:199], v[84:87]\n"
    "ds_read_b64_tr_b16 a[226:227], v7 offset:32768\n"
    "v_subrev_u32 v13, 2048, v13\n"
    "v_add_u32 v13, s74, v13\n"
    "v_mfma_f32_16x16x32_bf16 v[84:87], a[204:207], v[164:167], v[84:87]\n"
    "v_exp_f32 v95, v95\n"
    "v_mfma_f32_16x16x32_bf16 v[84:87], a[200:203], v[132:135], v[84:87]\n"
    "ds_read_b64_tr_b16 a[228:229], v7 offset:38912\n"
    "v_cvt_pk_bf16_f32 v223, v94, v95\n"
    "v_lshrrev_b32 v16, 4, v9\n"
    "v_mfma_f32_16x16x32_bf16 v[84:87], a[196:199], v[100:103], v[84:87]\n"
    "ds_read_b64_tr_b16 a[230:231], v7 offset:45056\n"
    "v_add_f32 v51, v51, v95\n"
    "v_add_u32 v16, 192, v16\n"
    "v_mfma_f32_16x16x32_bf16 v[84:87], a[192:195], v[68:71], v[84:87]\n"
    "v_exp_f32 v80, v80\n"
    "v_mfma_f32_16x16x32_bf16 v[88:91], a[192:195], v[72:75], v[120:123]\n"
    "ds_read_b64_tr_b16 a[232:233], v4 offset:28672\n"
    "v_add_f32 v48, v48, v80\n"
    "v_mul_u32_u24 v17, 3277, v16\n"
    "v_mfma_f32_16x16x32_bf16 v[88:91], a[196:199], v[104:107], v[88:91]\n"
    "ds_read_b64_tr_b16 a[234:235], v4 offset:34816\n"
    "v_lshrrev_b32 v17, 16, v17\n"
    "v_mul_u32_u24 v18, 20, v17\n"
    "v_mfma_f32_16x16x32_bf16 v[88:91], a[200:203], v[136:139], v[88:91]\n"
    "v_exp_f32 v81, v81\n"
    "v_mfma_f32_16x16x32_bf16 v[88:91], a[204:207], v[168:171], v[88:91]\n"
    "ds_read_b64_tr_b16 a[236:237], v4 offset:40960\n"
    "v_cvt_pk_bf16_f32 v240, v80, v81\n"
    "v_sub_u32 v16, v16, v18\n"
    "v_mfma_f32_16x16x32_bf16 v[88:91], a[208:211], v[200:203], v[88:91]\n"
    "ds_read_b64_tr_b16 a[238:239], v4 offset:47104\n"
    "v_add_f32 v48, v48, v81\n"
    "v_lshrrev_b32 v18, 1, v17\n"
    "v_mfma_f32_16x16x32_bf16 v[88:91], a[212:215], v[232:235], v[88:91]\n"
    "s_add_u32 s90, s90, 196608\n"
    "v_exp_f32 v82, v82\n"
    "v_mfma_f32_16x16x32_bf16 v[92:95], a[212:215], v[236:239], v[124:127]\n"
    "s_add_u32 m0, s64, 147456\n"
    "v_add_f32 v48, v48, v82\n"
    "v_and_b32 v18, 2, v18\n"
    "v_mfma_f32_16x16x32_bf16 v[92:95], a[208:211], v[204:207], v[92:95]\n"
    "buffer_load_dwordx4 v58, s[44:47], s90 offen offset:0 lds\n"
    "v_xor_b32 v16, v16, v18\n"
    "v_mul_u32_u24 v17, 1280, v17\n"
    "v_mfma_f32_16x16x32_bf16 v[92:95], a[204:207], v[172:175], v[92:95]\n"
    "v_exp_f32 v83, v83\n"
    "v_mfma_f32_16x16x32_bf16 v[92:95], a[200:203], v[140:143], v[92:95]\n"
    "v_cvt_pk_bf16_f32 v241, v82, v83\n"
    "v_add_f32 v48, v48, v83\n"
    "v_mfma_f32_16x16x32_bf16 v[92:95], a[196:199], v[108:111], v[92:95]\n"
    "s_waitcnt lgkmcnt(0)\n"
    "v_lshl_add_u32 v14, v16, 4, v17\n"
    "v_subrev_u32 v14, 3072, v14\n"
    "v_mfma_f32_16x16x32_bf16 v[92:95], a[192:195], v[76:79], v[92:95]\n"
    "v_exp_f32 v84, v84\n"
    "v_mfma_f32_16x16x32_bf16 a[96:99], a[216:219], v[144:147], a[96:99]\n"
    "ds_read_b128 a[192:195], v2 offset:18432\n"
    "v_add_f32 v49, v49, v84\n"
    "v_add_u32 v14, s74, v14\n"
    "v_mfma_f32_16x16x32_bf16 a[96:99], a[220:223], v[176:179], a[96:99]\n"
    "buffer_load_dwordx4 v60, s[44:47], s90 offen offset:1024 lds\n"
    "v_lshrrev_b32 v16, 4, v9\n"
    "v_add_u32 v16, 256, v16\n"
    "v_mfma_f32_16x16x32_bf16 a[100:103], a[220:223], v[180:183], a[100:103]\n"
    "v_exp_f32 v85, v85\n"
    "v_mfma_f32_16x16x32_bf16 a[100:103], a[216:219], v[148:151], a[100:103]\n"
    "ds_read_b128 a[196:199], v3 offset:18432\n"
    "v_cvt_pk_bf16_f32 v244, v84, v85\n"
    "v_mul_u32_u24 v17, 3277, v16\n"
    "v_mfma_f32_16x16x32_bf16 a[104:107], a[216:219], v[152:155], a[104:107]\n"
    "buffer_load_dwordx4 v61, s[44:47], s90 offen offset:2048 lds\n"
    "v_add_f32 v49, v49, v85\n"
    "v_lshrrev_b32 v17, 16, v17\n"
    "v_mfma_f32_16x16x32_bf16 a[104:107], a[220:223], v[184:187], a[104:107]\n"
    "v_exp_f32 v86, v86\n"
    "v_mfma_f32_16x16x32_bf16 a[108:111], a[220:223], v[188:191], a[108:111]\n"
    "ds_read_b128 a[200:203], v2 offset:20480\n"
    "v_add_f32 v49, v49, v86\n"
    "v_mul_u32_u24 v18, 20, v17\n"
    "v_mfma_f32_16x16x32_bf16 a[108:111], a[216:219], v[156:159], a[108:111]\n"
    "v_sub_u32 v16, v16, v18\n"
    "v_lshrrev_b32 v18, 1, v17\n"
    "v_mfma_f32_16x16x32_bf16 a[124:127], a[224:227], v[156:159], a[124:127]\n"
    "v_exp_f32 v87, v87\n"
    "v_mfma_f32_16x16x32_bf16 a[124:127], a[228:231], v[188:191], a[124:127]\n"
    "ds_read_b128 a[204:207], v3 offset:20480\n"
    "v_cvt_pk_bf16_f32 v245, v86, v87\n"
    "v_and_b32 v18, 2, v18\n"
    "v_mfma_f32_16x16x32_bf16 a[120:123], a[228:231], v[184:187], a[120:123]\n"
    "v_add_f32 v49, v49, v87\n"
    "v_xor_b32 v16, v16, v18\n"
    "v_mfma_f32_16x16x32_bf16 a[120:123], a[224:227], v[152:155], a[120:123]\n"
    "v_exp_f32 v88, v88\n"
    "v_mfma_f32_16x16x32_bf16 a[116:119], a[224:227], v[148:151], a[116:119]\n"
    "ds_read_b128 a[208:211], v2 offset:22528\n"
    "v_add_f32 v50, v50, v88\n"
    "v_mul_u32_u24 v17, 1280, v17\n"
    "v_mfma_f32_16x16x32_bf16 a[116:119], a[228:231], v[180:183], a[116:119]\n"
    "v_lshl_add_u32 v15, v16, 4, v17\n"
    "v_add_u32 v15, s74, v15\n"
    "v_mfma_f32_16x16x32_bf16 a[112:115], a[228:231], v[176:179], a[112:115]\n"
    "v_exp_f32 v89, v89\n"
    "v_mfma_f32_16x16x32_bf16 a[112:115], a[224:227], v[144:147], a[112:115]\n"
    "ds_read_b128 a[212:215], v3 offset:22528\n"
    "v_cvt_pk_bf16_f32 v248, v88, v89\n"
    "v_mfma_f32_16x16x32_bf16 a[128:131], a[232:235], v[144:147], a[128:131]\n"
    "v_add_f32 v50, v50, v89\n"
    "v_mfma_f32_16x16x32_bf16 a[128:131], a[236:239], v[176:179], a[128:131]\n"
    "v_exp_f32 v90, v90\n"
    "v_mfma_f32_16x16x32_bf16 a[132:135], a[236:239], v[180:183], a[132:135]\n"
    "v_add_f32 v50, v50, v90\n"
    "v_mfma_f32_16x16x32_bf16 a[132:135], a[232:235], v[148:151], a[132:135]\n"
    "v_mfma_f32_16x16x32_bf16 a[136:139], a[232:235], v[152:155], a[136:139]\n"
    "v_exp_f32 v91, v91\n"
    "v_mfma_f32_16x16x32_bf16 a[136:139], a[236:239], v[184:187], a[136:139]\n"
    "v_cvt_pk_bf16_f32 v249, v90, v91\n"
    "v_add_f32 v50, v50, v91\n"
    "v_mfma_f32_16x16x32_bf16 a[140:143], a[236:239], v[188:191], a[140:143]\n"
    "s_waitcnt lgkmcnt(0)\n"
    "v_mfma_f32_16x16x32_bf16 a[140:143], a[232:235], v[156:159], a[140:143]\n"
    "v_exp_f32 v92, v92\n"
    "v_mfma_f32_16x16x32_bf16 v[80:83], a[192:195], v[64:67], v[112:115]\n"
    "ds_read_b64_tr_b16 a[216:217], v5 offset:28672\n"
    "v_add_f32 v51, v51, v92\n"
    "v_mfma_f32_16x16x32_bf16 v[80:83], a[196:199], v[96:99], v[80:83]\n"
    "s_add_u32 s70, s69, s68\n"
    "ds_read_b64_tr_b16 a[218:219], v5 offset:34816\n"
    "v_mfma_f32_16x16x32_bf16 v[80:83], a[200:203], v[128:131], v[80:83]\n"
    "s_add_u32 s73, s69, 49152\n"
    "v_exp_f32 v93, v93\n"
    "v_mfma_f32_16x16x32_bf16 v[80:83], a[204:207], v[160:163], v[80:83]\n"
    "s_cmp_eq_u32 s73, 147456\n"
    "ds_read_b64_tr_b16 a[220:221], v5 offset:40960\n"
    "v_cvt_pk_bf16_f32 v252, v92, v93\n"
    "v_mfma_f32_16x16x32_bf16 v[80:83], a[208:211], v[192:195], v[80:83]\n"
    "s_cselect_b32 s73, 0, s73\n"
    "ds_read_b64_tr_b16 a[222:223], v5 offset:47104\n"
    "v_add_f32 v51, v51, v93\n"
    "v_mfma_f32_16x16x32_bf16 v[80:83], a[212:215], v[224:227], v[80:83]\n"
    "s_sub_u32 s71, s73, s69\n"
    "v_exp_f32 v94, v94\n"
    "v_mfma_f32_16x16x32_bf16 v[84:87], a[212:215], v[228:231], v[116:119]\n"
    "s_mov_b32 s69, s73\n"
    "ds_read_b64_tr_b16 a[224:225], v6 offset:28672\n"
    "v_add_f32 v51, v51, v94\n"
    "v_mfma_f32_16x16x32_bf16 v[84:87], a[208:211], v[196:199], v[84:87]\n"
    "s_lshr_b32 s74, s63, 2\n"
    "ds_read_b64_tr_b16 a[226:227], v6 offset:34816\n"
    "v_add_u32 v8, 24576, v8\n"
    "v_mfma_f32_16x16x32_bf16 v[84:87], a[204:207], v[164:167], v[84:87]\n"
    "s_lshl_b32 s74, s74, 3\n"
    "v_exp_f32 v95, v95\n"
    "v_mfma_f32_16x16x32_bf16 v[84:87], a[200:203], v[132:135], v[84:87]\n"
    "ds_read_b64_tr_b16 a[228:229], v6 offset:40960\n"
    "v_cvt_pk_bf16_f32 v253, v94, v95\n"
    "v_lshrrev_b32 v56, 4, v9\n"
    "v_mfma_f32_16x16x32_bf16 v[84:87], a[196:199], v[100:103], v[84:87]\n"
    "ds_read_b64_tr_b16 a[230:231], v6 offset:47104\n"
    "v_add_f32 v51, v51, v95\n"
    "v_min_u32 v56, 7, v56\n"
    "v_mfma_f32_16x16x32_bf16 v[84:87], a[192:195], v[68:71], v[84:87]\n"
    "v_exp_f32 v80, v80\n"
    "v_mfma_f32_16x16x32_bf16 v[88:91], a[192:195], v[72:75], v[120:123]\n"
    "ds_read_b64_tr_b16 a[232:233], v7 offset:28672\n"
    "v_add_f32 v48, v48, v80\n"
    "v_add_u32 v56, s74, v56\n"
    "v_mfma_f32_16x16x32_bf16 v[88:91], a[196:199], v[104:107], v[88:91]\n"
    "ds_read_b64_tr_b16 a[234:235], v7 offset:34816\n"
    "v_lshlrev_b32 v56, 6, v56\n"
    "v_add_u32 v56, 53248, v56\n"
    "v_mfma_f32_16x16x32_bf16 v[88:91], a[200:203], v[136:139], v[88:91]\n"
    "v_exp_f32 v81, v81\n"
    "v_mfma_f32_16x16x32_bf16 v[88:91], a[204:207], v[168:171], v[88:91]\n"
    "buffer_load_dwordx2 v[62:63], v56, s[52:55], 0 offen sc1\n"
    "ds_read_b64_tr_b16 a[236:237], v7 offset:40960\n"
    "v_cvt_pk_bf16_f32 v242, v80, v81\n"
    "v_mfma_f32_16x16x32_bf16 v[88:91], a[208:211], v[200:203], v[88:91]\n"
    "ds_read_b64_tr_b16 a[238:239], v7 offset:47104\n"
    "v_add_f32 v48, v48, v81\n"
    "v_mfma_f32_16x16x32_bf16 v[88:91], a[212:215], v[232:235], v[88:91]\n"
    "v_exp_f32 v82, v82\n"
    "v_mfma_f32_16x16x32_bf16 v[92:95], a[212:215], v[236:239], v[124:127]\n"
    "v_add_f32 v48, v48, v82\n"
    "v_mfma_f32_16x16x32_bf16 v[92:95], a[208:211], v[204:207], v[92:95]\n"
    "v_add_u32 v4, s71, v4\n"
    "v_add_u32 v5, s71, v5\n"
    "v_add_u32 v6, s71, v6\n"
    "v_add_u32 v7, s71, v7\n"
    "v_mfma_f32_16x16x32_bf16 v[92:95], a[204:207], v[172:175], v[92:95]\n"
    "v_exp_f32 v83, v83\n"
    "v_mfma_f32_16x16x32_bf16 v[92:95], a[200:203], v[140:143], v[92:95]\n"
    "v_cvt_pk_bf16_f32 v243, v82, v83\n"
    "v_add_f32 v48, v48, v83\n"
    "v_mfma_f32_16x16x32_bf16 v[92:95], a[196:199], v[108:111], v[92:95]\n"
    "s_waitcnt vmcnt(23) lgkmcnt(0)\n"
    "s_barrier\n"
    "v_mfma_f32_16x16x32_bf16 v[92:95], a[192:195], v[76:79], v[92:95]\n"
    "v_exp_f32 v84, v84\n"
    "v_mfma_f32_16x16x32_bf16 a[144:147], a[216:219], v[144:147], a[144:147]\n"
    "ds_read_b64_tr_b16 a[192:193], v4 offset:24576\n"
    "v_add_f32 v49, v49, v84\n"
    "v_mfma_f32_16x16x32_bf16 a[144:147], a[220:223], v[176:179], a[144:147]\n"
    "ds_read_b64_tr_b16 a[194:195], v4 offset:30720\n"
    "v_add_u32 v2, s71, v2\n"
    "v_mfma_f32_16x16x32_bf16 a[148:151], a[220:223], v[180:183], a[148:151]\n"
    "v_exp_f32 v85, v85\n"
    "v_mfma_f32_16x16x32_bf16 a[148:151], a[216:219], v[148:151], a[148:151]\n"
    "ds_read_b64_tr_b16 a[196:197], v4 offset:36864\n"
    "v_cvt_pk_bf16_f32 v246, v84, v85\n"
    "v_mfma_f32_16x16x32_bf16 a[152:155], a[216:219], v[152:155], a[152:155]\n"
    "ds_read_b64_tr_b16 a[198:199], v4 offset:43008\n"
    "v_add_f32 v49, v49, v85\n"
    "v_mfma_f32_16x16x32_bf16 a[152:155], a[220:223], v[184:187], a[152:155]\n"
    "v_exp_f32 v86, v86\n"
    "v_mfma_f32_16x16x32_bf16 a[156:159], a[220:223], v[188:191], a[156:159]\n"
    "ds_read_b64_tr_b16 a[200:201], v5 offset:24576\n"
    "v_add_f32 v49, v49, v86\n"
    "v_mfma_f32_16x16x32_bf16 a[156:159], a[216:219], v[156:159], a[156:159]\n"
    "ds_read_b64_tr_b16 a[202:203], v5 offset:30720\n"
    "v_add_u32 v3, s71, v3\n"
    "v_mfma_f32_16x16x32_bf16 a[172:175], a[224:227], v[156:159], a[172:175]\n"
    "v_exp_f32 v87, v87\n"
    "v_mfma_f32_16x16x32_bf16 a[172:175], a[228:231], v[188:191], a[172:175]\n"
    "ds_read_b64_tr_b16 a[204:205], v5 offset:36864\n"
    "v_cvt_pk_bf16_f32 v247, v86, v87\n"
    "v_mfma_f32_16x16x32_bf16 a[168:171], a[228:231], v[184:187], a[168:171]\n"
    "ds_read_b64_tr_b16 a[206:207], v5 offset:43008\n"
    "v_add_f32 v49, v49, v87\n"
    "v_mfma_f32_16x16x32_bf16 a[168:171], a[224:227], v[152:155], a[168:171]\n"
    "v_exp_f32 v88, v88\n"
    "v_mfma_f32_16x16x32_bf16 a[164:167], a[224:227], v[148:151], a[164:167]\n"
    "ds_read_b64_tr_b16 a[208:209], v6 offset:24576\n"
    "v_add_f32 v50, v50, v88\n"
    "v_mfma_f32_16x16x32_bf16 a[164:167], a[228:231], v[180:183], a[164:167]\n"
    "ds_read_b64_tr_b16 a[210:211], v6 offset:30720\n"
    "v_mfma_f32_16x16x32_bf16 a[160:163], a[228:231], v[176:179], a[160:163]\n"
    "v_exp_f32 v89, v89\n"
    "v_mfma_f32_16x16x32_bf16 a[160:163], a[224:227], v[144:147], a[160:163]\n"
    "ds_read_b64_tr_b16 a[212:213], v6 offset:36864\n"
    "v_cvt_pk_bf16_f32 v250, v88, v89\n"
    "v_mfma_f32_16x16x32_bf16 a[176:179], a[232:235], v[144:147], a[176:179]\n"
    "ds_read_b64_tr_b16 a[214:215], v6 offset:43008\n"
    "v_add_f32 v50, v50, v89\n"
    "v_mfma_f32_16x16x32_bf16 a[176:179], a[236:239], v[176:179], a[176:179]\n"
    "v_exp_f32 v90, v90\n"
    "v_mfma_f32_16x16x32_bf16 a[180:183], a[236:239], v[180:183], a[180:183]\n"
    "v_add_f32 v50, v50, v90\n"
    "v_mfma_f32_16x16x32_bf16 a[180:183], a[232:235], v[148:151], a[180:183]\n"
    "v_mfma_f32_16x16x32_bf16 a[184:187], a[232:235], v[152:155], a[184:187]\n"
    "v_exp_f32 v91, v91\n"
    "v_mfma_f32_16x16x32_bf16 a[184:187], a[236:239], v[184:187], a[184:187]\n"
    "v_cvt_pk_bf16_f32 v251, v90, v91\n"
    "v_add_f32 v50, v50, v91\n"
    "v_mfma_f32_16x16x32_bf16 a[188:191], a[236:239], v[188:191], a[188:191]\n"
    "s_waitcnt lgkmcnt(0)\n"
    "v_mfma_f32_16x16x32_bf16 a[188:191], a[232:235], v[156:159], a[188:191]\n"
    "v_exp_f32 v92, v92\n"
    "v_mfma_f32_16x16x32_bf16 a[0:3], a[192:195], v[208:211], a[0:3]\n"
    "v_exp_f32 v93, v93\n"
    "v_mfma_f32_16x16x32_bf16 a[0:3], a[196:199], v[240:243], a[0:3]\n"
    "s_add_u32 s90, s90, 196608\n"
    "v_exp_f32 v94, v94\n"
    "v_mfma_f32_16x16x32_bf16 a[4:7], a[196:199], v[244:247], a[4:7]\n"
    "s_add_u32 m0, s64, 98304\n"
    "v_exp_f32 v95, v95\n"
    "v_mfma_f32_16x16x32_bf16 a[4:7], a[192:195], v[212:215], a[4:7]\n"
    "v_cvt_pk_bf16_f32 v254, v92, v93\n"
    "v_cvt_pk_bf16_f32 v255, v94, v95\n"
    "v_mfma_f32_16x16x32_bf16 a[8:11], a[192:195], v[216:219], a[8:11]\n"
    "ds_read_b64_tr_b16 a[216:217], v7 offset:24576\n"
    "v_add_f32 v51, v51, v92\n"
    "v_mfma_f32_16x16x32_bf16 a[8:11], a[196:199], v[248:251], a[8:11]\n"
    "ds_read_b64_tr_b16 a[218:219], v7 offset:30720\n"
    "v_add_f32 v51, v51, v93\n"
    "v_mfma_f32_16x16x32_bf16 a[12:15], a[196:199], v[252:255], a[12:15]\n"
    "v_add_f32 v51, v51, v94\n"
    "v_add_f32 v51, v51, v95\n"
    "v_mfma_f32_16x16x32_bf16 a[12:15], a[192:195], v[220:223], a[12:15]\n"
    "ds_read_b64_tr_b16 a[220:221], v7 offset:36864\n"
    "v_add_f32 v16, v48, v49\n"
    "v_mfma_f32_16x16x32_bf16 a[28:31], a[200:203], v[220:223], a[28:31]\n"
    "ds_read_b64_tr_b16 a[222:223], v7 offset:43008\n"
    "v_add_f32 v17, v50, v51\n"
    "v_mfma_f32_16x16x32_bf16 a[28:31], a[204:207], v[252:255], a[28:31]\n"
    "v_add_f32 v16, v16, v17\n"
    "v_cmp_nge_f32 vcc, 0x45800000, v16\n"
    "v_mfma_f32_16x16x32_bf16 a[24:27], a[204:207], v[248:251], a[24:27]\n"
    "s_lshl_b32 s74, s76, 2\n"
    "buffer_load_dwordx4 v58, s[44:47], s90 offen offset:0 lds\n"
    "ds_read_b64_tr_b16 a[224:225], v4 offset:26624\n"
    "v_mfma_f32_16x16x32_bf16 a[24:27], a[200:203], v[216:219], a[24:27]\n"
    "s_or_b32 s93, vcc_lo, vcc_hi\n"
    "buffer_load_dwordx4 v60, s[44:47], s90 offen offset:1024 lds\n"
    "ds_read_b64_tr_b16 a[226:227], v4 offset:32768\n"
    "v_mfma_f32_16x16x32_bf16 a[20:23], a[200:203], v[212:215], a[20:23]\n"
    "s_add_u32 s74, s74, 163824\n"
    "v_mov_b32 v16, s93\n"
    "v_mov_b32 v17, s74\n"
    "v_mfma_f32_16x16x32_bf16 a[20:23], a[204:207], v[244:247], a[20:23]\n"
    "ds_read_b64_tr_b16 a[228:229], v4 offset:38912\n"
    "v_mov_b32 v18, 163824\n"
    "v_mfma_f32_16x16x32_bf16 a[16:19], a[204:207], v[240:243], a[16:19]\n"
    "buffer_load_dwordx4 v61, s[44:47], s90 offen offset:2048 lds\n"
    "ds_read_b64_tr_b16 a[230:231], v4 offset:45056\n"
    "v_mfma_f32_16x16x32_bf16 a[16:19], a[200:203], v[208:211], a[16:19]\n"
    "s_add_u32 s90, s90, 196608\n"
    "ds_write_b32 v17, v16\n"
    "v_mfma_f32_16x16x32_bf16 a[32:35], a[208:211], v[208:211], a[32:35]\n"
    "s_add_u32 m0, s64, 110592\n"
    "ds_read_b64_tr_b16 a[232:233], v5 offset:26624\n"
    "v_mfma_f32_16x16x32_bf16 a[32:35], a[212:215], v[240:243], a[32:35]\n"
    "buffer_load_dwordx4 v58, s[44:47], s90 offen offset:0 lds\n"
    "ds_read_b64_tr_b16 a[234:235], v5 offset:32768\n"
    "v_mfma_f32_16x16x32_bf16 a[36:39], a[212:215], v[244:247], a[36:39]\n"
    "buffer_load_dwordx4 v60, s[44:47], s90 offen offset:1024 lds\n"
    "v_mfma_f32_16x16x32_bf16 a[36:39], a[208:211], v[212:215], a[36:39]\n"
    "buffer_load_dwordx4 v61, s[44:47], s90 offen offset:2048 lds\n"
    "ds_read_b64_tr_b16 a[236:237], v5 offset:38912\n"
    "v_mfma_f32_16x16x32_bf16 a[40:43], a[208:211], v[216:219], a[40:43]\n"
    "ds_read_b64_tr_b16 a[238:239], v5 offset:45056\n"
    "v_mfma_f32_16x16x32_bf16 a[40:43], a[212:215], v[248:251], a[40:43]\n"
    "v_mfma_f32_16x16x32_bf16 a[44:47], a[212:215], v[252:255], a[44:47]\n"
    "s_waitcnt lgkmcnt(0)\n"
    "v_mfma_f32_16x16x32_bf16 a[44:47], a[208:211], v[220:223], a[44:47]\n"
    "s_barrier\n"
    "s_waitcnt vmcnt(6)\n"
    "v_cmp_ne_u64 vcc, s[80:81], v[62:63]\n"
    "s_nop 1\n"
    "s_cbranch_vccnz L_slow4\n"
    "L_back4:\n"
    "v_mfma_f32_16x16x32_bf16 a[48:51], a[216:219], v[208:211], a[48:51]\n"
    "ds_read_b64_tr_b16 a[192:193], v6 offset:26624\n"
    "v_lshrrev_b32 v59, 1, v9\n"
    "v_mfma_f32_16x16x32_bf16 a[48:51], a[220:223], v[240:243], a[48:51]\n"
    "ds_read_b64_tr_b16 a[194:195], v6 offset:32768\n"
    "v_add_u32 v59, 2048, v59\n"
    "v_mfma_f32_16x16x32_bf16 a[52:55], a[220:223], v[244:247], a[52:55]\n"
    "s_add_u32 s73, s63, 0\n"
    "ds_read_b128 v[20:23], v18\n"
    "v_mfma_f32_16x16x32_bf16 a[52:55], a[216:219], v[212:215], a[52:55]\n"
    "s_mul_i32 s73, s73, 20480\n"
    "ds_read_b64_tr_b16 a[196:197], v6 offset:38912\n"
    "v_mfma_f32_16x16x32_bf16 a[56:59], a[216:219], v[216:219], a[56:59]\n"
    "s_add_u32 s88, s73, 7340032\n"
    "ds_read_b64_tr_b16 a[198:199], v6 offset:45056\n"
    "v_mfma_f32_16x16x32_bf16 a[56:59], a[220:223], v[248:251], a[56:59]\n"
    "s_add_u32 s73, s63, 1\n"
    "v_mfma_f32_16x16x32_bf16 a[60:63], a[220:223], v[252:255], a[60:63]\n"
    "s_mul_i32 s73, s73, 20480\n"
    "ds_read_b64_tr_b16 a[200:201], v7 offset:26624\n"
    "v_mfma_f32_16x16x32_bf16 a[60:63], a[216:219], v[220:223], a[60:63]\n"
    "s_add_u32 s89, s73, 7340032\n"
    "ds_read_b64_tr_b16 a[202:203], v7 offset:32768\n"
    "v_mfma_f32_16x16x32_bf16 a[76:79], a[224:227], v[220:223], a[76:79]\n"
    "buffer_load_dwordx4 v[80:83], v9, s[52:55], s88 offen offset:0 sc1\n"
    "v_mfma_f32_16x16x32_bf16 a[76:79], a[228:231], v[252:255], a[76:79]\n"
    "buffer_load_dwordx4 v[84:87], v9, s[52:55], s89 offen offset:0 sc1\n"
    "ds_read_b64_tr_b16 a[204:205], v7 offset:38912\n"
    "v_mfma_f32_16x16x32_bf16 a[72:75], a[228:231], v[248:251], a[72:75]\n"
    "buffer_load_dwordx4 v[88:91], v9, s[52:55], s88 offen offset:1024 sc1\n"
    "ds_read_b64_tr_b16 a[206:207], v7 offset:45056\n"
    "v_mfma_f32_16x16x32_bf16 a[72:75], a[224:227], v[216:219], a[72:75]\n"
    "s_waitcnt lgkmcnt(6)\n"
    "buffer_load_dwordx4 v[92:95], v9, s[52:55], s89 offen offset:1024 sc1\n"
    "v_or3_b32 v20, v20, v21, v22\n"
    "v_or_b32 v20, v20, v23\n"
    "v_mfma_f32_16x16x32_bf16 a[68:71], a[224:227], v[212:215], a[68:71]\n"
    "ds_read_b64_tr_b16 a[208:209], v4 offset:28672\n"
    "v_readfirstlane_b32 s93, v20\n"
    "v_mfma_f32_16x16x32_bf16 a[68:71], a[228:231], v[244:247], a[68:71]\n"
    "buffer_load_dwordx2 v[112:113], v59, s[52:55], s88 offen offset:0 sc1\n"
    "ds_read_b64_tr_b16 a[210:211], v4 offset:34816\n"
    "v_mfma_f32_16x16x32_bf16 a[64:67], a[228:231], v[240:243], a[64:67]\n"
    "s_andn2_b32 s93, s93, s75\n"
    "s_cbranch_scc1 L_retry\n"
    "buffer_load_dwordx2 v[114:115], v59, s[52:55], s88 offen offset:2560 sc1\n"
    "v_mfma_f32_16x16x32_bf16 a[64:67], a[224:227], v[208:211], a[64:67]\n"
    "ds_read_b64_tr_b16 a[212:213], v4 offset:40960\n"
    "v_mov_b32 v16, v48\n"
    "v_mfma_f32_16x16x32_bf16 a[80:83], a[232:235], v[208:211], a[80:83]\n"
    "ds_read_b64_tr_b16 a[214:215], v4 offset:47104\n"
    "v_mov_b32 v17, v49\n"
    "v_mfma_f32_16x16x32_bf16 a[80:83], a[236:239], v[240:243], a[80:83]\n"
    "buffer_load_dwordx2 v[116:117], v59, s[52:55], s89 offen offset:0 sc1\n"
    "v_mov_b32 v18, v50\n"
    "v_mfma_f32_16x16x32_bf16 a[84:87], a[236:239], v[244:247], a[84:87]\n"
    "v_mov_b32 v19, v51\n"
    "v_permlane16_swap_b32 v48, v16\n"
    "v_mfma_f32_16x16x32_bf16 a[84:87], a[232:235], v[212:215], a[84:87]\n"
    "v_permlane16_swap_b32 v49, v17\n"
    "v_permlane16_swap_b32 v50, v18\n"
    "v_mfma_f32_16x16x32_bf16 a[88:91], a[232:235], v[216:219], a[88:91]\n"
    "buffer_load_dwordx2 v[118:119], v59, s[52:55], s89 offen offset:2560 sc1\n"
    "v_permlane16_swap_b32 v51, v19\n"
    "v_mfma_f32_16x16x32_bf16 a[88:91], a[236:239], v[248:251], a[88:91]\n"
    "v_add_f32 v48, v48, v16\n"
    "v_add_f32 v49, v49, v17\n"
    "v_mfma_f32_16x16x32_bf16 a[92:95], a[236:239], v[252:255], a[92:95]\n"
    "s_waitcnt lgkmcnt(0)\n"
    "v_add_f32 v50, v50, v18\n"
    "v_add_f32 v51, v51, v19\n"
    "v_mfma_f32_16x16x32_bf16 a[92:95], a[232:235], v[220:223], a[92:95]\n"
    "buffer_load_dwordx4 v[120:123], v9, s[52:55], s88 offen offset:2560 sc1\n"
    "v_mov_b32 v16, v48\n"
    "v_mfma_f32_16x16x32_bf16 a[96:99], a[192:195], v[208:211], a[96:99]\n"
    "ds_read_b64_tr_b16 a[216:217], v5 offset:28672\n"
    "v_mov_b32 v17, v49\n"
    "v_mfma_f32_16x16x32_bf16 a[96:99], a[196:199], v[240:243], a[96:99]\n"
    "ds_read_b64_tr_b16 a[218:219], v5 offset:34816\n"
    "v_mov_b32 v18, v50\n"
    "v_mfma_f32_16x16x32_bf16 a[100:103], a[196:199], v[244:247], a[100:103]\n"
    "buffer_load_dwordx4 v[124:127], v9, s[52:55], s89 offen offset:2560 sc1\n"
    "v_mov_b32 v19, v51\n"
    "v_mfma_f32_16x16x32_bf16 a[100:103], a[192:195], v[212:215], a[100:103]\n"
    "ds_read_b64_tr_b16 a[220:221], v5 offset:40960\n"
    "v_permlane32_swap_b32 v48, v16\n"
    "v_mfma_f32_16x16x32_bf16 a[104:107], a[192:195], v[216:219], a[104:107]\n"
    "ds_read_b64_tr_b16 a[222:223], v5 offset:47104\n"
    "v_permlane32_swap_b32 v49, v17\n"
    "v_mfma_f32_16x16x32_bf16 a[104:107], a[196:199], v[248:251], a[104:107]\n"
    "buffer_load_dwordx4 v[144:147], v9, s[52:55], s88 offen offset:3584 sc1\n"
    "v_permlane32_swap_b32 v50, v18\n"
    "v_mfma_f32_16x16x32_bf16 a[108:111], a[196:199], v[252:255], a[108:111]\n"
    "ds_read_b64_tr_b16 a[224:225], v6 offset:28672\n"
    "v_permlane32_swap_b32 v51, v19\n"
    "v_mfma_f32_16x16x32_bf16 a[108:111], a[192:195], v[220:223], a[108:111]\n"
    "ds_read_b64_tr_b16 a[226:227], v6 offset:34816\n"
    "v_add_f32 v48, v48, v16\n"
    "v_mfma_f32_16x16x32_bf16 a[124:127], a[200:203], v[220:223], a[124:127]\n"
    "buffer_load_dwordx4 v[148:151], v9, s[52:55], s89 offen offset:3584 sc1\n"
    "v_add_f32 v49, v49, v17\n"
    "v_mfma_f32_16x16x32_bf16 a[124:127], a[204:207], v[252:255], a[124:127]\n"
    "ds_read_b64_tr_b16 a[228:229], v6 offset:40960\n"
    "v_add_f32 v50, v50, v18\n"
    "v_mfma_f32_16x16x32_bf16 a[120:123], a[204:207], v[248:251], a[120:123]\n"
    "ds_read_b64_tr_b16 a[230:231], v6 offset:47104\n"
    "v_add_f32 v51, v51, v19\n"
    "v_mfma_f32_16x16x32_bf16 a[120:123], a[200:203], v[216:219], a[120:123]\n"
    "s_add_u32 s73, s63, 0\n"
    "v_rcp_f32 v48, v48\n"
    "v_mfma_f32_16x16x32_bf16 a[116:119], a[200:203], v[212:215], a[116:119]\n"
    "s_mul_i32 s73, s73, 20480\n"
    "ds_read_b64_tr_b16 a[232:233], v7 offset:28672\n"
    "v_mfma_f32_16x16x32_bf16 a[116:119], a[204:207], v[244:247], a[116:119]\n"
    "s_add_u32 s88, s73, 7345152\n"
    "ds_read_b64_tr_b16 a[234:235], v7 offset:34816\n"
    "v_mfma_f32_16x16x32_bf16 a[112:115], a[204:207], v[240:243], a[112:115]\n"
    "s_add_u32 s73, s63, 1\n"
    "v_rcp_f32 v49, v49\n"
    "v_mfma_f32_16x16x32_bf16 a[112:115], a[200:203], v[208:211], a[112:115]\n"
    "s_mul_i32 s73, s73, 20480\n"
    "ds_read_b64_tr_b16 a[236:237], v7 offset:40960\n"
    "v_mfma_f32_16x16x32_bf16 a[128:131], a[208:211], v[208:211], a[128:131]\n"
    "s_add_u32 s89, s73, 7345152\n"
    "ds_read_b64_tr_b16 a[238:239], v7 offset:47104\n"
    "v_mfma_f32_16x16x32_bf16 a[128:131], a[212:215], v[240:243], a[128:131]\n"
    "buffer_load_dwordx4 v[152:155], v9, s[52:55], s88 offen offset:0 sc1\n"
    "v_mfma_f32_16x16x32_bf16 a[132:135], a[212:215], v[244:247], a[132:135]\n"
    "buffer_load_dwordx4 v[156:159], v9, s[52:55], s89 offen offset:0 sc1\n"
    "v_mfma_f32_16x16x32_bf16 a[132:135], a[208:211], v[212:215], a[132:135]\n"
    "buffer_load_dwordx4 v[176:179], v9, s[52:55], s88 offen offset:1024 sc1\n"
    "v_mfma_f32_16x16x32_bf16 a[136:139], a[208:211], v[216:219], a[136:139]\n"
    "buffer_load_dwordx4 v[180:183], v9, s[52:55], s89 offen offset:1024 sc1\n"
    "v_mfma_f32_16x16x32_bf16 a[136:139], a[212:215], v[248:251], a[136:139]\n"
    "v_rcp_f32 v50, v50\n"
    "v_mfma_f32_16x16x32_bf16 a[140:143], a[212:215], v[252:255], a[140:143]\n"
    "s_waitcnt lgkmcnt(0)\n"
    "buffer_load_dwordx2 v[184:185], v59, s[52:55], s88 offen offset:0 sc1\n"
    "v_mfma_f32_16x16x32_bf16 a[140:143], a[208:211], v[220:223], a[140:143]\n"
    "v_rcp_f32 v51, v51\n"
    "v_mfma_f32_16x16x32_bf16 a[144:147], a[216:219], v[208:211], a[144:147]\n"
    "s_lshl_b32 s65, s77, 7\n"
    "v_mfma_f32_16x16x32_bf16 a[144:147], a[220:223], v[240:243], a[144:147]\n"
    "s_lshl_b32 s92, s78, 2\n"
    "buffer_load_dwordx2 v[186:187], v59, s[52:55], s88 offen offset:2560 sc1\n"
    "v_mfma_f32_16x16x32_bf16 a[148:151], a[220:223], v[244:247], a[148:151]\n"
    "s_add_u32 s65, s65, s92\n"
    "v_mfma_f32_16x16x32_bf16 a[148:151], a[216:219], v[212:215], a[148:151]\n"
    "s_add_u32 s65, s65, s76\n"
    "v_mfma_f32_16x16x32_bf16 a[152:155], a[216:219], v[216:219], a[152:155]\n"
    "s_mul_i32 s65, s65, 24576\n"
    "buffer_load_dwordx2 v[188:189], v59, s[52:55], s89 offen offset:0 sc1\n"
    "v_mfma_f32_16x16x32_bf16 a[152:155], a[220:223], v[248:251], a[152:155]\n"
    "s_add_u32 s65, s65, 18874368\n"
    "v_mfma_f32_16x16x32_bf16 a[156:159], a[220:223], v[252:255], a[156:159]\n"
    "s_mul_i32 s92, s79, 786432\n"
    "v_mfma_f32_16x16x32_bf16 a[156:159], a[216:219], v[220:223], a[156:159]\n"
    "s_add_u32 s92, s92, s65\n"
    "buffer_load_dwordx2 v[190:191], v59, s[52:55], s89 offen offset:2560 sc1\n"
    "v_mfma_f32_16x16x32_bf16 a[172:175], a[224:227], v[220:223], a[172:175]\n"
    "s_add_u32 s66, s92, 0\n"
    "v_mfma_f32_16x16x32_bf16 a[172:175], a[228:231], v[252:255], a[172:175]\n"
    "s_add_u32 s67, s66, 4096\n"
    "v_accvgpr_read_b32 v16, a0\n"
    "v_accvgpr_read_b32 v17, a1\n"
    "v_mfma_f32_16x16x32_bf16 a[168:171], a[228:231], v[248:251], a[168:171]\n"
    "buffer_load_dwordx4 v[64:67], v9, s[52:55], s88 offen offset:2560 sc1\n"
    "v_accvgpr_read_b32 v18, a2\n"
    "v_mfma_f32_16x16x32_bf16 a[168:171], a[224:227], v[216:219], a[168:171]\n"
    "v_accvgpr_read_b32 v19, a3\n"
    "v_mul_f32 v16, v48, v16\n"
    "v_mfma_f32_16x16x32_bf16 a[164:167], a[224:227], v[212:215], a[164:167]\n"
    "v_mul_f32 v17, v48, v17\n"
    "v_mul_f32 v18, v48, v18\n"
    "v_mfma_f32_16x16x32_bf16 a[164:167], a[228:231], v[244:247], a[164:167]\n"
    "buffer_load_dwordx4 v[68:71], v9, s[52:55], s89 offen offset:2560 sc1\n"
    "v_mul_f32 v19, v48, v19\n"
    "v_mfma_f32_16x16x32_bf16 a[160:163], a[228:231], v[240:243], a[160:163]\n"
    "v_cvt_pk_bf16_f32 v52, v16, v17\n"
    "v_cvt_pk_bf16_f32 v53, v18, v19\n"
    "v_mfma_f32_16x16x32_bf16 a[160:163], a[224:227], v[208:211], a[160:163]\n"
    "v_accvgpr_read_b32 v20, a16\n"
    "v_accvgpr_read_b32 v21, a17\n"
    "v_mfma_f32_16x16x32_bf16 a[176:179], a[232:235], v[208:211], a[176:179]\n"
    "buffer_load_dwordx4 v[72:75], v9, s[52:55], s88 offen offset:3584 sc1\n"
    "v_accvgpr_read_b32 v22, a18\n"
    "v_mfma_f32_16x16x32_bf16 a[176:179], a[236:239], v[240:243], a[176:179]\n"
    "v_accvgpr_read_b32 v23, a19\n"
    "v_mul_f32 v20, v48, v20\n"
    "v_mfma_f32_16x16x32_bf16 a[180:183], a[236:239], v[244:247], a[180:183]\n"
    "v_mul_f32 v21, v48, v21\n"
    "v_mul_f32 v22, v48, v22\n"
    "v_mfma_f32_16x16x32_bf16 a[180:183], a[232:235], v[212:215], a[180:183]\n"
    "buffer_load_dwordx4 v[76:79], v9, s[52:55], s89 offen offset:3584 sc1\n"
    "v_mul_f32 v23, v48, v23\n"
    "v_mfma_f32_16x16x32_bf16 a[184:187], a[232:235], v[216:219], a[184:187]\n"
    "v_cvt_pk_bf16_f32 v54, v20, v21\n"
    "v_cvt_pk_bf16_f32 v55, v22, v23\n"
    "v_mfma_f32_16x16x32_bf16 a[184:187], a[236:239], v[248:251], a[184:187]\n"
    "v_accvgpr_write_b32 a0, v52\n"
    "v_accvgpr_write_b32 a1, v53\n"
    "v_mfma_f32_16x16x32_bf16 a[188:191], a[236:239], v[252:255], a[188:191]\n"
    "v_accvgpr_write_b32 a2, v54\n"
    "v_accvgpr_write_b32 a3, v55\n"
    "v_mfma_f32_16x16x32_bf16 a[188:191], a[232:235], v[220:223], a[188:191]\n"
    "buffer_store_dwordx4 v[52:55], v9, s[52:55], s66 offen offset:0 sc1\n"
    "v_accvgpr_read_b32 v16, a4\n"
    "v_lshrrev_b32 v57, 6, v9\n"
    "v_mul_u32_u24 v57, 192, v57\n"
    "v_bfe_u32 v8, v9, 4, 2\n"
    "v_lshl_add_u32 v57, v8, 3, v57\n"
    "v_bfe_u32 v8, v9, 8, 1\n"
    "v_lshl_add_u32 v2, v8, 5, v57\n"
    "v_xor_b32 v8, 1, v8\n"
    "v_lshl_add_u32 v3, v8, 5, v57\n"
    "v_add_u32 v4, 49152, v2\n"
    "v_add_u32 v5, 49152, v3\n"
    "v_add_u32 v6, 98304, v2\n"
    "v_add_u32 v7, 98304, v3\n"
    "s_waitcnt vmcnt(21)\n"
    "s_barrier\n"
    "ds_read_b64_tr_b16 a[192:193], v2 offset:0\n"
    "ds_read_b64_tr_b16 a[194:195], v2 offset:3072\n"
    "ds_read_b64_tr_b16 a[196:197], v2 offset:6144\n"
    "ds_read_b64_tr_b16 a[198:199], v2 offset:9216\n"
    "ds_read_b64_tr_b16 a[200:201], v3 offset:0\n"
    "ds_read_b64_tr_b16 a[202:203], v3 offset:3072\n"
    "ds_read_b64_tr_b16 a[204:205], v3 offset:6144\n"
    "ds_read_b64_tr_b16 a[206:207], v3 offset:9216\n"
    "s_waitcnt lgkmcnt(6)\n"
    "v_mfma_f32_16x16x32_bf16 v[208:211], a[192:195], v[80:83], 0\n"
    "s_waitcnt lgkmcnt(4)\n"
    "s_add_u32 s90, s90, 196608\n"
    "ds_read_b64_tr_b16 a[208:209], v2 offset:64\n"
    "v_accvgpr_read_b32 v17, a5\n"
    "v_accvgpr_read_b32 v18, a6\n"
    "v_mfma_f32_16x16x32_bf16 v[208:211], a[196:199], v[88:91], v[208:211]\n"
    "s_add_u32 m0, s64, 122880\n"
    "ds_read_b64_tr_b16 a[210:211], v2 offset:3136\n"
    "v_accvgpr_read_b32 v19, a7\n"
    "v_mul_f32 v16, v49, v16\n"
    "v_mfma_f32_16x16x32_bf16 v[212:215], a[196:199], v[92:95], 0\n"
    "buffer_load_dwordx4 v58, s[44:47], s90 offen offset:0 lds\n"
    "ds_read_b64_tr_b16 a[212:213], v2 offset:6208\n"
    "v_mul_f32 v17, v49, v17\n"
    "v_mfma_f32_16x16x32_bf16 v[212:215], a[192:195], v[84:87], v[212:215]\n"
    "s_waitcnt lgkmcnt(5)\n"
    "ds_read_b64_tr_b16 a[214:215], v2 offset:9280\n"
    "v_mul_f32 v18, v49, v18\n"
    "v_mul_f32 v19, v49, v19\n"
    "v_mfma_f32_16x16x32_bf16 v[216:219], a[200:203], v[80:83], 0\n"
    "s_waitcnt lgkmcnt(4)\n"
    "buffer_load_dwordx4 v60, s[44:47], s90 offen offset:1024 lds\n"
    "ds_read_b64_tr_b16 a[216:217], v3 offset:64\n"
    "v_cvt_pk_bf16_f32 v24, v16, v17\n"
    "v_mfma_f32_16x16x32_bf16 v[216:219], a[204:207], v[88:91], v[216:219]\n"
    "ds_read_b64_tr_b16 a[218:219], v3 offset:3136\n"
    "v_cvt_pk_bf16_f32 v25, v18, v19\n"
    "v_accvgpr_read_b32 v20, a20\n"
    "v_mfma_f32_16x16x32_bf16 v[220:223], a[204:207], v[92:95], 0\n"
    "ds_read_b64_tr_b16 a[220:221], v3 offset:6208\n"
    "v_accvgpr_read_b32 v21, a21\n"
    "v_accvgpr_read_b32 v22, a22\n"
    "v_mfma_f32_16x16x32_bf16 v[220:223], a[200:203], v[84:87], v[220:223]\n"
    "s_waitcnt lgkmcnt(5)\n"
    "ds_read_b64_tr_b16 a[222:223], v3 offset:9280\n"
    "v_accvgpr_read_b32 v23, a23\n"
    "v_mul_f32 v20, v49, v20\n"
    "v_mfma_f32_16x16x32_bf16 v[240:243], a[208:211], v[80:83], 0\n"
    "s_waitcnt lgkmcnt(4)\n"
    "buffer_load_dwordx4 v61, s[44:47], s90 offen offset:2048 lds\n"
    "ds_read_b64_tr_b16 a[224:225], v2 offset:128\n"
    "v_mul_f32 v21, v49, v21\n"
    "v_mfma_f32_16x16x32_bf16 v[240:243], a[212:215], v[88:91], v[240:243]\n"
    "ds_read_b64_tr_b16 a[226:227], v2 offset:3200\n"
    "v_mul_f32 v22, v49, v22\n"
    "v_mul_f32 v23, v49, v23\n"
    "v_mfma_f32_16x16x32_bf16 v[244:247], a[212:215], v[92:95], 0\n"
    "s_add_u32 s73, s63, 0\n"
    "ds_read_b64_tr_b16 a[228:229], v2 offset:6272\n"
    "v_cvt_pk_bf16_f32 v26, v20, v21\n"
    "v_cvt_pk_bf16_f32 v27, v22, v23\n"
    "v_mfma_f32_16x16x32_bf16 v[244:247], a[208:211], v[84:87], v[244:247]\n"
    "s_waitcnt lgkmcnt(5)\n"
    "s_mul_i32 s73, s73, 20480\n"
    "ds_read_b64_tr_b16 a[230:231], v2 offset:9344\n"
    "v_accvgpr_write_b32 a4, v24\n"
    "v_accvgpr_write_b32 a5, v25\n"
    "v_mfma_f32_16x16x32_bf16 v[248:251], a[216:219], v[80:83], 0\n"
    "s_waitcnt lgkmcnt(4)\n"
    "s_add_u32 s88, s73, 7350272\n"
    "ds_read_b64_tr_b16 a[232:233], v3 offset:128\n"
    "v_accvgpr_write_b32 a6, v26\n"
    "v_accvgpr_write_b32 a7, v27\n"
    "v_mfma_f32_16x16x32_bf16 v[248:251], a[220:223], v[88:91], v[248:251]\n"
    "s_add_u32 s73, s63, 1\n"
    "buffer_store_dwordx4 v[24:27], v9, s[52:55], s66 offen offset:2048 sc1\n"
    "ds_read_b64_tr_b16 a[234:235], v3 offset:3200\n"
    "v_accvgpr_read_b32 v16, a8\n"
    "v_mfma_f32_16x16x32_bf16 v[252:255], a[220:223], v[92:95], 0\n"
    "s_mul_i32 s73, s73, 20480\n"
    "ds_read_b64_tr_b16 a[236:237], v3 offset:6272\n"
    "v_accvgpr_read_b32 v17, a9\n"
    "v_accvgpr_read_b32 v18, a10\n"
    "v_mfma_f32_16x16x32_bf16 v[252:255], a[216:219], v[84:87], v[252:255]\n"
    "s_waitcnt lgkmcnt(5)\n"
    "s_add_u32 s89, s73, 7350272\n"
    "ds_read_b64_tr_b16 a[238:239], v3 offset:9344\n"
    "v_accvgpr_read_b32 v19, a11\n"
    "v_mul_f32 v16, v50, v16\n"
    "v_mfma_f32_16x16x32_bf16 a[240:243], a[224:227], v[80:83], 0\n"
    "s_waitcnt lgkmcnt(4)\n"
    "buffer_load_dwordx4 v[96:99], v9, s[52:55], s88 offen offset:0 sc1\n"
    "ds_read_b64_tr_b16 a[192:193], v2 offset:12288\n"
    "v_mul_f32 v17, v50, v17\n"
    "v_mfma_f32_16x16x32_bf16 a[240:243], a[228:231], v[88:91], a[240:243]\n"
    "buffer_load_dwordx4 v[100:103], v9, s[52:55], s89 offen offset:0 sc1\n"
    "ds_read_b64_tr_b16 a[194:195], v4 offset:3072\n"
    "v_mul_f32 v18, v50, v18\n"
    "v_mfma_f32_16x16x32_bf16 a[244:247], a[228:231], v[92:95], 0\n"
    "buffer_load_dwordx4 v[104:107], v9, s[52:55], s88 offen offset:1024 sc1\n"
    "ds_read_b64_tr_b16 a[196:197], v2 offset:15360\n"
    "v_mul_f32 v19, v50, v19\n"
    "v_mfma_f32_16x16x32_bf16 a[244:247], a[224:227], v[84:87], a[244:247]\n"
    "s_waitcnt lgkmcnt(5)\n"
    "ds_read_b64_tr_b16 a[198:199], v2 offset:18432\n"
    "v_cvt_pk_bf16_f32 v52, v16, v17\n"
    "v_cvt_pk_bf16_f32 v53, v18, v19\n"
    "v_mfma_f32_16x16x32_bf16 a[248:251], a[232:235], v[80:83], 0\n"
    "s_waitcnt lgkmcnt(4)\n"
    "ds_read_b64_tr_b16 a[200:201], v3 offset:12288\n"
    "v_accvgpr_read_b32 v20, a24\n"
    "v_accvgpr_read_b32 v21, a25\n"
    "v_mfma_f32_16x16x32_bf16 a[248:251], a[236:239], v[88:91], a[248:251]\n"
    "buffer_load_dwordx4 v[108:111], v9, s[52:55], s89 offen offset:1024 sc1\n"
    "ds_read_b64_tr_b16 a[202:203], v5 offset:3072\n"
    "v_accvgpr_read_b32 v22, a26\n"
    "v_mfma_f32_16x16x32_bf16 a[252:255], a[236:239], v[92:95], 0\n"
    "s_waitcnt vmcnt(23)\n"
    "ds_read_b64_tr_b16 a[204:205], v3 offset:15360\n"
    "v_accvgpr_read_b32 v23, a27\n"
    "v_mul_f32 v20, v50, v20\n"
    "v_mfma_f32_16x16x32_bf16 a[252:255], a[232:235], v[84:87], a[252:255]\n"
    "s_waitcnt lgkmcnt(5)\n"
    "ds_read_b64_tr_b16 a[206:207], v3 offset:18432\n"
    "v_mul_f32 v21, v50, v21\n"
    "v_mul_f32 v22, v50, v22\n"
    "v_mfma_f32_16x16x32_bf16 v[208:211], a[192:195], v[112:115], v[208:211]\n"
    "s_waitcnt lgkmcnt(4)\n"
    "ds_read_b64_tr_b16 a[208:209], v2 offset:12352\n"
    "v_mul_f32 v23, v50, v23\n"
    "v_cvt_pk_bf16_f32 v54, v20, v21\n"
    "v_mfma_f32_16x16x32_bf16 v[208:211], a[196:199], v[120:123], v[208:211]\n"
    "buffer_load_dwordx2 v[128:129], v59, s[52:55], s88 offen offset:0 sc1\n"
    "ds_read_b64_tr_b16 a[210:211], v4 offset:3136\n"
    "v_cvt_pk_bf16_f32 v55, v22, v23\n"
    "v_mfma_f32_16x16x32_bf16 v[212:215], a[196:199], v[124:127], v[212:215]\n"
    "ds_read_b64_tr_b16 a[212:213], v2 offset:15424\n"
    "v_accvgpr_write_b32 a8, v52\n"
    "v_accvgpr_write_b32 a9, v53\n"
    "v_mfma_f32_16x16x32_bf16 v[212:215], a[192:195], v[116:119], v[212:215]\n"
    "s_waitcnt lgkmcnt(5)\n"
    "ds_read_b64_tr_b16 a[214:215], v2 offset:18496\n"
    "v_accvgpr_write_b32 a10, v54\n"
    "v_accvgpr_write_b32 a11, v55\n"
    "v_mfma_f32_16x16x32_bf16 v[216:219], a[200:203], v[112:115], v[216:219]\n"
    "s_waitcnt lgkmcnt(4)\n"
    "buffer_load_dwordx2 v[130:131], v59, s[52:55], s88 offen offset:2560 sc1\n"
    "ds_read_b64_tr_b16 a[216:217], v3 offset:12352\n"
    "v_mfma_f32_16x16x32_bf16 v[216:219], a[204:207], v[120:123], v[216:219]\n"
    "buffer_store_dwordx4 v[52:55], v9, s[52:55], s67 offen offset:0 sc1\n"
    "ds_read_b64_tr_b16 a[218:219], v5 offset:3136\n"
    "v_accvgpr_read_b32 v16, a12\n"
    "v_mfma_f32_16x16x32_bf16 v[220:223], a[204:207], v[124:127], v[220:223]\n"
    "ds_read_b64_tr_b16 a[220:221], v3 offset:15424\n"
    "v_accvgpr_read_b32 v17, a13\n"
    "v_accvgpr_read_b32 v18, a14\n"
    "v_mfma_f32_16x16x32_bf16 v[220:223], a[200:203], v[116:119], v[220:223]\n"
    "s_waitcnt lgkmcnt(5)\n"
    "ds_read_b64_tr_b16 a[222:223], v3 offset:18496\n"
    "v_accvgpr_read_b32 v19, a15\n"
    "v_mul_f32 v16, v51, v16\n"
    "v_mfma_f32_16x16x32_bf16 v[240:243], a[208:211], v[112:115], v[240:243]\n"
    "s_waitcnt lgkmcnt(4)\n"
    "buffer_load_dwordx2 v[132:133], v59, s[52:55], s89 offen offset:0 sc1\n"
    "ds_read_b64_tr_b16 a[224:225], v2 offset:12416\n"
    "v_mul_f32 v17, v51, v17\n"
    "v_mfma_f32_16x16x32_bf16 v[240:243], a[212:215], v[120:123], v[240:243]\n"
    "ds_read_b64_tr_b16 a[226:227], v4 offset:3200\n"
    "v_mul_f32 v18, v51, v18\n"
    "v_mul_f32 v19, v51, v19\n"
    "v_mfma_f32_16x16x32_bf16 v[244:247], a[212:215], v[124:127], v[244:247]\n"
    "ds_read_b64_tr_b16 a[228:229], v2 offset:15488\n"
    "v_cvt_pk_bf16_f32 v24, v16, v17\n"
    "v_cvt_pk_bf16_f32 v25, v18, v19\n"
    "v_mfma_f32_16x16x32_bf16 v[244:247], a[208:211], v[116:119], v[244:247]\n"
    "s_waitcnt lgkmcnt(5)\n"
    "ds_read_b64_tr_b16 a[230:231], v2 offset:18560\n"
    "v_accvgpr_read_b32 v20, a28\n"
    "v_accvgpr_read_b32 v21, a29\n"
    "v_mfma_f32_16x16x32_bf16 v[248:251], a[216:219], v[112:115], v[248:251]\n"
    "s_waitcnt lgkmcnt(4)\n"
    "buffer_load_dwordx2 v[134:135], v59, s[52:55], s89 offen offset:2560 sc1\n"
    "ds_read_b64_tr_b16 a[232:233], v3 offset:12416\n"
    "v_accvgpr_read_b32 v22, a30\n"
    "v_mfma_f32_16x16x32_bf16 v[248:251], a[220:223], v[120:123], v[248:251]\n"
    "ds_read_b64_tr_b16 a[234:235], v5 offset:3200\n"
    "v_accvgpr_read_b32 v23, a31\n"
    "v_mul_f32 v20, v51, v20\n"
    "v_mfma_f32_16x16x32_bf16 v[252:255], a[220:223], v[124:127], v[252:255]\n"
    "ds_read_b64_tr_b16 a[236:237], v3 offset:15488\n"
    "v_mul_f32 v21, v51, v21\n"
    "v_mul_f32 v22, v51, v22\n"
    "v_mfma_f32_16x16x32_bf16 v[252:255], a[216:219], v[116:119], v[252:255]\n"
    "s_waitcnt lgkmcnt(5)\n"
    "ds_read_b64_tr_b16 a[238:239], v3 offset:18560\n"
    "v_mul_f32 v23, v51, v23\n"
    "v_cvt_pk_bf16_f32 v26, v20, v21\n"
    "v_mfma_f32_16x16x32_bf16 a[240:243], a[224:227], v[112:115], a[240:243]\n"
    "s_waitcnt lgkmcnt(4)\n"
    "buffer_load_dwordx4 v[136:139], v9, s[52:55], s88 offen offset:2560 sc1\n"
    "ds_read_b64_tr_b16 a[192:193], v2 offset:21504\n"
    "v_cvt_pk_bf16_f32 v27, v22, v23\n"
    "v_mfma_f32_16x16x32_bf16 a[240:243], a[228:231], v[120:123], a[240:243]\n"
    "ds_read_b64_tr_b16 a[194:195], v4 offset:0\n"
    "v_accvgpr_write_b32 a12, v24\n"
    "v_accvgpr_write_b32 a13, v25\n"
    "v_mfma_f32_16x16x32_bf16 a[244:247], a[228:231], v[124:127], a[244:247]\n"
    "ds_read_b64_tr_b16 a[196:197], v4 offset:6144\n"
    "v_accvgpr_write_b32 a14, v26\n"
    "v_accvgpr_write_b32 a15, v27\n"
    "v_mfma_f32_16x16x32_bf16 a[244:247], a[224:227], v[116:119], a[244:247]\n"
    "s_waitcnt lgkmcnt(5)\n"
    "buffer_load_dwordx4 v[140:143], v9, s[52:55], s89 offen offset:2560 sc1\n"
    "ds_read_b64_tr_b16 a[198:199], v4 offset:9216\n"
    "v_mfma_f32_16x16x32_bf16 a[248:251], a[232:235], v[112:115], a[248:251]\n"
    "s_waitcnt lgkmcnt(4)\n"
    "buffer_store_dwordx4 v[24:27], v9, s[52:55], s67 offen offset:2048 sc1\n"
    "ds_read_b64_tr_b16 a[200:201], v3 offset:21504\n"
    "v_mfma_f32_16x16x32_bf16 a[248:251], a[236:239], v[120:123], a[248:251]\n"
    "s_add_u32 s66, s92, 0\n"
    "ds_read_b64_tr_b16 a[202:203], v5 offset:0\n"
    "v_mfma_f32_16x16x32_bf16 a[252:255], a[236:239], v[124:127], a[252:255]\n"
    "s_waitcnt vmcnt(27)\n"
    "ds_read_b64_tr_b16 a[204:205], v5 offset:6144\n"
    "v_mfma_f32_16x16x32_bf16 a[252:255], a[232:235], v[116:119], a[252:255]\n"
    "s_waitcnt lgkmcnt(5)\n"
    "s_add_u32 s67, s66, 4096\n"
    "buffer_load_dwordx4 v[160:163], v9, s[52:55], s88 offen offset:3584 sc1\n"
    "ds_read_b64_tr_b16 a[206:207], v5 offset:9216\n"
    "v_accvgpr_read_b32 v16, a32\n"
    "v_mfma_f32_16x16x32_bf16 v[208:211], a[192:195], v[144:147], v[208:211]\n"
    "s_waitcnt lgkmcnt(4)\n"
    "ds_read_b64_tr_b16 a[208:209], v2 offset:21568\n"
    "v_accvgpr_read_b32 v17, a33\n"
    "v_accvgpr_read_b32 v18, a34\n"
    "v_mfma_f32_16x16x32_bf16 v[208:211], a[196:199], v[152:155], v[208:211]\n"
    "ds_read_b64_tr_b16 a[210:211], v4 offset:64\n"
    "v_accvgpr_read_b32 v19, a35\n"
    "v_mul_f32 v16, v48, v16\n"
    "v_mfma_f32_16x16x32_bf16 v[212:215], a[196:199], v[156:159], v[212:215]\n"
    "ds_read_b64_tr_b16 a[212:213], v4 offset:6208\n"
    "v_mul_f32 v17, v48, v17\n"
    "v_mul_f32 v18, v48, v18\n"
    "v_mfma_f32_16x16x32_bf16 v[212:215], a[192:195], v[148:151], v[212:215]\n"
    "s_waitcnt lgkmcnt(5)\n"
    "buffer_load_dwordx4 v[164:167], v9, s[52:55], s89 offen offset:3584 sc1\n"
    "ds_read_b64_tr_b16 a[214:215], v4 offset:9280\n"
    "v_mul_f32 v19, v48, v19\n"
    "v_mfma_f32_16x16x32_bf16 v[216:219], a[200:203], v[144:147], v[216:219]\n"
    "s_waitcnt lgkmcnt(4)\n"
    "ds_read_b64_tr_b16 a[216:217], v3 offset:21568\n"
    "v_cvt_pk_bf16_f32 v52, v16, v17\n"
    "v_cvt_pk_bf16_f32 v53, v18, v19\n"
    "v_mfma_f32_16x16x32_bf16 v[216:219], a[204:207], v[152:155], v[216:219]\n"
    "ds_read_b64_tr_b16 a[218:219], v5 offset:64\n"
    "v_accvgpr_read_b32 v20, a48\n"
    "v_accvgpr_read_b32 v21, a49\n"
    "v_mfma_f32_16x16x32_bf16 v[220:223], a[204:207], v[156:159], v[220:223]\n"
    "ds_read_b64_tr_b16 a[220:221], v5 offset:6208\n"
    "v_accvgpr_read_b32 v22, a50\n"
    "v_accvgpr_read_b32 v23, a51\n"
    "v_mfma_f32_16x16x32_bf16 v[220:223], a[200:203], v[148:151], v[220:223]\n"
    "s_waitcnt lgkmcnt(5)\n"
    "s_add_u32 s73, s63, 0\n"
    "ds_read_b64_tr_b16 a[222:223], v5 offset:9280\n"
    "v_mul_f32 v20, v48, v20\n"
    "v_mul_f32 v21, v48, v21\n"
    "v_mfma_f32_16x16x32_bf16 v[240:243], a[208:211], v[144:147], v[240:243]\n"
    "s_waitcnt lgkmcnt(4)\n"
    "s_mul_i32 s73, s73, 20480\n"
    "ds_read_b64_tr_b16 a[224:225], v2 offset:21632\n"
    "v_mul_f32 v22, v48, v22\n"
    "v_mul_f32 v23, v48, v23\n"
    "v_mfma_f32_16x16x32_bf16 v[240:243], a[212:215], v[152:155], v[240:243]\n"
    "s_add_u32 s88, s73, 7355392\n"
    "ds_read_b64_tr_b16 a[226:227], v4 offset:128\n"
    "v_cvt_pk_bf16_f32 v54, v20, v21\n"
    "v_cvt_pk_bf16_f32 v55, v22, v23\n"
    "v_mfma_f32_16x16x32_bf16 v[244:247], a[212:215], v[156:159], v[244:247]\n"
    "s_add_u32 s73, s63, 1\n"
    "ds_read_b64_tr_b16 a[228:229], v4 offset:6272\n"
    "v_accvgpr_write_b32 a32, v52\n"
    "v_accvgpr_write_b32 a33, v53\n"
    "v_mfma_f32_16x16x32_bf16 v[244:247], a[208:211], v[148:151], v[244:247]\n"
    "s_waitcnt lgkmcnt(5)\n"
    "s_mul_i32 s73, s73, 20480\n"
    "ds_read_b64_tr_b16 a[230:231], v4 offset:9344\n"
    "v_accvgpr_write_b32 a34, v54\n"
    "v_accvgpr_write_b32 a35, v55\n"
    "v_mfma_f32_16x16x32_bf16 v[248:251], a[216:219], v[144:147], v[248:251]\n"
    "s_waitcnt lgkmcnt(4)\n"
    "s_add_u32 s89, s73, 7355392\n"
    "buffer_store_dwordx4 v[52:55], v9, s[52:55], s66 offen offset:1024 sc1\n"
    "ds_read_b64_tr_b16 a[232:233], v3 offset:21632\n"
    "v_accvgpr_read_b32 v16, a36\n"
    "v_mfma_f32_16x16x32_bf16 v[248:251], a[220:223], v[152:155], v[248:251]\n"
    "buffer_load_dwordx4 v[168:171], v9, s[52:55], s88 offen offset:0 sc1\n"
    "ds_read_b64_tr_b16 a[234:235], v5 offset:128\n"
    "v_accvgpr_read_b32 v17, a37\n"
    "v_mfma_f32_16x16x32_bf16 v[252:255], a[220:223], v[156:159], v[252:255]\n"
    "buffer_load_dwordx4 v[172:175], v9, s[52:55], s89 offen offset:0 sc1\n"
    "ds_read_b64_tr_b16 a[236:237], v5 offset:6272\n"
    "v_accvgpr_read_b32 v18, a38\n"
    "v_mfma_f32_16x16x32_bf16 v[252:255], a[216:219], v[148:151], v[252:255]\n"
    "s_waitcnt lgkmcnt(5)\n"
    "buffer_load_dwordx4 v[192:195], v9, s[52:55], s88 offen offset:1024 sc1\n"
    "ds_read_b64_tr_b16 a[238:239], v5 offset:9344\n"
    "v_accvgpr_read_b32 v19, a39\n"
    "v_mfma_f32_16x16x32_bf16 a[240:243], a[224:227], v[144:147], a[240:243]\n"
    "s_waitcnt lgkmcnt(4)\n"
    "ds_read_b64_tr_b16 a[192:193], v4 offset:12288\n"
    "v_mul_f32 v16, v49, v16\n"
    "v_mul_f32 v17, v49, v17\n"
    "v_mfma_f32_16x16x32_bf16 a[240:243], a[228:231], v[152:155], a[240:243]\n"
    "ds_read_b64_tr_b16 a[194:195], v4 offset:15360\n"
    "v_mul_f32 v18, v49, v18\n"
    "v_mul_f32 v19, v49, v19\n"
    "v_mfma_f32_16x16x32_bf16 a[244:247], a[228:231], v[156:159], a[244:247]\n"
    "buffer_load_dwordx4 v[196:199], v9, s[52:55], s89 offen offset:1024 sc1\n"
    "ds_read_b64_tr_b16 a[196:197], v4 offset:18432\n"
    "v_cvt_pk_bf16_f32 v24, v16, v17\n"
    "v_mfma_f32_16x16x32_bf16 a[244:247], a[224:227], v[148:151], a[244:247]\n"
    "s_waitcnt lgkmcnt(5)\n"
    "ds_read_b64_tr_b16 a[198:199], v4 offset:33792\n"
    "v_cvt_pk_bf16_f32 v25, v18, v19\n"
    "v_accvgpr_read_b32 v20, a52\n"
    "v_mfma_f32_16x16x32_bf16 a[248:251], a[232:235], v[144:147], a[248:251]\n"
    "s_waitcnt lgkmcnt(4)\n"
    "ds_read_b64_tr_b16 a[200:201], v5 offset:12288\n"
    "v_accvgpr_read_b32 v21, a53\n"
    "v_accvgpr_read_b32 v22, a54\n"
    "v_mfma_f32_16x16x32_bf16 a[248:251], a[236:239], v[152:155], a[248:251]\n"
    "ds_read_b64_tr_b16 a[202:203], v5 offset:15360\n"
    "v_accvgpr_read_b32 v23, a55\n"
    "v_mul_f32 v20, v49, v20\n"
    "v_mfma_f32_16x16x32_bf16 a[252:255], a[236:239], v[156:159], a[252:255]\n"
    "s_waitcnt vmcnt(28)\n"
    "buffer_load_dwordx2 v[200:201], v59, s[52:55], s88 offen offset:0 sc1\n"
    "ds_read_b64_tr_b16 a[204:205], v5 offset:18432\n"
    "v_mul_f32 v21, v49, v21\n"
    "v_mfma_f32_16x16x32_bf16 a[252:255], a[232:235], v[148:151], a[252:255]\n"
    "s_waitcnt lgkmcnt(5)\n"
    "ds_read_b64_tr_b16 a[206:207], v5 offset:33792\n"
    "v_mul_f32 v22, v49, v22\n"
    "v_mul_f32 v23, v49, v23\n"
    "v_mfma_f32_16x16x32_bf16 v[208:211], a[192:195], v[176:179], v[208:211]\n"
    "s_waitcnt lgkmcnt(4)\n"
    "ds_read_b64_tr_b16 a[208:209], v4 offset:12352\n"
    "v_cvt_pk_bf16_f32 v26, v20, v21\n"
    "v_cvt_pk_bf16_f32 v27, v22, v23\n"
    "v_mfma_f32_16x16x32_bf16 v[208:211], a[196:199], v[184:187], v[208:211]\n"
    "buffer_load_dwordx2 v[202:203], v59, s[52:55], s88 offen offset:2560 sc1\n"
    "ds_read_b64_tr_b16 a[210:211], v4 offset:15424\n"
    "v_accvgpr_write_b32 a36, v24\n"
    "v_mfma_f32_16x16x32_bf16 v[212:215], a[196:199], v[188:191], v[212:215]\n"
    "ds_read_b64_tr_b16 a[212:213], v4 offset:18496\n"
    "v_accvgpr_write_b32 a37, v25\n"
    "v_accvgpr_write_b32 a38, v26\n"
    "v_mfma_f32_16x16x32_bf16 v[212:215], a[192:195], v[180:183], v[212:215]\n"
    "s_waitcnt lgkmcnt(5)\n"
    "ds_read_b64_tr_b16 a[214:215], v4 offset:33856\n"
    "v_accvgpr_write_b32 a39, v27\n"
    "v_mfma_f32_16x16x32_bf16 v[216:219], a[200:203], v[176:179], v[216:219]\n"
    "s_waitcnt lgkmcnt(4)\n"
    "buffer_store_dwordx4 v[24:27], v9, s[52:55], s66 offen offset:3072 sc1\n"
    "ds_read_b64_tr_b16 a[216:217], v5 offset:12352\n"
    "v_accvgpr_read_b32 v16, a40\n"
    "v_mfma_f32_16x16x32_bf16 v[216:219], a[204:207], v[184:187], v[216:219]\n"
    "buffer_load_dwordx2 v[204:205], v59, s[52:55], s89 offen offset:0 sc1\n"
    "ds_read_b64_tr_b16 a[218:219], v5 offset:15424\n"
    "v_accvgpr_read_b32 v17, a41\n"
    "v_mfma_f32_16x16x32_bf16 v[220:223], a[204:207], v[188:191], v[220:223]\n"
    "ds_read_b64_tr_b16 a[220:221], v5 offset:18496\n"
    "v_accvgpr_read_b32 v18, a42\n"
    "v_accvgpr_read_b32 v19, a43\n"
    "v_mfma_f32_16x16x32_bf16 v[220:223], a[200:203], v[180:183], v[220:223]\n"
    "s_waitcnt lgkmcnt(5)\n"
    "ds_read_b64_tr_b16 a[222:223], v5 offset:33856\n"
    "v_mul_f32 v16, v50, v16\n"
    "v_mul_f32 v17, v50, v17\n"
    "v_mfma_f32_16x16x32_bf16 v[240:243], a[208:211], v[176:179], v[240:243]\n"
    "s_waitcnt lgkmcnt(4)\n"
    "ds_read_b64_tr_b16 a[224:225], v4 offset:12416\n"
    "v_mul_f32 v18, v50, v18\n"
    "v_mul_f32 v19, v50, v19\n"
    "v_mfma_f32_16x16x32_bf16 v[240:243], a[212:215], v[184:187], v[240:243]\n"
    "buffer_load_dwordx2 v[206:207], v59, s[52:55], s89 offen offset:2560 sc1\n"
    "ds_read_b64_tr_b16 a[226:227], v4 offset:15488\n"
    "v_cvt_pk_bf16_f32 v52, v16, v17\n"
    "v_mfma_f32_16x16x32_bf16 v[244:247], a[212:215], v[188:191], v[244:247]\n"
    "ds_read_b64_tr_b16 a[228:229], v4 offset:18560\n"
    "v_cvt_pk_bf16_f32 v53, v18, v19\n"
    "v_accvgpr_read_b32 v20, a56\n"
    "v_mfma_f32_16x16x32_bf16 v[244:247], a[208:211], v[180:183], v[244:247]\n"
    "s_waitcnt lgkmcnt(5)\n"
    "ds_read_b64_tr_b16 a[230:231], v4 offset:33920\n"
    "v_accvgpr_read_b32 v21, a57\n"
    "v_accvgpr_read_b32 v22, a58\n"
    "v_mfma_f32_16x16x32_bf16 v[248:251], a[216:219], v[176:179], v[248:251]\n"
    "s_waitcnt lgkmcnt(4)\n"
    "ds_read_b64_tr_b16 a[232:233], v5 offset:12416\n"
    "v_accvgpr_read_b32 v23, a59\n"
    "v_mul_f32 v20, v50, v20\n"
    "v_mfma_f32_16x16x32_bf16 v[248:251], a[220:223], v[184:187], v[248:251]\n"
    "buffer_load_dwordx4 v[224:227], v9, s[52:55], s88 offen offset:2560 sc1\n"
    "ds_read_b64_tr_b16 a[234:235], v5 offset:15488\n"
    "v_mul_f32 v21, v50, v21\n"
    "v_mfma_f32_16x16x32_bf16 v[252:255], a[220:223], v[188:191], v[252:255]\n"
    "ds_read_b64_tr_b16 a[236:237], v5 offset:18560\n"
    "v_mul_f32 v22, v50, v22\n"
    "v_mul_f32 v23, v50, v23\n"
    "v_mfma_f32_16x16x32_bf16 v[252:255], a[216:219], v[180:183], v[252:255]\n"
    "s_waitcnt lgkmcnt(5)\n"
    "ds_read_b64_tr_b16 a[238:239], v5 offset:33920\n"
    "v_cvt_pk_bf16_f32 v54, v20, v21\n"
    "v_cvt_pk_bf16_f32 v55, v22, v23\n"
    "v_mfma_f32_16x16x32_bf16 a[240:243], a[224:227], v[176:179], a[240:243]\n"
    "s_waitcnt lgkmcnt(4)\n"
    "buffer_load_dwordx4 v[228:231], v9, s[52:55], s89 offen offset:2560 sc1\n"
    "ds_read_b64_tr_b16 a[192:193], v4 offset:21504\n"
    "v_accvgpr_write_b32 a40, v52\n"
    "v_mfma_f32_16x16x32_bf16 a[240:243], a[228:231], v[184:187], a[240:243]\n"
    "ds_read_b64_tr_b16 a[194:195], v4 offset:24576\n"
    "v_accvgpr_write_b32 a41, v53\n"
    "v_accvgpr_write_b32 a42, v54\n"
    "v_mfma_f32_16x16x32_bf16 a[244:247], a[228:231], v[188:191], a[244:247]\n"
    "ds_read_b64_tr_b16 a[196:197], v4 offset:27648\n"
    "v_accvgpr_write_b32 a43, v55\n"
    "v_mfma_f32_16x16x32_bf16 a[244:247], a[224:227], v[180:183], a[244:247]\n"
    "s_waitcnt lgkmcnt(5)\n"
    "buffer_store_dwordx4 v[52:55], v9, s[52:55], s67 offen offset:1024 sc1\n"
    "ds_read_b64_tr_b16 a[198:199], v4 offset:30720\n"
    "v_accvgpr_read_b32 v16, a44\n"
    "v_mfma_f32_16x16x32_bf16 a[248:251], a[232:235], v[176:179], a[248:251]\n"
    "s_waitcnt lgkmcnt(4)\n"
    "buffer_load_dwordx4 v[232:235], v9, s[52:55], s88 offen offset:3584 sc1\n"
    "ds_read_b64_tr_b16 a[200:201], v5 offset:21504\n"
    "v_accvgpr_read_b32 v17, a45\n"
    "v_mfma_f32_16x16x32_bf16 a[248:251], a[236:239], v[184:187], a[248:251]\n"
    "ds_read_b64_tr_b16 a[202:203], v5 offset:24576\n"
    "v_accvgpr_read_b32 v18, a46\n"
    "v_accvgpr_read_b32 v19, a47\n"
    "v_mfma_f32_16x16x32_bf16 a[252:255], a[236:239], v[188:191], a[252:255]\n"
    "s_waitcnt vmcnt(33)\n"
    "ds_read_b64_tr_b16 a[204:205], v5 offset:27648\n"
    "v_mul_f32 v16, v51, v16\n"
    "v_mul_f32 v17, v51, v17\n"
    "v_mfma_f32_16x16x32_bf16 a[252:255], a[232:235], v[180:183], a[252:255]\n"
    "s_waitcnt lgkmcnt(5)\n"
    "ds_read_b64_tr_b16 a[206:207], v5 offset:30720\n"
    "v_mul_f32 v18, v51, v18\n"
    "v_mul_f32 v19, v51, v19\n"
    "v_mfma_f32_16x16x32_bf16 v[208:211], a[192:195], v[64:67], v[208:211]\n"
    "s_waitcnt lgkmcnt(4)\n"
    "buffer_load_dwordx4 v[236:239], v9, s[52:55], s89 offen offset:3584 sc1\n"
    "ds_read_b64_tr_b16 a[208:209], v4 offset:21568\n"
    "v_cvt_pk_bf16_f32 v24, v16, v17\n"
    "v_mfma_f32_16x16x32_bf16 v[208:211], a[196:199], v[72:75], v[208:211]\n"
    "ds_read_b64_tr_b16 a[210:211], v4 offset:24640\n"
    "v_cvt_pk_bf16_f32 v25, v18, v19\n"
    "v_accvgpr_read_b32 v20, a60\n"
    "v_mfma_f32_16x16x32_bf16 v[212:215], a[196:199], v[76:79], v[212:215]\n"
    "s_mov_b32 s90, s79\n"
    "ds_read_b64_tr_b16 a[212:213], v4 offset:27712\n"
    "v_accvgpr_read_b32 v21, a61\n"
    "v_accvgpr_read_b32 v22, a62\n"
    "v_mfma_f32_16x16x32_bf16 v[212:215], a[192:195], v[68:71], v[212:215]\n"
    "s_waitcnt lgkmcnt(5)\n"
    "s_mul_i32 s90, s90, 245760\n"
    "ds_read_b64_tr_b16 a[214:215], v4 offset:30784\n"
    "v_accvgpr_read_b32 v23, a63\n"
    "v_mul_f32 v20, v51, v20\n"
    "v_mfma_f32_16x16x32_bf16 v[216:219], a[200:203], v[64:67], v[216:219]\n"
    "s_waitcnt lgkmcnt(4)\n"
    "s_add_u32 s90, s90, 0\n"
    "ds_read_b64_tr_b16 a[216:217], v5 offset:21568\n"
    "v_mul_f32 v21, v51, v21\n"
    "v_mul_f32 v22, v51, v22\n"
    "v_mfma_f32_16x16x32_bf16 v[216:219], a[204:207], v[72:75], v[216:219]\n"
    "s_add_u32 m0, s91, 24576\n"
    "ds_read_b64_tr_b16 a[218:219], v5 offset:24640\n"
    "v_mul_f32 v23, v51, v23\n"
    "v_cvt_pk_bf16_f32 v26, v20, v21\n"
    "v_mfma_f32_16x16x32_bf16 v[220:223], a[204:207], v[76:79], v[220:223]\n"
    "ds_read_b64_tr_b16 a[220:221], v5 offset:27712\n"
    "v_cvt_pk_bf16_f32 v27, v22, v23\n"
    "v_accvgpr_write_b32 a44, v24\n"
    "v_mfma_f32_16x16x32_bf16 v[220:223], a[200:203], v[68:71], v[220:223]\n"
    "s_waitcnt lgkmcnt(5)\n"
    "ds_read_b64_tr_b16 a[222:223], v5 offset:30784\n"
    "v_accvgpr_write_b32 a45, v25\n"
    "v_accvgpr_write_b32 a46, v26\n"
    "v_mfma_f32_16x16x32_bf16 v[240:243], a[208:211], v[64:67], v[240:243]\n"
    "s_waitcnt lgkmcnt(4)\n"
    "buffer_load_dwordx4 v11, s[48:51], s90 offen offset:0 lds\n"
    "ds_read_b64_tr_b16 a[224:225], v4 offset:21632\n"
    "v_accvgpr_write_b32 a47, v27\n"
    "v_mfma_f32_16x16x32_bf16 v[240:243], a[212:215], v[72:75], v[240:243]\n"
    "buffer_store_dwordx4 v[24:27], v9, s[52:55], s67 offen offset:3072 sc1\n"
    "ds_read_b64_tr_b16 a[226:227], v4 offset:24704\n"
    "v_mfma_f32_16x16x32_bf16 v[244:247], a[212:215], v[76:79], v[244:247]\n"
    "s_add_u32 s66, s92, 8192\n"
    "buffer_load_dwordx4 v12, s[48:51], s90 offen offset:1024 lds\n"
    "ds_read_b64_tr_b16 a[228:229], v4 offset:27776\n"
    "v_mfma_f32_16x16x32_bf16 v[244:247], a[208:211], v[68:71], v[244:247]\n"
    "s_waitcnt lgkmcnt(5)\n"
    "s_add_u32 s67, s66, 4096\n"
    "ds_read_b64_tr_b16 a[230:231], v4 offset:30848\n"
    "v_accvgpr_read_b32 v16, a64\n"
    "v_accvgpr_read_b32 v17, a65\n"
    "v_mfma_f32_16x16x32_bf16 v[248:251], a[216:219], v[64:67], v[248:251]\n"
    "s_waitcnt lgkmcnt(4)\n"
    "ds_read_b64_tr_b16 a[232:233], v5 offset:21632\n"
    "v_accvgpr_read_b32 v18, a66\n"
    "v_accvgpr_read_b32 v19, a67\n"
    "v_mfma_f32_16x16x32_bf16 v[248:251], a[220:223], v[72:75], v[248:251]\n"
    "ds_read_b64_tr_b16 a[234:235], v5 offset:24704\n"
    "v_mul_f32 v16, v48, v16\n"
    "v_mul_f32 v17, v48, v17\n"
    "v_mfma_f32_16x16x32_bf16 v[252:255], a[220:223], v[76:79], v[252:255]\n"
    "ds_read_b64_tr_b16 a[236:237], v5 offset:27776\n"
    "v_mul_f32 v18, v48, v18\n"
    "v_mul_f32 v19, v48, v19\n"
    "v_mfma_f32_16x16x32_bf16 v[252:255], a[216:219], v[68:71], v[252:255]\n"
    "s_waitcnt lgkmcnt(5)\n"
    "ds_read_b64_tr_b16 a[238:239], v5 offset:30848\n"
    "v_cvt_pk_bf16_f32 v52, v16, v17\n"
    "v_cvt_pk_bf16_f32 v53, v18, v19\n"
    "v_mfma_f32_16x16x32_bf16 a[240:243], a[224:227], v[64:67], a[240:243]\n"
    "s_waitcnt lgkmcnt(4)\n"
    "ds_read_b64_tr_b16 a[192:193], v4 offset:36864\n"
    "v_accvgpr_read_b32 v20, a80\n"
    "v_accvgpr_read_b32 v21, a81\n"
    "v_mfma_f32_16x16x32_bf16 a[240:243], a[228:231], v[72:75], a[240:243]\n"
    "ds_read_b64_tr_b16 a[194:195], v4 offset:39936\n"
    "v_accvgpr_read_b32 v22, a82\n"
    "v_accvgpr_read_b32 v23, a83\n"
    "v_mfma_f32_16x16x32_bf16 a[244:247], a[228:231], v[76:79], a[244:247]\n"
    "ds_read_b64_tr_b16 a[196:197], v4 offset:43008\n"
    "v_mul_f32 v20, v48, v20\n"
    "v_mul_f32 v21, v48, v21\n"
    "v_mfma_f32_16x16x32_bf16 a[244:247], a[224:227], v[68:71], a[244:247]\n"
    "s_waitcnt lgkmcnt(5)\n"
    "buffer_load_dwordx4 v13, s[48:51], s90 offen offset:2048 lds\n"
    "ds_read_b64_tr_b16 a[198:199], v4 offset:46080\n"
    "v_mul_f32 v22, v48, v22\n"
    "v_mfma_f32_16x16x32_bf16 a[248:251], a[232:235], v[64:67], a[248:251]\n"
    "s_waitcnt lgkmcnt(4)\n"
    "ds_read_b64_tr_b16 a[200:201], v5 offset:36864\n"
    "v_mul_f32 v23, v48, v23\n"
    "v_cvt_pk_bf16_f32 v54, v20, v21\n"
    "v_mfma_f32_16x16x32_bf16 a[248:251], a[236:239], v[72:75], a[248:251]\n"
    "ds_read_b64_tr_b16 a[202:203], v5 offset:39936\n"
    "v_cvt_pk_bf16_f32 v55, v22, v23\n"
    "v_accvgpr_write_b32 a64, v52\n"
    "v_mfma_f32_16x16x32_bf16 a[252:255], a[236:239], v[76:79], a[252:255]\n"
    "s_waitcnt vmcnt(29)\n"
    "ds_read_b64_tr_b16 a[204:205], v5 offset:43008\n"
    "v_accvgpr_write_b32 a65, v53\n"
    "v_accvgpr_write_b32 a66, v54\n"
    "v_mfma_f32_16x16x32_bf16 a[252:255], a[232:235], v[68:71], a[252:255]\n"
    "s_waitcnt lgkmcnt(5)\n"
    "ds_read_b64_tr_b16 a[206:207], v5 offset:46080\n"
    "v_accvgpr_write_b32 a67, v55\n"
    "v_mfma_f32_16x16x32_bf16 v[208:211], a[192:195], v[96:99], v[208:211]\n"
    "s_waitcnt lgkmcnt(4)\n"
    "buffer_store_dwordx4 v[52:55], v9, s[52:55], s66 offen offset:0 sc1\n"
    "ds_read_b64_tr_b16 a[208:209], v4 offset:36928\n"
    "v_accvgpr_read_b32 v16, a68\n"
    "v_mfma_f32_16x16x32_bf16 v[208:211], a[196:199], v[104:107], v[208:211]\n"
    "s_barrier\n"
    "ds_read_b64_tr_b16 a[210:211], v4 offset:40000\n"
    "v_accvgpr_read_b32 v17, a69\n"
    "v_accvgpr_read_b32 v18, a70\n"
    "v_mfma_f32_16x16x32_bf16 v[212:215], a[196:199], v[108:111], v[212:215]\n"
    "ds_read_b64_tr_b16 a[212:213], v4 offset:43072\n"
    "v_accvgpr_read_b32 v19, a71\n"
    "v_mul_f32 v16, v49, v16\n"
    "v_mfma_f32_16x16x32_bf16 v[212:215], a[192:195], v[100:103], v[212:215]\n"
    "s_waitcnt lgkmcnt(5)\n"
    "ds_read_b64_tr_b16 a[214:215], v4 offset:46144\n"
    "v_mul_f32 v17, v49, v17\n"
    "v_mul_f32 v18, v49, v18\n"
    "v_mfma_f32_16x16x32_bf16 v[216:219], a[200:203], v[96:99], v[216:219]\n"
    "s_waitcnt lgkmcnt(4)\n"
    "buffer_load_dwordx4 v14, s[48:51], s90 offen offset:3072 lds\n"
    "ds_read_b64_tr_b16 a[216:217], v5 offset:36928\n"
    "v_mul_f32 v19, v49, v19\n"
    "v_mfma_f32_16x16x32_bf16 v[216:219], a[204:207], v[104:107], v[216:219]\n"
    "ds_read_b64_tr_b16 a[218:219], v5 offset:40000\n"
    "v_cvt_pk_bf16_f32 v24, v16, v17\n"
    "v_cvt_pk_bf16_f32 v25, v18, v19\n"
    "v_mfma_f32_16x16x32_bf16 v[220:223], a[204:207], v[108:111], v[220:223]\n"
    "ds_read_b64_tr_b16 a[220:221], v5 offset:43072\n"
    "v_accvgpr_read_b32 v20, a84\n"
    "v_accvgpr_read_b32 v21, a85\n"
    "v_mfma_f32_16x16x32_bf16 v[220:223], a[200:203], v[100:103], v[220:223]\n"
    "s_waitcnt lgkmcnt(5)\n"
    "ds_read_b64_tr_b16 a[222:223], v5 offset:46144\n"
    "v_accvgpr_read_b32 v22, a86\n"
    "v_accvgpr_read_b32 v23, a87\n"
    "v_mfma_f32_16x16x32_bf16 v[240:243], a[208:211], v[96:99], v[240:243]\n"
    "s_waitcnt lgkmcnt(4)\n"
    "ds_read_b64_tr_b16 a[224:225], v4 offset:36992\n"
    "v_mul_f32 v20, v49, v20\n"
    "v_mul_f32 v21, v49, v21\n"
    "v_mfma_f32_16x16x32_bf16 v[240:243], a[212:215], v[104:107], v[240:243]\n"
    "ds_read_b64_tr_b16 a[226:227], v4 offset:40064\n"
    "v_mul_f32 v22, v49, v22\n"
    "v_mul_f32 v23, v49, v23\n"
    "v_mfma_f32_16x16x32_bf16 v[244:247], a[212:215], v[108:111], v[244:247]\n"
    "ds_read_b64_tr_b16 a[228:229], v4 offset:43136\n"
    "v_cvt_pk_bf16_f32 v26, v20, v21\n"
    "v_cvt_pk_bf16_f32 v27, v22, v23\n"
    "v_mfma_f32_16x16x32_bf16 v[244:247], a[208:211], v[100:103], v[244:247]\n"
    "s_waitcnt lgkmcnt(5)\n"
    "ds_read_b64_tr_b16 a[230:231], v4 offset:46208\n"
    "v_accvgpr_write_b32 a68, v24\n"
    "v_accvgpr_write_b32 a69, v25\n"
    "v_mfma_f32_16x16x32_bf16 v[248:251], a[216:219], v[96:99], v[248:251]\n"
    "s_waitcnt lgkmcnt(4)\n"
    "ds_read_b64_tr_b16 a[232:233], v5 offset:36992\n"
    "v_accvgpr_write_b32 a70, v26\n"
    "v_accvgpr_write_b32 a71, v27\n"
    "v_mfma_f32_16x16x32_bf16 v[248:251], a[220:223], v[104:107], v[248:251]\n"
    "s_add_u32 m0, s91, 28672\n"
    "buffer_store_dwordx4 v[24:27], v9, s[52:55], s66 offen offset:2048 sc1\n"
    "ds_read_b64_tr_b16 a[234:235], v5 offset:40064\n"
    "v_accvgpr_read_b32 v16, a72\n"
    "v_mfma_f32_16x16x32_bf16 v[252:255], a[220:223], v[108:111], v[252:255]\n"
    "buffer_load_dwordx4 v15, s[48:51], s90 offen offset:0 lds\n"
    "ds_read_b64_tr_b16 a[236:237], v5 offset:43136\n"
    "v_accvgpr_read_b32 v17, a73\n"
    "v_mfma_f32_16x16x32_bf16 v[252:255], a[216:219], v[100:103], v[252:255]\n"
    "s_waitcnt lgkmcnt(5)\n"
    "ds_read_b64_tr_b16 a[238:239], v5 offset:46208\n"
    "v_accvgpr_read_b32 v18, a74\n"
    "v_accvgpr_read_b32 v19, a75\n"
    "v_mfma_f32_16x16x32_bf16 a[240:243], a[224:227], v[96:99], a[240:243]\n"
    "s_waitcnt lgkmcnt(4)\n"
    "ds_read_b64_tr_b16 a[192:193], v6 offset:49152\n"
    "v_mul_f32 v16, v50, v16\n"
    "v_mul_f32 v17, v50, v17\n"
    "v_mfma_f32_16x16x32_bf16 a[240:243], a[228:231], v[104:107], a[240:243]\n"
    "ds_read_b64_tr_b16 a[194:195], v6 offset:3072\n"
    "v_mul_f32 v18, v50, v18\n"
    "v_mul_f32 v19, v50, v19\n"
    "v_mfma_f32_16x16x32_bf16 a[244:247], a[228:231], v[108:111], a[244:247]\n"
    "ds_read_b64_tr_b16 a[196:197], v6 offset:52224\n"
    "v_cvt_pk_bf16_f32 v52, v16, v17\n"
    "v_cvt_pk_bf16_f32 v53, v18, v19\n"
    "v_mfma_f32_16x16x32_bf16 a[244:247], a[224:227], v[100:103], a[244:247]\n"
    "s_waitcnt lgkmcnt(5)\n"
    "ds_read_b64_tr_b16 a[198:199], v6 offset:55296\n"
    "v_accvgpr_read_b32 v20, a88\n"
    "v_accvgpr_read_b32 v21, a89\n"
    "v_mfma_f32_16x16x32_bf16 a[248:251], a[232:235], v[96:99], a[248:251]\n"
    "s_waitcnt lgkmcnt(4)\n"
    "ds_read_b64_tr_b16 a[200:201], v7 offset:49152\n"
    "v_accvgpr_read_b32 v22, a90\n"
    "v_accvgpr_read_b32 v23, a91\n"
    "v_mfma_f32_16x16x32_bf16 a[248:251], a[236:239], v[104:107], a[248:251]\n"
    "ds_read_b64_tr_b16 a[202:203], v7 offset:3072\n"
    "v_mul_f32 v20, v50, v20\n"
    "v_mul_f32 v21, v50, v21\n"
    "v_mfma_f32_16x16x32_bf16 a[252:255], a[236:239], v[108:111], a[252:255]\n"
    "s_waitcnt vmcnt(26)\n"
    "ds_read_b64_tr_b16 a[204:205], v7 offset:52224\n"
    "v_mul_f32 v22, v50, v22\n"
    "v_mul_f32 v23, v50, v23\n"
    "v_mfma_f32_16x16x32_bf16 a[252:255], a[232:235], v[100:103], a[252:255]\n"
    "s_waitcnt lgkmcnt(5)\n"
    "ds_read_b64_tr_b16 a[206:207], v7 offset:55296\n"
    "v_cvt_pk_bf16_f32 v54, v20, v21\n"
    "v_cvt_pk_bf16_f32 v55, v22, v23\n"
    "v_mfma_f32_16x16x32_bf16 v[208:211], a[192:195], v[128:131], v[208:211]\n"
    "s_waitcnt lgkmcnt(4)\n"
    "ds_read_b64_tr_b16 a[208:209], v6 offset:49216\n"
    "v_accvgpr_write_b32 a72, v52\n"
    "v_accvgpr_write_b32 a73, v53\n"
    "v_mfma_f32_16x16x32_bf16 v[208:211], a[196:199], v[136:139], v[208:211]\n"
    "s_waitcnt vmcnt(6)\n"
    "s_barrier\n"
    "ds_read_b64_tr_b16 a[210:211], v6 offset:3136\n"
    "v_accvgpr_write_b32 a74, v54\n"
    "v_accvgpr_write_b32 a75, v55\n"
    "v_mfma_f32_16x16x32_bf16 v[212:215], a[196:199], v[140:143], v[212:215]\n"
    "s_lshl_b32 s69, s77, 3\n"
    "buffer_store_dwordx4 v[52:55], v9, s[52:55], s67 offen offset:0 sc1\n"
    "ds_read_b64_tr_b16 a[212:213], v6 offset:52288\n"
    "v_accvgpr_read_b32 v16, a76\n"
    "v_mfma_f32_16x16x32_bf16 v[212:215], a[192:195], v[132:135], v[212:215]\n"
    "s_waitcnt lgkmcnt(5)\n"
    "s_add_u32 s69, s69, s78\n"
    "ds_read_b64_tr_b16 a[214:215], v6 offset:55360\n"
    "v_accvgpr_read_b32 v17, a77\n"
    "v_accvgpr_read_b32 v18, a78\n"
    "v_mfma_f32_16x16x32_bf16 v[216:219], a[200:203], v[128:131], v[216:219]\n"
    "s_waitcnt lgkmcnt(4)\n"
    "s_lshl_b32 s69, s69, 2\n"
    "ds_read_b64_tr_b16 a[216:217], v7 offset:49216\n"
    "v_accvgpr_read_b32 v19, a79\n"
    "v_mul_f32 v16, v51, v16\n"
    "v_mfma_f32_16x16x32_bf16 v[216:219], a[204:207], v[136:139], v[216:219]\n"
    "s_add_u32 s70, s69, s79\n"
    "ds_read_b64_tr_b16 a[218:219], v7 offset:3136\n"
    "v_mul_f32 v17, v51, v17\n"
    "v_mul_f32 v18, v51, v18\n"
    "v_mfma_f32_16x16x32_bf16 v[220:223], a[204:207], v[140:143], v[220:223]\n"
    "s_lshl_b32 s95, s70, 6\n"
    "ds_read_b64_tr_b16 a[220:221], v7 offset:52288\n"
    "v_mul_f32 v19, v51, v19\n"
    "v_cvt_pk_bf16_f32 v24, v16, v17\n"
    "v_mfma_f32_16x16x32_bf16 v[220:223], a[200:203], v[132:135], v[220:223]\n"
    "s_waitcnt lgkmcnt(5)\n"
    "s_add_u32 s95, s95, 69632\n"
    "ds_read_b64_tr_b16 a[222:223], v7 offset:55360\n"
    "v_mov_b32 v62, s80\n"
    "v_mov_b32 v63, s81\n"
    "v_mfma_f32_16x16x32_bf16 v[240:243], a[208:211], v[128:131], v[240:243]\n"
    "s_waitcnt lgkmcnt(4)\n"
    "buffer_store_dwordx2 v[62:63], v10, s[52:55], s95 offen sc1\n"
    "ds_read_b64_tr_b16 a[224:225], v6 offset:49280\n"
    "v_cvt_pk_bf16_f32 v25, v18, v19\n"
    "v_mfma_f32_16x16x32_bf16 v[240:243], a[212:215], v[136:139], v[240:243]\n"
    "s_mov_b32 s90, s79\n"
    "ds_read_b64_tr_b16 a[226:227], v6 offset:3200\n"
    "v_accvgpr_read_b32 v20, a92\n"
    "v_accvgpr_read_b32 v21, a93\n"
    "v_mfma_f32_16x16x32_bf16 v[244:247], a[212:215], v[140:143], v[244:247]\n"
    "s_mul_i32 s90, s90, 245760\n"
    "ds_read_b64_tr_b16 a[228:229], v6 offset:52352\n"
    "v_accvgpr_read_b32 v22, a94\n"
    "v_accvgpr_read_b32 v23, a95\n"
    "v_mfma_f32_16x16x32_bf16 v[244:247], a[208:211], v[132:135], v[244:247]\n"
    "s_waitcnt lgkmcnt(5)\n"
    "s_add_u32 s90, s90, 81920\n"
    "ds_read_b64_tr_b16 a[230:231], v6 offset:55424\n"
    "v_mul_f32 v20, v51, v20\n"
    "v_mul_f32 v21, v51, v21\n"
    "v_mfma_f32_16x16x32_bf16 v[248:251], a[216:219], v[128:131], v[248:251]\n"
    "s_waitcnt lgkmcnt(4)\n"
    "s_add_u32 m0, s91, 0\n"
    "ds_read_b64_tr_b16 a[232:233], v7 offset:49280\n"
    "v_mul_f32 v22, v51, v22\n"
    "v_mul_f32 v23, v51, v23\n"
    "v_mfma_f32_16x16x32_bf16 v[248:251], a[220:223], v[136:139], v[248:251]\n"
    "buffer_load_dwordx4 v11, s[48:51], s90 offen offset:0 lds\n"
    "ds_read_b64_tr_b16 a[234:235], v7 offset:3200\n"
    "v_cvt_pk_bf16_f32 v26, v20, v21\n"
    "v_mfma_f32_16x16x32_bf16 v[252:255], a[220:223], v[140:143], v[252:255]\n"
    "buffer_load_dwordx4 v12, s[48:51], s90 offen offset:1024 lds\n"
    "ds_read_b64_tr_b16 a[236:237], v7 offset:52352\n"
    "v_cvt_pk_bf16_f32 v27, v22, v23\n"
    "v_mfma_f32_16x16x32_bf16 v[252:255], a[216:219], v[132:135], v[252:255]\n"
    "s_waitcnt lgkmcnt(5)\n"
    "ds_read_b64_tr_b16 a[238:239], v7 offset:55424\n"
    "v_accvgpr_write_b32 a76, v24\n"
    "v_accvgpr_write_b32 a77, v25\n"
    "v_mfma_f32_16x16x32_bf16 a[240:243], a[224:227], v[128:131], a[240:243]\n"
    "s_waitcnt lgkmcnt(4)\n"
    "buffer_load_dwordx4 v13, s[48:51], s90 offen offset:2048 lds\n"
    "ds_read_b64_tr_b16 a[192:193], v6 offset:58368\n"
    "v_accvgpr_write_b32 a78, v26\n"
    "v_mfma_f32_16x16x32_bf16 a[240:243], a[228:231], v[136:139], a[240:243]\n"
    "ds_read_b64_tr_b16 a[194:195], v6 offset:0\n"
    "v_accvgpr_write_b32 a79, v27\n"
    "v_mfma_f32_16x16x32_bf16 a[244:247], a[228:231], v[140:143], a[244:247]\n"
    "buffer_store_dwordx4 v[24:27], v9, s[52:55], s67 offen offset:2048 sc1\n"
    "ds_read_b64_tr_b16 a[196:197], v6 offset:6144\n"
    "v_mfma_f32_16x16x32_bf16 a[244:247], a[224:227], v[132:135], a[244:247]\n"
    "s_waitcnt lgkmcnt(5)\n"
    "s_add_u32 s66, s92, 8192\n"
    "ds_read_b64_tr_b16 a[198:199], v6 offset:9216\n"
    "v_mfma_f32_16x16x32_bf16 a[248:251], a[232:235], v[128:131], a[248:251]\n"
    "s_waitcnt lgkmcnt(4)\n"
    "s_add_u32 s67, s66, 4096\n"
    "buffer_load_dwordx4 v14, s[48:51], s90 offen offset:3072 lds\n"
    "ds_read_b64_tr_b16 a[200:201], v7 offset:58368\n"
    "v_accvgpr_read_b32 v16, a96\n"
    "v_mfma_f32_16x16x32_bf16 a[248:251], a[236:239], v[136:139], a[248:251]\n"
    "ds_read_b64_tr_b16 a[202:203], v7 offset:0\n"
    "v_accvgpr_read_b32 v17, a97\n"
    "v_accvgpr_read_b32 v18, a98\n"
    "v_mfma_f32_16x16x32_bf16 a[252:255], a[236:239], v[140:143], a[252:255]\n"
    "ds_read_b64_tr_b16 a[204:205], v7 offset:6144\n"
    "v_accvgpr_read_b32 v19, a99\n"
    "v_mul_f32 v16, v48, v16\n"
    "v_mfma_f32_16x16x32_bf16 a[252:255], a[232:235], v[132:135], a[252:255]\n"
    "s_waitcnt lgkmcnt(5)\n"
    "ds_read_b64_tr_b16 a[206:207], v7 offset:9216\n"
    "v_mul_f32 v17, v48, v17\n"
    "v_mul_f32 v18, v48, v18\n"
    "v_mfma_f32_16x16x32_bf16 v[208:211], a[192:195], v[160:163], v[208:211]\n"
    "s_waitcnt lgkmcnt(4)\n"
    "s_add_u32 m0, s91, 4096\n"
    "ds_read_b64_tr_b16 a[208:209], v6 offset:58432\n"
    "v_mul_f32 v19, v48, v19\n"
    "v_cvt_pk_bf16_f32 v52, v16, v17\n"
    "v_mfma_f32_16x16x32_bf16 v[208:211], a[196:199], v[168:171], v[208:211]\n"
    "buffer_load_dwordx4 v15, s[48:51], s90 offen offset:0 lds\n"
    "ds_read_b64_tr_b16 a[210:211], v6 offset:64\n"
    "v_cvt_pk_bf16_f32 v53, v18, v19\n"
    "v_mfma_f32_16x16x32_bf16 v[212:215], a[196:199], v[172:175], v[212:215]\n"
    "ds_read_b64_tr_b16 a[212:213], v6 offset:6208\n"
    "v_lshrrev_b32 v57, 6, v9\n"
    "v_mul_u32_u24 v57, 320, v57\n"
    "v_mfma_f32_16x16x32_bf16 v[212:215], a[192:195], v[164:167], v[212:215]\n"
    "s_waitcnt lgkmcnt(5)\n"
    "ds_read_b64_tr_b16 a[214:215], v6 offset:9280\n"
    "v_bfe_u32 v8, v9, 4, 2\n"
    "v_lshl_add_u32 v57, v8, 3, v57\n"
    "v_mfma_f32_16x16x32_bf16 v[216:219], a[200:203], v[160:163], v[216:219]\n"
    "s_waitcnt lgkmcnt(4)\n"
    "ds_read_b64_tr_b16 a[216:217], v7 offset:58432\n"
    "v_add_u32 v57, 24576, v57\n"
    "v_bfe_u32 v8, v9, 8, 1\n"
    "v_mfma_f32_16x16x32_bf16 v[216:219], a[204:207], v[168:171], v[216:219]\n"
    "s_cmp_lt_u32 s79, 1\n"
    "s_cselect_b32 s90, 1, 0\n"
    "ds_read_b64_tr_b16 a[218:219], v7 offset:64\n"
    "v_accvgpr_read_b32 v20, a112\n"
    "v_accvgpr_read_b32 v21, a113\n"
    "v_mfma_f32_16x16x32_bf16 v[220:223], a[204:207], v[172:175], v[220:223]\n"
    "s_mul_i32 s90, s90, 245760\n"
    "ds_read_b64_tr_b16 a[220:221], v7 offset:6208\n"
    "v_accvgpr_read_b32 v22, a114\n"
    "v_accvgpr_read_b32 v23, a115\n"
    "v_mfma_f32_16x16x32_bf16 v[220:223], a[200:203], v[164:167], v[220:223]\n"
    "s_waitcnt lgkmcnt(5)\n"
    "s_add_u32 s90, s90, 0\n"
    "ds_read_b64_tr_b16 a[222:223], v7 offset:9280\n"
    "v_mul_f32 v20, v48, v20\n"
    "v_mul_f32 v21, v48, v21\n"
    "v_mfma_f32_16x16x32_bf16 v[240:243], a[208:211], v[160:163], v[240:243]\n"
    "s_waitcnt lgkmcnt(4)\n"
    "s_add_u32 m0, s91, 49152\n"
    "ds_read_b64_tr_b16 a[224:225], v6 offset:58496\n"
    "v_mul_f32 v22, v48, v22\n"
    "v_mul_f32 v23, v48, v23\n"
    "v_mfma_f32_16x16x32_bf16 v[240:243], a[212:215], v[168:171], v[240:243]\n"
    "s_and_b32 s73, s77, 1\n"
    "buffer_load_dwordx4 v11, s[48:51], s90 offen offset:0 lds\n"
    "ds_read_b64_tr_b16 a[226:227], v6 offset:128\n"
    "v_cvt_pk_bf16_f32 v54, v20, v21\n"
    "v_mfma_f32_16x16x32_bf16 v[244:247], a[212:215], v[172:175], v[244:247]\n"
    "s_mul_i32 s73, s73, 5\n"
    "buffer_load_dwordx4 v12, s[48:51], s90 offen offset:1024 lds\n"
    "ds_read_b64_tr_b16 a[228:229], v6 offset:6272\n"
    "v_cvt_pk_bf16_f32 v55, v22, v23\n"
    "v_mfma_f32_16x16x32_bf16 v[244:247], a[208:211], v[164:167], v[244:247]\n"
    "s_waitcnt lgkmcnt(5)\n"
    "s_sub_u32 s74, 5, s73\n"
    "ds_read_b64_tr_b16 a[230:231], v6 offset:9344\n"
    "v_accvgpr_write_b32 a96, v52\n"
    "v_accvgpr_write_b32 a97, v53\n"
    "v_mfma_f32_16x16x32_bf16 v[248:251], a[216:219], v[160:163], v[248:251]\n"
    "s_waitcnt lgkmcnt(4)\n"
    "s_add_u32 s93, s73, 0\n"
    "buffer_load_dwordx4 v13, s[48:51], s90 offen offset:2048 lds\n"
    "ds_read_b64_tr_b16 a[232:233], v7 offset:58496\n"
    "v_xor_b32 v60, s93, v8\n"
    "v_mfma_f32_16x16x32_bf16 v[248:251], a[220:223], v[168:171], v[248:251]\n"
    "ds_read_b64_tr_b16 a[234:235], v7 offset:128\n"
    "v_lshl_add_u32 v2, v60, 5, v57\n"
    "v_accvgpr_write_b32 a98, v54\n"
    "v_mfma_f32_16x16x32_bf16 v[252:255], a[220:223], v[172:175], v[252:255]\n"
    "s_add_u32 s93, s73, 1\n"
    "ds_read_b64_tr_b16 a[236:237], v7 offset:6272\n"
    "v_xor_b32 v60, s93, v8\n"
    "v_lshl_add_u32 v3, v60, 5, v57\n"
    "v_mfma_f32_16x16x32_bf16 v[252:255], a[216:219], v[164:167], v[252:255]\n"
    "s_waitcnt lgkmcnt(5)\n"
    "s_add_u32 s93, s74, 0\n"
    "ds_read_b64_tr_b16 a[238:239], v7 offset:9344\n"
    "v_xor_b32 v60, s93, v8\n"
    "v_lshl_add_u32 v4, v60, 5, v57\n"
    "v_mfma_f32_16x16x32_bf16 a[240:243], a[224:227], v[160:163], a[240:243]\n"
    "s_waitcnt lgkmcnt(4)\n"
    "s_add_u32 s93, s74, 1\n"
    "buffer_load_dwordx4 v14, s[48:51], s90 offen offset:3072 lds\n"
    "ds_read_b64_tr_b16 a[192:193], v6 offset:12288\n"
    "v_xor_b32 v60, s93, v8\n"
    "v_mfma_f32_16x16x32_bf16 a[240:243], a[228:231], v[168:171], a[240:243]\n"
    "ds_read_b64_tr_b16 a[194:195], v6 offset:15360\n"
    "v_lshl_add_u32 v5, v60, 5, v57\n"
    "v_accvgpr_write_b32 a99, v55\n"
    "v_mfma_f32_16x16x32_bf16 a[244:247], a[228:231], v[172:175], a[244:247]\n"
    "buffer_store_dwordx4 v[52:55], v9, s[52:55], s66 offen offset:1024 sc1\n"
    "ds_read_b64_tr_b16 a[196:197], v6 offset:18432\n"
    "v_accvgpr_read_b32 v16, a100\n"
    "v_mfma_f32_16x16x32_bf16 a[244:247], a[224:227], v[164:167], a[244:247]\n"
    "s_waitcnt lgkmcnt(5)\n"
    "ds_read_b64_tr_b16 a[198:199], v6 offset:33792\n"
    "v_accvgpr_read_b32 v17, a101\n"
    "v_accvgpr_read_b32 v18, a102\n"
    "v_mfma_f32_16x16x32_bf16 a[248:251], a[232:235], v[160:163], a[248:251]\n"
    "s_waitcnt lgkmcnt(4)\n"
    "s_add_u32 m0, s91, 53248\n"
    "ds_read_b64_tr_b16 a[200:201], v7 offset:12288\n"
    "v_accvgpr_read_b32 v19, a103\n"
    "v_mul_f32 v16, v49, v16\n"
    "v_mfma_f32_16x16x32_bf16 a[248:251], a[236:239], v[168:171], a[248:251]\n"
    "buffer_load_dwordx4 v15, s[48:51], s90 offen offset:0 lds\n"
    "ds_read_b64_tr_b16 a[202:203], v7 offset:15360\n"
    "v_mul_f32 v17, v49, v17\n"
    "v_mfma_f32_16x16x32_bf16 a[252:255], a[236:239], v[172:175], a[252:255]\n"
    "ds_read_b64_tr_b16 a[204:205], v7 offset:18432\n"
    "v_mul_f32 v18, v49, v18\n"
    "v_mul_f32 v19, v49, v19\n"
    "v_mfma_f32_16x16x32_bf16 a[252:255], a[232:235], v[164:167], a[252:255]\n"
    "s_waitcnt lgkmcnt(5)\n"
    "ds_read_b64_tr_b16 a[206:207], v7 offset:33792\n"
    "v_cvt_pk_bf16_f32 v24, v16, v17\n"
    "v_cvt_pk_bf16_f32 v25, v18, v19\n"
    "v_mfma_f32_16x16x32_bf16 v[208:211], a[192:195], v[192:195], v[208:211]\n"
    "s_waitcnt lgkmcnt(4)\n"
    "ds_read_b64_tr_b16 a[208:209], v6 offset:12352\n"
    "v_accvgpr_read_b32 v20, a116\n"
    "v_accvgpr_read_b32 v21, a117\n"
    "v_mfma_f32_16x16x32_bf16 v[208:211], a[196:199], v[200:203], v[208:211]\n"
    "s_barrier\n"
    "ds_read_b64_tr_b16 a[210:211], v6 offset:15424\n"
    "v_accvgpr_read_b32 v22, a118\n"
    "v_accvgpr_read_b32 v23, a119\n"
    "v_mfma_f32_16x16x32_bf16 v[212:215], a[196:199], v[204:207], v[212:215]\n"
    "ds_read_b64_tr_b16 a[212:213], v6 offset:18496\n"
    "v_mul_f32 v20, v49, v20\n"
    "v_mul_f32 v21, v49, v21\n"
    "v_mfma_f32_16x16x32_bf16 v[212:215], a[192:195], v[196:199], v[212:215]\n"
    "s_waitcnt lgkmcnt(5)\n"
    "ds_read_b64_tr_b16 a[214:215], v6 offset:33856\n"
    "v_mul_f32 v22, v49, v22\n"
    "v_mul_f32 v23, v49, v23\n"
    "v_mfma_f32_16x16x32_bf16 v[216:219], a[200:203], v[192:195], v[216:219]\n"
    "s_waitcnt lgkmcnt(4)\n"
    "ds_read_b64_tr_b16 a[216:217], v7 offset:12352\n"
    "v_cvt_pk_bf16_f32 v26, v20, v21\n"
    "v_cvt_pk_bf16_f32 v27, v22, v23\n"
    "v_mfma_f32_16x16x32_bf16 v[216:219], a[204:207], v[200:203], v[216:219]\n"
    "s_cmp_lt_u32 s79, 2\n"
    "s_cselect_b32 s90, 2, 1\n"
    "ds_read_b64_tr_b16 a[218:219], v7 offset:15424\n"
    "v_accvgpr_write_b32 a100, v24\n"
    "v_accvgpr_write_b32 a101, v25\n"
    "v_mfma_f32_16x16x32_bf16 v[220:223], a[204:207], v[204:207], v[220:223]\n"
    "s_mul_i32 s90, s90, 245760\n"
    "ds_read_b64_tr_b16 a[220:221], v7 offset:18496\n"
    "v_accvgpr_write_b32 a102, v26\n"
    "v_accvgpr_write_b32 a103, v27\n"
    "v_mfma_f32_16x16x32_bf16 v[220:223], a[200:203], v[196:199], v[220:223]\n"
    "s_waitcnt lgkmcnt(5)\n"
    "s_add_u32 s90, s90, 0\n"
    "buffer_store_dwordx4 v[24:27], v9, s[52:55], s66 offen offset:3072 sc1\n"
    "ds_read_b64_tr_b16 a[222:223], v7 offset:33856\n"
    "v_accvgpr_read_b32 v16, a104\n"
    "v_mfma_f32_16x16x32_bf16 v[240:243], a[208:211], v[192:195], v[240:243]\n"
    "s_waitcnt lgkmcnt(4)\n"
    "s_add_u32 m0, s91, 73728\n"
    "ds_read_b64_tr_b16 a[224:225], v6 offset:12416\n"
    "v_accvgpr_read_b32 v17, a105\n"
    "v_accvgpr_read_b32 v18, a106\n"
    "v_mfma_f32_16x16x32_bf16 v[240:243], a[212:215], v[200:203], v[240:243]\n"
    "buffer_load_dwordx4 v11, s[48:51], s90 offen offset:0 lds\n"
    "ds_read_b64_tr_b16 a[226:227], v6 offset:15488\n"
    "v_accvgpr_read_b32 v19, a107\n"
    "v_mfma_f32_16x16x32_bf16 v[244:247], a[212:215], v[204:207], v[244:247]\n"
    "buffer_load_dwordx4 v12, s[48:51], s90 offen offset:1024 lds\n"
    "ds_read_b64_tr_b16 a[228:229], v6 offset:18560\n"
    "v_mul_f32 v16, v50, v16\n"
    "v_mfma_f32_16x16x32_bf16 v[244:247], a[208:211], v[196:199], v[244:247]\n"
    "s_waitcnt lgkmcnt(5)\n"
    "ds_read_b64_tr_b16 a[230:231], v6 offset:33920\n"
    "v_mul_f32 v17, v50, v17\n"
    "v_mul_f32 v18, v50, v18\n"
    "v_mfma_f32_16x16x32_bf16 v[248:251], a[216:219], v[192:195], v[248:251]\n"
    "s_waitcnt lgkmcnt(4)\n"
    "ds_read_b64_tr_b16 a[232:233], v7 offset:12416\n"
    "v_mul_f32 v19, v50, v19\n"
    "v_cvt_pk_bf16_f32 v52, v16, v17\n"
    "v_mfma_f32_16x16x32_bf16 v[248:251], a[220:223], v[200:203], v[248:251]\n"
    "ds_read_b64_tr_b16 a[234:235], v7 offset:15488\n"
    "v_cvt_pk_bf16_f32 v53, v18, v19\n"
    "v_accvgpr_read_b32 v20, a120\n"
    "v_mfma_f32_16x16x32_bf16 v[252:255], a[220:223], v[204:207], v[252:255]\n"
    "buffer_load_dwordx4 v13, s[48:51], s90 offen offset:2048 lds\n"
    "ds_read_b64_tr_b16 a[236:237], v7 offset:18560\n"
    "v_accvgpr_read_b32 v21, a121\n"
    "v_mfma_f32_16x16x32_bf16 v[252:255], a[216:219], v[196:199], v[252:255]\n"
    "s_waitcnt lgkmcnt(5)\n"
    "ds_read_b64_tr_b16 a[238:239], v7 offset:33920\n"
    "v_accvgpr_read_b32 v22, a122\n"
    "v_accvgpr_read_b32 v23, a123\n"
    "v_mfma_f32_16x16x32_bf16 a[240:243], a[224:227], v[192:195], a[240:243]\n"
    "s_waitcnt lgkmcnt(4)\n"
    "ds_read_b64_tr_b16 a[192:193], v6 offset:21504\n"
    "v_mul_f32 v20, v50, v20\n"
    "v_mul_f32 v21, v50, v21\n"
    "v_mfma_f32_16x16x32_bf16 a[240:243], a[228:231], v[200:203], a[240:243]\n"
    "ds_read_b64_tr_b16 a[194:195], v6 offset:24576\n"
    "v_mul_f32 v22, v50, v22\n"
    "v_mul_f32 v23, v50, v23\n"
    "v_mfma_f32_16x16x32_bf16 a[244:247], a[228:231], v[204:207], a[244:247]\n"
    "ds_read_b64_tr_b16 a[196:197], v6 offset:27648\n"
    "v_cvt_pk_bf16_f32 v54, v20, v21\n"
    "v_cvt_pk_bf16_f32 v55, v22, v23\n"
    "v_mfma_f32_16x16x32_bf16 a[244:247], a[224:227], v[196:199], a[244:247]\n"
    "s_waitcnt lgkmcnt(5)\n"
    "buffer_load_dwordx4 v14, s[48:51], s90 offen offset:3072 lds\n"
    "ds_read_b64_tr_b16 a[198:199], v6 offset:30720\n"
    "v_accvgpr_write_b32 a104, v52\n"
    "v_mfma_f32_16x16x32_bf16 a[248:251], a[232:235], v[192:195], a[248:251]\n"
    "s_waitcnt lgkmcnt(4)\n"
    "ds_read_b64_tr_b16 a[200:201], v7 offset:21504\n"
    "v_accvgpr_write_b32 a105, v53\n"
    "v_accvgpr_write_b32 a106, v54\n"
    "v_mfma_f32_16x16x32_bf16 a[248:251], a[236:239], v[200:203], a[248:251]\n"
    "ds_read_b64_tr_b16 a[202:203], v7 offset:24576\n"
    "v_accvgpr_write_b32 a107, v55\n"
    "v_mfma_f32_16x16x32_bf16 a[252:255], a[236:239], v[204:207], a[252:255]\n"
    "buffer_store_dwordx4 v[52:55], v9, s[52:55], s67 offen offset:1024 sc1\n"
    "ds_read_b64_tr_b16 a[204:205], v7 offset:27648\n"
    "v_accvgpr_read_b32 v16, a108\n"
    "v_mfma_f32_16x16x32_bf16 a[252:255], a[232:235], v[196:199], a[252:255]\n"
    "s_waitcnt lgkmcnt(5)\n"
    "ds_read_b64_tr_b16 a[206:207], v7 offset:30720\n"
    "v_accvgpr_read_b32 v17, a109\n"
    "v_accvgpr_read_b32 v18, a110\n"
    "v_mfma_f32_16x16x32_bf16 v[208:211], a[192:195], v[224:227], v[208:211]\n"
    "s_waitcnt lgkmcnt(4)\n"
    "s_add_u32 m0, s91, 77824\n"
    "ds_read_b64_tr_b16 a[208:209], v6 offset:21568\n"
    "v_accvgpr_read_b32 v19, a111\n"
    "v_mul_f32 v16, v51, v16\n"
    "v_mfma_f32_16x16x32_bf16 v[208:211], a[196:199], v[232:235], v[208:211]\n"
    "buffer_load_dwordx4 v15, s[48:51], s90 offen offset:0 lds\n"
    "ds_read_b64_tr_b16 a[210:211], v6 offset:24640\n"
    "v_mul_f32 v17, v51, v17\n"
    "v_mfma_f32_16x16x32_bf16 v[212:215], a[196:199], v[236:239], v[212:215]\n"
    "ds_read_b64_tr_b16 a[212:213], v6 offset:27712\n"
    "v_lshrrev_b32 v56, 4, v9\n"
    "v_min_u32 v56, 3, v56\n"
    "v_mfma_f32_16x16x32_bf16 v[212:215], a[192:195], v[228:231], v[212:215]\n"
    "s_waitcnt lgkmcnt(5)\n"
    "ds_read_b64_tr_b16 a[214:215], v6 offset:30784\n"
    "v_add_u32 v56, s69, v56\n"
    "v_lshlrev_b32 v56, 6, v56\n"
    "v_mfma_f32_16x16x32_bf16 v[216:219], a[200:203], v[224:227], v[216:219]\n"
    "s_waitcnt lgkmcnt(4)\n"
    "ds_read_b64_tr_b16 a[216:217], v7 offset:21568\n"
    "v_add_u32 v56, 69632, v56\n"
    "v_mul_f32 v18, v51, v18\n"
    "v_mfma_f32_16x16x32_bf16 v[216:219], a[204:207], v[232:235], v[216:219]\n"
    "buffer_load_dwordx2 v[62:63], v56, s[52:55], 0 offen sc1\n"
    "ds_read_b64_tr_b16 a[218:219], v7 offset:24640\n"
    "v_mul_f32 v19, v51, v19\n"
    "v_mfma_f32_16x16x32_bf16 v[220:223], a[204:207], v[236:239], v[220:223]\n"
    "ds_read_b64_tr_b16 a[220:221], v7 offset:27712\n"
    "v_cvt_pk_bf16_f32 v24, v16, v17\n"
    "v_cvt_pk_bf16_f32 v25, v18, v19\n"
    "v_mfma_f32_16x16x32_bf16 v[220:223], a[200:203], v[228:231], v[220:223]\n"
    "s_waitcnt lgkmcnt(5)\n"
    "ds_read_b64_tr_b16 a[222:223], v7 offset:30784\n"
    "v_accvgpr_read_b32 v20, a124\n"
    "v_accvgpr_read_b32 v21, a125\n"
    "v_mfma_f32_16x16x32_bf16 v[240:243], a[208:211], v[224:227], v[240:243]\n"
    "s_waitcnt lgkmcnt(4)\n"
    "ds_read_b64_tr_b16 a[224:225], v6 offset:21632\n"
    "v_accvgpr_read_b32 v22, a126\n"
    "v_accvgpr_read_b32 v23, a127\n"
    "v_mfma_f32_16x16x32_bf16 v[240:243], a[212:215], v[232:235], v[240:243]\n"
    "ds_read_b64_tr_b16 a[226:227], v6 offset:24704\n"
    "v_mul_f32 v20, v51, v20\n"
    "v_mul_f32 v21, v51, v21\n"
    "v_mfma_f32_16x16x32_bf16 v[244:247], a[212:215], v[236:239], v[244:247]\n"
    "ds_read_b64_tr_b16 a[228:229], v6 offset:27776\n"
    "v_mul_f32 v22, v51, v22\n"
    "v_mul_f32 v23, v51, v23\n"
    "v_mfma_f32_16x16x32_bf16 v[244:247], a[208:211], v[228:231], v[244:247]\n"
    "s_waitcnt lgkmcnt(5)\n"
    "ds_read_b64_tr_b16 a[230:231], v6 offset:30848\n"
    "v_cvt_pk_bf16_f32 v26, v20, v21\n"
    "v_cvt_pk_bf16_f32 v27, v22, v23\n"
    "v_mfma_f32_16x16x32_bf16 v[248:251], a[216:219], v[224:227], v[248:251]\n"
    "s_waitcnt lgkmcnt(4)\n"
    "ds_read_b64_tr_b16 a[232:233], v7 offset:21632\n"
    "v_accvgpr_write_b32 a108, v24\n"
    "v_accvgpr_write_b32 a109, v25\n"
    "v_mfma_f32_16x16x32_bf16 v[248:251], a[220:223], v[232:235], v[248:251]\n"
    "ds_read_b64_tr_b16 a[234:235], v7 offset:24704\n"
    "v_accvgpr_write_b32 a110, v26\n"
    "v_accvgpr_write_b32 a111, v27\n"
    "v_mfma_f32_16x16x32_bf16 v[252:255], a[220:223], v[236:239], v[252:255]\n"
    "buffer_store_dwordx4 v[24:27], v9, s[52:55], s67 offen offset:3072 sc1\n"
    "ds_read_b64_tr_b16 a[236:237], v7 offset:27776\n"
    "v_mfma_f32_16x16x32_bf16 v[252:255], a[216:219], v[228:231], v[252:255]\n"
    "s_waitcnt lgkmcnt(5)\n"
    "ds_read_b64_tr_b16 a[238:239], v7 offset:30848\n"
    "s_waitcnt vmcnt(23)\n"
    "s_barrier\n"
    "v_mfma_f32_16x16x32_bf16 a[240:243], a[224:227], v[224:227], a[240:243]\n"
    "s_waitcnt lgkmcnt(4)\n"
    "ds_read_b64_tr_b16 a[192:193], v2 offset:0\n"
    "v_mfma_f32_16x16x32_bf16 a[240:243], a[228:231], v[232:235], a[240:243]\n"
    "ds_read_b64_tr_b16 a[194:195], v2 offset:5120\n"
    "v_mfma_f32_16x16x32_bf16 a[244:247], a[228:231], v[236:239], a[244:247]\n"
    "ds_read_b64_tr_b16 a[196:197], v2 offset:10240\n"
    "v_mfma_f32_16x16x32_bf16 a[244:247], a[224:227], v[228:231], a[244:247]\n"
    "s_waitcnt lgkmcnt(5)\n"
    "ds_read_b64_tr_b16 a[198:199], v2 offset:15360\n"
    "v_mfma_f32_16x16x32_bf16 a[248:251], a[232:235], v[224:227], a[248:251]\n"
    "s_waitcnt lgkmcnt(4)\n"
    "ds_read_b64_tr_b16 a[200:201], v3 offset:0\n"
    "v_mfma_f32_16x16x32_bf16 a[248:251], a[236:239], v[232:235], a[248:251]\n"
    "ds_read_b64_tr_b16 a[202:203], v3 offset:5120\n"
    "v_mfma_f32_16x16x32_bf16 a[252:255], a[236:239], v[236:239], a[252:255]\n"
    "ds_read_b64_tr_b16 a[204:205], v3 offset:10240\n"
    "v_mfma_f32_16x16x32_bf16 a[252:255], a[232:235], v[228:231], a[252:255]\n"
    "ds_read_b64_tr_b16 a[206:207], v3 offset:15360\n"
    "s_waitcnt lgkmcnt(6)\n"
    "v_mfma_f32_16x16x32_bf16 v[64:67], a[192:195], a[0:3], 0\n"
    "s_waitcnt lgkmcnt(4)\n"
    "ds_read_b64_tr_b16 a[208:209], v2 offset:64\n"
    "v_mfma_f32_16x16x32_bf16 v[64:67], a[196:199], a[32:35], v[64:67]\n"
    "v_mfma_f32_16x16x32_bf16 v[68:71], a[196:199], a[36:39], 0\n"
    "ds_read_b64_tr_b16 a[210:211], v2 offset:5184\n"
    "v_mfma_f32_16x16x32_bf16 v[68:71], a[192:195], a[4:7], v[68:71]\n"
    "v_mfma_f32_16x16x32_bf16 v[72:75], a[192:195], a[8:11], 0\n"
    "ds_read_b64_tr_b16 a[212:213], v2 offset:10304\n"
    "v_mfma_f32_16x16x32_bf16 v[72:75], a[196:199], a[40:43], v[72:75]\n"
    "v_mfma_f32_16x16x32_bf16 v[76:79], a[196:199], a[44:47], 0\n"
    "ds_read_b64_tr_b16 a[214:215], v2 offset:15424\n"
    "v_mfma_f32_16x16x32_bf16 v[76:79], a[192:195], a[12:15], v[76:79]\n"
    "s_waitcnt lgkmcnt(6)\n"
    "v_mfma_f32_16x16x32_bf16 v[92:95], a[200:203], a[12:15], 0\n"
    "s_waitcnt lgkmcnt(4)\n"
    "ds_read_b64_tr_b16 a[216:217], v3 offset:64\n"
    "v_mfma_f32_16x16x32_bf16 v[92:95], a[204:207], a[44:47], v[92:95]\n"
    "v_mfma_f32_16x16x32_bf16 v[88:91], a[204:207], a[40:43], 0\n"
    "ds_read_b64_tr_b16 a[218:219], v3 offset:5184\n"
    "v_mfma_f32_16x16x32_bf16 v[88:91], a[200:203], a[8:11], v[88:91]\n"
    "v_mfma_f32_16x16x32_bf16 v[84:87], a[200:203], a[4:7], 0\n"
    "ds_read_b64_tr_b16 a[220:221], v3 offset:10304\n"
    "v_mfma_f32_16x16x32_bf16 v[84:87], a[204:207], a[36:39], v[84:87]\n"
    "s_waitcnt vmcnt(1)\n"
    "v_cmp_ne_u64 vcc, s[80:81], v[62:63]\n"
    "s_nop 1\n"
    "s_cbranch_vccnz L_slow5\n"
    "L_back5:\n"
    "s_barrier\n"
    "v_mfma_f32_16x16x32_bf16 v[80:83], a[204:207], a[32:35], 0\n"
    "s_cmp_lt_u32 s79, 1\n"
    "s_cselect_b32 s88, 1, 0\n"
    "ds_read_b64_tr_b16 a[222:223], v3 offset:15424\n"
    "v_mfma_f32_16x16x32_bf16 v[80:83], a[200:203], a[0:3], v[80:83]\n"
    "s_waitcnt lgkmcnt(6)\n"
    "s_mul_i32 s88, s88, 786432\n"
    "v_mfma_f32_16x16x32_bf16 v[96:99], a[208:211], a[0:3], 0\n"
    "s_waitcnt lgkmcnt(4)\n"
    "s_add_u32 s88, s88, s65\n"
    "ds_read_b64_tr_b16 a[224:225], v2 offset:128\n"
    "v_mfma_f32_16x16x32_bf16 v[96:99], a[212:215], a[32:35], v[96:99]\n"
    "s_add_u32 s88, s88, 0\n"
    "v_mfma_f32_16x16x32_bf16 v[100:103], a[212:215], a[36:39], 0\n"
    "s_add_u32 s89, s88, 4096\n"
    "ds_read_b64_tr_b16 a[226:227], v2 offset:5248\n"
    "v_mfma_f32_16x16x32_bf16 v[100:103], a[208:211], a[4:7], v[100:103]\n"
    "v_mfma_f32_16x16x32_bf16 v[104:107], a[208:211], a[8:11], 0\n"
    "s_add_u32 s66, s92, 16384\n"
    "buffer_load_dwordx4 a[16:19], v9, s[52:55], s88 offen offset:0 sc1\n"
    "ds_read_b64_tr_b16 a[228:229], v2 offset:10368\n"
    "v_mfma_f32_16x16x32_bf16 v[104:107], a[212:215], a[40:43], v[104:107]\n"
    "s_add_u32 s67, s66, 4096\n"
    "v_accvgpr_read_b32 v16, a128\n"
    "v_accvgpr_read_b32 v17, a129\n"
    "v_accvgpr_read_b32 v18, a130\n"
    "v_mfma_f32_16x16x32_bf16 v[108:111], a[212:215], a[44:47], 0\n"
    "buffer_load_dwordx4 a[48:51], v9, s[52:55], s88 offen offset:1024 sc1\n"
    "ds_read_b64_tr_b16 a[230:231], v2 offset:15488\n"
    "v_accvgpr_read_b32 v19, a131\n"
    "v_mfma_f32_16x16x32_bf16 v[108:111], a[208:211], a[12:15], v[108:111]\n"
    "s_waitcnt lgkmcnt(6)\n"
    "v_mul_f32 v16, v48, v16\n"
    "v_mul_f32 v17, v48, v17\n"
    "v_mul_f32 v18, v48, v18\n"
    "v_mfma_f32_16x16x32_bf16 v[124:127], a[216:219], a[12:15], 0\n"
    "s_waitcnt lgkmcnt(4)\n"
    "buffer_load_dwordx4 a[20:23], v9, s[52:55], s88 offen offset:2048 sc1\n"
    "ds_read_b64_tr_b16 a[232:233], v4 offset:0\n"
    "v_mul_f32 v19, v48, v19\n"
    "v_mfma_f32_16x16x32_bf16 v[124:127], a[220:223], a[44:47], v[124:127]\n"
    "v_cvt_pk_bf16_f32 v52, v16, v17\n"
    "v_cvt_pk_bf16_f32 v53, v18, v19\n"
    "v_accvgpr_read_b32 v20, a144\n"
    "v_mfma_f32_16x16x32_bf16 v[120:123], a[220:223], a[40:43], 0\n"
    "buffer_load_dwordx4 a[52:55], v9, s[52:55], s88 offen offset:3072 sc1\n"
    "ds_read_b64_tr_b16 a[234:235], v4 offset:5120\n"
    "v_accvgpr_read_b32 v21, a145\n"
    "v_mfma_f32_16x16x32_bf16 v[120:123], a[216:219], a[8:11], v[120:123]\n"
    "v_accvgpr_read_b32 v22, a146\n"
    "v_accvgpr_read_b32 v23, a147\n"
    "v_mul_f32 v20, v48, v20\n"
    "v_mfma_f32_16x16x32_bf16 v[116:119], a[216:219], a[4:7], 0\n"
    "buffer_load_dwordx4 a[24:27], v9, s[52:55], s89 offen offset:0 sc1\n"
    "ds_read_b64_tr_b16 a[236:237], v4 offset:10240\n"
    "v_mul_f32 v21, v48, v21\n"
    "v_mfma_f32_16x16x32_bf16 v[116:119], a[220:223], a[36:39], v[116:119]\n"
    "v_mul_f32 v22, v48, v22\n"
    "v_mul_f32 v23, v48, v23\n"
    "v_cvt_pk_bf16_f32 v54, v20, v21\n"
    "v_mfma_f32_16x16x32_bf16 v[112:115], a[220:223], a[32:35], 0\n"
    "ds_read_b64_tr_b16 a[238:239], v4 offset:15360\n"
    "v_cvt_pk_bf16_f32 v55, v22, v23\n"
    "v_accvgpr_write_b32 a128, v52\n"
    "v_mfma_f32_16x16x32_bf16 v[112:115], a[216:219], a[0:3], v[112:115]\n"
    "s_waitcnt lgkmcnt(6)\n"
    "v_accvgpr_write_b32 a129, v53\n"
    "v_accvgpr_write_b32 a130, v54\n"
    "v_accvgpr_write_b32 a131, v55\n"
    "v_mfma_f32_16x16x32_bf16 v[128:131], a[224:227], a[0:3], 0\n"
    "s_waitcnt lgkmcnt(4)\n"
    "buffer_load_dwordx4 a[56:59], v9, s[52:55], s89 offen offset:1024 sc1\n"
    "ds_read_b64_tr_b16 a[192:193], v5 offset:0\n"
    "v_mfma_f32_16x16x32_bf16 v[128:131], a[228:231], a[32:35], v[128:131]\n"
    "buffer_store_dwordx4 v[52:55], v9, s[52:55], s66 offen offset:0 sc1\n"
    "v_mfma_f32_16x16x32_bf16 v[132:135], a[228:231], a[36:39], 0\n"
    "ds_read_b64_tr_b16 a[194:195], v5 offset:5120\n"
    "v_accvgpr_read_b32 v16, a132\n"
    "v_accvgpr_read_b32 v17, a133\n"
    "v_mfma_f32_16x16x32_bf16 v[132:135], a[224:227], a[4:7], v[132:135]\n"
    "v_accvgpr_read_b32 v18, a134\n"
    "v_accvgpr_read_b32 v19, a135\n"
    "v_mul_f32 v16, v49, v16\n"
    "v_mfma_f32_16x16x32_bf16 v[136:139], a[224:227], a[8:11], 0\n"
    "buffer_load_dwordx4 a[28:31], v9, s[52:55], s89 offen offset:2048 sc1\n"
    "ds_read_b64_tr_b16 a[196:197], v5 offset:10240\n"
    "v_mul_f32 v17, v49, v17\n"
    "v_mfma_f32_16x16x32_bf16 v[136:139], a[228:231], a[40:43], v[136:139]\n"
    "v_mul_f32 v18, v49, v18\n"
    "v_mul_f32 v19, v49, v19\n"
    "v_cvt_pk_bf16_f32 v24, v16, v17\n"
    "v_mfma_f32_16x16x32_bf16 v[140:143], a[228:231], a[44:47], 0\n"
    "buffer_load_dwordx4 a[60:63], v9, s[52:55], s89 offen offset:3072 sc1\n"
    "ds_read_b64_tr_b16 a[198:199], v5 offset:15360\n"
    "v_cvt_pk_bf16_f32 v25, v18, v19\n"
    "v_mfma_f32_16x16x32_bf16 v[140:143], a[224:227], a[12:15], v[140:143]\n"
    "s_waitcnt lgkmcnt(6)\n"
    "v_accvgpr_read_b32 v20, a148\n"
    "v_accvgpr_read_b32 v21, a149\n"
    "v_accvgpr_read_b32 v22, a150\n"
    "v_mfma_f32_16x16x32_bf16 v[156:159], a[232:235], a[12:15], 0\n"
    "s_waitcnt lgkmcnt(4)\n"
    "ds_read_b64_tr_b16 a[200:201], v4 offset:64\n"
    "v_accvgpr_read_b32 v23, a151\n"
    "v_mul_f32 v20, v49, v20\n"
    "v_mfma_f32_16x16x32_bf16 v[156:159], a[236:239], a[44:47], v[156:159]\n"
    "v_mul_f32 v21, v49, v21\n"
    "v_mul_f32 v22, v49, v22\n"
    "v_mul_f32 v23, v49, v23\n"
    "v_mfma_f32_16x16x32_bf16 v[152:155], a[236:239], a[40:43], 0\n"
    "ds_read_b64_tr_b16 a[202:203], v4 offset:5184\n"
    "v_cvt_pk_bf16_f32 v26, v20, v21\n"
    "v_cvt_pk_bf16_f32 v27, v22, v23\n"
    "v_mfma_f32_16x16x32_bf16 v[152:155], a[232:235], a[8:11], v[152:155]\n"
    "s_cmp_lt_u32 s79, 3\n"
    "s_cselect_b32 s90, 3, 2\n"
    "v_accvgpr_write_b32 a132, v24\n"
    "v_accvgpr_write_b32 a133, v25\n"
    "v_accvgpr_write_b32 a134, v26\n"
    "v_mfma_f32_16x16x32_bf16 v[148:151], a[232:235], a[4:7], 0\n"
    "s_mul_i32 s90, s90, 245760\n"
    "ds_read_b64_tr_b16 a[204:205], v4 offset:10304\n"
    "v_accvgpr_write_b32 a135, v27\n"
    "v_mfma_f32_16x16x32_bf16 v[148:151], a[236:239], a[36:39], v[148:151]\n"
    "s_add_u32 s90, s90, 0\n"
    "buffer_store_dwordx4 v[24:27], v9, s[52:55], s66 offen offset:2048 sc1\n"
    "v_mfma_f32_16x16x32_bf16 v[144:147], a[236:239], a[32:35], 0\n"
    "s_add_u32 m0, s91, 102400\n"
    "ds_read_b64_tr_b16 a[206:207], v4 offset:15424\n"
    "v_mfma_f32_16x16x32_bf16 v[144:147], a[232:235], a[0:3], v[144:147]\n"
    "s_waitcnt lgkmcnt(6)\n"
    "v_accvgpr_read_b32 v16, a136\n"
    "v_accvgpr_read_b32 v17, a137\n"
    "v_accvgpr_read_b32 v18, a138\n"
    "v_mfma_f32_16x16x32_bf16 v[160:163], a[192:195], a[0:3], 0\n"
    "s_waitcnt lgkmcnt(4)\n"
    "buffer_load_dwordx4 v11, s[48:51], s90 offen offset:0 lds\n"
    "ds_read_b64_tr_b16 a[208:209], v5 offset:64\n"
    "v_accvgpr_read_b32 v19, a139\n"
    "v_mfma_f32_16x16x32_bf16 v[160:163], a[196:199], a[32:35], v[160:163]\n"
    "v_mul_f32 v16, v50, v16\n"
    "v_mul_f32 v17, v50, v17\n"
    "v_mul_f32 v18, v50, v18\n"
    "v_mfma_f32_16x16x32_bf16 v[164:167], a[196:199], a[36:39], 0\n"
    "buffer_load_dwordx4 v12, s[48:51], s90 offen offset:1024 lds\n"
    "ds_read_b64_tr_b16 a[210:211], v5 offset:5184\n"
    "v_mul_f32 v19, v50, v19\n"
    "v_mfma_f32_16x16x32_bf16 v[164:167], a[192:195], a[4:7], v[164:167]\n"
    "v_cvt_pk_bf16_f32 v52, v16, v17\n"
    "v_cvt_pk_bf16_f32 v53, v18, v19\n"
    "v_accvgpr_read_b32 v20, a152\n"
    "v_mfma_f32_16x16x32_bf16 v[168:171], a[192:195], a[8:11], 0\n"
    "buffer_load_dwordx4 v13, s[48:51], s90 offen offset:2048 lds\n"
    "ds_read_b64_tr_b16 a[212:213], v5 offset:10304\n"
    "v_accvgpr_read_b32 v21, a153\n"
    "v_mfma_f32_16x16x32_bf16 v[168:171], a[196:199], a[40:43], v[168:171]\n"
    "v_accvgpr_read_b32 v22, a154\n"
    "v_accvgpr_read_b32 v23, a155\n"
    "v_mul_f32 v20, v50, v20\n"
    "v_mfma_f32_16x16x32_bf16 v[172:175], a[196:199], a[44:47], 0\n"
    "buffer_load_dwordx4 v14, s[48:51], s90 offen offset:3072 lds\n"
    "ds_read_b64_tr_b16 a[214:215], v5 offset:15424\n"
    "v_mul_f32 v21, v50, v21\n"
    "v_mfma_f32_16x16x32_bf16 v[172:175], a[192:195], a[12:15], v[172:175]\n"
    "s_waitcnt lgkmcnt(6)\n"
    "v_mul_f32 v22, v50, v22\n"
    "v_mul_f32 v23, v50, v23\n"
    "v_cvt_pk_bf16_f32 v54, v20, v21\n"
    "v_mfma_f32_16x16x32_bf16 v[188:191], a[200:203], a[12:15], 0\n"
    "s_waitcnt lgkmcnt(4)\n"
    "ds_read_b64_tr_b16 a[216:217], v4 offset:128\n"
    "v_cvt_pk_bf16_f32 v55, v22, v23\n"
    "v_accvgpr_write_b32 a136, v52\n"
    "v_mfma_f32_16x16x32_bf16 v[188:191], a[204:207], a[44:47], v[188:191]\n"
    "v_accvgpr_write_b32 a137, v53\n"
    "v_accvgpr_write_b32 a138, v54\n"
    "v_accvgpr_write_b32 a139, v55\n"
    "v_mfma_f32_16x16x32_bf16 v[184:187], a[204:207], a[40:43], 0\n"
    "s_add_u32 m0, s91, 106496\n"
    "buffer_store_dwordx4 v[52:55], v9, s[52:55], s67 offen offset:0 sc1\n"
    "ds_read_b64_tr_b16 a[218:219], v4 offset:5248\n"
    "v_mfma_f32_16x16x32_bf16 v[184:187], a[200:203], a[8:11], v[184:187]\n"
    "v_mfma_f32_16x16x32_bf16 v[180:183], a[200:203], a[4:7], 0\n"
    "buffer_load_dwordx4 v15, s[48:51], s90 offen offset:0 lds\n"
    "ds_read_b64_tr_b16 a[220:221], v4 offset:10368\n"
    "v_mfma_f32_16x16x32_bf16 v[180:183], a[204:207], a[36:39], v[180:183]\n"
    "v_accvgpr_read_b32 v16, a140\n"
    "v_accvgpr_read_b32 v17, a141\n"
    "v_accvgpr_read_b32 v18, a142\n"
    "v_mfma_f32_16x16x32_bf16 v[176:179], a[204:207], a[32:35], 0\n"
    "ds_read_b64_tr_b16 a[222:223], v4 offset:15488\n"
    "v_accvgpr_read_b32 v19, a143\n"
    "v_mul_f32 v16, v51, v16\n"
    "v_mfma_f32_16x16x32_bf16 v[176:179], a[200:203], a[0:3], v[176:179]\n"
    "s_waitcnt lgkmcnt(6)\n"
    "s_waitcnt vmcnt(16)\n"
    "s_barrier\n"
    "v_add_u32 v2, 4294942720, v2\n"
    "v_add_u32 v3, 4294942720, v3\n"
    "v_mul_f32 v17, v51, v17\n"
    "v_mfma_f32_16x16x32_bf16 v[192:195], a[208:211], a[0:3], 0\n"
    "s_waitcnt lgkmcnt(4)\n"
    "ds_read_b64_tr_b16 a[224:225], v2 offset:0\n"
    "v_mul_f32 v18, v51, v18\n"
    "v_mul_f32 v19, v51, v19\n"
    "v_mfma_f32_16x16x32_bf16 v[192:195], a[212:215], a[32:35], v[192:195]\n"
    "v_add_u32 v4, 4294942720, v4\n"
    "v_add_u32 v5, 4294942720, v5\n"
    "v_cvt_pk_bf16_f32 v24, v16, v17\n"
    "v_mfma_f32_16x16x32_bf16 v[196:199], a[212:215], a[36:39], 0\n"
    "s_lshl_b32 s95, s70, 6\n"
    "ds_read_b64_tr_b16 a[226:227], v2 offset:5120\n"
    "v_cvt_pk_bf16_f32 v25, v18, v19\n"
    "v_accvgpr_read_b32 v20, a156\n"
    "v_mfma_f32_16x16x32_bf16 v[196:199], a[208:211], a[4:7], v[196:199]\n"
    "s_add_u32 s95, s95, 135168\n"
    "v_mov_b32 v62, s80\n"
    "v_mov_b32 v63, s81\n"
    "v_accvgpr_read_b32 v21, a157\n"
    "v_mfma_f32_16x16x32_bf16 v[200:203], a[208:211], a[8:11], 0\n"
    "buffer_store_dwordx2 v[62:63], v10, s[52:55], s95 offen sc1\n"
    "ds_read_b64_tr_b16 a[228:229], v2 offset:10240\n"
    "v_accvgpr_read_b32 v22, a158\n"
    "v_mfma_f32_16x16x32_bf16 v[200:203], a[212:215], a[40:43], v[200:203]\n"
    "v_accvgpr_read_b32 v23, a159\n"
    "v_mul_f32 v20, v51, v20\n"
    "v_mul_f32 v21, v51, v21\n"
    "v_mfma_f32_16x16x32_bf16 v[204:207], a[212:215], a[44:47], 0\n"
    "ds_read_b64_tr_b16 a[230:231], v2 offset:15360\n"
    "v_mul_f32 v22, v51, v22\n"
    "v_mul_f32 v23, v51, v23\n"
    "v_mfma_f32_16x16x32_bf16 v[204:207], a[208:211], a[12:15], v[204:207]\n"
    "s_waitcnt lgkmcnt(6)\n"
    "v_cvt_pk_bf16_f32 v26, v20, v21\n"
    "v_cvt_pk_bf16_f32 v27, v22, v23\n"
    "v_accvgpr_write_b32 a140, v24\n"
    "v_mfma_f32_16x16x32_bf16 v[236:239], a[216:219], a[12:15], 0\n"
    "s_waitcnt lgkmcnt(4)\n"
    "ds_read_b64_tr_b16 a[232:233], v3 offset:0\n"
    "v_accvgpr_write_b32 a141, v25\n"
    "v_accvgpr_write_b32 a142, v26\n"
    "v_mfma_f32_16x16x32_bf16 v[236:239], a[220:223], a[44:47], v[236:239]\n"
    "v_accvgpr_write_b32 a143, v27\n"
    "v_mfma_f32_16x16x32_bf16 v[232:235], a[220:223], a[40:43], 0\n"
    "buffer_store_dwordx4 v[24:27], v9, s[52:55], s67 offen offset:2048 sc1\n"
    "ds_read_b64_tr_b16 a[234:235], v3 offset:5120\n"
    "v_mfma_f32_16x16x32_bf16 v[232:235], a[216:219], a[8:11], v[232:235]\n"
    "s_add_u32 s66, s92, 16384\n"
    "v_mfma_f32_16x16x32_bf16 v[228:231], a[216:219], a[4:7], 0\n"
    "s_add_u32 s67, s66, 4096\n"
    "ds_read_b64_tr_b16 a[236:237], v3 offset:10240\n"
    "v_accvgpr_read_b32 v16, a160\n"
    "v_accvgpr_read_b32 v17, a161\n"
    "v_mfma_f32_16x16x32_bf16 v[228:231], a[220:223], a[36:39], v[228:231]\n"
    "v_accvgpr_read_b32 v18, a162\n"
    "v_accvgpr_read_b32 v19, a163\n"
    "v_mul_f32 v16, v48, v16\n"
    "v_mfma_f32_16x16x32_bf16 v[224:227], a[220:223], a[32:35], 0\n"
    "ds_read_b64_tr_b16 a[238:239], v3 offset:15360\n"
    "v_mul_f32 v17, v48, v17\n"
    "v_mul_f32 v18, v48, v18\n"
    "v_mfma_f32_16x16x32_bf16 v[224:227], a[216:219], a[0:3], v[224:227]\n"
    "s_waitcnt lgkmcnt(6)\n"
    "v_mul_f32 v19, v48, v19\n"
    "v_cvt_pk_bf16_f32 v52, v16, v17\n"
    "v_cvt_pk_bf16_f32 v53, v18, v19\n"
    "v_mfma_f32_16x16x32_bf16 v[64:67], a[224:227], a[64:67], v[64:67]\n"
    "s_waitcnt lgkmcnt(4)\n"
    "ds_read_b64_tr_b16 a[192:193], v2 offset:64\n"
    "v_accvgpr_read_b32 v20, a176\n"
    "v_accvgpr_read_b32 v21, a177\n"
    "v_mfma_f32_16x16x32_bf16 v[64:67], a[228:231], a[96:99], v[64:67]\n"
    "v_accvgpr_read_b32 v22, a178\n"
    "v_accvgpr_read_b32 v23, a179\n"
    "v_mul_f32 v20, v48, v20\n"
    "v_mfma_f32_16x16x32_bf16 v[68:71], a[228:231], a[100:103], v[68:71]\n"
    "s_cmp_lt_u32 s79, 2\n"
    "s_cselect_b32 s88, 2, 1\n"
    "ds_read_b64_tr_b16 a[194:195], v2 offset:5184\n"
    "v_mul_f32 v21, v48, v21\n"
    "v_mul_f32 v22, v48, v22\n"
    "v_mfma_f32_16x16x32_bf16 v[68:71], a[224:227], a[68:71], v[68:71]\n"
    "s_mul_i32 s88, s88, 786432\n"
    "v_mul_f32 v23, v48, v23\n"
    "v_cvt_pk_bf16_f32 v54, v20, v21\n"
    "v_cvt_pk_bf16_f32 v55, v22, v23\n"
    "v_mfma_f32_16x16x32_bf16 v[72:75], a[224:227], a[72:75], v[72:75]\n"
    "s_add_u32 s88, s88, s65\n"
    "ds_read_b64_tr_b16 a[196:197], v2 offset:10304\n"
    "v_accvgpr_write_b32 a160, v52\n"
    "v_accvgpr_write_b32 a161, v53\n"
    "v_mfma_f32_16x16x32_bf16 v[72:75], a[228:231], a[104:107], v[72:75]\n"
    "s_add_u32 s88, s88, 0\n"
    "v_accvgpr_write_b32 a162, v54\n"
    "v_accvgpr_write_b32 a163, v55\n"
    "v_mfma_f32_16x16x32_bf16 v[76:79], a[228:231], a[108:111], v[76:79]\n"
    "s_add_u32 s89, s88, 4096\n"
    "buffer_store_dwordx4 v[52:55], v9, s[52:55], s66 offen offset:1024 sc1\n"
    "ds_read_b64_tr_b16 a[198:199], v2 offset:15424\n"
    "v_mfma_f32_16x16x32_bf16 v[76:79], a[224:227], a[76:79], v[76:79]\n"
    "s_waitcnt lgkmcnt(6)\n"
    "v_mfma_f32_16x16x32_bf16 v[92:95], a[232:235], a[76:79], v[92:95]\n"
    "s_waitcnt lgkmcnt(4)\n"
    "buffer_load_dwordx4 a[80:83], v9, s[52:55], s88 offen offset:0 sc1\n"
    "ds_read_b64_tr_b16 a[200:201], v3 offset:64\n"
    "v_accvgpr_read_b32 v16, a164\n"
    "v_mfma_f32_16x16x32_bf16 v[92:95], a[236:239], a[108:111], v[92:95]\n"
    "v_accvgpr_read_b32 v17, a165\n"
    "v_accvgpr_read_b32 v18, a166\n"
    "v_accvgpr_read_b32 v19, a167\n"
    "v_mfma_f32_16x16x32_bf16 v[88:91], a[236:239], a[104:107], v[88:91]\n"
    "buffer_load_dwordx4 a[112:115], v9, s[52:55], s88 offen offset:1024 sc1\n"
    "ds_read_b64_tr_b16 a[202:203], v3 offset:5184\n"
    "v_mul_f32 v16, v49, v16\n"
    "v_mfma_f32_16x16x32_bf16 v[88:91], a[232:235], a[72:75], v[88:91]\n"
    "v_mul_f32 v17, v49, v17\n"
    "v_mul_f32 v18, v49, v18\n"
    "v_mul_f32 v19, v49, v19\n"
    "v_mfma_f32_16x16x32_bf16 v[84:87], a[232:235], a[68:71], v[84:87]\n"
    "buffer_load_dwordx4 a[84:87], v9, s[52:55], s88 offen offset:2048 sc1\n"
    "ds_read_b64_tr_b16 a[204:205], v3 offset:10304\n"
    "v_cvt_pk_bf16_f32 v24, v16, v17\n"
    "v_mfma_f32_16x16x32_bf16 v[84:87], a[236:239], a[100:103], v[84:87]\n"
    "v_cvt_pk_bf16_f32 v25, v18, v19\n"
    "v_accvgpr_read_b32 v20, a180\n"
    "v_accvgpr_read_b32 v21, a181\n"
    "v_mfma_f32_16x16x32_bf16 v[80:83], a[236:239], a[96:99], v[80:83]\n"
    "ds_read_b64_tr_b16 a[206:207], v3 offset:15424\n"
    "v_accvgpr_read_b32 v22, a182\n"
    "v_accvgpr_read_b32 v23, a183\n"
    "v_mfma_f32_16x16x32_bf16 v[80:83], a[232:235], a[64:67], v[80:83]\n"
    "s_waitcnt lgkmcnt(6)\n"
    "v_mul_f32 v20, v49, v20\n"
    "v_mul_f32 v21, v49, v21\n"
    "v_mul_f32 v22, v49, v22\n"
    "v_mfma_f32_16x16x32_bf16 v[96:99], a[192:195], a[64:67], v[96:99]\n"
    "s_waitcnt lgkmcnt(4)\n"
    "ds_read_b64_tr_b16 a[208:209], v2 offset:128\n"
    "v_mul_f32 v23, v49, v23\n"
    "v_cvt_pk_bf16_f32 v26, v20, v21\n"
    "v_mfma_f32_16x16x32_bf16 v[96:99], a[196:199], a[96:99], v[96:99]\n"
    "v_cvt_pk_bf16_f32 v27, v22, v23\n"
    "v_accvgpr_write_b32 a164, v24\n"
    "v_accvgpr_write_b32 a165, v25\n"
    "v_mfma_f32_16x16x32_bf16 v[100:103], a[196:199], a[100:103], v[100:103]\n"
    "buffer_load_dwordx4 a[116:119], v9, s[52:55], s88 offen offset:3072 sc1\n"
    "ds_read_b64_tr_b16 a[210:211], v2 offset:5248\n"
    "v_accvgpr_write_b32 a166, v26\n"
    "v_mfma_f32_16x16x32_bf16 v[100:103], a[192:195], a[68:71], v[100:103]\n"
    "v_accvgpr_write_b32 a167, v27\n"
    "v_mfma_f32_16x16x32_bf16 v[104:107], a[192:195], a[72:75], v[104:107]\n"
    "buffer_store_dwordx4 v[24:27], v9, s[52:55], s66 offen offset:3072 sc1\n"
    "ds_read_b64_tr_b16 a[212:213], v2 offset:10368\n"
    "v_mfma_f32_16x16x32_bf16 v[104:107], a[196:199], a[104:107], v[104:107]\n"
    "v_mfma_f32_16x16x32_bf16 v[108:111], a[196:199], a[108:111], v[108:111]\n"
    "buffer_load_dwordx4 a[88:91], v9, s[52:55], s89 offen offset:0 sc1\n"
    "ds_read_b64_tr_b16 a[214:215], v2 offset:15488\n"
    "v_accvgpr_read_b32 v16, a168\n"
    "v_mfma_f32_16x16x32_bf16 v[108:111], a[192:195], a[76:79], v[108:111]\n"
    "s_waitcnt lgkmcnt(6)\n"
    "v_accvgpr_read_b32 v17, a169\n"
    "v_accvgpr_read_b32 v18, a170\n"
    "v_accvgpr_read_b32 v19, a171\n"
    "v_mfma_f32_16x16x32_bf16 v[124:127], a[200:203], a[76:79], v[124:127]\n"
    "s_waitcnt lgkmcnt(4)\n"
    "ds_read_b64_tr_b16 a[216:217], v4 offset:0\n"
    "v_mul_f32 v16, v50, v16\n"
    "v_mul_f32 v17, v50, v17\n"
    "v_mfma_f32_16x16x32_bf16 v[124:127], a[204:207], a[108:111], v[124:127]\n"
    "v_mul_f32 v18, v50, v18\n"
    "v_mul_f32 v19, v50, v19\n"
    "v_cvt_pk_bf16_f32 v52, v16, v17\n"
    "v_mfma_f32_16x16x32_bf16 v[120:123], a[204:207], a[104:107], v[120:123]\n"
    "ds_read_b64_tr_b16 a[218:219], v4 offset:5120\n"
    "v_cvt_pk_bf16_f32 v53, v18, v19\n"
    "v_accvgpr_read_b32 v20, a184\n"
    "v_mfma_f32_16x16x32_bf16 v[120:123], a[200:203], a[72:75], v[120:123]\n"
    "v_accvgpr_read_b32 v21, a185\n"
    "v_accvgpr_read_b32 v22, a186\n"
    "v_accvgpr_read_b32 v23, a187\n"
    "v_mfma_f32_16x16x32_bf16 v[116:119], a[200:203], a[68:71], v[116:119]\n"
    "buffer_load_dwordx4 a[120:123], v9, s[52:55], s89 offen offset:1024 sc1\n"
    "ds_read_b64_tr_b16 a[220:221], v4 offset:10240\n"
    "v_mul_f32 v20, v50, v20\n"
    "v_mfma_f32_16x16x32_bf16 v[116:119], a[204:207], a[100:103], v[116:119]\n"
    "v_mul_f32 v21, v50, v21\n"
    "v_mul_f32 v22, v50, v22\n"
    "v_mul_f32 v23, v50, v23\n"
    "v_mfma_f32_16x16x32_bf16 v[112:115], a[204:207], a[96:99], v[112:115]\n"
    "ds_read_b64_tr_b16 a[222:223], v4 offset:15360\n"
    "v_cvt_pk_bf16_f32 v54, v20, v21\n"
    "v_cvt_pk_bf16_f32 v55, v22, v23\n"
    "v_mfma_f32_16x16x32_bf16 v[112:115], a[200:203], a[64:67], v[112:115]\n"
    "s_waitcnt lgkmcnt(6)\n"
    "v_accvgpr_write_b32 a168, v52\n"
    "v_accvgpr_write_b32 a169, v53\n"
    "v_accvgpr_write_b32 a170, v54\n"
    "v_mfma_f32_16x16x32_bf16 v[128:131], a[208:211], a[64:67], v[128:131]\n"
    "s_waitcnt lgkmcnt(4)\n"
    "ds_read_b64_tr_b16 a[224:225], v5 offset:0\n"
    "v_accvgpr_write_b32 a171, v55\n"
    "v_mfma_f32_16x16x32_bf16 v[128:131], a[212:215], a[96:99], v[128:131]\n"
    "buffer_store_dwordx4 v[52:55], v9, s[52:55], s67 offen offset:1024 sc1\n"
    "v_mfma_f32_16x16x32_bf16 v[132:135], a[212:215], a[100:103], v[132:135]\n"
    "buffer_load_dwordx4 a[92:95], v9, s[52:55], s89 offen offset:2048 sc1\n"
    "ds_read_b64_tr_b16 a[226:227], v5 offset:5120\n"
    "v_mfma_f32_16x16x32_bf16 v[132:135], a[208:211], a[68:71], v[132:135]\n"
    "v_mfma_f32_16x16x32_bf16 v[136:139], a[208:211], a[72:75], v[136:139]\n"
    "ds_read_b64_tr_b16 a[228:229], v5 offset:10240\n"
    "v_accvgpr_read_b32 v16, a172\n"
    "v_accvgpr_read_b32 v17, a173\n"
    "v_mfma_f32_16x16x32_bf16 v[136:139], a[212:215], a[104:107], v[136:139]\n"
    "v_accvgpr_read_b32 v18, a174\n"
    "v_accvgpr_read_b32 v19, a175\n"
    "v_mul_f32 v16, v51, v16\n"
    "v_mfma_f32_16x16x32_bf16 v[140:143], a[212:215], a[108:111], v[140:143]\n"
    "ds_read_b64_tr_b16 a[230:231], v5 offset:15360\n"
    "v_mul_f32 v17, v51, v17\n"
    "v_mul_f32 v18, v51, v18\n"
    "v_mfma_f32_16x16x32_bf16 v[140:143], a[208:211], a[76:79], v[140:143]\n"
    "s_waitcnt lgkmcnt(6)\n"
    "v_mul_f32 v19, v51, v19\n"
    "v_cvt_pk_bf16_f32 v24, v16, v17\n"
    "v_cvt_pk_bf16_f32 v25, v18, v19\n"
    "v_mfma_f32_16x16x32_bf16 v[156:159], a[216:219], a[76:79], v[156:159]\n"
    "s_waitcnt lgkmcnt(4)\n"
    "buffer_load_dwordx4 a[124:127], v9, s[52:55], s89 offen offset:3072 sc1\n"
    "ds_read_b64_tr_b16 a[232:233], v4 offset:64\n"
    "v_accvgpr_read_b32 v20, a188\n"
    "v_mfma_f32_16x16x32_bf16 v[156:159], a[220:223], a[108:111], v[156:159]\n"
    "v_accvgpr_read_b32 v21, a189\n"
    "v_accvgpr_read_b32 v22, a190\n"
    "v_accvgpr_read_b32 v23, a191\n"
    "v_mfma_f32_16x16x32_bf16 v[152:155], a[220:223], a[104:107], v[152:155]\n"
    "ds_read_b64_tr_b16 a[234:235], v4 offset:5184\n"
    "v_mul_f32 v20, v51, v20\n"
    "v_mul_f32 v21, v51, v21\n"
    "v_mfma_f32_16x16x32_bf16 v[152:155], a[216:219], a[72:75], v[152:155]\n"
    "v_mul_f32 v22, v51, v22\n"
    "v_mul_f32 v23, v51, v23\n"
    "v_cvt_pk_bf16_f32 v26, v20, v21\n"
    "v_mfma_f32_16x16x32_bf16 v[148:151], a[216:219], a[68:71], v[148:151]\n"
    "ds_read_b64_tr_b16 a[236:237], v4 offset:10304\n"
    "v_cvt_pk_bf16_f32 v27, v22, v23\n"
    "v_accvgpr_write_b32 a172, v24\n"
    "v_mfma_f32_16x16x32_bf16 v[148:151], a[220:223], a[100:103], v[148:151]\n"
    "s_cmp_lt_u32 s79, 1\n"
    "s_cselect_b32 s90, 1, 0\n"
    "v_accvgpr_write_b32 a173, v25\n"
    "v_accvgpr_write_b32 a174, v26\n"
    "v_accvgpr_write_b32 a175, v27\n"
    "v_mfma_f32_16x16x32_bf16 v[144:147], a[220:223], a[96:99], v[144:147]\n"
    "s_mul_i32 s90, s90, 245760\n"
    "buffer_store_dwordx4 v[24:27], v9, s[52:55], s67 offen offset:3072 sc1\n"
    "ds_read_b64_tr_b16 a[238:239], v4 offset:15424\n"
    "v_mfma_f32_16x16x32_bf16 v[144:147], a[216:219], a[64:67], v[144:147]\n"
    "s_waitcnt lgkmcnt(6)\n"
    "s_add_u32 s90, s90, 81920\n"
    "v_mfma_f32_16x16x32_bf16 v[160:163], a[224:227], a[64:67], v[160:163]\n"
    "s_waitcnt lgkmcnt(4)\n"
    "s_add_u32 m0, s91, 122880\n"
    "ds_read_b64_tr_b16 a[192:193], v5 offset:64\n"
    "v_mfma_f32_16x16x32_bf16 v[160:163], a[228:231], a[96:99], v[160:163]\n"
    "s_mul_i32 s93, s63, 16\n"
    "v_mfma_f32_16x16x32_bf16 v[164:167], a[228:231], a[100:103], v[164:167]\n"
    "s_add_u32 s93, s93, s62\n"
    "buffer_load_dwordx4 v11, s[48:51], s90 offen offset:0 lds\n"
    "ds_read_b64_tr_b16 a[194:195], v5 offset:5184\n"
    "v_mfma_f32_16x16x32_bf16 v[164:167], a[224:227], a[68:71], v[164:167]\n"
    "s_mul_i32 s93, s93, 3072\n"
    "v_mfma_f32_16x16x32_bf16 v[168:171], a[224:227], a[72:75], v[168:171]\n"
    "s_add_u32 s93, s93, 11534336\n"
    "buffer_load_dwordx4 v12, s[48:51], s90 offen offset:1024 lds\n"
    "ds_read_b64_tr_b16 a[196:197], v5 offset:10304\n"
    "v_mfma_f32_16x16x32_bf16 v[168:171], a[228:231], a[104:107], v[168:171]\n"
    "s_add_u32 s94, s93, 49152\n"
    "v_mfma_f32_16x16x32_bf16 v[172:175], a[228:231], a[108:111], v[172:175]\n"
    "s_mov_b32 s92, 0xbdd2d3e8\n"
    "ds_read_b64_tr_b16 a[198:199], v5 offset:15424\n"
    "v_mov_b32 v20, 0xc0135761\n"
    "v_mul_f32 v16, v208, v208\n"
    "v_mfma_f32_16x16x32_bf16 v[172:175], a[224:227], a[76:79], v[172:175]\n"
    "s_waitcnt lgkmcnt(6)\n"
    "v_fma_f32 v16, v16, s92, v20\n"
    "v_mul_f32 v16, v16, v208\n"
    "v_mfma_f32_16x16x32_bf16 v[188:191], a[232:235], a[76:79], v[188:191]\n"
    "s_waitcnt lgkmcnt(4)\n"
    "buffer_load_dwordx4 v13, s[48:51], s90 offen offset:2048 lds\n"
    "ds_read_b64_tr_b16 a[200:201], v4 offset:128\n"
    "v_mfma_f32_16x16x32_bf16 v[188:191], a[236:239], a[108:111], v[188:191]\n"
    "v_exp_f32 v16, v16\n"
    "v_mul_f32 v17, v209, v209\n"
    "v_mfma_f32_16x16x32_bf16 v[184:187], a[236:239], a[104:107], v[184:187]\n"
    "ds_read_b64_tr_b16 a[202:203], v4 offset:5248\n"
    "v_fma_f32 v17, v17, s92, v20\n"
    "v_add_f32 v16, 1.0, v16\n"
    "v_mfma_f32_16x16x32_bf16 v[184:187], a[232:235], a[72:75], v[184:187]\n"
    "v_mul_f32 v17, v17, v209\n"
    "v_exp_f32 v17, v17\n"
    "v_mfma_f32_16x16x32_bf16 v[180:183], a[232:235], a[68:71], v[180:183]\n"
    "buffer_load_dwordx4 v14, s[48:51], s90 offen offset:3072 lds\n"
    "ds_read_b64_tr_b16 a[204:205], v4 offset:10368\n"
    "v_mul_f32 v18, v210, v210\n"
    "v_mfma_f32_16x16x32_bf16 v[180:183], a[236:239], a[100:103], v[180:183]\n"
    "v_rcp_f32 v16, v16\n"
    "v_fma_f32 v18, v18, s92, v20\n"
    "v_mfma_f32_16x16x32_bf16 v[176:179], a[236:239], a[96:99], v[176:179]\n"
    "ds_read_b64_tr_b16 a[206:207], v4 offset:15488\n"
    "v_add_f32 v17, 1.0, v17\n"
    "v_mul_f32 v18, v18, v210\n"
    "v_mfma_f32_16x16x32_bf16 v[176:179], a[232:235], a[64:67], v[176:179]\n"
    "s_waitcnt lgkmcnt(6)\n"
    "s_waitcnt vmcnt(25)\n"
    "s_barrier\n"
    "v_add_u32 v2, 49152, v2\n"
    "v_add_u32 v3, 49152, v3\n"
    "v_mul_f32 v208, v208, v16\n"
    "v_mfma_f32_16x16x32_bf16 v[192:195], a[192:195], a[64:67], v[192:195]\n"
    "s_waitcnt lgkmcnt(4)\n"
    "ds_read_b64_tr_b16 a[208:209], v2 offset:0\n"
    "v_exp_f32 v18, v18\n"
    "v_mfma_f32_16x16x32_bf16 v[192:195], a[196:199], a[96:99], v[192:195]\n"
    "v_add_u32 v4, 49152, v4\n"
    "v_add_u32 v5, 49152, v5\n"
    "v_mul_f32 v16, v211, v211\n"
    "v_mfma_f32_16x16x32_bf16 v[196:199], a[196:199], a[100:103], v[196:199]\n"
    "s_add_u32 m0, s91, 126976\n"
    "ds_read_b64_tr_b16 a[210:211], v2 offset:5120\n"
    "v_rcp_f32 v17, v17\n"
    "v_mfma_f32_16x16x32_bf16 v[196:199], a[192:195], a[68:71], v[196:199]\n"
    "v_fma_f32 v16, v16, s92, v20\n"
    "v_add_f32 v18, 1.0, v18\n"
    "v_mul_f32 v16, v16, v211\n"
    "v_mfma_f32_16x16x32_bf16 v[200:203], a[192:195], a[72:75], v[200:203]\n"
    "buffer_load_dwordx4 v15, s[48:51], s90 offen offset:0 lds\n"
    "ds_read_b64_tr_b16 a[212:213], v2 offset:10240\n"
    "v_mul_f32 v209, v209, v17\n"
    "v_mfma_f32_16x16x32_bf16 v[200:203], a[196:199], a[104:107], v[200:203]\n"
    "v_exp_f32 v16, v16\n"
    "v_mul_f32 v17, v212, v212\n"
    "v_mfma_f32_16x16x32_bf16 v[204:207], a[196:199], a[108:111], v[204:207]\n"
    "ds_read_b64_tr_b16 a[214:215], v2 offset:15360\n"
    "v_rcp_f32 v18, v18\n"
    "v_mfma_f32_16x16x32_bf16 v[204:207], a[192:195], a[76:79], v[204:207]\n"
    "s_waitcnt lgkmcnt(6)\n"
    "v_lshrrev_b32 v56, 4, v9\n"
    "v_min_u32 v56, 3, v56\n"
    "v_fma_f32 v17, v17, s92, v20\n"
    "v_mfma_f32_16x16x32_bf16 v[236:239], a[200:203], a[76:79], v[236:239]\n"
    "s_waitcnt lgkmcnt(4)\n"
    "ds_read_b64_tr_b16 a[216:217], v3 offset:0\n"
    "v_add_u32 v56, s69, v56\n"
    "v_add_f32 v16, 1.0, v16\n"
    "v_mfma_f32_16x16x32_bf16 v[236:239], a[204:207], a[108:111], v[236:239]\n"
    "v_lshlrev_b32 v56, 6, v56\n"
    "v_add_u32 v56, 135168, v56\n"
    "v_mul_f32 v17, v17, v212\n"
    "v_mfma_f32_16x16x32_bf16 v[232:235], a[204:207], a[104:107], v[232:235]\n"
    "buffer_load_dwordx2 v[62:63], v56, s[52:55], 0 offen sc1\n"
    "ds_read_b64_tr_b16 a[218:219], v3 offset:5120\n"
    "v_mul_f32 v210, v210, v18\n"
    "v_mfma_f32_16x16x32_bf16 v[232:235], a[200:203], a[72:75], v[232:235]\n"
    "v_exp_f32 v17, v17\n"
    "v_mul_f32 v18, v213, v213\n"
    "v_mfma_f32_16x16x32_bf16 v[228:231], a[200:203], a[68:71], v[228:231]\n"
    "ds_read_b64_tr_b16 a[220:221], v3 offset:10240\n"
    "v_rcp_f32 v16, v16\n"
    "v_mfma_f32_16x16x32_bf16 v[228:231], a[204:207], a[100:103], v[228:231]\n"
    "v_fma_f32 v18, v18, s92, v20\n"
    "v_add_f32 v17, 1.0, v17\n"
    "v_mul_f32 v18, v18, v213\n"
    "v_mfma_f32_16x16x32_bf16 v[224:227], a[204:207], a[96:99], v[224:227]\n"
    "ds_read_b64_tr_b16 a[222:223], v3 offset:15360\n"
    "v_mul_f32 v211, v211, v16\n"
    "v_mfma_f32_16x16x32_bf16 v[224:227], a[200:203], a[64:67], v[224:227]\n"
    "s_waitcnt lgkmcnt(6)\n"
    "v_exp_f32 v18, v18\n"
    "v_mul_f32 v16, v214, v214\n"
    "v_mfma_f32_16x16x32_bf16 v[64:67], a[208:211], a[16:19], v[64:67]\n"
    "s_waitcnt lgkmcnt(4)\n"
    "ds_read_b64_tr_b16 a[224:225], v2 offset:64\n"
    "v_rcp_f32 v17, v17\n"
    "v_mfma_f32_16x16x32_bf16 v[64:67], a[212:215], a[48:51], v[64:67]\n"
    "v_fma_f32 v16, v16, s92, v20\n"
    "v_add_f32 v18, 1.0, v18\n"
    "v_mul_f32 v16, v16, v214\n"
    "v_mfma_f32_16x16x32_bf16 v[68:71], a[212:215], a[52:55], v[68:71]\n"
    "s_cmp_lt_u32 s79, 3\n"
    "s_cselect_b32 s88, 3, 2\n"
    "ds_read_b64_tr_b16 a[226:227], v2 offset:5184\n"
    "v_mul_f32 v212, v212, v17\n"
    "v_mfma_f32_16x16x32_bf16 v[68:71], a[208:211], a[20:23], v[68:71]\n"
    "s_mul_i32 s88, s88, 786432\n"
    "v_exp_f32 v16, v16\n"
    "v_mul_f32 v17, v215, v215\n"
    "v_mfma_f32_16x16x32_bf16 v[72:75], a[208:211], a[24:27], v[72:75]\n"
    "s_add_u32 s88, s88, s65\n"
    "ds_read_b64_tr_b16 a[228:229], v2 offset:10304\n"
    "v_rcp_f32 v18, v18\n"
    "v_mfma_f32_16x16x32_bf16 v[72:75], a[212:215], a[56:59], v[72:75]\n"
    "s_add_u32 s88, s88, 0\n"
    "v_fma_f32 v17, v17, s92, v20\n"
    "v_add_f32 v16, 1.0, v16\n"
    "v_mul_f32 v17, v17, v215\n"
    "v_mfma_f32_16x16x32_bf16 v[76:79], a[212:215], a[60:63], v[76:79]\n"
    "s_add_u32 s89, s88, 4096\n"
    "ds_read_b64_tr_b16 a[230:231], v2 offset:15424\n"
    "v_mul_f32 v213, v213, v18\n"
    "v_mfma_f32_16x16x32_bf16 v[76:79], a[208:211], a[28:31], v[76:79]\n"
    "s_waitcnt lgkmcnt(6)\n"
    "v_exp_f32 v17, v17\n"
    "v_mul_f32 v18, v216, v216\n"
    "v_mfma_f32_16x16x32_bf16 v[92:95], a[216:219], a[28:31], v[92:95]\n"
    "s_waitcnt lgkmcnt(4)\n"
    "buffer_load_dwordx4 a[0:3], v9, s[52:55], s88 offen offset:0 sc1\n"
    "ds_read_b64_tr_b16 a[232:233], v3 offset:64\n"
    "v_mfma_f32_16x16x32_bf16 v[92:95], a[220:223], a[60:63], v[92:95]\n"
    "v_rcp_f32 v16, v16\n"
    "v_fma_f32 v18, v18, s92, v20\n"
    "v_mfma_f32_16x16x32_bf16 v[88:91], a[220:223], a[56:59], v[88:91]\n"
    "buffer_load_dwordx4 a[32:35], v9, s[52:55], s88 offen offset:1024 sc1\n"
    "ds_read_b64_tr_b16 a[234:235], v3 offset:5184\n"
    "v_add_f32 v17, 1.0, v17\n"
    "v_mfma_f32_16x16x32_bf16 v[88:91], a[216:219], a[24:27], v[88:91]\n"
    "v_mul_f32 v18, v18, v216\n"
    "v_mul_f32 v214, v214, v16\n"
    "v_mfma_f32_16x16x32_bf16 v[84:87], a[216:219], a[20:23], v[84:87]\n"
    "buffer_load_dwordx4 a[4:7], v9, s[52:55], s88 offen offset:2048 sc1\n"
    "ds_read_b64_tr_b16 a[236:237], v3 offset:10304\n"
    "v_mfma_f32_16x16x32_bf16 v[84:87], a[220:223], a[52:55], v[84:87]\n"
    "v_exp_f32 v18, v18\n"
    "v_mul_f32 v16, v217, v217\n"
    "v_mfma_f32_16x16x32_bf16 v[80:83], a[220:223], a[48:51], v[80:83]\n"
    "buffer_load_dwordx4 a[36:39], v9, s[52:55], s88 offen offset:3072 sc1\n"
    "ds_read_b64_tr_b16 a[238:239], v3 offset:15424\n"
    "v_mfma_f32_16x16x32_bf16 v[80:83], a[216:219], a[16:19], v[80:83]\n"
    "s_waitcnt lgkmcnt(6)\n"
    "v_rcp_f32 v17, v17\n"
    "v_fma_f32 v16, v16, s92, v20\n"
    "v_mfma_f32_16x16x32_bf16 v[96:99], a[224:227], a[16:19], v[96:99]\n"
    "s_waitcnt lgkmcnt(4)\n"
    "ds_read_b64_tr_b16 a[192:193], v2 offset:128\n"
    "v_add_f32 v18, 1.0, v18\n"
    "v_mul_f32 v16, v16, v217\n"
    "v_mfma_f32_16x16x32_bf16 v[96:99], a[228:231], a[48:51], v[96:99]\n"
    "v_mul_f32 v215, v215, v17\n"
    "v_exp_f32 v16, v16\n"
    "v_mfma_f32_16x16x32_bf16 v[100:103], a[228:231], a[52:55], v[100:103]\n"
    "buffer_load_dwordx4 a[8:11], v9, s[52:55], s89 offen offset:0 sc1\n"
    "ds_read_b64_tr_b16 a[194:195], v2 offset:5248\n"
    "v_mul_f32 v17, v218, v218\n"
    "v_mfma_f32_16x16x32_bf16 v[100:103], a[224:227], a[20:23], v[100:103]\n"
    "v_rcp_f32 v18, v18\n"
    "v_fma_f32 v17, v17, s92, v20\n"
    "v_mfma_f32_16x16x32_bf16 v[104:107], a[224:227], a[24:27], v[104:107]\n"
    "ds_read_b64_tr_b16 a[196:197], v2 offset:10368\n"
    "v_add_f32 v16, 1.0, v16\n"
    "v_mul_f32 v17, v17, v218\n"
    "v_mfma_f32_16x16x32_bf16 v[104:107], a[228:231], a[56:59], v[104:107]\n"
    "v_mul_f32 v216, v216, v18\n"
    "v_exp_f32 v17, v17\n"
    "v_mfma_f32_16x16x32_bf16 v[108:111], a[228:231], a[60:63], v[108:111]\n"
    "buffer_load_dwordx4 a[40:43], v9, s[52:55], s89 offen offset:1024 sc1\n"
    "ds_read_b64_tr_b16 a[198:199], v2 offset:15488\n"
    "v_mul_f32 v18, v219, v219\n"
    "v_mfma_f32_16x16x32_bf16 v[108:111], a[224:227], a[28:31], v[108:111]\n"
    "s_waitcnt lgkmcnt(6)\n"
    "v_rcp_f32 v16, v16\n"
    "v_fma_f32 v18, v18, s92, v20\n"
    "v_mfma_f32_16x16x32_bf16 v[124:127], a[232:235], a[28:31], v[124:127]\n"
    "s_waitcnt lgkmcnt(4)\n"
    "ds_read_b64_tr_b16 a[200:201], v4 offset:0\n"
    "v_add_f32 v17, 1.0, v17\n"
    "v_mul_f32 v18, v18, v219\n"
    "v_mfma_f32_16x16x32_bf16 v[124:127], a[236:239], a[60:63], v[124:127]\n"
    "v_mul_f32 v217, v217, v16\n"
    "v_exp_f32 v18, v18\n"
    "v_mfma_f32_16x16x32_bf16 v[120:123], a[236:239], a[56:59], v[120:123]\n"
    "ds_read_b64_tr_b16 a[202:203], v4 offset:5120\n"
    "v_mul_f32 v16, v220, v220\n"
    "v_mfma_f32_16x16x32_bf16 v[120:123], a[232:235], a[24:27], v[120:123]\n"
    "v_rcp_f32 v17, v17\n"
    "v_fma_f32 v16, v16, s92, v20\n"
    "v_mfma_f32_16x16x32_bf16 v[116:119], a[232:235], a[20:23], v[116:119]\n"
    "buffer_load_dwordx4 a[12:15], v9, s[52:55], s89 offen offset:2048 sc1\n"
    "ds_read_b64_tr_b16 a[204:205], v4 offset:10240\n"
    "v_add_f32 v18, 1.0, v18\n"
    "v_mfma_f32_16x16x32_bf16 v[116:119], a[236:239], a[52:55], v[116:119]\n"
    "v_mul_f32 v16, v16, v220\n"
    "v_mul_f32 v218, v218, v17\n"
    "v_mfma_f32_16x16x32_bf16 v[112:115], a[236:239], a[48:51], v[112:115]\n"
    "ds_read_b64_tr_b16 a[206:207], v4 offset:15360\n"
    "v_exp_f32 v16, v16\n"
    "v_mfma_f32_16x16x32_bf16 v[112:115], a[232:235], a[16:19], v[112:115]\n"
    "s_waitcnt lgkmcnt(6)\n"
    "v_mul_f32 v17, v221, v221\n"
    "v_rcp_f32 v18, v18\n"
    "v_mfma_f32_16x16x32_bf16 v[128:131], a[192:195], a[16:19], v[128:131]\n"
    "s_waitcnt lgkmcnt(4)\n"
    "buffer_load_dwordx4 a[44:47], v9, s[52:55], s89 offen offset:3072 sc1\n"
    "ds_read_b64_tr_b16 a[208:209], v5 offset:0\n"
    "v_fma_f32 v17, v17, s92, v20\n"
    "v_mfma_f32_16x16x32_bf16 v[128:131], a[196:199], a[48:51], v[128:131]\n"
    "v_add_f32 v16, 1.0, v16\n"
    "v_mul_f32 v17, v17, v221\n"
    "v_mul_f32 v219, v219, v18\n"
    "v_mfma_f32_16x16x32_bf16 v[132:135], a[196:199], a[52:55], v[132:135]\n"
    "ds_read_b64_tr_b16 a[210:211], v5 offset:5120\n"
    "v_exp_f32 v17, v17\n"
    "v_mfma_f32_16x16x32_bf16 v[132:135], a[192:195], a[20:23], v[132:135]\n"
    "v_mul_f32 v18, v222, v222\n"
    "v_rcp_f32 v16, v16\n"
    "v_mfma_f32_16x16x32_bf16 v[136:139], a[192:195], a[24:27], v[136:139]\n"
    "s_cmp_lt_u32 s79, 2\n"
    "s_cselect_b32 s90, 2, 1\n"
    "ds_read_b64_tr_b16 a[212:213], v5 offset:10240\n"
    "v_fma_f32 v18, v18, s92, v20\n"
    "v_add_f32 v17, 1.0, v17\n"
    "v_mfma_f32_16x16x32_bf16 v[136:139], a[196:199], a[56:59], v[136:139]\n"
    "s_mul_i32 s90, s90, 245760\n"
    "v_mul_f32 v18, v18, v222\n"
    "v_mul_f32 v220, v220, v16\n"
    "v_mfma_f32_16x16x32_bf16 v[140:143], a[196:199], a[60:63], v[140:143]\n"
    "s_add_u32 s90, s90, 81920\n"
    "ds_read_b64_tr_b16 a[214:215], v5 offset:15360\n"
    "v_exp_f32 v18, v18\n"
    "v_mfma_f32_16x16x32_bf16 v[140:143], a[192:195], a[28:31], v[140:143]\n"
    "s_waitcnt lgkmcnt(6)\n"
    "s_add_u32 m0, s91, 143360\n"
    "v_mul_f32 v16, v223, v223\n"
    "v_rcp_f32 v17, v17\n"
    "v_mfma_f32_16x16x32_bf16 v[156:159], a[200:203], a[28:31], v[156:159]\n"
    "s_waitcnt lgkmcnt(4)\n"
    "buffer_load_dwordx4 v11, s[48:51], s90 offen offset:0 lds\n"
    "ds_read_b64_tr_b16 a[216:217], v4 offset:64\n"
    "v_fma_f32 v16, v16, s92, v20\n"
    "v_mfma_f32_16x16x32_bf16 v[156:159], a[204:207], a[60:63], v[156:159]\n"
    "v_add_f32 v18, 1.0, v18\n"
    "v_mul_f32 v16, v16, v223\n"
    "v_mul_f32 v221, v221, v17\n"
    "v_mfma_f32_16x16x32_bf16 v[152:155], a[204:207], a[56:59], v[152:155]\n"
    "buffer_load_dwordx4 v12, s[48:51], s90 offen offset:1024 lds\n"
    "ds_read_b64_tr_b16 a[218:219], v4 offset:5184\n"
    "v_mfma_f32_16x16x32_bf16 v[152:155], a[200:203], a[24:27], v[152:155]\n"
    "v_exp_f32 v16, v16\n"
    "v_mfma_f32_16x16x32_bf16 v[148:151], a[200:203], a[20:23], v[148:151]\n"
    "buffer_load_dwordx4 v13, s[48:51], s90 offen offset:2048 lds\n"
    "ds_read_b64_tr_b16 a[220:221], v4 offset:10304\n"
    "v_mfma_f32_16x16x32_bf16 v[148:151], a[204:207], a[52:55], v[148:151]\n"
    "v_rcp_f32 v18, v18\n"
    "v_add_f32 v16, 1.0, v16\n"
    "v_mfma_f32_16x16x32_bf16 v[144:147], a[204:207], a[48:51], v[144:147]\n"
    "ds_read_b64_tr_b16 a[222:223], v4 offset:15424\n"
    "v_mul_f32 v222, v222, v18\n"
    "v_mfma_f32_16x16x32_bf16 v[144:147], a[200:203], a[16:19], v[144:147]\n"
    "s_waitcnt lgkmcnt(6)\n"
    "v_rcp_f32 v16, v16\n"
    "v_mfma_f32_16x16x32_bf16 v[160:163], a[208:211], a[16:19], v[160:163]\n"
    "s_waitcnt lgkmcnt(4)\n"
    "s_nop 0\n"
    "buffer_load_dwordx4 v14, s[48:51], s90 offen offset:3072 lds\n"
    "ds_read_b64_tr_b16 a[224:225], v5 offset:64\n"
    "v_mul_f32 v223, v223, v16\n"
    "v_mfma_f32_16x16x32_bf16 v[160:163], a[212:215], a[48:51], v[160:163]\n"
    "v_cvt_pk_bf16_f32 v208, v208, v209\n"
    "v_cvt_pk_bf16_f32 v209, v210, v211\n"
    "v_cvt_pk_bf16_f32 v210, v216, v217\n"
    "v_mfma_f32_16x16x32_bf16 v[164:167], a[212:215], a[52:55], v[164:167]\n"
    "ds_read_b64_tr_b16 a[226:227], v5 offset:5184\n"
    "v_cvt_pk_bf16_f32 v211, v218, v219\n"
    "v_mfma_f32_16x16x32_bf16 v[164:167], a[208:211], a[20:23], v[164:167]\n"
    "buffer_store_dwordx4 v[208:211], v9, s[52:55], s93 offen offset:0 sc1\n"
    "v_cvt_pk_bf16_f32 v212, v212, v213\n"
    "v_cvt_pk_bf16_f32 v213, v214, v215\n"
    "v_mfma_f32_16x16x32_bf16 v[168:171], a[208:211], a[24:27], v[168:171]\n"
    "ds_read_b64_tr_b16 a[228:229], v5 offset:10304\n"
    "v_cvt_pk_bf16_f32 v214, v220, v221\n"
    "v_cvt_pk_bf16_f32 v215, v222, v223\n"
    "v_mfma_f32_16x16x32_bf16 v[168:171], a[212:215], a[56:59], v[168:171]\n"
    "s_add_u32 m0, s91, 147456\n"
    "buffer_store_dwordx4 v[212:215], v9, s[52:55], s94 offen offset:0 sc1\n"
    "v_mul_f32 v16, v240, v240\n"
    "v_fma_f32 v16, v16, s92, v20\n"
    "v_mfma_f32_16x16x32_bf16 v[172:175], a[212:215], a[60:63], v[172:175]\n"
    "buffer_load_dwordx4 v15, s[48:51], s90 offen offset:0 lds\n"
    "ds_read_b64_tr_b16 a[230:231], v5 offset:15424\n"
    "v_mul_f32 v16, v16, v240\n"
    "v_mfma_f32_16x16x32_bf16 v[172:175], a[208:211], a[28:31], v[172:175]\n"
    "s_waitcnt lgkmcnt(6)\n"
    "v_exp_f32 v16, v16\n"
    "v_mul_f32 v17, v241, v241\n"
    "v_mfma_f32_16x16x32_bf16 v[188:191], a[216:219], a[28:31], v[188:191]\n"
    "s_waitcnt lgkmcnt(4)\n"
    "ds_read_b64_tr_b16 a[232:233], v4 offset:128\n"
    "v_fma_f32 v17, v17, s92, v20\n"
    "v_add_f32 v16, 1.0, v16\n"
    "v_mfma_f32_16x16x32_bf16 v[188:191], a[220:223], a[60:63], v[188:191]\n"
    "v_mul_f32 v17, v17, v241\n"
    "v_exp_f32 v17, v17\n"
    "v_mfma_f32_16x16x32_bf16 v[184:187], a[220:223], a[56:59], v[184:187]\n"
    "s_waitcnt vmcnt(15)\n"
    "v_cmp_ne_u64 vcc, s[80:81], v[62:63]\n"
    "s_nop 1\n"
    "s_cbranch_vccnz L_slow6\n"
    "L_back6:\n"
    "ds_read_b64_tr_b16 a[234:235], v4 offset:5248\n"
    "v_mul_f32 v18, v242, v242\n"
    "v_mfma_f32_16x16x32_bf16 v[184:187], a[216:219], a[24:27], v[184:187]\n"
    "v_rcp_f32 v16, v16\n"
    "v_fma_f32 v18, v18, s92, v20\n"
    "v_mfma_f32_16x16x32_bf16 v[180:183], a[216:219], a[20:23], v[180:183]\n"
    "ds_read_b64_tr_b16 a[236:237], v4 offset:10368\n"
    "v_add_f32 v17, 1.0, v17\n"
    "v_mul_f32 v18, v18, v242\n"
    "v_mfma_f32_16x16x32_bf16 v[180:183], a[220:223], a[52:55], v[180:183]\n"
    "v_mul_f32 v240, v240, v16\n"
    "v_exp_f32 v18, v18\n"
    "v_mfma_f32_16x16x32_bf16 v[176:179], a[220:223], a[48:51], v[176:179]\n"
    "ds_read_b64_tr_b16 a[238:239], v4 offset:15488\n"
    "v_mul_f32 v16, v243, v243\n"
    "v_mfma_f32_16x16x32_bf16 v[176:179], a[216:219], a[16:19], v[176:179]\n"
    "s_waitcnt lgkmcnt(6)\n"
    "s_barrier\n"
    "v_add_u32 v2, 24576, v2\n"
    "v_add_u32 v3, 24576, v3\n"
    "v_mfma_f32_16x16x32_bf16 v[192:195], a[224:227], a[16:19], v[192:195]\n"
    "s_waitcnt lgkmcnt(4)\n"
    "ds_read_b64_tr_b16 a[192:193], v2 offset:0\n"
    "v_rcp_f32 v17, v17\n"
    "v_mfma_f32_16x16x32_bf16 v[192:195], a[228:231], a[48:51], v[192:195]\n"
    "v_add_u32 v4, 24576, v4\n"
    "v_add_u32 v5, 24576, v5\n"
    "v_fma_f32 v16, v16, s92, v20\n"
    "v_mfma_f32_16x16x32_bf16 v[196:199], a[228:231], a[52:55], v[196:199]\n"
    "s_lshl_b32 s95, s70, 6\n"
    "ds_read_b64_tr_b16 a[194:195], v2 offset:5120\n"
    "v_add_f32 v18, 1.0, v18\n"
    "v_mul_f32 v16, v16, v243\n"
    "v_mfma_f32_16x16x32_bf16 v[196:199], a[224:227], a[20:23], v[196:199]\n"
    "s_add_u32 s95, s95, 151552\n"
    "v_mov_b32 v62, s80\n"
    "v_mov_b32 v63, s81\n"
    "v_mul_f32 v241, v241, v17\n"
    "v_mfma_f32_16x16x32_bf16 v[200:203], a[224:227], a[24:27], v[200:203]\n"
    "buffer_store_dwordx2 v[62:63], v10, s[52:55], s95 offen sc1\n"
    "ds_read_b64_tr_b16 a[196:197], v2 offset:10240\n"
    "v_mfma_f32_16x16x32_bf16 v[200:203], a[228:231], a[56:59], v[200:203]\n"
    "v_exp_f32 v16, v16\n"
    "v_mul_f32 v17, v244, v244\n"
    "v_mfma_f32_16x16x32_bf16 v[204:207], a[228:231], a[60:63], v[204:207]\n"
    "ds_read_b64_tr_b16 a[198:199], v2 offset:15360\n"
    "v_rcp_f32 v18, v18\n"
    "v_mfma_f32_16x16x32_bf16 v[204:207], a[224:227], a[28:31], v[204:207]\n"
    "s_waitcnt lgkmcnt(6)\n"
    "v_fma_f32 v17, v17, s92, v20\n"
    "v_add_f32 v16, 1.0, v16\n"
    "v_mul_f32 v17, v17, v244\n"
    "v_mfma_f32_16x16x32_bf16 v[236:239], a[232:235], a[28:31], v[236:239]\n"
    "s_waitcnt lgkmcnt(4)\n"
    "ds_read_b64_tr_b16 a[200:201], v3 offset:0\n"
    "v_mul_f32 v242, v242, v18\n"
    "v_mfma_f32_16x16x32_bf16 v[236:239], a[236:239], a[60:63], v[236:239]\n"
    "v_exp_f32 v17, v17\n"
    "v_mul_f32 v18, v245, v245\n"
    "v_mfma_f32_16x16x32_bf16 v[232:235], a[236:239], a[56:59], v[232:235]\n"
    "ds_read_b64_tr_b16 a[202:203], v3 offset:5120\n"
    "v_rcp_f32 v16, v16\n"
    "v_mfma_f32_16x16x32_bf16 v[232:235], a[232:235], a[24:27], v[232:235]\n"
    "v_fma_f32 v18, v18, s92, v20\n"
    "v_add_f32 v17, 1.0, v17\n"
    "v_mul_f32 v18, v18, v245\n"
    "v_mfma_f32_16x16x32_bf16 v[228:231], a[232:235], a[20:23], v[228:231]\n"
    "ds_read_b64_tr_b16 a[204:205], v3 offset:10240\n"
    "v_mul_f32 v243, v243, v16\n"
    "v_mfma_f32_16x16x32_bf16 v[228:231], a[236:239], a[52:55], v[228:231]\n"
    "v_exp_f32 v18, v18\n"
    "v_mul_f32 v16, v246, v246\n"
    "v_mfma_f32_16x16x32_bf16 v[224:227], a[236:239], a[48:51], v[224:227]\n"
    "ds_read_b64_tr_b16 a[206:207], v3 offset:15360\n"
    "v_rcp_f32 v17, v17\n"
    "v_mfma_f32_16x16x32_bf16 v[224:227], a[232:235], a[16:19], v[224:227]\n"
    "s_waitcnt lgkmcnt(6)\n"
    "v_fma_f32 v16, v16, s92, v20\n"
    "v_add_f32 v18, 1.0, v18\n"
    "v_mul_f32 v16, v16, v246\n"
    "v_mfma_f32_16x16x32_bf16 v[64:67], a[192:195], a[80:83], v[64:67]\n"
    "s_waitcnt lgkmcnt(4)\n"
    "ds_read_b64_tr_b16 a[208:209], v2 offset:64\n"
    "v_mul_f32 v244, v244, v17\n"
    "v_mfma_f32_16x16x32_bf16 v[64:67], a[196:199], a[112:115], v[64:67]\n"
    "v_exp_f32 v16, v16\n"
    "v_mul_f32 v17, v247, v247\n"
    "v_mfma_f32_16x16x32_bf16 v[68:71], a[196:199], a[116:119], v[68:71]\n"
    "s_cmp_lt_u32 s79, 1\n"
    "s_cselect_b32 s88, 1, 0\n"
    "ds_read_b64_tr_b16 a[210:211], v2 offset:5184\n"
    "v_rcp_f32 v18, v18\n"
    "v_mfma_f32_16x16x32_bf16 v[68:71], a[192:195], a[84:87], v[68:71]\n"
    "s_mul_i32 s88, s88, 786432\n"
    "v_fma_f32 v17, v17, s92, v20\n"
    "v_add_f32 v16, 1.0, v16\n"
    "v_mul_f32 v17, v17, v247\n"
    "v_mfma_f32_16x16x32_bf16 v[72:75], a[192:195], a[88:91], v[72:75]\n"
    "s_add_u32 s88, s88, s65\n"
    "ds_read_b64_tr_b16 a[212:213], v2 offset:10304\n"
    "v_mul_f32 v245, v245, v18\n"
    "v_mfma_f32_16x16x32_bf16 v[72:75], a[196:199], a[120:123], v[72:75]\n"
    "s_add_u32 s88, s88, 8192\n"
    "v_exp_f32 v17, v17\n"
    "v_mul_f32 v18, v248, v248\n"
    "v_mfma_f32_16x16x32_bf16 v[76:79], a[196:199], a[124:127], v[76:79]\n"
    "s_add_u32 s89, s88, 4096\n"
    "ds_read_b64_tr_b16 a[214:215], v2 offset:15424\n"
    "v_rcp_f32 v16, v16\n"
    "v_mfma_f32_16x16x32_bf16 v[76:79], a[192:195], a[92:95], v[76:79]\n"
    "s_waitcnt lgkmcnt(6)\n"
    "v_fma_f32 v18, v18, s92, v20\n"
    "v_add_f32 v17, 1.0, v17\n"
    "v_mul_f32 v18, v18, v248\n"
    "v_mfma_f32_16x16x32_bf16 v[92:95], a[200:203], a[92:95], v[92:95]\n"
    "s_waitcnt lgkmcnt(4)\n"
    "buffer_load_dwordx4 a[64:67], v9, s[52:55], s88 offen offset:0 sc1\n"
    "ds_read_b64_tr_b16 a[216:217], v3 offset:64\n"
    "v_mul_f32 v246, v246, v16\n"
    "v_mfma_f32_16x16x32_bf16 v[92:95], a[204:207], a[124:127], v[92:95]\n"
    "v_exp_f32 v18, v18\n"
    "v_mul_f32 v16, v249, v249\n"
    "v_mfma_f32_16x16x32_bf16 v[88:91], a[204:207], a[120:123], v[88:91]\n"
    "buffer_load_dwordx4 a[96:99], v9, s[52:55], s88 offen offset:1024 sc1\n"
    "ds_read_b64_tr_b16 a[218:219], v3 offset:5184\n"
    "v_mfma_f32_16x16x32_bf16 v[88:91], a[200:203], a[88:91], v[88:91]\n"
    "v_rcp_f32 v17, v17\n"
    "v_fma_f32 v16, v16, s92, v20\n"
    "v_mfma_f32_16x16x32_bf16 v[84:87], a[200:203], a[84:87], v[84:87]\n"
    "buffer_load_dwordx4 a[68:71], v9, s[52:55], s88 offen offset:2048 sc1\n"
    "ds_read_b64_tr_b16 a[220:221], v3 offset:10304\n"
    "v_add_f32 v18, 1.0, v18\n"
    "v_mfma_f32_16x16x32_bf16 v[84:87], a[204:207], a[116:119], v[84:87]\n"
    "v_mul_f32 v16, v16, v249\n"
    "v_mul_f32 v247, v247, v17\n"
    "v_mfma_f32_16x16x32_bf16 v[80:83], a[204:207], a[112:115], v[80:83]\n"
    "ds_read_b64_tr_b16 a[222:223], v3 offset:15424\n"
    "v_exp_f32 v16, v16\n"
    "v_mfma_f32_16x16x32_bf16 v[80:83], a[200:203], a[80:83], v[80:83]\n"
    "s_waitcnt lgkmcnt(6)\n"
    "v_mul_f32 v17, v250, v250\n"
    "v_rcp_f32 v18, v18\n"
    "v_mfma_f32_16x16x32_bf16 v[96:99], a[208:211], a[80:83], v[96:99]\n"
    "s_waitcnt lgkmcnt(4)\n"
    "ds_read_b64_tr_b16 a[224:225], v2 offset:128\n"
    "v_fma_f32 v17, v17, s92, v20\n"
    "v_add_f32 v16, 1.0, v16\n"
    "v_mfma_f32_16x16x32_bf16 v[96:99], a[212:215], a[112:115], v[96:99]\n"
    "v_mul_f32 v17, v17, v250\n"
    "v_mul_f32 v248, v248, v18\n"
    "v_mfma_f32_16x16x32_bf16 v[100:103], a[212:215], a[116:119], v[100:103]\n"
    "buffer_load_dwordx4 a[100:103], v9, s[52:55], s88 offen offset:3072 sc1\n"
    "ds_read_b64_tr_b16 a[226:227], v2 offset:5248\n"
    "v_mfma_f32_16x16x32_bf16 v[100:103], a[208:211], a[84:87], v[100:103]\n"
    "v_exp_f32 v17, v17\n"
    "v_mul_f32 v18, v251, v251\n"
    "v_mfma_f32_16x16x32_bf16 v[104:107], a[208:211], a[88:91], v[104:107]\n"
    "ds_read_b64_tr_b16 a[228:229], v2 offset:10368\n"
    "v_rcp_f32 v16, v16\n"
    "v_mfma_f32_16x16x32_bf16 v[104:107], a[212:215], a[120:123], v[104:107]\n"
    "v_fma_f32 v18, v18, s92, v20\n"
    "v_add_f32 v17, 1.0, v17\n"
    "v_mul_f32 v18, v18, v251\n"
    "v_mfma_f32_16x16x32_bf16 v[108:111], a[212:215], a[124:127], v[108:111]\n"
    "buffer_load_dwordx4 a[72:75], v9, s[52:55], s89 offen offset:0 sc1\n"
    "ds_read_b64_tr_b16 a[230:231], v2 offset:15488\n"
    "v_mul_f32 v249, v249, v16\n"
    "v_mfma_f32_16x16x32_bf16 v[108:111], a[208:211], a[92:95], v[108:111]\n"
    "s_waitcnt lgkmcnt(6)\n"
    "v_exp_f32 v18, v18\n"
    "v_mul_f32 v16, v252, v252\n"
    "v_mfma_f32_16x16x32_bf16 v[124:127], a[216:219], a[92:95], v[124:127]\n"
    "s_waitcnt lgkmcnt(4)\n"
    "ds_read_b64_tr_b16 a[232:233], v4 offset:0\n"
    "v_rcp_f32 v17, v17\n"
    "v_mfma_f32_16x16x32_bf16 v[124:127], a[220:223], a[124:127], v[124:127]\n"
    "v_fma_f32 v16, v16, s92, v20\n"
    "v_add_f32 v18, 1.0, v18\n"
    "v_mul_f32 v16, v16, v252\n"
    "v_mfma_f32_16x16x32_bf16 v[120:123], a[220:223], a[120:123], v[120:123]\n"
    "ds_read_b64_tr_b16 a[234:235], v4 offset:5120\n"
    "v_mul_f32 v250, v250, v17\n"
    "v_mfma_f32_16x16x32_bf16 v[120:123], a[216:219], a[88:91], v[120:123]\n"
    "v_exp_f32 v16, v16\n"
    "v_mul_f32 v17, v253, v253\n"
    "v_mfma_f32_16x16x32_bf16 v[116:119], a[216:219], a[84:87], v[116:119]\n"
    "buffer_load_dwordx4 a[104:107], v9, s[52:55], s89 offen offset:1024 sc1\n"
    "ds_read_b64_tr_b16 a[236:237], v4 offset:10240\n"
    "v_mfma_f32_16x16x32_bf16 v[116:119], a[220:223], a[116:119], v[116:119]\n"
    "v_rcp_f32 v18, v18\n"
    "v_fma_f32 v17, v17, s92, v20\n"
    "v_mfma_f32_16x16x32_bf16 v[112:115], a[220:223], a[112:115], v[112:115]\n"
    "ds_read_b64_tr_b16 a[238:239], v4 offset:15360\n"
    "v_add_f32 v16, 1.0, v16\n"
    "v_mul_f32 v17, v17, v253\n"
    "v_mfma_f32_16x16x32_bf16 v[112:115], a[216:219], a[80:83], v[112:115]\n"
    "s_waitcnt lgkmcnt(6)\n"
    "v_mul_f32 v251, v251, v18\n"
    "v_exp_f32 v17, v17\n"
    "v_mfma_f32_16x16x32_bf16 v[128:131], a[224:227], a[80:83], v[128:131]\n"
    "s_waitcnt lgkmcnt(4)\n"
    "ds_read_b64_tr_b16 a[192:193], v5 offset:0\n"
    "v_mul_f32 v18, v254, v254\n"
    "v_mfma_f32_16x16x32_bf16 v[128:131], a[228:231], a[112:115], v[128:131]\n"
    "v_rcp_f32 v16, v16\n"
    "v_fma_f32 v18, v18, s92, v20\n"
    "v_mfma_f32_16x16x32_bf16 v[132:135], a[228:231], a[116:119], v[132:135]\n"
    "buffer_load_dwordx4 a[76:79], v9, s[52:55], s89 offen offset:2048 sc1\n"
    "ds_read_b64_tr_b16 a[194:195], v5 offset:5120\n"
    "v_add_f32 v17, 1.0, v17\n"
    "v_mfma_f32_16x16x32_bf16 v[132:135], a[224:227], a[84:87], v[132:135]\n"
    "v_mul_f32 v18, v18, v254\n"
    "v_mul_f32 v252, v252, v16\n"
    "v_mfma_f32_16x16x32_bf16 v[136:139], a[224:227], a[88:91], v[136:139]\n"
    "ds_read_b64_tr_b16 a[196:197], v5 offset:10240\n"
    "v_exp_f32 v18, v18\n"
    "v_mfma_f32_16x16x32_bf16 v[136:139], a[228:231], a[120:123], v[136:139]\n"
    "v_mul_f32 v16, v255, v255\n"
    "v_rcp_f32 v17, v17\n"
    "v_mfma_f32_16x16x32_bf16 v[140:143], a[228:231], a[124:127], v[140:143]\n"
    "ds_read_b64_tr_b16 a[198:199], v5 offset:15360\n"
    "v_fma_f32 v16, v16, s92, v20\n"
    "v_add_f32 v18, 1.0, v18\n"
    "v_mfma_f32_16x16x32_bf16 v[140:143], a[224:227], a[92:95], v[140:143]\n"
    "s_waitcnt lgkmcnt(6)\n"
    "v_mul_f32 v16, v16, v255\n"
    "v_mul_f32 v253, v253, v17\n"
    "v_mfma_f32_16x16x32_bf16 v[156:159], a[232:235], a[92:95], v[156:159]\n"
    "s_waitcnt lgkmcnt(4)\n"
    "buffer_load_dwordx4 a[108:111], v9, s[52:55], s89 offen offset:3072 sc1\n"
    "ds_read_b64_tr_b16 a[200:201], v4 offset:64\n"
    "v_mfma_f32_16x16x32_bf16 v[156:159], a[236:239], a[124:127], v[156:159]\n"
    "v_exp_f32 v16, v16\n"
    "v_mfma_f32_16x16x32_bf16 v[152:155], a[236:239], a[120:123], v[152:155]\n"
    "ds_read_b64_tr_b16 a[202:203], v4 offset:5184\n"
    "v_rcp_f32 v18, v18\n"
    "v_mfma_f32_16x16x32_bf16 v[152:155], a[232:235], a[88:91], v[152:155]\n"
    "v_add_f32 v16, 1.0, v16\n"
    "v_mul_f32 v254, v254, v18\n"
    "v_mfma_f32_16x16x32_bf16 v[148:151], a[232:235], a[84:87], v[148:151]\n"
    "ds_read_b64_tr_b16 a[204:205], v4 offset:10304\n"
    "v_rcp_f32 v16, v16\n"
    "v_mfma_f32_16x16x32_bf16 v[148:151], a[236:239], a[116:119], v[148:151]\n"
    "s_cmp_lt_u32 s79, 3\n"
    "s_cselect_b32 s90, 3, 2\n"
    "v_mfma_f32_16x16x32_bf16 v[144:147], a[236:239], a[112:115], v[144:147]\n"
    "s_mul_i32 s90, s90, 245760\n"
    "ds_read_b64_tr_b16 a[206:207], v4 offset:15424\n"
    "v_mfma_f32_16x16x32_bf16 v[144:147], a[232:235], a[80:83], v[144:147]\n"
    "s_waitcnt lgkmcnt(6)\n"
    "s_add_u32 s90, s90, 81920\n"
    "v_mfma_f32_16x16x32_bf16 v[160:163], a[192:195], a[80:83], v[160:163]\n"
    "s_waitcnt lgkmcnt(4)\n"
    "s_add_u32 m0, s91, 0\n"
    "ds_read_b64_tr_b16 a[208:209], v5 offset:64\n"
    "v_mfma_f32_16x16x32_bf16 v[160:163], a[196:199], a[112:115], v[160:163]\n"
    "s_nop 0\n"
    "v_mul_f32 v255, v255, v16\n"
    "v_cvt_pk_bf16_f32 v240, v240, v241\n"
    "v_cvt_pk_bf16_f32 v241, v242, v243\n"
    "v_mfma_f32_16x16x32_bf16 v[164:167], a[196:199], a[116:119], v[164:167]\n"
    "buffer_load_dwordx4 v11, s[48:51], s90 offen offset:0 lds\n"
    "ds_read_b64_tr_b16 a[210:211], v5 offset:5184\n"
    "v_cvt_pk_bf16_f32 v242, v248, v249\n"
    "v_mfma_f32_16x16x32_bf16 v[164:167], a[192:195], a[84:87], v[164:167]\n"
    "v_cvt_pk_bf16_f32 v243, v250, v251\n"
    "v_mfma_f32_16x16x32_bf16 v[168:171], a[192:195], a[88:91], v[168:171]\n"
    "buffer_load_dwordx4 v12, s[48:51], s90 offen offset:1024 lds\n"
    "ds_read_b64_tr_b16 a[212:213], v5 offset:10304\n"
    "v_mfma_f32_16x16x32_bf16 v[168:171], a[196:199], a[120:123], v[168:171]\n"
    "buffer_store_dwordx4 v[240:243], v9, s[52:55], s93 offen offset:1024 sc1\n"
    "v_cvt_pk_bf16_f32 v244, v244, v245\n"
    "v_cvt_pk_bf16_f32 v245, v246, v247\n"
    "v_mfma_f32_16x16x32_bf16 v[172:175], a[196:199], a[124:127], v[172:175]\n"
    "ds_read_b64_tr_b16 a[214:215], v5 offset:15424\n"
    "v_cvt_pk_bf16_f32 v246, v252, v253\n"
    "v_cvt_pk_bf16_f32 v247, v254, v255\n"
    "v_mfma_f32_16x16x32_bf16 v[172:175], a[192:195], a[92:95], v[172:175]\n"
    "s_waitcnt lgkmcnt(6)\n"
    "buffer_store_dwordx4 v[244:247], v9, s[52:55], s94 offen offset:1024 sc1\n"
    "v_accvgpr_read_b32 v208, a240\n"
    "v_accvgpr_read_b32 v209, a241\n"
    "v_mfma_f32_16x16x32_bf16 v[188:191], a[200:203], a[92:95], v[188:191]\n"
    "s_waitcnt lgkmcnt(4)\n"
    "buffer_load_dwordx4 v13, s[48:51], s90 offen offset:2048 lds\n"
    "ds_read_b64_tr_b16 a[216:217], v4 offset:128\n"
    "v_accvgpr_read_b32 v210, a242\n"
    "v_mfma_f32_16x16x32_bf16 v[188:191], a[204:207], a[124:127], v[188:191]\n"
    "v_accvgpr_read_b32 v211, a243\n"
    "v_accvgpr_read_b32 v212, a244\n"
    "v_accvgpr_read_b32 v213, a245\n"
    "v_mfma_f32_16x16x32_bf16 v[184:187], a[204:207], a[120:123], v[184:187]\n"
    "ds_read_b64_tr_b16 a[218:219], v4 offset:5248\n"
    "v_accvgpr_read_b32 v214, a246\n"
    "v_accvgpr_read_b32 v215, a247\n"
    "v_mfma_f32_16x16x32_bf16 v[184:187], a[200:203], a[88:91], v[184:187]\n"
    "v_accvgpr_read_b32 v216, a248\n"
    "v_accvgpr_read_b32 v217, a249\n"
    "v_accvgpr_read_b32 v218, a250\n"
    "v_mfma_f32_16x16x32_bf16 v[180:183], a[200:203], a[84:87], v[180:183]\n"
    "buffer_load_dwordx4 v14, s[48:51], s90 offen offset:3072 lds\n"
    "ds_read_b64_tr_b16 a[220:221], v4 offset:10368\n"
    "v_accvgpr_read_b32 v219, a251\n"
    "v_mfma_f32_16x16x32_bf16 v[180:183], a[204:207], a[116:119], v[180:183]\n"
    "v_accvgpr_read_b32 v220, a252\n"
    "v_accvgpr_read_b32 v221, a253\n"
    "v_accvgpr_read_b32 v222, a254\n"
    "v_mfma_f32_16x16x32_bf16 v[176:179], a[204:207], a[112:115], v[176:179]\n"
    "ds_read_b64_tr_b16 a[222:223], v4 offset:15488\n"
    "v_accvgpr_read_b32 v223, a255\n"
    "v_mul_f32 v16, v208, v208\n"
    "v_mfma_f32_16x16x32_bf16 v[176:179], a[200:203], a[80:83], v[176:179]\n"
    "s_waitcnt lgkmcnt(6)\n"
    "s_waitcnt vmcnt(22)\n"
    "s_barrier\n"
    "v_add_u32 v2, 28672, v2\n"
    "v_add_u32 v3, 28672, v3\n"
    "v_fma_f32 v16, v16, s92, v20\n"
    "v_mfma_f32_16x16x32_bf16 v[192:195], a[208:211], a[80:83], v[192:195]\n"
    "s_waitcnt lgkmcnt(4)\n"
    "ds_read_b64_tr_b16 a[224:225], v2 offset:0\n"
    "v_mul_f32 v16, v16, v208\n"
    "v_mfma_f32_16x16x32_bf16 v[192:195], a[212:215], a[112:115], v[192:195]\n"
    "v_add_u32 v4, 28672, v4\n"
    "v_add_u32 v5, 28672, v5\n"
    "v_mfma_f32_16x16x32_bf16 v[196:199], a[212:215], a[116:119], v[196:199]\n"
    "s_add_u32 m0, s91, 4096\n"
    "ds_read_b64_tr_b16 a[226:227], v2 offset:5120\n"
    "v_exp_f32 v16, v16\n"
    "v_mfma_f32_16x16x32_bf16 v[196:199], a[208:211], a[84:87], v[196:199]\n"
    "v_mul_f32 v17, v209, v209\n"
    "v_fma_f32 v17, v17, s92, v20\n"
    "v_add_f32 v16, 1.0, v16\n"
    "v_mfma_f32_16x16x32_bf16 v[200:203], a[208:211], a[88:91], v[200:203]\n"
    "buffer_load_dwordx4 v15, s[48:51], s90 offen offset:0 lds\n"
    "ds_read_b64_tr_b16 a[228:229], v2 offset:10240\n"
    "v_mul_f32 v17, v17, v209\n"
    "v_mfma_f32_16x16x32_bf16 v[200:203], a[212:215], a[120:123], v[200:203]\n"
    "v_exp_f32 v17, v17\n"
    "v_mul_f32 v18, v210, v210\n"
    "v_mfma_f32_16x16x32_bf16 v[204:207], a[212:215], a[124:127], v[204:207]\n"
    "ds_read_b64_tr_b16 a[230:231], v2 offset:15360\n"
    "v_rcp_f32 v16, v16\n"
    "v_mfma_f32_16x16x32_bf16 v[204:207], a[208:211], a[92:95], v[204:207]\n"
    "s_waitcnt lgkmcnt(6)\n"
    "v_lshrrev_b32 v56, 4, v9\n"
    "v_min_u32 v56, 3, v56\n"
    "v_fma_f32 v18, v18, s92, v20\n"
    "v_mfma_f32_16x16x32_bf16 v[236:239], a[216:219], a[92:95], v[236:239]\n"
    "s_waitcnt lgkmcnt(4)\n"
    "ds_read_b64_tr_b16 a[232:233], v3 offset:0\n"
    "v_add_u32 v56, s69, v56\n"
    "v_add_f32 v17, 1.0, v17\n"
    "v_mfma_f32_16x16x32_bf16 v[236:239], a[220:223], a[124:127], v[236:239]\n"
    "v_lshlrev_b32 v56, 6, v56\n"
    "v_add_u32 v56, 151552, v56\n"
    "v_mul_f32 v18, v18, v210\n"
    "v_mfma_f32_16x16x32_bf16 v[232:235], a[220:223], a[120:123], v[232:235]\n"
    "buffer_load_dwordx2 v[62:63], v56, s[52:55], 0 offen sc1\n"
    "ds_read_b64_tr_b16 a[234:235], v3 offset:5120\n"
    "v_mul_f32 v208, v208, v16\n"
    "v_mfma_f32_16x16x32_bf16 v[232:235], a[216:219], a[88:91], v[232:235]\n"
    "v_exp_f32 v18, v18\n"
    "v_mul_f32 v16, v211, v211\n"
    "v_mfma_f32_16x16x32_bf16 v[228:231], a[216:219], a[84:87], v[228:231]\n"
    "ds_read_b64_tr_b16 a[236:237], v3 offset:10240\n"
    "v_rcp_f32 v17, v17\n"
    "v_mfma_f32_16x16x32_bf16 v[228:231], a[220:223], a[116:119], v[228:231]\n"
    "v_fma_f32 v16, v16, s92, v20\n"
    "v_add_f32 v18, 1.0, v18\n"
    "v_mul_f32 v16, v16, v211\n"
    "v_mfma_f32_16x16x32_bf16 v[224:227], a[220:223], a[112:115], v[224:227]\n"
    "ds_read_b64_tr_b16 a[238:239], v3 offset:15360\n"
    "v_mul_f32 v209, v209, v17\n"
    "v_mfma_f32_16x16x32_bf16 v[224:227], a[216:219], a[80:83], v[224:227]\n"
    "s_waitcnt lgkmcnt(6)\n"
    "v_exp_f32 v16, v16\n"
    "v_mul_f32 v17, v212, v212\n"
    "v_mfma_f32_16x16x32_bf16 v[64:67], a[224:227], a[0:3], v[64:67]\n"
    "s_waitcnt lgkmcnt(4)\n"
    "ds_read_b64_tr_b16 a[192:193], v2 offset:64\n"
    "v_rcp_f32 v18, v18\n"
    "v_mfma_f32_16x16x32_bf16 v[64:67], a[228:231], a[32:35], v[64:67]\n"
    "v_fma_f32 v17, v17, s92, v20\n"
    "v_add_f32 v16, 1.0, v16\n"
    "v_mul_f32 v17, v17, v212\n"
    "v_mfma_f32_16x16x32_bf16 v[68:71], a[228:231], a[36:39], v[68:71]\n"
    "s_cmp_lt_u32 s79, 2\n"
    "s_cselect_b32 s88, 2, 1\n"
    "ds_read_b64_tr_b16 a[194:195], v2 offset:5184\n"
    "v_mul_f32 v210, v210, v18\n"
    "v_mfma_f32_16x16x32_bf16 v[68:71], a[224:227], a[4:7], v[68:71]\n"
    "s_mul_i32 s88, s88, 786432\n"
    "v_exp_f32 v17, v17\n"
    "v_mul_f32 v18, v213, v213\n"
    "v_mfma_f32_16x16x32_bf16 v[72:75], a[224:227], a[8:11], v[72:75]\n"
    "s_add_u32 s88, s88, s65\n"
    "ds_read_b64_tr_b16 a[196:197], v2 offset:10304\n"
    "v_rcp_f32 v16, v16\n"
    "v_mfma_f32_16x16x32_bf16 v[72:75], a[228:231], a[40:43], v[72:75]\n"
    "s_add_u32 s88, s88, 8192\n"
    "v_fma_f32 v18, v18, s92, v20\n"
    "v_add_f32 v17, 1.0, v17\n"
    "v_mul_f32 v18, v18, v213\n"
    "v_mfma_f32_16x16x32_bf16 v[76:79], a[228:231], a[44:47], v[76:79]\n"
    "s_add_u32 s89, s88, 4096\n"
    "ds_read_b64_tr_b16 a[198:199], v2 offset:15424\n"
    "v_mul_f32 v211, v211, v16\n"
    "v_mfma_f32_16x16x32_bf16 v[76:79], a[224:227], a[12:15], v[76:79]\n"
    "s_waitcnt lgkmcnt(6)\n"
    "v_exp_f32 v18, v18\n"
    "v_mul_f32 v16, v214, v214\n"
    "v_mfma_f32_16x16x32_bf16 v[92:95], a[232:235], a[12:15], v[92:95]\n"
    "s_waitcnt lgkmcnt(4)\n"
    "buffer_load_dwordx4 a[16:19], v9, s[52:55], s88 offen offset:0 sc1\n"
    "ds_read_b64_tr_b16 a[200:201], v3 offset:64\n"
    "v_mfma_f32_16x16x32_bf16 v[92:95], a[236:239], a[44:47], v[92:95]\n"
    "v_rcp_f32 v17, v17\n"
    "v_fma_f32 v16, v16, s92, v20\n"
    "v_mfma_f32_16x16x32_bf16 v[88:91], a[236:239], a[40:43], v[88:91]\n"
    "buffer_load_dwordx4 a[48:51], v9, s[52:55], s88 offen offset:1024 sc1\n"
    "ds_read_b64_tr_b16 a[202:203], v3 offset:5184\n"
    "v_add_f32 v18, 1.0, v18\n"
    "v_mfma_f32_16x16x32_bf16 v[88:91], a[232:235], a[8:11], v[88:91]\n"
    "v_mul_f32 v16, v16, v214\n"
    "v_mul_f32 v212, v212, v17\n"
    "v_mfma_f32_16x16x32_bf16 v[84:87], a[232:235], a[4:7], v[84:87]\n"
    "buffer_load_dwordx4 a[20:23], v9, s[52:55], s88 offen offset:2048 sc1\n"
    "ds_read_b64_tr_b16 a[204:205], v3 offset:10304\n"
    "v_mfma_f32_16x16x32_bf16 v[84:87], a[236:239], a[36:39], v[84:87]\n"
    "v_exp_f32 v16, v16\n"
    "v_mul_f32 v17, v215, v215\n"
    "v_mfma_f32_16x16x32_bf16 v[80:83], a[236:239], a[32:35], v[80:83]\n"
    "ds_read_b64_tr_b16 a[206:207], v3 offset:15424\n"
    "v_rcp_f32 v18, v18\n"
    "v_mfma_f32_16x16x32_bf16 v[80:83], a[232:235], a[0:3], v[80:83]\n"
    "s_waitcnt lgkmcnt(6)\n"
    "v_fma_f32 v17, v17, s92, v20\n"
    "v_add_f32 v16, 1.0, v16\n"
    "v_mul_f32 v17, v17, v215\n"
    "v_mfma_f32_16x16x32_bf16 v[96:99], a[192:195], a[0:3], v[96:99]\n"
    "s_waitcnt lgkmcnt(4)\n"
    "ds_read_b64_tr_b16 a[208:209], v2 offset:128\n"
    "v_mul_f32 v213, v213, v18\n"
    "v_mfma_f32_16x16x32_bf16 v[96:99], a[196:199], a[32:35], v[96:99]\n"
    "v_exp_f32 v17, v17\n"
    "v_mul_f32 v18, v216, v216\n"
    "v_mfma_f32_16x16x32_bf16 v[100:103], a[196:199], a[36:39], v[100:103]\n"
    "buffer_load_dwordx4 a[52:55], v9, s[52:55], s88 offen offset:3072 sc1\n"
    "ds_read_b64_tr_b16 a[210:211], v2 offset:5248\n"
    "v_mfma_f32_16x16x32_bf16 v[100:103], a[192:195], a[4:7], v[100:103]\n"
    "v_rcp_f32 v16, v16\n"
    "v_fma_f32 v18, v18, s92, v20\n"
    "v_mfma_f32_16x16x32_bf16 v[104:107], a[192:195], a[8:11], v[104:107]\n"
    "ds_read_b64_tr_b16 a[212:213], v2 offset:10368\n"
    "v_add_f32 v17, 1.0, v17\n"
    "v_mul_f32 v18, v18, v216\n"
    "v_mfma_f32_16x16x32_bf16 v[104:107], a[196:199], a[40:43], v[104:107]\n"
    "v_mul_f32 v214, v214, v16\n"
    "v_exp_f32 v18, v18\n"
    "v_mfma_f32_16x16x32_bf16 v[108:111], a[196:199], a[44:47], v[108:111]\n"
    "buffer_load_dwordx4 a[24:27], v9, s[52:55], s89 offen offset:0 sc1\n"
    "ds_read_b64_tr_b16 a[214:215], v2 offset:15488\n"
    "v_mul_f32 v16, v217, v217\n"
    "v_mfma_f32_16x16x32_bf16 v[108:111], a[192:195], a[12:15], v[108:111]\n"
    "s_waitcnt lgkmcnt(6)\n"
    "v_rcp_f32 v17, v17\n"
    "v_fma_f32 v16, v16, s92, v20\n"
    "v_mfma_f32_16x16x32_bf16 v[124:127], a[200:203], a[12:15], v[124:127]\n"
    "s_waitcnt lgkmcnt(4)\n"
    "ds_read_b64_tr_b16 a[216:217], v4 offset:0\n"
    "v_add_f32 v18, 1.0, v18\n"
    "v_mul_f32 v16, v16, v217\n"
    "v_mfma_f32_16x16x32_bf16 v[124:127], a[204:207], a[44:47], v[124:127]\n"
    "v_mul_f32 v215, v215, v17\n"
    "v_exp_f32 v16, v16\n"
    "v_mfma_f32_16x16x32_bf16 v[120:123], a[204:207], a[40:43], v[120:123]\n"
    "ds_read_b64_tr_b16 a[218:219], v4 offset:5120\n"
    "v_mul_f32 v17, v218, v218\n"
    "v_mfma_f32_16x16x32_bf16 v[120:123], a[200:203], a[8:11], v[120:123]\n"
    "v_rcp_f32 v18, v18\n"
    "v_fma_f32 v17, v17, s92, v20\n"
    "v_mfma_f32_16x16x32_bf16 v[116:119], a[200:203], a[4:7], v[116:119]\n"
    "buffer_load_dwordx4 a[56:59], v9, s[52:55], s89 offen offset:1024 sc1\n"
    "ds_read_b64_tr_b16 a[220:221], v4 offset:10240\n"
    "v_add_f32 v16, 1.0, v16\n"
    "v_mfma_f32_16x16x32_bf16 v[116:119], a[204:207], a[36:39], v[116:119]\n"
    "v_mul_f32 v17, v17, v218\n"
    "v_mul_f32 v216, v216, v18\n"
    "v_mfma_f32_16x16x32_bf16 v[112:115], a[204:207], a[32:35], v[112:115]\n"
    "ds_read_b64_tr_b16 a[222:223], v4 offset:15360\n"
    "v_exp_f32 v17, v17\n"
    "v_mfma_f32_16x16x32_bf16 v[112:115], a[200:203], a[0:3], v[112:115]\n"
    "s_waitcnt lgkmcnt(6)\n"
    "v_mul_f32 v18, v219, v219\n"
    "v_rcp_f32 v16, v16\n"
    "v_mfma_f32_16x16x32_bf16 v[128:131], a[208:211], a[0:3], v[128:131]\n"
    "s_waitcnt lgkmcnt(4)\n"
    "ds_read_b64_tr_b16 a[224:225], v5 offset:0\n"
    "v_fma_f32 v18, v18, s92, v20\n"
    "v_add_f32 v17, 1.0, v17\n"
    "v_mfma_f32_16x16x32_bf16 v[128:131], a[212:215], a[32:35], v[128:131]\n"
    "v_mul_f32 v18, v18, v219\n"
    "v_mul_f32 v217, v217, v16\n"
    "v_mfma_f32_16x16x32_bf16 v[132:135], a[212:215], a[36:39], v[132:135]\n"
    "buffer_load_dwordx4 a[28:31], v9, s[52:55], s89 offen offset:2048 sc1\n"
    "ds_read_b64_tr_b16 a[226:227], v5 offset:5120\n"
    "v_mfma_f32_16x16x32_bf16 v[132:135], a[208:211], a[4:7], v[132:135]\n"
    "v_exp_f32 v18, v18\n"
    "v_mul_f32 v16, v220, v220\n"
    "v_mfma_f32_16x16x32_bf16 v[136:139], a[208:211], a[8:11], v[136:139]\n"
    "ds_read_b64_tr_b16 a[228:229], v5 offset:10240\n"
    "v_rcp_f32 v17, v17\n"
    "v_mfma_f32_16x16x32_bf16 v[136:139], a[212:215], a[40:43], v[136:139]\n"
    "v_fma_f32 v16, v16, s92, v20\n"
    "v_add_f32 v18, 1.0, v18\n"
    "v_mul_f32 v16, v16, v220\n"
    "v_mfma_f32_16x16x32_bf16 v[140:143], a[212:215], a[44:47], v[140:143]\n"
    "ds_read_b64_tr_b16 a[230:231], v5 offset:15360\n"
    "v_mul_f32 v218, v218, v17\n"
    "v_mfma_f32_16x16x32_bf16 v[140:143], a[208:211], a[12:15], v[140:143]\n"
    "s_waitcnt lgkmcnt(6)\n"
    "v_exp_f32 v16, v16\n"
    "v_mul_f32 v17, v221, v221\n"
    "v_mfma_f32_16x16x32_bf16 v[156:159], a[216:219], a[12:15], v[156:159]\n"
    "s_waitcnt lgkmcnt(4)\n"
    "buffer_load_dwordx4 a[60:63], v9, s[52:55], s89 offen offset:3072 sc1\n"
    "ds_read_b64_tr_b16 a[232:233], v4 offset:64\n"
    "v_mfma_f32_16x16x32_bf16 v[156:159], a[220:223], a[44:47], v[156:159]\n"
    "v_rcp_f32 v18, v18\n"
    "v_fma_f32 v17, v17, s92, v20\n"
    "v_mfma_f32_16x16x32_bf16 v[152:155], a[220:223], a[40:43], v[152:155]\n"
    "ds_read_b64_tr_b16 a[234:235], v4 offset:5184\n"
    "v_add_f32 v16, 1.0, v16\n"
    "v_mul_f32 v17, v17, v221\n"
    "v_mfma_f32_16x16x32_bf16 v[152:155], a[216:219], a[8:11], v[152:155]\n"
    "v_mul_f32 v219, v219, v18\n"
    "v_exp_f32 v17, v17\n"
    "v_mfma_f32_16x16x32_bf16 v[148:151], a[216:219], a[4:7], v[148:151]\n"
    "s_mov_b32 s90, s79\n"
    "ds_read_b64_tr_b16 a[236:237], v4 offset:10304\n"
    "v_mul_f32 v18, v222, v222\n"
    "v_mfma_f32_16x16x32_bf16 v[148:151], a[220:223], a[36:39], v[148:151]\n"
    "s_mul_i32 s90, s90, 245760\n"
    "v_rcp_f32 v16, v16\n"
    "v_fma_f32 v18, v18, s92, v20\n"
    "v_mfma_f32_16x16x32_bf16 v[144:147], a[220:223], a[32:35], v[144:147]\n"
    "s_add_u32 s90, s90, 163840\n"
    "ds_read_b64_tr_b16 a[238:239], v4 offset:15424\n"
    "v_add_f32 v17, 1.0, v17\n"
    "v_mul_f32 v18, v18, v222\n"
    "v_mfma_f32_16x16x32_bf16 v[144:147], a[216:219], a[0:3], v[144:147]\n"
    "s_waitcnt lgkmcnt(6)\n"
    "s_add_u32 m0, s91, 20480\n"
    "v_mul_f32 v220, v220, v16\n"
    "v_exp_f32 v18, v18\n"
    "v_mfma_f32_16x16x32_bf16 v[160:163], a[224:227], a[0:3], v[160:163]\n"
    "s_waitcnt lgkmcnt(4)\n"
    "buffer_load_dwordx4 v11, s[48:51], s90 offen offset:0 lds\n"
    "ds_read_b64_tr_b16 a[192:193], v5 offset:64\n"
    "v_mul_f32 v16, v223, v223\n"
    "v_mfma_f32_16x16x32_bf16 v[160:163], a[228:231], a[32:35], v[160:163]\n"
    "v_rcp_f32 v17, v17\n"
    "v_fma_f32 v16, v16, s92, v20\n"
    "v_mfma_f32_16x16x32_bf16 v[164:167], a[228:231], a[36:39], v[164:167]\n"
    "buffer_load_dwordx4 v12, s[48:51], s90 offen offset:1024 lds\n"
    "ds_read_b64_tr_b16 a[194:195], v5 offset:5184\n"
    "v_add_f32 v18, 1.0, v18\n"
    "v_mfma_f32_16x16x32_bf16 v[164:167], a[224:227], a[4:7], v[164:167]\n"
    "v_mul_f32 v16, v16, v223\n"
    "v_mul_f32 v221, v221, v17\n"
    "v_mfma_f32_16x16x32_bf16 v[168:171], a[224:227], a[8:11], v[168:171]\n"
    "ds_read_b64_tr_b16 a[196:197], v5 offset:10304\n"
    "v_exp_f32 v16, v16\n"
    "v_mfma_f32_16x16x32_bf16 v[168:171], a[228:231], a[40:43], v[168:171]\n"
    "v_rcp_f32 v18, v18\n"
    "v_add_f32 v16, 1.0, v16\n"
    "v_mfma_f32_16x16x32_bf16 v[172:175], a[228:231], a[44:47], v[172:175]\n"
    "ds_read_b64_tr_b16 a[198:199], v5 offset:15424\n"
    "v_mul_f32 v222, v222, v18\n"
    "v_mfma_f32_16x16x32_bf16 v[172:175], a[224:227], a[12:15], v[172:175]\n"
    "s_waitcnt lgkmcnt(6)\n"
    "v_rcp_f32 v16, v16\n"
    "v_mfma_f32_16x16x32_bf16 v[188:191], a[232:235], a[12:15], v[188:191]\n"
    "s_waitcnt lgkmcnt(4)\n"
    "s_nop 0\n"
    "buffer_load_dwordx4 v13, s[48:51], s90 offen offset:2048 lds\n"
    "ds_read_b64_tr_b16 a[200:201], v4 offset:128\n"
    "v_mul_f32 v223, v223, v16\n"
    "v_mfma_f32_16x16x32_bf16 v[188:191], a[236:239], a[44:47], v[188:191]\n"
    "v_cvt_pk_bf16_f32 v208, v208, v209\n"
    "v_cvt_pk_bf16_f32 v209, v210, v211\n"
    "v_cvt_pk_bf16_f32 v210, v216, v217\n"
    "v_mfma_f32_16x16x32_bf16 v[184:187], a[236:239], a[40:43], v[184:187]\n"
    "ds_read_b64_tr_b16 a[202:203], v4 offset:5248\n"
    "v_cvt_pk_bf16_f32 v211, v218, v219\n"
    "v_mfma_f32_16x16x32_bf16 v[184:187], a[232:235], a[8:11], v[184:187]\n"
    "buffer_store_dwordx4 v[208:211], v9, s[52:55], s93 offen offset:2048 sc1\n"
    "v_cvt_pk_bf16_f32 v212, v212, v213\n"
    "v_cvt_pk_bf16_f32 v213, v214, v215\n"
    "v_mfma_f32_16x16x32_bf16 v[180:183], a[232:235], a[4:7], v[180:183]\n"
    "buffer_load_dwordx4 v14, s[48:51], s90 offen offset:3072 lds\n"
    "ds_read_b64_tr_b16 a[204:205], v4 offset:10368\n"
    "v_cvt_pk_bf16_f32 v214, v220, v221\n"
    "v_mfma_f32_16x16x32_bf16 v[180:183], a[236:239], a[36:39], v[180:183]\n"
    "v_cvt_pk_bf16_f32 v215, v222, v223\n"
    "v_mfma_f32_16x16x32_bf16 v[176:179], a[236:239], a[32:35], v[176:179]\n"
    "buffer_store_dwordx4 v[212:215], v9, s[52:55], s94 offen offset:2048 sc1\n"
    "ds_read_b64_tr_b16 a[206:207], v4 offset:15488\n"
    "v_mfma_f32_16x16x32_bf16 v[176:179], a[232:235], a[0:3], v[176:179]\n"
    "s_waitcnt lgkmcnt(6)\n"
    "s_waitcnt vmcnt(22)\n"
    "s_barrier\n"
    "v_add_u32 v2, 20480, v2\n"
    "v_add_u32 v3, 20480, v3\n"
    "v_mfma_f32_16x16x32_bf16 v[192:195], a[192:195], a[0:3], v[192:195]\n"
    "s_waitcnt lgkmcnt(4)\n"
    "ds_read_b64_tr_b16 a[208:209], v2 offset:0\n"
    "v_mfma_f32_16x16x32_bf16 v[192:195], a[196:199], a[32:35], v[192:195]\n"
    "v_add_u32 v4, 20480, v4\n"
    "v_add_u32 v5, 20480, v5\n"
    "v_mfma_f32_16x16x32_bf16 v[196:199], a[196:199], a[36:39], v[196:199]\n"
    "s_add_u32 m0, s91, 24576\n"
    "ds_read_b64_tr_b16 a[210:211], v2 offset:5120\n"
    "v_mfma_f32_16x16x32_bf16 v[196:199], a[192:195], a[4:7], v[196:199]\n"
    "v_mfma_f32_16x16x32_bf16 v[200:203], a[192:195], a[8:11], v[200:203]\n"
    "buffer_load_dwordx4 v15, s[48:51], s90 offen offset:0 lds\n"
    "ds_read_b64_tr_b16 a[212:213], v2 offset:10240\n"
    "v_mfma_f32_16x16x32_bf16 v[200:203], a[196:199], a[40:43], v[200:203]\n"
    "v_mfma_f32_16x16x32_bf16 v[204:207], a[196:199], a[44:47], v[204:207]\n"
    "ds_read_b64_tr_b16 a[214:215], v2 offset:15360\n"
    "v_mfma_f32_16x16x32_bf16 v[204:207], a[192:195], a[12:15], v[204:207]\n"
    "s_waitcnt lgkmcnt(6)\n"
    "v_mfma_f32_16x16x32_bf16 v[236:239], a[200:203], a[12:15], v[236:239]\n"
    "s_waitcnt lgkmcnt(4)\n"
    "s_waitcnt vmcnt(15)\n"
    "v_cmp_ne_u64 vcc, s[80:81], v[62:63]\n"
    "s_nop 1\n"
    "s_cbranch_vccnz L_slow7\n"
    "L_back7:\n"
    "ds_read_b64_tr_b16 a[216:217], v3 offset:0\n"
    "v_mfma_f32_16x16x32_bf16 v[236:239], a[204:207], a[44:47], v[236:239]\n"
    "v_mfma_f32_16x16x32_bf16 v[232:235], a[204:207], a[40:43], v[232:235]\n"
    "ds_read_b64_tr_b16 a[218:219], v3 offset:5120\n"
    "v_mfma_f32_16x16x32_bf16 v[232:235], a[200:203], a[8:11], v[232:235]\n"
    "v_mfma_f32_16x16x32_bf16 v[228:231], a[200:203], a[4:7], v[228:231]\n"
    "ds_read_b64_tr_b16 a[220:221], v3 offset:10240\n"
    "v_mfma_f32_16x16x32_bf16 v[228:231], a[204:207], a[36:39], v[228:231]\n"
    "v_mfma_f32_16x16x32_bf16 v[224:227], a[204:207], a[32:35], v[224:227]\n"
    "ds_read_b64_tr_b16 a[222:223], v3 offset:15360\n"
    "v_mfma_f32_16x16x32_bf16 v[224:227], a[200:203], a[0:3], v[224:227]\n"
    "s_waitcnt lgkmcnt(6)\n"
    "v_mfma_f32_16x16x32_bf16 v[64:67], a[208:211], a[64:67], v[64:67]\n"
    "s_waitcnt lgkmcnt(4)\n"
    "ds_read_b64_tr_b16 a[224:225], v2 offset:64\n"
    "v_mfma_f32_16x16x32_bf16 v[64:67], a[212:215], a[96:99], v[64:67]\n"
    "v_mfma_f32_16x16x32_bf16 v[68:71], a[212:215], a[100:103], v[68:71]\n"
    "s_getpc_b64 s[84:85]\n"
    "L_pc2:\n"
    "s_add_u32 s84, s84, L_g4-L_pc2\n"
    "s_addc_u32 s85, s85, 0\n"
    "s_mov_b32 s86, L_end-L_g4\n"
    "s_mov_b32 s87, 0x20000\n"
    "s_lshr_b32 s95, %1, 3\n"
    "s_lshl_b32 s95, s95, 7\n"
    "ds_read_b64_tr_b16 a[226:227], v2 offset:5184\n"
    "v_lshl_add_u32 v61, %2, 12, s95\n"
    "v_mfma_f32_16x16x32_bf16 v[68:71], a[208:211], a[68:71], v[68:71]\n"
    "buffer_load_dword v8, v61, s[84:87], 0 offen\n"
    "v_mfma_f32_16x16x32_bf16 v[72:75], a[208:211], a[72:75], v[72:75]\n"
    "ds_read_b64_tr_b16 a[228:229], v2 offset:10304\n"
    "v_mfma_f32_16x16x32_bf16 v[72:75], a[212:215], a[104:107], v[72:75]\n"
    "v_mfma_f32_16x16x32_bf16 v[76:79], a[212:215], a[108:111], v[76:79]\n"
    "s_cmp_lt_u32 s79, 3\n"
    "s_cselect_b32 s88, 3, 2\n"
    "ds_read_b64_tr_b16 a[230:231], v2 offset:15424\n"
    "v_mfma_f32_16x16x32_bf16 v[76:79], a[208:211], a[76:79], v[76:79]\n"
    "s_waitcnt lgkmcnt(6)\n"
    "s_mul_i32 s88, s88, 786432\n"
    "v_mfma_f32_16x16x32_bf16 v[92:95], a[216:219], a[76:79], v[92:95]\n"
    "s_waitcnt lgkmcnt(4)\n"
    "s_add_u32 s88, s88, s65\n"
    "ds_read_b64_tr_b16 a[232:233], v3 offset:64\n"
    "v_mfma_f32_16x16x32_bf16 v[92:95], a[220:223], a[108:111], v[92:95]\n"
    "s_add_u32 s88, s88, 8192\n"
    "v_mfma_f32_16x16x32_bf16 v[88:91], a[220:223], a[104:107], v[88:91]\n"
    "s_add_u32 s89, s88, 4096\n"
    "ds_read_b64_tr_b16 a[234:235], v3 offset:5184\n"
    "v_mfma_f32_16x16x32_bf16 v[88:91], a[216:219], a[72:75], v[88:91]\n"
    "buffer_load_dwordx4 a[80:83], v9, s[52:55], s88 offen offset:0 sc1\n"
    "v_mfma_f32_16x16x32_bf16 v[84:87], a[216:219], a[68:71], v[84:87]\n"
    "buffer_load_dwordx4 a[112:115], v9, s[52:55], s88 offen offset:1024 sc1\n"
    "ds_read_b64_tr_b16 a[236:237], v3 offset:10304\n"
    "v_mfma_f32_16x16x32_bf16 v[84:87], a[220:223], a[100:103], v[84:87]\n"
    "buffer_load_dwordx4 a[84:87], v9, s[52:55], s88 offen offset:2048 sc1\n"
    "v_mfma_f32_16x16x32_bf16 v[80:83], a[220:223], a[96:99], v[80:83]\n"
    "ds_read_b64_tr_b16 a[238:239], v3 offset:15424\n"
    "v_mfma_f32_16x16x32_bf16 v[80:83], a[216:219], a[64:67], v[80:83]\n"
    "s_waitcnt lgkmcnt(6)\n"
    "v_mfma_f32_16x16x32_bf16 v[96:99], a[224:227], a[64:67], v[96:99]\n"
    "s_waitcnt lgkmcnt(4)\n"
    "ds_read_b64_tr_b16 a[192:193], v2 offset:128\n"
    "v_mfma_f32_16x16x32_bf16 v[96:99], a[228:231], a[96:99], v[96:99]\n"
    "buffer_load_dwordx4 a[116:119], v9, s[52:55], s88 offen offset:3072 sc1\n"
    "v_mfma_f32_16x16x32_bf16 v[100:103], a[228:231], a[100:103], v[100:103]\n"
    "ds_read_b64_tr_b16 a[194:195], v2 offset:5248\n"
    "v_mfma_f32_16x16x32_bf16 v[100:103], a[224:227], a[68:71], v[100:103]\n"
    "v_mfma_f32_16x16x32_bf16 v[104:107], a[224:227], a[72:75], v[104:107]\n"
    "ds_read_b64_tr_b16 a[196:197], v2 offset:10368\n"
    "v_mfma_f32_16x16x32_bf16 v[104:107], a[228:231], a[104:107], v[104:107]\n"
    "v_mfma_f32_16x16x32_bf16 v[108:111], a[228:231], a[108:111], v[108:111]\n"
    "buffer_load_dwordx4 a[88:91], v9, s[52:55], s89 offen offset:0 sc1\n"
    "ds_read_b64_tr_b16 a[198:199], v2 offset:15488\n"
    "v_mfma_f32_16x16x32_bf16 v[108:111], a[224:227], a[76:79], v[108:111]\n"
    "s_waitcnt lgkmcnt(6)\n"
    "v_mfma_f32_16x16x32_bf16 v[124:127], a[232:235], a[76:79], v[124:127]\n"
    "s_waitcnt lgkmcnt(4)\n"
    "ds_read_b64_tr_b16 a[200:201], v4 offset:0\n"
    "v_mfma_f32_16x16x32_bf16 v[124:127], a[236:239], a[108:111], v[124:127]\n"
    "v_mfma_f32_16x16x32_bf16 v[120:123], a[236:239], a[104:107], v[120:123]\n"
    "buffer_load_dwordx4 a[120:123], v9, s[52:55], s89 offen offset:1024 sc1\n"
    "ds_read_b64_tr_b16 a[202:203], v4 offset:5120\n"
    "v_mfma_f32_16x16x32_bf16 v[120:123], a[232:235], a[72:75], v[120:123]\n"
    "v_mfma_f32_16x16x32_bf16 v[116:119], a[232:235], a[68:71], v[116:119]\n"
    "ds_read_b64_tr_b16 a[204:205], v4 offset:10240\n"
    "v_mfma_f32_16x16x32_bf16 v[116:119], a[236:239], a[100:103], v[116:119]\n"
    "v_mfma_f32_16x16x32_bf16 v[112:115], a[236:239], a[96:99], v[112:115]\n"
    "buffer_load_dwordx4 a[92:95], v9, s[52:55], s89 offen offset:2048 sc1\n"
    "ds_read_b64_tr_b16 a[206:207], v4 offset:15360\n"
    "v_mfma_f32_16x16x32_bf16 v[112:115], a[232:235], a[64:67], v[112:115]\n"
    "s_waitcnt lgkmcnt(6)\n"
    "v_mfma_f32_16x16x32_bf16 v[128:131], a[192:195], a[64:67], v[128:131]\n"
    "s_waitcnt lgkmcnt(4)\n"
    "ds_read_b64_tr_b16 a[208:209], v5 offset:0\n"
    "v_mfma_f32_16x16x32_bf16 v[128:131], a[196:199], a[96:99], v[128:131]\n"
    "v_mfma_f32_16x16x32_bf16 v[132:135], a[196:199], a[100:103], v[132:135]\n"
    "buffer_load_dwordx4 a[124:127], v9, s[52:55], s89 offen offset:3072 sc1\n"
    "ds_read_b64_tr_b16 a[210:211], v5 offset:5120\n"
    "v_mfma_f32_16x16x32_bf16 v[132:135], a[192:195], a[68:71], v[132:135]\n"
    "v_mfma_f32_16x16x32_bf16 v[136:139], a[192:195], a[72:75], v[136:139]\n"
    "ds_read_b64_tr_b16 a[212:213], v5 offset:10240\n"
    "v_mfma_f32_16x16x32_bf16 v[136:139], a[196:199], a[104:107], v[136:139]\n"
    "v_mfma_f32_16x16x32_bf16 v[140:143], a[196:199], a[108:111], v[140:143]\n"
    "ds_read_b64_tr_b16 a[214:215], v5 offset:15360\n"
    "v_mfma_f32_16x16x32_bf16 v[140:143], a[192:195], a[76:79], v[140:143]\n"
    "s_waitcnt lgkmcnt(6)\n"
    "s_cmp_lt_u32 s79, 1\n"
    "s_cselect_b32 s90, 1, 0\n"
    "v_mfma_f32_16x16x32_bf16 v[156:159], a[200:203], a[76:79], v[156:159]\n"
    "s_waitcnt lgkmcnt(4)\n"
    "s_mul_i32 s90, s90, 245760\n"
    "ds_read_b64_tr_b16 a[216:217], v4 offset:64\n"
    "v_mfma_f32_16x16x32_bf16 v[156:159], a[204:207], a[108:111], v[156:159]\n"
    "s_add_u32 s90, s90, 163840\n"
    "v_mfma_f32_16x16x32_bf16 v[152:155], a[204:207], a[104:107], v[152:155]\n"
    "s_add_u32 m0, s91, 40960\n"
    "ds_read_b64_tr_b16 a[218:219], v4 offset:5184\n"
    "v_mfma_f32_16x16x32_bf16 v[152:155], a[200:203], a[72:75], v[152:155]\n"
    "buffer_load_dwordx4 v11, s[48:51], s90 offen offset:0 lds\n"
    "v_mfma_f32_16x16x32_bf16 v[148:151], a[200:203], a[68:71], v[148:151]\n"
    "buffer_load_dwordx4 v12, s[48:51], s90 offen offset:1024 lds\n"
    "ds_read_b64_tr_b16 a[220:221], v4 offset:10304\n"
    "v_mfma_f32_16x16x32_bf16 v[148:151], a[204:207], a[100:103], v[148:151]\n"
    "v_mfma_f32_16x16x32_bf16 v[144:147], a[204:207], a[96:99], v[144:147]\n"
    "ds_read_b64_tr_b16 a[222:223], v4 offset:15424\n"
    "v_mfma_f32_16x16x32_bf16 v[144:147], a[200:203], a[64:67], v[144:147]\n"
    "s_waitcnt lgkmcnt(6)\n"
    "buffer_load_dwordx4 v13, s[48:51], s90 offen offset:2048 lds\n"
    "v_mfma_f32_16x16x32_bf16 v[160:163], a[208:211], a[64:67], v[160:163]\n"
    "s_waitcnt lgkmcnt(4)\n"
    "ds_read_b64_tr_b16 a[224:225], v5 offset:64\n"
    "v_mfma_f32_16x16x32_bf16 v[160:163], a[212:215], a[96:99], v[160:163]\n"
    "v_mfma_f32_16x16x32_bf16 v[164:167], a[212:215], a[100:103], v[164:167]\n"
    "ds_read_b64_tr_b16 a[226:227], v5 offset:5184\n"
    "v_mfma_f32_16x16x32_bf16 v[164:167], a[208:211], a[68:71], v[164:167]\n"
    "buffer_load_dwordx4 v14, s[48:51], s90 offen offset:3072 lds\n"
    "v_mfma_f32_16x16x32_bf16 v[168:171], a[208:211], a[72:75], v[168:171]\n"
    "ds_read_b64_tr_b16 a[228:229], v5 offset:10304\n"
    "v_mfma_f32_16x16x32_bf16 v[168:171], a[212:215], a[104:107], v[168:171]\n"
    "v_mfma_f32_16x16x32_bf16 v[172:175], a[212:215], a[108:111], v[172:175]\n"
    "ds_read_b64_tr_b16 a[230:231], v5 offset:15424\n"
    "v_mfma_f32_16x16x32_bf16 v[172:175], a[208:211], a[76:79], v[172:175]\n"
    "s_waitcnt lgkmcnt(6)\n"
    "s_add_u32 m0, s91, 45056\n"
    "v_mfma_f32_16x16x32_bf16 v[188:191], a[216:219], a[76:79], v[188:191]\n"
    "s_waitcnt lgkmcnt(4)\n"
    "buffer_load_dwordx4 v15, s[48:51], s90 offen offset:0 lds\n"
    "ds_read_b64_tr_b16 a[232:233], v4 offset:128\n"
    "v_mfma_f32_16x16x32_bf16 v[188:191], a[220:223], a[108:111], v[188:191]\n"
    "v_mfma_f32_16x16x32_bf16 v[184:187], a[220:223], a[104:107], v[184:187]\n"
    "ds_read_b64_tr_b16 a[234:235], v4 offset:5248\n"
    "v_mfma_f32_16x16x32_bf16 v[184:187], a[216:219], a[72:75], v[184:187]\n"
    "v_mfma_f32_16x16x32_bf16 v[180:183], a[216:219], a[68:71], v[180:183]\n"
    "ds_read_b64_tr_b16 a[236:237], v4 offset:10368\n"
    "v_mfma_f32_16x16x32_bf16 v[180:183], a[220:223], a[100:103], v[180:183]\n"
    "v_mfma_f32_16x16x32_bf16 v[176:179], a[220:223], a[96:99], v[176:179]\n"
    "ds_read_b64_tr_b16 a[238:239], v4 offset:15488\n"
    "v_mfma_f32_16x16x32_bf16 v[176:179], a[216:219], a[64:67], v[176:179]\n"
    "s_waitcnt lgkmcnt(6)\n"
    "s_waitcnt vmcnt(15)\n"
    "s_barrier\n"
    "v_add_u32 v2, 20480, v2\n"
    "v_add_u32 v3, 20480, v3\n"
    "v_mfma_f32_16x16x32_bf16 v[192:195], a[224:227], a[64:67], v[192:195]\n"
    "s_waitcnt lgkmcnt(4)\n"
    "ds_read_b64_tr_b16 a[192:193], v2 offset:0\n"
    "v_mfma_f32_16x16x32_bf16 v[192:195], a[228:231], a[96:99], v[192:195]\n"
    "v_add_u32 v4, 20480, v4\n"
    "v_add_u32 v5, 20480, v5\n"
    "v_mfma_f32_16x16x32_bf16 v[196:199], a[228:231], a[100:103], v[196:199]\n"
    "ds_read_b64_tr_b16 a[194:195], v2 offset:5120\n"
    "v_mfma_f32_16x16x32_bf16 v[196:199], a[224:227], a[68:71], v[196:199]\n"
    "v_mfma_f32_16x16x32_bf16 v[200:203], a[224:227], a[72:75], v[200:203]\n"
    "s_lshr_b32 s73, s63, 3\n"
    "ds_read_b64_tr_b16 a[196:197], v2 offset:10240\n"
    "v_mfma_f32_16x16x32_bf16 v[200:203], a[228:231], a[104:107], v[200:203]\n"
    "s_lshl_b32 s73, s73, 4\n"
    "v_mfma_f32_16x16x32_bf16 v[204:207], a[228:231], a[108:111], v[204:207]\n"
    "s_add_u32 s73, s73, s62\n"
    "ds_read_b64_tr_b16 a[198:199], v2 offset:15360\n"
    "v_mfma_f32_16x16x32_bf16 v[204:207], a[224:227], a[76:79], v[204:207]\n"
    "s_waitcnt lgkmcnt(6)\n"
    "s_lshl_b32 s95, s73, 6\n"
    "v_mfma_f32_16x16x32_bf16 v[236:239], a[232:235], a[76:79], v[236:239]\n"
    "s_waitcnt lgkmcnt(4)\n"
    "s_add_u32 s95, s95, 86016\n"
    "ds_read_b64_tr_b16 a[200:201], v3 offset:0\n"
    "v_mov_b32 v62, s80\n"
    "v_mfma_f32_16x16x32_bf16 v[236:239], a[236:239], a[108:111], v[236:239]\n"
    "v_mov_b32 v63, s81\n"
    "v_mfma_f32_16x16x32_bf16 v[232:235], a[236:239], a[104:107], v[232:235]\n"
    "buffer_store_dwordx2 v[62:63], v10, s[52:55], s95 offen sc1\n"
    "ds_read_b64_tr_b16 a[202:203], v3 offset:5120\n"
    "v_mfma_f32_16x16x32_bf16 v[232:235], a[232:235], a[72:75], v[232:235]\n"
    "v_mfma_f32_16x16x32_bf16 v[228:231], a[232:235], a[68:71], v[228:231]\n"
    "ds_read_b64_tr_b16 a[204:205], v3 offset:10240\n"
    "v_mfma_f32_16x16x32_bf16 v[228:231], a[236:239], a[100:103], v[228:231]\n"
    "v_mfma_f32_16x16x32_bf16 v[224:227], a[236:239], a[96:99], v[224:227]\n"
    "ds_read_b64_tr_b16 a[206:207], v3 offset:15360\n"
    "v_mfma_f32_16x16x32_bf16 v[224:227], a[232:235], a[64:67], v[224:227]\n"
    "s_waitcnt lgkmcnt(6)\n"
    "v_mfma_f32_16x16x32_bf16 v[64:67], a[192:195], a[16:19], v[64:67]\n"
    "s_waitcnt lgkmcnt(4)\n"
    "ds_read_b64_tr_b16 a[208:209], v2 offset:64\n"
    "v_mfma_f32_16x16x32_bf16 v[64:67], a[196:199], a[48:51], v[64:67]\n"
    "v_mfma_f32_16x16x32_bf16 v[68:71], a[196:199], a[52:55], v[68:71]\n"
    "s_cmp_lt_u32 s79, 1\n"
    "s_cselect_b32 s88, 1, 0\n"
    "ds_read_b64_tr_b16 a[210:211], v2 offset:5184\n"
    "v_mfma_f32_16x16x32_bf16 v[68:71], a[192:195], a[20:23], v[68:71]\n"
    "s_mul_i32 s88, s88, 786432\n"
    "v_mfma_f32_16x16x32_bf16 v[72:75], a[192:195], a[24:27], v[72:75]\n"
    "s_add_u32 s88, s88, s65\n"
    "ds_read_b64_tr_b16 a[212:213], v2 offset:10304\n"
    "v_mfma_f32_16x16x32_bf16 v[72:75], a[196:199], a[56:59], v[72:75]\n"
    "s_add_u32 s88, s88, 16384\n"
    "v_mfma_f32_16x16x32_bf16 v[76:79], a[196:199], a[60:63], v[76:79]\n"
    "s_add_u32 s89, s88, 4096\n"
    "ds_read_b64_tr_b16 a[214:215], v2 offset:15424\n"
    "v_mfma_f32_16x16x32_bf16 v[76:79], a[192:195], a[28:31], v[76:79]\n"
    "s_waitcnt lgkmcnt(6)\n"
    "buffer_load_dwordx4 a[144:147], v9, s[52:55], s88 offen offset:0 sc1\n"
    "v_mfma_f32_16x16x32_bf16 v[92:95], a[200:203], a[28:31], v[92:95]\n"
    "s_waitcnt lgkmcnt(4)\n"
    "buffer_load_dwordx4 a[176:179], v9, s[52:55], s88 offen offset:1024 sc1\n"
    "ds_read_b64_tr_b16 a[216:217], v3 offset:64\n"
    "v_mfma_f32_16x16x32_bf16 v[92:95], a[204:207], a[60:63], v[92:95]\n"
    "buffer_load_dwordx4 a[148:151], v9, s[52:55], s88 offen offset:2048 sc1\n"
    "v_mfma_f32_16x16x32_bf16 v[88:91], a[204:207], a[56:59], v[88:91]\n"
    "ds_read_b64_tr_b16 a[218:219], v3 offset:5184\n"
    "v_mfma_f32_16x16x32_bf16 v[88:91], a[200:203], a[24:27], v[88:91]\n"
    "buffer_load_dwordx4 a[180:183], v9, s[52:55], s88 offen offset:3072 sc1\n"
    "v_mfma_f32_16x16x32_bf16 v[84:87], a[200:203], a[20:23], v[84:87]\n"
    "ds_read_b64_tr_b16 a[220:221], v3 offset:10304\n"
    "v_mfma_f32_16x16x32_bf16 v[84:87], a[204:207], a[52:55], v[84:87]\n"
    "v_mfma_f32_16x16x32_bf16 v[80:83], a[204:207], a[48:51], v[80:83]\n"
    "ds_read_b64_tr_b16 a[222:223], v3 offset:15424\n"
    "v_mfma_f32_16x16x32_bf16 v[80:83], a[200:203], a[16:19], v[80:83]\n"
    "s_waitcnt lgkmcnt(6)\n"
    "buffer_load_dwordx4 a[152:155], v9, s[52:55], s89 offen offset:0 sc1\n"
    "v_mfma_f32_16x16x32_bf16 v[96:99], a[208:211], a[16:19], v[96:99]\n"
    "s_waitcnt lgkmcnt(4)\n"
    "ds_read_b64_tr_b16 a[224:225], v2 offset:128\n"
    "v_mfma_f32_16x16x32_bf16 v[96:99], a[212:215], a[48:51], v[96:99]\n"
    "v_mfma_f32_16x16x32_bf16 v[100:103], a[212:215], a[52:55], v[100:103]\n"
    "ds_read_b64_tr_b16 a[226:227], v2 offset:5248\n"
    "v_mfma_f32_16x16x32_bf16 v[100:103], a[208:211], a[20:23], v[100:103]\n"
    "buffer_load_dwordx4 a[184:187], v9, s[52:55], s89 offen offset:1024 sc1\n"
    "v_mfma_f32_16x16x32_bf16 v[104:107], a[208:211], a[24:27], v[104:107]\n"
    "ds_read_b64_tr_b16 a[228:229], v2 offset:10368\n"
    "v_mfma_f32_16x16x32_bf16 v[104:107], a[212:215], a[56:59], v[104:107]\n"
    "v_mfma_f32_16x16x32_bf16 v[108:111], a[212:215], a[60:63], v[108:111]\n"
    "buffer_load_dwordx4 a[156:159], v9, s[52:55], s89 offen offset:2048 sc1\n"
    "ds_read_b64_tr_b16 a[230:231], v2 offset:15488\n"
    "v_mfma_f32_16x16x32_bf16 v[108:111], a[208:211], a[28:31], v[108:111]\n"
    "s_waitcnt lgkmcnt(6)\n"
    "v_mfma_f32_16x16x32_bf16 v[124:127], a[216:219], a[28:31], v[124:127]\n"
    "s_waitcnt lgkmcnt(4)\n"
    "ds_read_b64_tr_b16 a[232:233], v4 offset:0\n"
    "v_mfma_f32_16x16x32_bf16 v[124:127], a[220:223], a[60:63], v[124:127]\n"
    "v_mfma_f32_16x16x32_bf16 v[120:123], a[220:223], a[56:59], v[120:123]\n"
    "buffer_load_dwordx4 a[188:191], v9, s[52:55], s89 offen offset:3072 sc1\n"
    "ds_read_b64_tr_b16 a[234:235], v4 offset:5120\n"
    "v_mfma_f32_16x16x32_bf16 v[120:123], a[216:219], a[24:27], v[120:123]\n"
    "v_mfma_f32_16x16x32_bf16 v[116:119], a[216:219], a[20:23], v[116:119]\n"
    "ds_read_b64_tr_b16 a[236:237], v4 offset:10240\n"
    "v_mfma_f32_16x16x32_bf16 v[116:119], a[220:223], a[52:55], v[116:119]\n"
    "v_mfma_f32_16x16x32_bf16 v[112:115], a[220:223], a[48:51], v[112:115]\n"
    "s_cmp_lt_u32 s79, 2\n"
    "s_cselect_b32 s90, 2, 1\n"
    "ds_read_b64_tr_b16 a[238:239], v4 offset:15360\n"
    "v_mfma_f32_16x16x32_bf16 v[112:115], a[216:219], a[16:19], v[112:115]\n"
    "s_waitcnt lgkmcnt(6)\n"
    "s_mul_i32 s90, s90, 245760\n"
    "v_mfma_f32_16x16x32_bf16 v[128:131], a[224:227], a[16:19], v[128:131]\n"
    "s_waitcnt lgkmcnt(4)\n"
    "s_add_u32 s90, s90, 163840\n"
    "ds_read_b64_tr_b16 a[192:193], v5 offset:0\n"
    "v_mfma_f32_16x16x32_bf16 v[128:131], a[228:231], a[48:51], v[128:131]\n"
    "s_add_u32 m0, s91, 61440\n"
    "v_mfma_f32_16x16x32_bf16 v[132:135], a[228:231], a[52:55], v[132:135]\n"
    "buffer_load_dwordx4 v11, s[48:51], s90 offen offset:0 lds\n"
    "ds_read_b64_tr_b16 a[194:195], v5 offset:5120\n"
    "v_mfma_f32_16x16x32_bf16 v[132:135], a[224:227], a[20:23], v[132:135]\n"
    "buffer_load_dwordx4 v12, s[48:51], s90 offen offset:1024 lds\n"
    "v_mfma_f32_16x16x32_bf16 v[136:139], a[224:227], a[24:27], v[136:139]\n"
    "ds_read_b64_tr_b16 a[196:197], v5 offset:10240\n"
    "v_mfma_f32_16x16x32_bf16 v[136:139], a[228:231], a[56:59], v[136:139]\n"
    "buffer_load_dwordx4 v13, s[48:51], s90 offen offset:2048 lds\n"
    "v_mfma_f32_16x16x32_bf16 v[140:143], a[228:231], a[60:63], v[140:143]\n"
    "ds_read_b64_tr_b16 a[198:199], v5 offset:15360\n"
    "v_mfma_f32_16x16x32_bf16 v[140:143], a[224:227], a[28:31], v[140:143]\n"
    "s_waitcnt lgkmcnt(6)\n"
    "v_mfma_f32_16x16x32_bf16 v[156:159], a[232:235], a[28:31], v[156:159]\n"
    "s_waitcnt lgkmcnt(4)\n"
    "buffer_load_dwordx4 v14, s[48:51], s90 offen offset:3072 lds\n"
    "ds_read_b64_tr_b16 a[200:201], v4 offset:64\n"
    "v_mfma_f32_16x16x32_bf16 v[156:159], a[236:239], a[60:63], v[156:159]\n"
    "v_mfma_f32_16x16x32_bf16 v[152:155], a[236:239], a[56:59], v[152:155]\n"
    "ds_read_b64_tr_b16 a[202:203], v4 offset:5184\n"
    "v_mfma_f32_16x16x32_bf16 v[152:155], a[232:235], a[24:27], v[152:155]\n"
    "v_mfma_f32_16x16x32_bf16 v[148:151], a[232:235], a[20:23], v[148:151]\n"
    "s_add_u32 m0, s91, 65536\n"
    "ds_read_b64_tr_b16 a[204:205], v4 offset:10304\n"
    "v_mfma_f32_16x16x32_bf16 v[148:151], a[236:239], a[52:55], v[148:151]\n"
    "buffer_load_dwordx4 v15, s[48:51], s90 offen offset:0 lds\n"
    "v_mfma_f32_16x16x32_bf16 v[144:147], a[236:239], a[48:51], v[144:147]\n"
    "ds_read_b64_tr_b16 a[206:207], v4 offset:15424\n"
    "v_mfma_f32_16x16x32_bf16 v[144:147], a[232:235], a[16:19], v[144:147]\n"
    "s_waitcnt lgkmcnt(6)\n"
    "v_mfma_f32_16x16x32_bf16 v[160:163], a[192:195], a[16:19], v[160:163]\n"
    "s_waitcnt lgkmcnt(4)\n"
    "ds_read_b64_tr_b16 a[208:209], v5 offset:64\n"
    "v_mfma_f32_16x16x32_bf16 v[160:163], a[196:199], a[48:51], v[160:163]\n"
    "s_cmp_lt_u32 s79, 2\n"
    "s_cselect_b32 s88, 2, 1\n"
    "v_mfma_f32_16x16x32_bf16 v[164:167], a[196:199], a[52:55], v[164:167]\n"
    "s_mul_i32 s88, s88, 786432\n"
    "ds_read_b64_tr_b16 a[210:211], v5 offset:5184\n"
    "v_mfma_f32_16x16x32_bf16 v[164:167], a[192:195], a[20:23], v[164:167]\n"
    "s_add_u32 s88, s88, s65\n"
    "v_mfma_f32_16x16x32_bf16 v[168:171], a[192:195], a[24:27], v[168:171]\n"
    "s_add_u32 s88, s88, 16384\n"
    "ds_read_b64_tr_b16 a[212:213], v5 offset:10304\n"
    "v_mfma_f32_16x16x32_bf16 v[168:171], a[196:199], a[56:59], v[168:171]\n"
    "s_add_u32 s89, s88, 4096\n"
    "v_mfma_f32_16x16x32_bf16 v[172:175], a[196:199], a[60:63], v[172:175]\n"
    "buffer_load_dwordx4 a[0:3], v9, s[52:55], s88 offen offset:0 sc1\n"
    "ds_read_b64_tr_b16 a[214:215], v5 offset:15424\n"
    "v_mfma_f32_16x16x32_bf16 v[172:175], a[192:195], a[28:31], v[172:175]\n"
    "s_waitcnt lgkmcnt(6)\n"
    "buffer_load_dwordx4 a[32:35], v9, s[52:55], s88 offen offset:1024 sc1\n"
    "v_mfma_f32_16x16x32_bf16 v[188:191], a[200:203], a[28:31], v[188:191]\n"
    "s_waitcnt lgkmcnt(4)\n"
    "buffer_load_dwordx4 a[4:7], v9, s[52:55], s88 offen offset:2048 sc1\n"
    "ds_read_b64_tr_b16 a[216:217], v4 offset:128\n"
    "v_mfma_f32_16x16x32_bf16 v[188:191], a[204:207], a[60:63], v[188:191]\n"
    "v_mfma_f32_16x16x32_bf16 v[184:187], a[204:207], a[56:59], v[184:187]\n"
    "buffer_load_dwordx4 a[36:39], v9, s[52:55], s88 offen offset:3072 sc1\n"
    "ds_read_b64_tr_b16 a[218:219], v4 offset:5248\n"
    "v_mfma_f32_16x16x32_bf16 v[184:187], a[200:203], a[24:27], v[184:187]\n"
    "v_mfma_f32_16x16x32_bf16 v[180:183], a[200:203], a[20:23], v[180:183]\n"
    "ds_read_b64_tr_b16 a[220:221], v4 offset:10368\n"
    "v_mfma_f32_16x16x32_bf16 v[180:183], a[204:207], a[52:55], v[180:183]\n"
    "v_mfma_f32_16x16x32_bf16 v[176:179], a[204:207], a[48:51], v[176:179]\n"
    "buffer_load_dwordx4 a[8:11], v9, s[52:55], s89 offen offset:0 sc1\n"
    "ds_read_b64_tr_b16 a[222:223], v4 offset:15488\n"
    "v_mfma_f32_16x16x32_bf16 v[176:179], a[200:203], a[16:19], v[176:179]\n"
    "s_waitcnt lgkmcnt(6)\n"
    "s_waitcnt vmcnt(24)\n"
    "s_barrier\n"
    "v_add_u32 v2, 4294823936, v2\n"
    "v_add_u32 v3, 4294823936, v3\n"
    "v_mfma_f32_16x16x32_bf16 v[192:195], a[208:211], a[16:19], v[192:195]\n"
    "s_waitcnt lgkmcnt(4)\n"
    "ds_read_b64_tr_b16 a[224:225], v2 offset:0\n"
    "v_mfma_f32_16x16x32_bf16 v[192:195], a[212:215], a[48:51], v[192:195]\n"
    "v_add_u32 v4, 4294823936, v4\n"
    "v_add_u32 v5, 4294823936, v5\n"
    "v_mfma_f32_16x16x32_bf16 v[196:199], a[212:215], a[52:55], v[196:199]\n"
    "buffer_load_dwordx4 a[40:43], v9, s[52:55], s89 offen offset:1024 sc1\n"
    "ds_read_b64_tr_b16 a[226:227], v2 offset:5120\n"
    "v_mfma_f32_16x16x32_bf16 v[196:199], a[208:211], a[20:23], v[196:199]\n"
    "v_mfma_f32_16x16x32_bf16 v[200:203], a[208:211], a[24:27], v[200:203]\n"
    "ds_read_b64_tr_b16 a[228:229], v2 offset:10240\n"
    "v_mfma_f32_16x16x32_bf16 v[200:203], a[212:215], a[56:59], v[200:203]\n"
    "buffer_load_dwordx4 a[12:15], v9, s[52:55], s89 offen offset:2048 sc1\n"
    "v_mfma_f32_16x16x32_bf16 v[204:207], a[212:215], a[60:63], v[204:207]\n"
    "ds_read_b64_tr_b16 a[230:231], v2 offset:15360\n"
    "v_mfma_f32_16x16x32_bf16 v[204:207], a[208:211], a[28:31], v[204:207]\n"
    "s_waitcnt lgkmcnt(6)\n"
    "v_mfma_f32_16x16x32_bf16 v[236:239], a[216:219], a[28:31], v[236:239]\n"
    "s_waitcnt lgkmcnt(4)\n"
    "ds_read_b64_tr_b16 a[232:233], v3 offset:0\n"
    "v_mfma_f32_16x16x32_bf16 v[236:239], a[220:223], a[60:63], v[236:239]\n"
    "buffer_load_dwordx4 a[44:47], v9, s[52:55], s89 offen offset:3072 sc1\n"
    "v_mfma_f32_16x16x32_bf16 v[232:235], a[220:223], a[56:59], v[232:235]\n"
    "ds_read_b64_tr_b16 a[234:235], v3 offset:5120\n"
    "v_mfma_f32_16x16x32_bf16 v[232:235], a[216:219], a[24:27], v[232:235]\n"
    "v_mfma_f32_16x16x32_bf16 v[228:231], a[216:219], a[20:23], v[228:231]\n"
    "ds_read_b64_tr_b16 a[236:237], v3 offset:10240\n"
    "v_mfma_f32_16x16x32_bf16 v[228:231], a[220:223], a[52:55], v[228:231]\n"
    "v_mfma_f32_16x16x32_bf16 v[224:227], a[220:223], a[48:51], v[224:227]\n"
    "ds_read_b64_tr_b16 a[238:239], v3 offset:15360\n"
    "v_mfma_f32_16x16x32_bf16 v[224:227], a[216:219], a[16:19], v[224:227]\n"
    "s_waitcnt lgkmcnt(6)\n"
    "v_mfma_f32_16x16x32_bf16 v[64:67], a[224:227], a[80:83], v[64:67]\n"
    "s_waitcnt lgkmcnt(4)\n"
    "ds_read_b64_tr_b16 a[192:193], v2 offset:64\n"
    "v_mfma_f32_16x16x32_bf16 v[64:67], a[228:231], a[112:115], v[64:67]\n"
    "s_lshr_b32 s74, s82, 1\n"
    "v_mfma_f32_16x16x32_bf16 v[68:71], a[228:231], a[116:119], v[68:71]\n"
    "s_lshl_b32 s74, s74, 4\n"
    "ds_read_b64_tr_b16 a[194:195], v2 offset:5184\n"
    "v_lshrrev_b32 v56, 4, v9\n"
    "v_mfma_f32_16x16x32_bf16 v[68:71], a[224:227], a[84:87], v[68:71]\n"
    "v_min_u32 v56, 15, v56\n"
    "v_add_u32 v56, s74, v56\n"
    "v_mfma_f32_16x16x32_bf16 v[72:75], a[224:227], a[88:91], v[72:75]\n"
    "ds_read_b64_tr_b16 a[196:197], v2 offset:10304\n"
    "v_lshlrev_b32 v56, 6, v56\n"
    "v_mfma_f32_16x16x32_bf16 v[72:75], a[228:231], a[120:123], v[72:75]\n"
    "v_add_u32 v56, 86016, v56\n"
    "v_mfma_f32_16x16x32_bf16 v[76:79], a[228:231], a[124:127], v[76:79]\n"
    "buffer_load_dwordx2 v[62:63], v56, s[52:55], 0 offen sc1\n"
    "ds_read_b64_tr_b16 a[198:199], v2 offset:15424\n"
    "v_mfma_f32_16x16x32_bf16 v[76:79], a[224:227], a[92:95], v[76:79]\n"
    "s_waitcnt lgkmcnt(6)\n"
    "v_mfma_f32_16x16x32_bf16 v[92:95], a[232:235], a[92:95], v[92:95]\n"
    "s_waitcnt lgkmcnt(4)\n"
    "s_cmp_lt_u32 s79, 3\n"
    "s_cselect_b32 s88, 3, 2\n"
    "ds_read_b64_tr_b16 a[200:201], v3 offset:64\n"
    "v_mfma_f32_16x16x32_bf16 v[92:95], a[236:239], a[124:127], v[92:95]\n"
    "s_mul_i32 s88, s88, 786432\n"
    "v_mfma_f32_16x16x32_bf16 v[88:91], a[236:239], a[120:123], v[88:91]\n"
    "s_add_u32 s88, s88, s65\n"
    "ds_read_b64_tr_b16 a[202:203], v3 offset:5184\n"
    "v_mfma_f32_16x16x32_bf16 v[88:91], a[232:235], a[88:91], v[88:91]\n"
    "s_add_u32 s88, s88, 16384\n"
    "v_mfma_f32_16x16x32_bf16 v[84:87], a[232:235], a[84:87], v[84:87]\n"
    "s_add_u32 s89, s88, 4096\n"
    "ds_read_b64_tr_b16 a[204:205], v3 offset:10304\n"
    "v_mfma_f32_16x16x32_bf16 v[84:87], a[236:239], a[116:119], v[84:87]\n"
    "buffer_load_dwordx4 a[64:67], v9, s[52:55], s88 offen offset:0 sc1\n"
    "v_mfma_f32_16x16x32_bf16 v[80:83], a[236:239], a[112:115], v[80:83]\n"
    "buffer_load_dwordx4 a[96:99], v9, s[52:55], s88 offen offset:1024 sc1\n"
    "ds_read_b64_tr_b16 a[206:207], v3 offset:15424\n"
    "v_mfma_f32_16x16x32_bf16 v[80:83], a[232:235], a[80:83], v[80:83]\n"
    "s_waitcnt lgkmcnt(6)\n"
    "buffer_load_dwordx4 a[68:71], v9, s[52:55], s88 offen offset:2048 sc1\n"
    "v_mfma_f32_16x16x32_bf16 v[96:99], a[192:195], a[80:83], v[96:99]\n"
    "s_waitcnt lgkmcnt(4)\n"
    "ds_read_b64_tr_b16 a[208:209], v2 offset:128\n"
    "v_mfma_f32_16x16x32_bf16 v[96:99], a[196:199], a[112:115], v[96:99]\n"
    "buffer_load_dwordx4 a[100:103], v9, s[52:55], s88 offen offset:3072 sc1\n"
    "v_mfma_f32_16x16x32_bf16 v[100:103], a[196:199], a[116:119], v[100:103]\n"
    "ds_read_b64_tr_b16 a[210:211], v2 offset:5248\n"
    "v_mfma_f32_16x16x32_bf16 v[100:103], a[192:195], a[84:87], v[100:103]\n"
    "v_mfma_f32_16x16x32_bf16 v[104:107], a[192:195], a[88:91], v[104:107]\n"
    "ds_read_b64_tr_b16 a[212:213], v2 offset:10368\n"
    "v_mfma_f32_16x16x32_bf16 v[104:107], a[196:199], a[120:123], v[104:107]\n"
    "buffer_load_dwordx4 a[72:75], v9, s[52:55], s89 offen offset:0 sc1\n"
    "v_mfma_f32_16x16x32_bf16 v[108:111], a[196:199], a[124:127], v[108:111]\n"
    "ds_read_b64_tr_b16 a[214:215], v2 offset:15488\n"
    "v_mfma_f32_16x16x32_bf16 v[108:111], a[192:195], a[92:95], v[108:111]\n"
    "s_waitcnt lgkmcnt(6)\n"
    "v_mfma_f32_16x16x32_bf16 v[124:127], a[200:203], a[92:95], v[124:127]\n"
    "s_waitcnt lgkmcnt(4)\n"
    "ds_read_b64_tr_b16 a[216:217], v4 offset:0\n"
    "v_mfma_f32_16x16x32_bf16 v[124:127], a[204:207], a[124:127], v[124:127]\n"
    "buffer_load_dwordx4 a[104:107], v9, s[52:55], s89 offen offset:1024 sc1\n"
    "v_mfma_f32_16x16x32_bf16 v[120:123], a[204:207], a[120:123], v[120:123]\n"
    "ds_read_b64_tr_b16 a[218:219], v4 offset:5120\n"
    "v_mfma_f32_16x16x32_bf16 v[120:123], a[200:203], a[88:91], v[120:123]\n"
    "v_mfma_f32_16x16x32_bf16 v[116:119], a[200:203], a[84:87], v[116:119]\n"
    "ds_read_b64_tr_b16 a[220:221], v4 offset:10240\n"
    "v_mfma_f32_16x16x32_bf16 v[116:119], a[204:207], a[116:119], v[116:119]\n"
    "buffer_load_dwordx4 a[76:79], v9, s[52:55], s89 offen offset:2048 sc1\n"
    "v_mfma_f32_16x16x32_bf16 v[112:115], a[204:207], a[112:115], v[112:115]\n"
    "ds_read_b64_tr_b16 a[222:223], v4 offset:15360\n"
    "v_mfma_f32_16x16x32_bf16 v[112:115], a[200:203], a[80:83], v[112:115]\n"
    "s_waitcnt lgkmcnt(6)\n"
    "v_mfma_f32_16x16x32_bf16 v[128:131], a[208:211], a[80:83], v[128:131]\n"
    "s_waitcnt lgkmcnt(4)\n"
    "ds_read_b64_tr_b16 a[224:225], v5 offset:0\n"
    "v_mfma_f32_16x16x32_bf16 v[128:131], a[212:215], a[112:115], v[128:131]\n"
    "buffer_load_dwordx4 a[108:111], v9, s[52:55], s89 offen offset:3072 sc1\n"
    "v_mfma_f32_16x16x32_bf16 v[132:135], a[212:215], a[116:119], v[132:135]\n"
    "ds_read_b64_tr_b16 a[226:227], v5 offset:5120\n"
    "v_mfma_f32_16x16x32_bf16 v[132:135], a[208:211], a[84:87], v[132:135]\n"
    "v_mfma_f32_16x16x32_bf16 v[136:139], a[208:211], a[88:91], v[136:139]\n"
    "ds_read_b64_tr_b16 a[228:229], v5 offset:10240\n"
    "v_mfma_f32_16x16x32_bf16 v[136:139], a[212:215], a[120:123], v[136:139]\n"
    "v_mfma_f32_16x16x32_bf16 v[140:143], a[212:215], a[124:127], v[140:143]\n"
    "s_cmp_lt_u32 s79, 3\n"
    "s_cselect_b32 s90, 3, 2\n"
    "ds_read_b64_tr_b16 a[230:231], v5 offset:15360\n"
    "v_mfma_f32_16x16x32_bf16 v[140:143], a[208:211], a[92:95], v[140:143]\n"
    "s_waitcnt lgkmcnt(6)\n"
    "s_mul_i32 s90, s90, 245760\n"
    "v_mfma_f32_16x16x32_bf16 v[156:159], a[216:219], a[92:95], v[156:159]\n"
    "s_waitcnt lgkmcnt(4)\n"
    "s_add_u32 s90, s90, 163840\n"
    "ds_read_b64_tr_b16 a[232:233], v4 offset:64\n"
    "v_mfma_f32_16x16x32_bf16 v[156:159], a[220:223], a[124:127], v[156:159]\n"
    "s_add_u32 m0, s91, 81920\n"
    "v_mfma_f32_16x16x32_bf16 v[152:155], a[220:223], a[120:123], v[152:155]\n"
    "s_lshl_b32 s73, s78, 2\n"
    "buffer_load_dwordx4 v11, s[48:51], s90 offen offset:0 lds\n"
    "ds_read_b64_tr_b16 a[234:235], v4 offset:5184\n"
    "v_mfma_f32_16x16x32_bf16 v[152:155], a[216:219], a[88:91], v[152:155]\n"
    "s_add_u32 s73, s73, s76\n"
    "buffer_load_dwordx4 v12, s[48:51], s90 offen offset:1024 lds\n"
    "v_mfma_f32_16x16x32_bf16 v[148:151], a[216:219], a[84:87], v[148:151]\n"
    "s_lshl_b32 s73, s73, 4\n"
    "ds_read_b64_tr_b16 a[236:237], v4 offset:10304\n"
    "v_mfma_f32_16x16x32_bf16 v[148:151], a[220:223], a[116:119], v[148:151]\n"
    "s_add_u32 s73, s73, s79\n"
    "buffer_load_dwordx4 v13, s[48:51], s90 offen offset:2048 lds\n"
    "v_mfma_f32_16x16x32_bf16 v[144:147], a[220:223], a[112:115], v[144:147]\n"
    "s_lshl_b32 s73, s73, 4\n"
    "ds_read_b64_tr_b16 a[238:239], v4 offset:15424\n"
    "v_mfma_f32_16x16x32_bf16 v[144:147], a[216:219], a[80:83], v[144:147]\n"
    "s_waitcnt lgkmcnt(6)\n"
    "s_lshr_b32 s56, s77, 1\n"
    "v_mfma_f32_16x16x32_bf16 v[160:163], a[224:227], a[80:83], v[160:163]\n"
    "s_waitcnt lgkmcnt(4)\n"
    "s_add_u32 s73, s73, s56\n"
    "ds_read_b64_tr_b16 a[192:193], v5 offset:64\n"
    "v_mfma_f32_16x16x32_bf16 v[160:163], a[228:231], a[112:115], v[160:163]\n"
    "s_mul_i32 s73, s73, 2560\n"
    "buffer_load_dwordx4 v14, s[48:51], s90 offen offset:3072 lds\n"
    "v_mfma_f32_16x16x32_bf16 v[164:167], a[228:231], a[116:119], v[164:167]\n"
    "s_add_u32 s56, s73, 44040192\n"
    "ds_read_b64_tr_b16 a[194:195], v5 offset:5184\n"
    "v_mfma_f32_16x16x32_bf16 v[164:167], a[224:227], a[84:87], v[164:167]\n"
    "s_add_u32 s57, s56, 163840\n"
    "v_mfma_f32_16x16x32_bf16 v[168:171], a[224:227], a[88:91], v[168:171]\n"
    "s_add_u32 s58, s56, 327680\n"
    "ds_read_b64_tr_b16 a[196:197], v5 offset:10304\n"
    "v_mfma_f32_16x16x32_bf16 v[168:171], a[228:231], a[120:123], v[168:171]\n"
    "s_add_u32 m0, s91, 86016\n"
    "v_mfma_f32_16x16x32_bf16 v[172:175], a[228:231], a[124:127], v[172:175]\n"
    "s_add_u32 s59, s56, 491520\n"
    "buffer_load_dwordx4 v15, s[48:51], s90 offen offset:0 lds\n"
    "ds_read_b64_tr_b16 a[198:199], v5 offset:15424\n"
    "v_mfma_f32_16x16x32_bf16 v[172:175], a[224:227], a[92:95], v[172:175]\n"
    "s_waitcnt lgkmcnt(6)\n"
    "s_and_b32 s73, s77, 1\n"
    "v_mfma_f32_16x16x32_bf16 v[188:191], a[232:235], a[92:95], v[188:191]\n"
    "s_waitcnt lgkmcnt(4)\n"
    "s_mul_i32 s73, s73, 20480\n"
    "ds_read_b64_tr_b16 a[200:201], v4 offset:128\n"
    "v_add_u32 v57, s73, v9\n"
    "v_mfma_f32_16x16x32_bf16 v[188:191], a[236:239], a[124:127], v[188:191]\n"
    "v_add_u32 v6, s73, v59\n"
    "v_mfma_f32_16x16x32_bf16 v[184:187], a[236:239], a[120:123], v[184:187]\n"
    "s_mov_b32 s35, 0\n"
    "ds_read_b64_tr_b16 a[202:203], v4 offset:5248\n"
    "v_mfma_f32_16x16x32_bf16 v[184:187], a[232:235], a[88:91], v[184:187]\n"
    "s_mul_i32 s71, s78, 245760\n"
    "v_mfma_f32_16x16x32_bf16 v[180:183], a[232:235], a[84:87], v[180:183]\n"
    "s_add_u32 s71, s71, s91\n"
    "ds_read_b64_tr_b16 a[204:205], v4 offset:10368\n"
    "v_mfma_f32_16x16x32_bf16 v[180:183], a[236:239], a[116:119], v[180:183]\n"
    "s_add_u32 s71, s71, 65011712\n"
    "v_mfma_f32_16x16x32_bf16 v[176:179], a[236:239], a[112:115], v[176:179]\n"
    "s_cmp_ge_u32 s35, 12\n"
    "s_cselect_b32 s90, -12, 0\n"
    "s_add_u32 s90, s90, s35\n"
    "ds_read_b64_tr_b16 a[206:207], v4 offset:15488\n"
    "v_mfma_f32_16x16x32_bf16 v[176:179], a[232:235], a[80:83], v[176:179]\n"
    "s_waitcnt lgkmcnt(6)\n"
    "s_barrier\n"
    "v_add_u32 v2, 20480, v2\n"
    "v_add_u32 v3, 20480, v3\n"
    "v_mfma_f32_16x16x32_bf16 v[192:195], a[192:195], a[80:83], v[192:195]\n"
    "s_waitcnt lgkmcnt(4)\n"
    "s_mul_i32 s90, s90, 20480\n"
    "ds_read_b64_tr_b16 a[208:209], v2 offset:0\n"
    "v_mfma_f32_16x16x32_bf16 v[192:195], a[196:199], a[112:115], v[192:195]\n"
    "s_add_u32 s90, s90, s71\n"
    "v_add_u32 v4, 20480, v4\n"
    "v_add_u32 v5, 20480, v5\n"
    "v_mfma_f32_16x16x32_bf16 v[196:199], a[196:199], a[116:119], v[196:199]\n"
    "s_add_u32 m0, s91, 102400\n"
    "ds_read_b64_tr_b16 a[210:211], v2 offset:5120\n"
    "v_mfma_f32_16x16x32_bf16 v[196:199], a[192:195], a[84:87], v[196:199]\n"
    "s_sub_u32 s73, 30720, s73\n"
    "buffer_load_dwordx4 v9, s[52:55], s90 offen offset:0 sc1 lds\n"
    "v_add_u32 v58, s73, v9\n"
    "v_mfma_f32_16x16x32_bf16 v[200:203], a[192:195], a[88:91], v[200:203]\n"
    "buffer_load_dwordx4 v9, s[52:55], s90 offen offset:1024 sc1 lds\n"
    "ds_read_b64_tr_b16 a[212:213], v2 offset:10240\n"
    "v_mfma_f32_16x16x32_bf16 v[200:203], a[196:199], a[120:123], v[200:203]\n"
    "buffer_load_dwordx4 v9, s[52:55], s90 offen offset:2048 sc1 lds\n"
    "v_add_u32 v7, s73, v59\n"
    "v_mfma_f32_16x16x32_bf16 v[204:207], a[196:199], a[124:127], v[204:207]\n"
    "buffer_load_dwordx4 v9, s[52:55], s90 offen offset:3072 sc1 lds\n"
    "ds_read_b64_tr_b16 a[214:215], v2 offset:15360\n"
    "v_mfma_f32_16x16x32_bf16 v[204:207], a[192:195], a[92:95], v[204:207]\n"
    "s_waitcnt lgkmcnt(6)\n"
    "v_mfma_f32_16x16x32_bf16 v[236:239], a[200:203], a[92:95], v[236:239]\n"
    "s_waitcnt lgkmcnt(4)\n"
    "ds_read_b64_tr_b16 a[216:217], v3 offset:0\n"
    "v_mfma_f32_16x16x32_bf16 v[236:239], a[204:207], a[124:127], v[236:239]\n"
    "s_add_u32 s90, s90, 4096\n"
    "v_mfma_f32_16x16x32_bf16 v[232:235], a[204:207], a[120:123], v[232:235]\n"
    "s_add_u32 m0, s91, 106496\n"
    "ds_read_b64_tr_b16 a[218:219], v3 offset:5120\n"
    "v_mfma_f32_16x16x32_bf16 v[232:235], a[200:203], a[88:91], v[232:235]\n"
    "buffer_load_dwordx4 v9, s[52:55], s90 offen offset:0 sc1 lds\n"
    "v_mfma_f32_16x16x32_bf16 v[228:231], a[200:203], a[84:87], v[228:231]\n"
    "ds_read_b64_tr_b16 a[220:221], v3 offset:10240\n"
    "v_mfma_f32_16x16x32_bf16 v[228:231], a[204:207], a[116:119], v[228:231]\n"
    "v_mfma_f32_16x16x32_bf16 v[224:227], a[204:207], a[112:115], v[224:227]\n"
    "ds_read_b64_tr_b16 a[222:223], v3 offset:15360\n"
    "v_mfma_f32_16x16x32_bf16 v[224:227], a[200:203], a[80:83], v[224:227]\n"
    "s_waitcnt lgkmcnt(6)\n"
    "v_mfma_f32_16x16x32_bf16 v[64:67], a[208:211], a[128:131], v[64:67]\n"
    "s_waitcnt lgkmcnt(4)\n"
    "ds_read_b64_tr_b16 a[224:225], v2 offset:64\n"
    "v_mfma_f32_16x16x32_bf16 v[64:67], a[212:215], a[160:163], v[64:67]\n"
    "v_mfma_f32_16x16x32_bf16 v[68:71], a[212:215], a[164:167], v[68:71]\n"
    "s_waitcnt vmcnt(18)\n"
    "v_cmp_ne_u64 vcc, s[80:81], v[62:63]\n"
    "s_nop 1\n"
    "s_cbranch_vccnz L_slow8\n"
    "L_back8:\n"
    "s_mul_i32 s66, s83, 49152\n"
    "s_add_u32 s66, s66, 11534336\n"
    "ds_read_b64_tr_b16 a[226:227], v2 offset:5184\n"
    "v_mfma_f32_16x16x32_bf16 v[68:71], a[208:211], a[132:135], v[68:71]\n"
    "s_cmp_ge_u32 s35, 12\n"
    "s_cselect_b32 s67, -12, 0\n"
    "s_add_u32 s67, s67, s35\n"
    "v_mfma_f32_16x16x32_bf16 v[72:75], a[208:211], a[136:139], v[72:75]\n"
    "s_lshl_b32 s67, s67, 12\n"
    "ds_read_b64_tr_b16 a[228:229], v2 offset:10304\n"
    "v_mfma_f32_16x16x32_bf16 v[72:75], a[212:215], a[168:171], v[72:75]\n"
    "s_add_u32 s67, s67, s66\n"
    "v_mfma_f32_16x16x32_bf16 v[76:79], a[212:215], a[172:175], v[76:79]\n"
    "buffer_load_dwordx4 a[16:19], v9, s[52:55], s67 offen offset:0 sc1\n"
    "ds_read_b64_tr_b16 a[230:231], v2 offset:15424\n"
    "v_mfma_f32_16x16x32_bf16 v[76:79], a[208:211], a[140:143], v[76:79]\n"
    "s_waitcnt lgkmcnt(6)\n"
    "buffer_load_dwordx4 a[20:23], v9, s[52:55], s67 offen offset:1024 sc1\n"
    "v_mfma_f32_16x16x32_bf16 v[92:95], a[216:219], a[140:143], v[92:95]\n"
    "s_waitcnt lgkmcnt(4)\n"
    "ds_read_b64_tr_b16 a[232:233], v3 offset:64\n"
    "v_mfma_f32_16x16x32_bf16 v[92:95], a[220:223], a[172:175], v[92:95]\n"
    "buffer_load_dwordx4 a[24:27], v9, s[52:55], s67 offen offset:2048 sc1\n"
    "v_mfma_f32_16x16x32_bf16 v[88:91], a[220:223], a[168:171], v[88:91]\n"
    "ds_read_b64_tr_b16 a[234:235], v3 offset:5184\n"
    "v_mfma_f32_16x16x32_bf16 v[88:91], a[216:219], a[136:139], v[88:91]\n"
    "v_mfma_f32_16x16x32_bf16 v[84:87], a[216:219], a[132:135], v[84:87]\n"
    "ds_read_b64_tr_b16 a[236:237], v3 offset:10304\n"
    "v_mfma_f32_16x16x32_bf16 v[84:87], a[220:223], a[164:167], v[84:87]\n"
    "v_mfma_f32_16x16x32_bf16 v[80:83], a[220:223], a[160:163], v[80:83]\n"
    "buffer_load_dwordx4 a[28:31], v9, s[52:55], s67 offen offset:3072 sc1\n"
    "ds_read_b64_tr_b16 a[238:239], v3 offset:15424\n"
    "v_mfma_f32_16x16x32_bf16 v[80:83], a[216:219], a[128:131], v[80:83]\n"
    "s_waitcnt lgkmcnt(6)\n"
    "v_mfma_f32_16x16x32_bf16 v[96:99], a[224:227], a[128:131], v[96:99]\n"
    "s_waitcnt lgkmcnt(4)\n"
    "ds_read_b64_tr_b16 a[192:193], v2 offset:128\n"
    "v_mfma_f32_16x16x32_bf16 v[96:99], a[228:231], a[160:163], v[96:99]\n"
    "v_mfma_f32_16x16x32_bf16 v[100:103], a[228:231], a[164:167], v[100:103]\n"
    "ds_read_b64_tr_b16 a[194:195], v2 offset:5248\n"
    "v_mfma_f32_16x16x32_bf16 v[100:103], a[224:227], a[132:135], v[100:103]\n"
    "s_cmp_ge_u32 s35, 11\n"
    "s_cselect_b32 s67, -11, 1\n"
    "s_add_u32 s67, s67, s35\n"
    "v_mfma_f32_16x16x32_bf16 v[104:107], a[224:227], a[136:139], v[104:107]\n"
    "s_lshl_b32 s67, s67, 12\n"
    "ds_read_b64_tr_b16 a[196:197], v2 offset:10368\n"
    "v_mfma_f32_16x16x32_bf16 v[104:107], a[228:231], a[168:171], v[104:107]\n"
    "s_add_u32 s67, s67, s66\n"
    "v_mfma_f32_16x16x32_bf16 v[108:111], a[228:231], a[172:175], v[108:111]\n"
    "buffer_load_dwordx4 a[48:51], v9, s[52:55], s67 offen offset:0 sc1\n"
    "ds_read_b64_tr_b16 a[198:199], v2 offset:15488\n"
    "v_mfma_f32_16x16x32_bf16 v[108:111], a[224:227], a[140:143], v[108:111]\n"
    "s_waitcnt lgkmcnt(6)\n"
    "buffer_load_dwordx4 a[52:55], v9, s[52:55], s67 offen offset:1024 sc1\n"
    "v_mfma_f32_16x16x32_bf16 v[124:127], a[232:235], a[140:143], v[124:127]\n"
    "s_waitcnt lgkmcnt(4)\n"
    "ds_read_b64_tr_b16 a[200:201], v4 offset:0\n"
    "v_mfma_f32_16x16x32_bf16 v[124:127], a[236:239], a[172:175], v[124:127]\n"
    "v_mfma_f32_16x16x32_bf16 v[120:123], a[236:239], a[168:171], v[120:123]\n"
    "ds_read_b64_tr_b16 a[202:203], v4 offset:5120\n"
    "v_mfma_f32_16x16x32_bf16 v[120:123], a[232:235], a[136:139], v[120:123]\n"
    "buffer_load_dwordx4 a[56:59], v9, s[52:55], s67 offen offset:2048 sc1\n"
    "v_mfma_f32_16x16x32_bf16 v[116:119], a[232:235], a[132:135], v[116:119]\n"
    "ds_read_b64_tr_b16 a[204:205], v4 offset:10240\n"
    "v_mfma_f32_16x16x32_bf16 v[116:119], a[236:239], a[164:167], v[116:119]\n"
    "v_mfma_f32_16x16x32_bf16 v[112:115], a[236:239], a[160:163], v[112:115]\n"
    "ds_read_b64_tr_b16 a[206:207], v4 offset:15360\n"
    "v_mfma_f32_16x16x32_bf16 v[112:115], a[232:235], a[128:131], v[112:115]\n"
    "s_waitcnt lgkmcnt(6)\n"
    "v_mfma_f32_16x16x32_bf16 v[128:131], a[192:195], a[128:131], v[128:131]\n"
    "s_waitcnt lgkmcnt(4)\n"
    "buffer_load_dwordx4 a[60:63], v9, s[52:55], s67 offen offset:3072 sc1\n"
    "ds_read_b64_tr_b16 a[208:209], v5 offset:0\n"
    "v_mfma_f32_16x16x32_bf16 v[128:131], a[196:199], a[160:163], v[128:131]\n"
    "v_mfma_f32_16x16x32_bf16 v[132:135], a[196:199], a[164:167], v[132:135]\n"
    "ds_read_b64_tr_b16 a[210:211], v5 offset:5120\n"
    "v_mfma_f32_16x16x32_bf16 v[132:135], a[192:195], a[132:135], v[132:135]\n"
    "v_mfma_f32_16x16x32_bf16 v[136:139], a[192:195], a[136:139], v[136:139]\n"
    "ds_read_b64_tr_b16 a[212:213], v5 offset:10240\n"
    "v_mfma_f32_16x16x32_bf16 v[136:139], a[196:199], a[168:171], v[136:139]\n"
    "s_cmp_ge_u32 s35, 11\n"
    "s_cselect_b32 s90, -11, 1\n"
    "s_add_u32 s90, s90, s35\n"
    "v_mfma_f32_16x16x32_bf16 v[140:143], a[196:199], a[172:175], v[140:143]\n"
    "s_mul_i32 s90, s90, 20480\n"
    "ds_read_b64_tr_b16 a[214:215], v5 offset:15360\n"
    "v_mfma_f32_16x16x32_bf16 v[140:143], a[192:195], a[140:143], v[140:143]\n"
    "s_waitcnt lgkmcnt(6)\n"
    "s_add_u32 s90, s90, s71\n"
    "v_mfma_f32_16x16x32_bf16 v[156:159], a[200:203], a[140:143], v[156:159]\n"
    "s_waitcnt lgkmcnt(4)\n"
    "s_add_u32 m0, s91, 122880\n"
    "ds_read_b64_tr_b16 a[216:217], v4 offset:64\n"
    "v_mfma_f32_16x16x32_bf16 v[156:159], a[204:207], a[172:175], v[156:159]\n"
    "buffer_load_dwordx4 v9, s[52:55], s90 offen offset:0 sc1 lds\n"
    "v_mfma_f32_16x16x32_bf16 v[152:155], a[204:207], a[168:171], v[152:155]\n"
    "buffer_load_dwordx4 v9, s[52:55], s90 offen offset:1024 sc1 lds\n"
    "ds_read_b64_tr_b16 a[218:219], v4 offset:5184\n"
    "v_mfma_f32_16x16x32_bf16 v[152:155], a[200:203], a[136:139], v[152:155]\n"
    "v_mfma_f32_16x16x32_bf16 v[148:151], a[200:203], a[132:135], v[148:151]\n"
    "ds_read_b64_tr_b16 a[220:221], v4 offset:10304\n"
    "v_mfma_f32_16x16x32_bf16 v[148:151], a[204:207], a[164:167], v[148:151]\n"
    "buffer_load_dwordx4 v9, s[52:55], s90 offen offset:2048 sc1 lds\n"
    "v_mfma_f32_16x16x32_bf16 v[144:147], a[204:207], a[160:163], v[144:147]\n"
    "ds_read_b64_tr_b16 a[222:223], v4 offset:15424\n"
    "v_mfma_f32_16x16x32_bf16 v[144:147], a[200:203], a[128:131], v[144:147]\n"
    "s_waitcnt lgkmcnt(6)\n"
    "v_mfma_f32_16x16x32_bf16 v[160:163], a[208:211], a[128:131], v[160:163]\n"
    "s_waitcnt lgkmcnt(4)\n"
    "ds_read_b64_tr_b16 a[224:225], v5 offset:64\n"
    "v_mfma_f32_16x16x32_bf16 v[160:163], a[212:215], a[160:163], v[160:163]\n"
    "v_mfma_f32_16x16x32_bf16 v[164:167], a[212:215], a[164:167], v[164:167]\n"
    "buffer_load_dwordx4 v9, s[52:55], s90 offen offset:3072 sc1 lds\n"
    "ds_read_b64_tr_b16 a[226:227], v5 offset:5184\n"
    "v_mfma_f32_16x16x32_bf16 v[164:167], a[208:211], a[132:135], v[164:167]\n"
    "v_mfma_f32_16x16x32_bf16 v[168:171], a[208:211], a[136:139], v[168:171]\n"
    "ds_read_b64_tr_b16 a[228:229], v5 offset:10304\n"
    "v_mfma_f32_16x16x32_bf16 v[168:171], a[212:215], a[168:171], v[168:171]\n"
    "v_mfma_f32_16x16x32_bf16 v[172:175], a[212:215], a[172:175], v[172:175]\n"
    "s_add_u32 s90, s90, 4096\n"
    "ds_read_b64_tr_b16 a[230:231], v5 offset:15424\n"
    "v_mfma_f32_16x16x32_bf16 v[172:175], a[208:211], a[140:143], v[172:175]\n"
    "s_waitcnt lgkmcnt(6)\n"
    "s_add_u32 m0, s91, 126976\n"
    "v_mfma_f32_16x16x32_bf16 v[188:191], a[216:219], a[140:143], v[188:191]\n"
    "s_waitcnt lgkmcnt(4)\n"
    "buffer_load_dwordx4 v9, s[52:55], s90 offen offset:0 sc1 lds\n"
    "ds_read_b64_tr_b16 a[232:233], v4 offset:128\n"
    "v_mfma_f32_16x16x32_bf16 v[188:191], a[220:223], a[172:175], v[188:191]\n"
    "v_mfma_f32_16x16x32_bf16 v[184:187], a[220:223], a[168:171], v[184:187]\n"
    "ds_read_b64_tr_b16 a[234:235], v4 offset:5248\n"
    "v_mfma_f32_16x16x32_bf16 v[184:187], a[216:219], a[136:139], v[184:187]\n"
    "v_mfma_f32_16x16x32_bf16 v[180:183], a[216:219], a[132:135], v[180:183]\n"
    "s_cmp_ge_u32 s35, 10\n"
    "s_cselect_b32 s67, -10, 2\n"
    "s_add_u32 s67, s67, s35\n"
    "ds_read_b64_tr_b16 a[236:237], v4 offset:10368\n"
    "v_mfma_f32_16x16x32_bf16 v[180:183], a[220:223], a[164:167], v[180:183]\n"
    "s_lshl_b32 s67, s67, 12\n"
    "v_mfma_f32_16x16x32_bf16 v[176:179], a[220:223], a[160:163], v[176:179]\n"
    "s_add_u32 s67, s67, s66\n"
    "ds_read_b64_tr_b16 a[238:239], v4 offset:15488\n"
    "v_mfma_f32_16x16x32_bf16 v[176:179], a[216:219], a[128:131], v[176:179]\n"
    "s_waitcnt lgkmcnt(6)\n"
    "s_waitcnt vmcnt(18)\n"
    "s_barrier\n"
    "v_add_u32 v2, 20480, v2\n"
    "v_add_u32 v3, 20480, v3\n"
    "v_mfma_f32_16x16x32_bf16 v[192:195], a[224:227], a[128:131], v[192:195]\n"
    "s_waitcnt lgkmcnt(4)\n"
    "buffer_load_dwordx4 a[80:83], v9, s[52:55], s67 offen offset:0 sc1\n"
    "ds_read_b64_tr_b16 a[192:193], v2 offset:0\n"
    "v_mfma_f32_16x16x32_bf16 v[192:195], a[228:231], a[160:163], v[192:195]\n"
    "v_add_u32 v4, 20480, v4\n"
    "v_add_u32 v5, 20480, v5\n"
    "v_mfma_f32_16x16x32_bf16 v[196:199], a[228:231], a[164:167], v[196:199]\n"
    "buffer_load_dwordx4 a[84:87], v9, s[52:55], s67 offen offset:1024 sc1\n"
    "ds_read_b64_tr_b16 a[194:195], v2 offset:5120\n"
    "v_mfma_f32_16x16x32_bf16 v[196:199], a[224:227], a[132:135], v[196:199]\n"
    "v_mfma_f32_16x16x32_bf16 v[200:203], a[224:227], a[136:139], v[200:203]\n"
    "buffer_load_dwordx4 a[88:91], v9, s[52:55], s67 offen offset:2048 sc1\n"
    "ds_read_b64_tr_b16 a[196:197], v2 offset:10240\n"
    "v_mfma_f32_16x16x32_bf16 v[200:203], a[228:231], a[168:171], v[200:203]\n"
    "v_mfma_f32_16x16x32_bf16 v[204:207], a[228:231], a[172:175], v[204:207]\n"
    "ds_read_b64_tr_b16 a[198:199], v2 offset:15360\n"
    "v_mfma_f32_16x16x32_bf16 v[204:207], a[224:227], a[140:143], v[204:207]\n"
    "s_waitcnt lgkmcnt(6)\n"
    "v_mfma_f32_16x16x32_bf16 v[236:239], a[232:235], a[140:143], v[236:239]\n"
    "s_waitcnt lgkmcnt(4)\n"
    "buffer_load_dwordx4 a[92:95], v9, s[52:55], s67 offen offset:3072 sc1\n"
    "ds_read_b64_tr_b16 a[200:201], v2 offset:20480\n"
    "v_mfma_f32_16x16x32_bf16 v[236:239], a[236:239], a[172:175], v[236:239]\n"
    "ds_read_b64_tr_b16 a[208:209], v2 offset:40960\n"
    "v_mfma_f32_16x16x32_bf16 v[232:235], a[236:239], a[168:171], v[232:235]\n"
    "ds_read_b64_tr_b16 a[202:203], v2 offset:25600\n"
    "v_mfma_f32_16x16x32_bf16 v[232:235], a[232:235], a[136:139], v[232:235]\n"
    "ds_read_b64_tr_b16 a[210:211], v2 offset:46080\n"
    "v_mfma_f32_16x16x32_bf16 v[228:231], a[232:235], a[132:135], v[228:231]\n"
    "ds_read_b64_tr_b16 a[204:205], v2 offset:30720\n"
    "v_mfma_f32_16x16x32_bf16 v[228:231], a[236:239], a[164:167], v[228:231]\n"
    "ds_read_b64_tr_b16 a[212:213], v2 offset:51200\n"
    "v_mfma_f32_16x16x32_bf16 v[224:227], a[236:239], a[160:163], v[224:227]\n"
    "ds_read_b64_tr_b16 a[206:207], v2 offset:35840\n"
    "v_mfma_f32_16x16x32_bf16 v[224:227], a[232:235], a[128:131], v[224:227]\n"
    "s_waitcnt lgkmcnt(9)\n"
    "ds_read_b64_tr_b16 a[214:215], v2 offset:56320\n"
    "v_mfma_f32_16x16x32_bf16 v[64:67], a[192:195], a[144:147], v[64:67]\n"
    "s_waitcnt lgkmcnt(8)\n"
    "ds_read_b64_tr_b16 a[216:217], v3 offset:0\n"
    "v_mfma_f32_16x16x32_bf16 v[64:67], a[196:199], a[176:179], v[64:67]\n"
    "s_waitcnt lgkmcnt(6)\n"
    "ds_read_b64_tr_b16 a[218:219], v3 offset:5120\n"
    "v_mfma_f32_16x16x32_bf16 v[64:67], a[200:203], a[0:3], v[64:67]\n"
    "s_waitcnt lgkmcnt(3)\n"
    "s_cmp_ge_u32 s35, 10\n"
    "s_cselect_b32 s90, -10, 2\n"
    "s_add_u32 s90, s90, s35\n"
    "v_mfma_f32_16x16x32_bf16 v[64:67], a[204:207], a[32:35], v[64:67]\n"
    "s_mul_i32 s90, s90, 20480\n"
    "ds_read_b64_tr_b16 a[220:221], v3 offset:10240\n"
    "v_mfma_f32_16x16x32_bf16 v[64:67], a[208:211], a[64:67], v[64:67]\n"
    "s_waitcnt lgkmcnt(3)\n"
    "s_add_u32 s90, s90, s71\n"
    "ds_read_b64_tr_b16 a[222:223], v3 offset:15360\n"
    "v_mfma_f32_16x16x32_bf16 v[64:67], a[212:215], a[96:99], v[64:67]\n"
    "s_add_u32 m0, s91, 143360\n"
    "v_mfma_f32_16x16x32_bf16 v[68:71], a[212:215], a[100:103], v[68:71]\n"
    "buffer_load_dwordx4 v9, s[52:55], s90 offen offset:0 sc1 lds\n"
    "ds_read_b64_tr_b16 a[224:225], v3 offset:20480\n"
    "v_mfma_f32_16x16x32_bf16 v[68:71], a[208:211], a[68:71], v[68:71]\n"
    "buffer_load_dwordx4 v9, s[52:55], s90 offen offset:1024 sc1 lds\n"
    "ds_read_b64_tr_b16 a[226:227], v3 offset:25600\n"
    "v_mfma_f32_16x16x32_bf16 v[68:71], a[204:207], a[36:39], v[68:71]\n"
    "v_lshrrev_b32 v16, 6, v9\n"
    "v_mul_u32_u24 v16, 160, v16\n"
    "v_mfma_f32_16x16x32_bf16 v[68:71], a[200:203], a[4:7], v[68:71]\n"
    "ds_read_b64_tr_b16 a[228:229], v3 offset:30720\n"
    "v_bfe_u32 v17, v9, 4, 2\n"
    "v_mfma_f32_16x16x32_bf16 v[68:71], a[196:199], a[180:183], v[68:71]\n"
    "buffer_load_dwordx4 v9, s[52:55], s90 offen offset:2048 sc1 lds\n"
    "ds_read_b64_tr_b16 a[230:231], v3 offset:35840\n"
    "v_mfma_f32_16x16x32_bf16 v[68:71], a[192:195], a[148:151], v[68:71]\n"
    "v_lshl_add_u32 v16, v17, 3, v16\n"
    "v_add_u32 v60, 102400, v16\n"
    "v_mfma_f32_16x16x32_bf16 v[72:75], a[192:195], a[152:155], v[72:75]\n"
    "ds_read_b64_tr_b16 a[232:233], v3 offset:40960\n"
    "v_lshrrev_b32 v16, 4, v9\n"
    "v_mfma_f32_16x16x32_bf16 v[72:75], a[196:199], a[184:187], v[72:75]\n"
    "ds_read_b64_tr_b16 a[234:235], v3 offset:46080\n"
    "v_add_u32 v16, 0, v16\n"
    "v_mfma_f32_16x16x32_bf16 v[72:75], a[200:203], a[8:11], v[72:75]\n"
    "v_mul_u32_u24 v17, 6554, v16\n"
    "v_lshrrev_b32 v17, 16, v17\n"
    "v_mfma_f32_16x16x32_bf16 v[72:75], a[204:207], a[40:43], v[72:75]\n"
    "buffer_load_dwordx4 v9, s[52:55], s90 offen offset:3072 sc1 lds\n"
    "ds_read_b64_tr_b16 a[236:237], v3 offset:51200\n"
    "v_mfma_f32_16x16x32_bf16 v[72:75], a[208:211], a[72:75], v[72:75]\n"
    "ds_read_b64_tr_b16 a[238:239], v3 offset:56320\n"
    "v_mul_u32_u24 v18, 10, v17\n"
    "v_mfma_f32_16x16x32_bf16 v[72:75], a[212:215], a[104:107], v[72:75]\n"
    "v_sub_u32 v16, v16, v18\n"
    "v_mul_u32_u24 v17, 1280, v17\n"
    "v_mfma_f32_16x16x32_bf16 v[76:79], a[212:215], a[108:111], v[76:79]\n"
    "v_lshl_add_u32 v11, v16, 4, v17\n"
    "v_lshrrev_b32 v16, 4, v9\n"
    "v_mfma_f32_16x16x32_bf16 v[76:79], a[208:211], a[76:79], v[76:79]\n"
    "v_add_u32 v16, 64, v16\n"
    "v_mul_u32_u24 v17, 6554, v16\n"
    "v_mfma_f32_16x16x32_bf16 v[76:79], a[204:207], a[44:47], v[76:79]\n"
    "s_add_u32 s90, s90, 4096\n"
    "v_lshrrev_b32 v17, 16, v17\n"
    "v_mul_u32_u24 v18, 10, v17\n"
    "v_mfma_f32_16x16x32_bf16 v[76:79], a[200:203], a[12:15], v[76:79]\n"
    "s_add_u32 m0, s91, 147456\n"
    "v_sub_u32 v16, v16, v18\n"
    "v_mul_u32_u24 v17, 1280, v17\n"
    "v_mfma_f32_16x16x32_bf16 v[76:79], a[196:199], a[188:191], v[76:79]\n"
    "s_waitcnt lgkmcnt(0)\n"
    "buffer_load_dwordx4 v9, s[52:55], s90 offen offset:0 sc1 lds\n"
    "v_lshl_add_u32 v12, v16, 4, v17\n"
    "v_mfma_f32_16x16x32_bf16 v[76:79], a[192:195], a[156:159], v[76:79]\n"
    "v_subrev_u32 v12, 1024, v12\n"
    "v_lshrrev_b32 v16, 4, v9\n"
    "v_mfma_f32_16x16x32_bf16 v[80:83], a[216:219], a[144:147], v[80:83]\n"
    "ds_read_b64_tr_b16 a[192:193], v2 offset:64\n"
    "v_add_u32 v16, 128, v16\n"
    "v_mfma_f32_16x16x32_bf16 v[80:83], a[220:223], a[176:179], v[80:83]\n"
    "ds_read_b64_tr_b16 a[194:195], v2 offset:5184\n"
    "v_mul_u32_u24 v17, 6554, v16\n"
    "v_mfma_f32_16x16x32_bf16 v[80:83], a[224:227], a[0:3], v[80:83]\n"
    "s_cmp_ge_u32 s35, 9\n"
    "s_cselect_b32 s67, -9, 3\n"
    "s_add_u32 s67, s67, s35\n"
    "v_lshrrev_b32 v17, 16, v17\n"
    "v_mul_u32_u24 v18, 10, v17\n"
    "v_mfma_f32_16x16x32_bf16 v[80:83], a[228:231], a[32:35], v[80:83]\n"
    "s_lshl_b32 s67, s67, 12\n"
    "ds_read_b64_tr_b16 a[196:197], v2 offset:10304\n"
    "v_sub_u32 v16, v16, v18\n"
    "v_mfma_f32_16x16x32_bf16 v[80:83], a[232:235], a[64:67], v[80:83]\n"
    "s_add_u32 s67, s67, s66\n"
    "ds_read_b64_tr_b16 a[198:199], v2 offset:15424\n"
    "v_mul_u32_u24 v17, 1280, v17\n"
    "v_mfma_f32_16x16x32_bf16 v[80:83], a[236:239], a[96:99], v[80:83]\n"
    "buffer_load_dwordx4 a[112:115], v9, s[52:55], s67 offen offset:0 sc1\n"
    "v_lshl_add_u32 v13, v16, 4, v17\n"
    "v_mfma_f32_16x16x32_bf16 v[84:87], a[236:239], a[100:103], v[84:87]\n"
    "buffer_load_dwordx4 a[116:119], v9, s[52:55], s67 offen offset:1024 sc1\n"
    "ds_read_b64_tr_b16 a[200:201], v2 offset:20544\n"
    "v_mfma_f32_16x16x32_bf16 v[84:87], a[232:235], a[68:71], v[84:87]\n"
    "ds_read_b64_tr_b16 a[202:203], v2 offset:25664\n"
    "v_subrev_u32 v13, 2048, v13\n"
    "v_mfma_f32_16x16x32_bf16 v[84:87], a[228:231], a[36:39], v[84:87]\n"
    "s_mul_i32 s73, s76, 2560\n"
    "v_mfma_f32_16x16x32_bf16 v[84:87], a[224:227], a[4:7], v[84:87]\n"
    "s_add_u32 s73, s73, 122880\n"
    "ds_read_b64_tr_b16 a[204:205], v2 offset:30784\n"
    "v_add_u32 v27, s73, v9\n"
    "v_mfma_f32_16x16x32_bf16 v[84:87], a[220:223], a[180:183], v[84:87]\n"
    "ds_read_b64_tr_b16 a[206:207], v2 offset:35904\n"
    "v_bfe_u32 v16, v9, 4, 4\n"
    "v_mfma_f32_16x16x32_bf16 v[84:87], a[216:219], a[148:151], v[84:87]\n"
    "buffer_load_dwordx4 a[120:123], v9, s[52:55], s67 offen offset:2048 sc1\n"
    "v_mul_u32_u24 v16, 160, v16\n"
    "v_mfma_f32_16x16x32_bf16 v[88:91], a[216:219], a[152:155], v[88:91]\n"
    "ds_read_b64_tr_b16 a[208:209], v2 offset:41024\n"
    "v_lshrrev_b32 v17, 8, v9\n"
    "v_mfma_f32_16x16x32_bf16 v[88:91], a[220:223], a[184:187], v[88:91]\n"
    "ds_read_b64_tr_b16 a[210:211], v2 offset:46144\n"
    "v_cvt_pk_bf16_f32 v64, v64, v65\n"
    "v_mfma_f32_16x16x32_bf16 v[88:91], a[224:227], a[8:11], v[88:91]\n"
    "v_cvt_pk_bf16_f32 v65, v66, v67\n"
    "v_cvt_pk_bf16_f32 v66, v80, v81\n"
    "v_mfma_f32_16x16x32_bf16 v[88:91], a[228:231], a[40:43], v[88:91]\n"
    "ds_read_b64_tr_b16 a[212:213], v2 offset:51264\n"
    "v_cvt_pk_bf16_f32 v67, v82, v83\n"
    "v_mfma_f32_16x16x32_bf16 v[88:91], a[232:235], a[72:75], v[88:91]\n"
    "buffer_load_dwordx4 a[124:127], v9, s[52:55], s67 offen offset:3072 sc1\n"
    "ds_read_b64_tr_b16 a[214:215], v2 offset:56384\n"
    "v_mfma_f32_16x16x32_bf16 v[88:91], a[236:239], a[104:107], v[88:91]\n"
    "buffer_store_dwordx4 v[64:67], v57, s[52:55], s56 offen offset:0 sc1\n"
    "v_lshl_add_u32 v16, v17, 3, v16\n"
    "v_mfma_f32_16x16x32_bf16 v[92:95], a[236:239], a[108:111], v[92:95]\n"
    "v_add_u32 v26, s73, v16\n"
    "v_mfma_f32_16x16x32_bf16 v[92:95], a[232:235], a[76:79], v[92:95]\n"
    "s_mul_i32 s94, s78, 160\n"
    "v_mfma_f32_16x16x32_bf16 v[92:95], a[228:231], a[44:47], v[92:95]\n"
    "v_mfma_f32_16x16x32_bf16 v[92:95], a[224:227], a[12:15], v[92:95]\n"
    "v_mfma_f32_16x16x32_bf16 v[92:95], a[220:223], a[188:191], v[92:95]\n"
    "s_waitcnt lgkmcnt(0)\n"
    "s_cmp_ge_u32 s35, 9\n"
    "s_cselect_b32 s90, -9, 3\n"
    "s_add_u32 s90, s90, s35\n"
    "v_mfma_f32_16x16x32_bf16 v[92:95], a[216:219], a[156:159], v[92:95]\n"
    "s_barrier\n"
    "v_cvt_pk_bf16_f32 v68, v68, v69\n"
    "v_cvt_pk_bf16_f32 v69, v70, v71\n"
    "v_mfma_f32_16x16x32_bf16 v[96:99], a[192:195], a[144:147], v[96:99]\n"
    "s_mul_i32 s90, s90, 20480\n"
    "ds_read_b64_tr_b16 a[216:217], v3 offset:64\n"
    "v_cvt_pk_bf16_f32 v70, v84, v85\n"
    "v_mfma_f32_16x16x32_bf16 v[96:99], a[196:199], a[176:179], v[96:99]\n"
    "s_add_u32 s90, s90, s71\n"
    "ds_read_b64_tr_b16 a[218:219], v3 offset:5184\n"
    "v_cvt_pk_bf16_f32 v71, v86, v87\n"
    "v_mfma_f32_16x16x32_bf16 v[96:99], a[200:203], a[0:3], v[96:99]\n"
    "s_add_u32 m0, s91, 0\n"
    "buffer_store_dwordx4 v[68:71], v57, s[52:55], s57 offen offset:0 sc1\n"
    "v_mfma_f32_16x16x32_bf16 v[96:99], a[204:207], a[32:35], v[96:99]\n"
    "buffer_load_dwordx4 v9, s[52:55], s90 offen offset:0 sc1 lds\n"
    "ds_read_b64_tr_b16 a[220:221], v3 offset:10304\n"
    "v_mfma_f32_16x16x32_bf16 v[96:99], a[208:211], a[64:67], v[96:99]\n"
    "buffer_load_dwordx4 v9, s[52:55], s90 offen offset:1024 sc1 lds\n"
    "ds_read_b64_tr_b16 a[222:223], v3 offset:15424\n"
    "v_mfma_f32_16x16x32_bf16 v[96:99], a[212:215], a[96:99], v[96:99]\n"
    "v_mfma_f32_16x16x32_bf16 v[100:103], a[212:215], a[100:103], v[100:103]\n"
    "ds_read_b64_tr_b16 a[224:225], v3 offset:20544\n"
    "v_mfma_f32_16x16x32_bf16 v[100:103], a[208:211], a[68:71], v[100:103]\n"
    "buffer_load_dwordx4 v9, s[52:55], s90 offen offset:2048 sc1 lds\n"
    "ds_read_b64_tr_b16 a[226:227], v3 offset:25664\n"
    "v_mfma_f32_16x16x32_bf16 v[100:103], a[204:207], a[36:39], v[100:103]\n"
    "v_cvt_pk_bf16_f32 v72, v72, v73\n"
    "v_cvt_pk_bf16_f32 v73, v74, v75\n"
    "v_mfma_f32_16x16x32_bf16 v[100:103], a[200:203], a[4:7], v[100:103]\n"
    "ds_read_b64_tr_b16 a[228:229], v3 offset:30784\n"
    "v_cvt_pk_bf16_f32 v74, v88, v89\n"
    "v_mfma_f32_16x16x32_bf16 v[100:103], a[196:199], a[180:183], v[100:103]\n"
    "ds_read_b64_tr_b16 a[230:231], v3 offset:35904\n"
    "v_cvt_pk_bf16_f32 v75, v90, v91\n"
    "v_mfma_f32_16x16x32_bf16 v[100:103], a[192:195], a[148:151], v[100:103]\n"
    "buffer_store_dwordx4 v[72:75], v57, s[52:55], s58 offen offset:0 sc1\n"
    "v_mfma_f32_16x16x32_bf16 v[104:107], a[192:195], a[152:155], v[104:107]\n"
    "buffer_load_dwordx4 v9, s[52:55], s90 offen offset:3072 sc1 lds\n"
    "ds_read_b64_tr_b16 a[232:233], v3 offset:41024\n"
    "v_mfma_f32_16x16x32_bf16 v[104:107], a[196:199], a[184:187], v[104:107]\n"
    "ds_read_b64_tr_b16 a[234:235], v3 offset:46144\n"
    "v_mfma_f32_16x16x32_bf16 v[104:107], a[200:203], a[8:11], v[104:107]\n"
    "v_mfma_f32_16x16x32_bf16 v[104:107], a[204:207], a[40:43], v[104:107]\n"
    "ds_read_b64_tr_b16 a[236:237], v3 offset:51264\n"
    "v_mfma_f32_16x16x32_bf16 v[104:107], a[208:211], a[72:75], v[104:107]\n"
    "ds_read_b64_tr_b16 a[238:239], v3 offset:56384\n"
    "v_mfma_f32_16x16x32_bf16 v[104:107], a[212:215], a[104:107], v[104:107]\n"
    "s_add_u32 s90, s90, 4096\n"
    "v_cvt_pk_bf16_f32 v76, v76, v77\n"
    "v_cvt_pk_bf16_f32 v77, v78, v79\n"
    "v_mfma_f32_16x16x32_bf16 v[108:111], a[212:215], a[108:111], v[108:111]\n"
    "s_add_u32 m0, s91, 4096\n"
    "v_cvt_pk_bf16_f32 v78, v92, v93\n"
    "v_cvt_pk_bf16_f32 v79, v94, v95\n"
    "v_mfma_f32_16x16x32_bf16 v[108:111], a[208:211], a[76:79], v[108:111]\n"
    "buffer_load_dwordx4 v9, s[52:55], s90 offen offset:0 sc1 lds\n"
    "v_mfma_f32_16x16x32_bf16 v[108:111], a[204:207], a[44:47], v[108:111]\n"
    "buffer_store_dwordx4 v[76:79], v57, s[52:55], s59 offen offset:0 sc1\n"
    "v_mfma_f32_16x16x32_bf16 v[108:111], a[200:203], a[12:15], v[108:111]\n"
    "s_cmp_ge_u32 s35, 8\n"
    "s_cselect_b32 s67, -8, 4\n"
    "s_add_u32 s67, s67, s35\n"
    "v_mfma_f32_16x16x32_bf16 v[108:111], a[196:199], a[188:191], v[108:111]\n"
    "s_waitcnt lgkmcnt(0)\n"
    "v_mfma_f32_16x16x32_bf16 v[108:111], a[192:195], a[156:159], v[108:111]\n"
    "s_lshl_b32 s67, s67, 12\n"
    "v_mfma_f32_16x16x32_bf16 v[112:115], a[216:219], a[144:147], v[112:115]\n"
    "s_add_u32 s67, s67, s66\n"
    "ds_read_b64_tr_b16 a[192:193], v2 offset:128\n"
    "v_mfma_f32_16x16x32_bf16 v[112:115], a[220:223], a[176:179], v[112:115]\n"
    "buffer_load_dwordx4 a[128:131], v9, s[52:55], s67 offen offset:0 sc1\n"
    "ds_read_b64_tr_b16 a[194:195], v2 offset:5248\n"
    "v_mfma_f32_16x16x32_bf16 v[112:115], a[224:227], a[0:3], v[112:115]\n"
    "v_mfma_f32_16x16x32_bf16 v[112:115], a[228:231], a[32:35], v[112:115]\n"
    "ds_read_b64_tr_b16 a[196:197], v2 offset:10368\n"
    "v_mfma_f32_16x16x32_bf16 v[112:115], a[232:235], a[64:67], v[112:115]\n"
    "ds_read_b64_tr_b16 a[198:199], v2 offset:15488\n"
    "v_mfma_f32_16x16x32_bf16 v[112:115], a[236:239], a[96:99], v[112:115]\n"
    "buffer_load_dwordx4 a[132:135], v9, s[52:55], s67 offen offset:1024 sc1\n"
    "v_mfma_f32_16x16x32_bf16 v[116:119], a[236:239], a[100:103], v[116:119]\n"
    "ds_read_b64_tr_b16 a[200:201], v2 offset:20608\n"
    "v_mfma_f32_16x16x32_bf16 v[116:119], a[232:235], a[68:71], v[116:119]\n"
    "ds_read_b64_tr_b16 a[202:203], v2 offset:25728\n"
    "v_mfma_f32_16x16x32_bf16 v[116:119], a[228:231], a[36:39], v[116:119]\n"
    "v_mfma_f32_16x16x32_bf16 v[116:119], a[224:227], a[4:7], v[116:119]\n"
    "ds_read_b64_tr_b16 a[204:205], v2 offset:30848\n"
    "v_mfma_f32_16x16x32_bf16 v[116:119], a[220:223], a[180:183], v[116:119]\n"
    "ds_read_b64_tr_b16 a[206:207], v2 offset:35968\n"
    "v_mfma_f32_16x16x32_bf16 v[116:119], a[216:219], a[148:151], v[116:119]\n"
    "v_mfma_f32_16x16x32_bf16 v[120:123], a[216:219], a[152:155], v[120:123]\n"
    "ds_read_b64_tr_b16 a[208:209], v2 offset:41088\n"
    "v_mfma_f32_16x16x32_bf16 v[120:123], a[220:223], a[184:187], v[120:123]\n"
    "ds_read_b64_tr_b16 a[210:211], v2 offset:46208\n"
    "v_cvt_pk_bf16_f32 v96, v96, v97\n"
    "v_mfma_f32_16x16x32_bf16 v[120:123], a[224:227], a[8:11], v[120:123]\n"
    "v_cvt_pk_bf16_f32 v97, v98, v99\n"
    "v_cvt_pk_bf16_f32 v98, v112, v113\n"
    "v_mfma_f32_16x16x32_bf16 v[120:123], a[228:231], a[40:43], v[120:123]\n"
    "buffer_load_dwordx4 a[136:139], v9, s[52:55], s67 offen offset:2048 sc1\n"
    "ds_read_b64_tr_b16 a[212:213], v2 offset:51328\n"
    "v_mfma_f32_16x16x32_bf16 v[120:123], a[232:235], a[72:75], v[120:123]\n"
    "ds_read_b64_tr_b16 a[214:215], v2 offset:56448\n"
    "v_cvt_pk_bf16_f32 v99, v114, v115\n"
    "v_mfma_f32_16x16x32_bf16 v[120:123], a[236:239], a[104:107], v[120:123]\n"
    "buffer_store_dwordx4 v[96:99], v57, s[52:55], s56 offen offset:1024 sc1\n"
    "v_mfma_f32_16x16x32_bf16 v[124:127], a[236:239], a[108:111], v[124:127]\n"
    "v_mfma_f32_16x16x32_bf16 v[124:127], a[232:235], a[76:79], v[124:127]\n"
    "v_mfma_f32_16x16x32_bf16 v[124:127], a[228:231], a[44:47], v[124:127]\n"
    "v_mfma_f32_16x16x32_bf16 v[124:127], a[224:227], a[12:15], v[124:127]\n"
    "v_mfma_f32_16x16x32_bf16 v[124:127], a[220:223], a[188:191], v[124:127]\n"
    "s_waitcnt lgkmcnt(0)\n"
    "v_mfma_f32_16x16x32_bf16 v[124:127], a[216:219], a[156:159], v[124:127]\n"
    "v_cvt_pk_bf16_f32 v100, v100, v101\n"
    "v_cvt_pk_bf16_f32 v101, v102, v103\n"
    "v_mfma_f32_16x16x32_bf16 v[128:131], a[192:195], a[144:147], v[128:131]\n"
    "buffer_load_dwordx4 a[140:143], v9, s[52:55], s67 offen offset:3072 sc1\n"
    "ds_read_b64_tr_b16 a[216:217], v4 offset:0\n"
    "v_mfma_f32_16x16x32_bf16 v[128:131], a[196:199], a[176:179], v[128:131]\n"
    "ds_read_b64_tr_b16 a[218:219], v4 offset:5120\n"
    "v_cvt_pk_bf16_f32 v102, v116, v117\n"
    "v_mfma_f32_16x16x32_bf16 v[128:131], a[200:203], a[0:3], v[128:131]\n"
    "v_cvt_pk_bf16_f32 v103, v118, v119\n"
    "v_mfma_f32_16x16x32_bf16 v[128:131], a[204:207], a[32:35], v[128:131]\n"
    "buffer_store_dwordx4 v[100:103], v57, s[52:55], s57 offen offset:1024 sc1\n"
    "ds_read_b64_tr_b16 a[220:221], v4 offset:10240\n"
    "v_mfma_f32_16x16x32_bf16 v[128:131], a[208:211], a[64:67], v[128:131]\n"
    "ds_read_b64_tr_b16 a[222:223], v4 offset:15360\n"
    "v_mfma_f32_16x16x32_bf16 v[128:131], a[212:215], a[96:99], v[128:131]\n"
    "v_mfma_f32_16x16x32_bf16 v[132:135], a[212:215], a[100:103], v[132:135]\n"
    "ds_read_b64_tr_b16 a[224:225], v4 offset:20480\n"
    "v_mfma_f32_16x16x32_bf16 v[132:135], a[208:211], a[68:71], v[132:135]\n"
    "ds_read_b64_tr_b16 a[226:227], v4 offset:25600\n"
    "v_mfma_f32_16x16x32_bf16 v[132:135], a[204:207], a[36:39], v[132:135]\n"
    "v_mfma_f32_16x16x32_bf16 v[132:135], a[200:203], a[4:7], v[132:135]\n"
    "ds_read_b64_tr_b16 a[228:229], v4 offset:30720\n"
    "v_cvt_pk_bf16_f32 v104, v104, v105\n"
    "v_mfma_f32_16x16x32_bf16 v[132:135], a[196:199], a[180:183], v[132:135]\n"
    "ds_read_b64_tr_b16 a[230:231], v4 offset:35840\n"
    "v_cvt_pk_bf16_f32 v105, v106, v107\n"
    "v_mfma_f32_16x16x32_bf16 v[132:135], a[192:195], a[148:151], v[132:135]\n"
    "s_cmp_ge_u32 s35, 7\n"
    "s_cselect_b32 s67, -7, 5\n"
    "s_add_u32 s67, s67, s35\n"
    "v_cvt_pk_bf16_f32 v106, v120, v121\n"
    "v_cvt_pk_bf16_f32 v107, v122, v123\n"
    "v_mfma_f32_16x16x32_bf16 v[136:139], a[192:195], a[152:155], v[136:139]\n"
    "s_lshl_b32 s67, s67, 12\n"
    "buffer_store_dwordx4 v[104:107], v57, s[52:55], s58 offen offset:1024 sc1\n"
    "ds_read_b64_tr_b16 a[232:233], v4 offset:40960\n"
    "v_mfma_f32_16x16x32_bf16 v[136:139], a[196:199], a[184:187], v[136:139]\n"
    "s_add_u32 s67, s67, s66\n"
    "ds_read_b64_tr_b16 a[234:235], v4 offset:46080\n"
    "v_mfma_f32_16x16x32_bf16 v[136:139], a[200:203], a[8:11], v[136:139]\n"
    "buffer_load_dwordx4 a[160:163], v9, s[52:55], s67 offen offset:0 sc1\n"
    "v_mfma_f32_16x16x32_bf16 v[136:139], a[204:207], a[40:43], v[136:139]\n"
    "ds_read_b64_tr_b16 a[236:237], v4 offset:51200\n"
    "v_mfma_f32_16x16x32_bf16 v[136:139], a[208:211], a[72:75], v[136:139]\n"
    "ds_read_b64_tr_b16 a[238:239], v4 offset:56320\n"
    "v_mfma_f32_16x16x32_bf16 v[136:139], a[212:215], a[104:107], v[136:139]\n"
    "v_mfma_f32_16x16x32_bf16 v[140:143], a[212:215], a[108:111], v[140:143]\n"
    "v_cvt_pk_bf16_f32 v108, v108, v109\n"
    "v_cvt_pk_bf16_f32 v109, v110, v111\n"
    "v_mfma_f32_16x16x32_bf16 v[140:143], a[208:211], a[76:79], v[140:143]\n"
    "buffer_load_dwordx4 a[164:167], v9, s[52:55], s67 offen offset:1024 sc1\n"
    "v_cvt_pk_bf16_f32 v110, v124, v125\n"
    "v_mfma_f32_16x16x32_bf16 v[140:143], a[204:207], a[44:47], v[140:143]\n"
    "v_cvt_pk_bf16_f32 v111, v126, v127\n"
    "v_mfma_f32_16x16x32_bf16 v[140:143], a[200:203], a[12:15], v[140:143]\n"
    "buffer_store_dwordx4 v[108:111], v57, s[52:55], s59 offen offset:1024 sc1\n"
    "v_mfma_f32_16x16x32_bf16 v[140:143], a[196:199], a[188:191], v[140:143]\n"
    "s_waitcnt lgkmcnt(0)\n"
    "v_mfma_f32_16x16x32_bf16 v[140:143], a[192:195], a[156:159], v[140:143]\n"
    "v_cvt_pk_bf16_f32 v128, v128, v129\n"
    "v_cvt_pk_bf16_f32 v129, v130, v131\n"
    "v_mfma_f32_16x16x32_bf16 v[144:147], a[216:219], a[144:147], v[144:147]\n"
    "buffer_store_dwordx2 v[128:129], v6, s[52:55], s56 offen sc1\n"
    "ds_read_b64_tr_b16 a[192:193], v5 offset:0\n"
    "v_mfma_f32_16x16x32_bf16 v[144:147], a[220:223], a[176:179], v[144:147]\n"
    "ds_read_b64_tr_b16 a[194:195], v5 offset:5120\n"
    "v_mfma_f32_16x16x32_bf16 v[144:147], a[224:227], a[0:3], v[144:147]\n"
    "v_cvt_pk_bf16_f32 v132, v132, v133\n"
    "v_cvt_pk_bf16_f32 v133, v134, v135\n"
    "v_mfma_f32_16x16x32_bf16 v[144:147], a[228:231], a[32:35], v[144:147]\n"
    "buffer_store_dwordx2 v[132:133], v6, s[52:55], s57 offen sc1\n"
    "ds_read_b64_tr_b16 a[196:197], v5 offset:10240\n"
    "v_mfma_f32_16x16x32_bf16 v[144:147], a[232:235], a[64:67], v[144:147]\n"
    "ds_read_b64_tr_b16 a[198:199], v5 offset:15360\n"
    "v_mfma_f32_16x16x32_bf16 v[144:147], a[236:239], a[96:99], v[144:147]\n"
    "buffer_load_dwordx4 a[168:171], v9, s[52:55], s67 offen offset:2048 sc1\n"
    "v_cvt_pk_bf16_f32 v136, v136, v137\n"
    "v_mfma_f32_16x16x32_bf16 v[148:151], a[236:239], a[100:103], v[148:151]\n"
    "ds_read_b64_tr_b16 a[200:201], v5 offset:20480\n"
    "v_cvt_pk_bf16_f32 v137, v138, v139\n"
    "v_mfma_f32_16x16x32_bf16 v[148:151], a[232:235], a[68:71], v[148:151]\n"
    "buffer_store_dwordx2 v[136:137], v6, s[52:55], s58 offen sc1\n"
    "ds_read_b64_tr_b16 a[202:203], v5 offset:25600\n"
    "v_mfma_f32_16x16x32_bf16 v[148:151], a[228:231], a[36:39], v[148:151]\n"
    "v_mfma_f32_16x16x32_bf16 v[148:151], a[224:227], a[4:7], v[148:151]\n"
    "ds_read_b64_tr_b16 a[204:205], v5 offset:30720\n"
    "v_cvt_pk_bf16_f32 v140, v140, v141\n"
    "v_mfma_f32_16x16x32_bf16 v[148:151], a[220:223], a[180:183], v[148:151]\n"
    "ds_read_b64_tr_b16 a[206:207], v5 offset:35840\n"
    "v_cvt_pk_bf16_f32 v141, v142, v143\n"
    "v_mfma_f32_16x16x32_bf16 v[148:151], a[216:219], a[148:151], v[148:151]\n"
    "buffer_store_dwordx2 v[140:141], v6, s[52:55], s59 offen sc1\n"
    "v_mfma_f32_16x16x32_bf16 v[152:155], a[216:219], a[152:155], v[152:155]\n"
    "ds_read_b64_tr_b16 a[208:209], v5 offset:40960\n"
    "v_mfma_f32_16x16x32_bf16 v[152:155], a[220:223], a[184:187], v[152:155]\n"
    "ds_read_b64_tr_b16 a[210:211], v5 offset:46080\n"
    "v_mfma_f32_16x16x32_bf16 v[152:155], a[224:227], a[8:11], v[152:155]\n"
    "buffer_load_dwordx4 a[172:175], v9, s[52:55], s67 offen offset:3072 sc1\n"
    "v_mfma_f32_16x16x32_bf16 v[152:155], a[228:231], a[40:43], v[152:155]\n"
    "ds_read_b64_tr_b16 a[212:213], v5 offset:51200\n"
    "v_mfma_f32_16x16x32_bf16 v[152:155], a[232:235], a[72:75], v[152:155]\n"
    "ds_read_b64_tr_b16 a[214:215], v5 offset:56320\n"
    "v_mfma_f32_16x16x32_bf16 v[152:155], a[236:239], a[104:107], v[152:155]\n"
    "v_mfma_f32_16x16x32_bf16 v[156:159], a[236:239], a[108:111], v[156:159]\n"
    "v_mfma_f32_16x16x32_bf16 v[156:159], a[232:235], a[76:79], v[156:159]\n"
    "v_mfma_f32_16x16x32_bf16 v[156:159], a[228:231], a[44:47], v[156:159]\n"
    "v_mfma_f32_16x16x32_bf16 v[156:159], a[224:227], a[12:15], v[156:159]\n"
    "v_mfma_f32_16x16x32_bf16 v[156:159], a[220:223], a[188:191], v[156:159]\n"
    "s_waitcnt lgkmcnt(0)\n"
    "v_mfma_f32_16x16x32_bf16 v[156:159], a[216:219], a[156:159], v[156:159]\n"
    "s_waitcnt vmcnt(43)\n"
    "v_mfma_f32_16x16x32_bf16 v[160:163], a[192:195], a[144:147], v[160:163]\n"
    "ds_read_b64_tr_b16 a[216:217], v4 offset:64\n"
    "v_mfma_f32_16x16x32_bf16 v[160:163], a[196:199], a[176:179], v[160:163]\n"
    "s_cmp_ge_u32 s35, 8\n"
    "s_cselect_b32 s90, -8, 4\n"
    "s_add_u32 s90, s90, s35\n"
    "ds_read_b64_tr_b16 a[218:219], v4 offset:5184\n"
    "v_mfma_f32_16x16x32_bf16 v[160:163], a[200:203], a[0:3], v[160:163]\n"
    "s_mul_i32 s90, s90, 20480\n"
    "v_mfma_f32_16x16x32_bf16 v[160:163], a[204:207], a[32:35], v[160:163]\n"
    "s_add_u32 s90, s90, s71\n"
    "ds_read_b64_tr_b16 a[220:221], v4 offset:10304\n"
    "v_mfma_f32_16x16x32_bf16 v[160:163], a[208:211], a[64:67], v[160:163]\n"
    "s_add_u32 m0, s91, 20480\n"
    "ds_read_b64_tr_b16 a[222:223], v4 offset:15424\n"
    "v_mfma_f32_16x16x32_bf16 v[160:163], a[212:215], a[96:99], v[160:163]\n"
    "buffer_load_dwordx4 v9, s[52:55], s90 offen offset:0 sc1 lds\n"
    "v_mfma_f32_16x16x32_bf16 v[164:167], a[212:215], a[100:103], v[164:167]\n"
    "ds_read_b64_tr_b16 a[224:225], v4 offset:20544\n"
    "v_mfma_f32_16x16x32_bf16 v[164:167], a[208:211], a[68:71], v[164:167]\n"
    "ds_read_b64_tr_b16 a[226:227], v4 offset:25664\n"
    "v_mfma_f32_16x16x32_bf16 v[164:167], a[204:207], a[36:39], v[164:167]\n"
    "v_mfma_f32_16x16x32_bf16 v[164:167], a[200:203], a[4:7], v[164:167]\n"
    "buffer_load_dwordx4 v9, s[52:55], s90 offen offset:1024 sc1 lds\n"
    "ds_read_b64_tr_b16 a[228:229], v4 offset:30784\n"
    "v_mfma_f32_16x16x32_bf16 v[164:167], a[196:199], a[180:183], v[164:167]\n"
    "ds_read_b64_tr_b16 a[230:231], v4 offset:35904\n"
    "v_mfma_f32_16x16x32_bf16 v[164:167], a[192:195], a[148:151], v[164:167]\n"
    "v_mfma_f32_16x16x32_bf16 v[168:171], a[192:195], a[152:155], v[168:171]\n"
    "ds_read_b64_tr_b16 a[232:233], v4 offset:41024\n"
    "v_mfma_f32_16x16x32_bf16 v[168:171], a[196:199], a[184:187], v[168:171]\n"
    "ds_read_b64_tr_b16 a[234:235], v4 offset:46144\n"
    "v_cvt_pk_bf16_f32 v144, v144, v145\n"
    "v_mfma_f32_16x16x32_bf16 v[168:171], a[200:203], a[8:11], v[168:171]\n"
    "v_cvt_pk_bf16_f32 v145, v146, v147\n"
    "v_cvt_pk_bf16_f32 v146, v160, v161\n"
    "v_mfma_f32_16x16x32_bf16 v[168:171], a[204:207], a[40:43], v[168:171]\n"
    "ds_read_b64_tr_b16 a[236:237], v4 offset:51264\n"
    "v_cvt_pk_bf16_f32 v147, v162, v163\n"
    "v_mfma_f32_16x16x32_bf16 v[168:171], a[208:211], a[72:75], v[168:171]\n"
    "buffer_store_dwordx4 v[144:147], v58, s[52:55], s56 offen offset:0 sc1\n"
    "ds_read_b64_tr_b16 a[238:239], v4 offset:56384\n"
    "v_mfma_f32_16x16x32_bf16 v[168:171], a[212:215], a[104:107], v[168:171]\n"
    "v_mfma_f32_16x16x32_bf16 v[172:175], a[212:215], a[108:111], v[172:175]\n"
    "v_mfma_f32_16x16x32_bf16 v[172:175], a[208:211], a[76:79], v[172:175]\n"
    "buffer_load_dwordx4 v9, s[52:55], s90 offen offset:2048 sc1 lds\n"
    "v_mfma_f32_16x16x32_bf16 v[172:175], a[204:207], a[44:47], v[172:175]\n"
    "v_mfma_f32_16x16x32_bf16 v[172:175], a[200:203], a[12:15], v[172:175]\n"
    "v_mfma_f32_16x16x32_bf16 v[172:175], a[196:199], a[188:191], v[172:175]\n"
    "s_waitcnt lgkmcnt(0)\n"
    "v_cvt_pk_bf16_f32 v148, v148, v149\n"
    "v_cvt_pk_bf16_f32 v149, v150, v151\n"
    "v_mfma_f32_16x16x32_bf16 v[172:175], a[192:195], a[156:159], v[172:175]\n"
    "v_cvt_pk_bf16_f32 v150, v164, v165\n"
    "v_cvt_pk_bf16_f32 v151, v166, v167\n"
    "v_mfma_f32_16x16x32_bf16 v[176:179], a[216:219], a[144:147], v[176:179]\n"
    "buffer_store_dwordx4 v[148:151], v58, s[52:55], s57 offen offset:0 sc1\n"
    "ds_read_b64_tr_b16 a[192:193], v5 offset:64\n"
    "v_mfma_f32_16x16x32_bf16 v[176:179], a[220:223], a[176:179], v[176:179]\n"
    "ds_read_b64_tr_b16 a[194:195], v5 offset:5184\n"
    "v_mfma_f32_16x16x32_bf16 v[176:179], a[224:227], a[0:3], v[176:179]\n"
    "v_mfma_f32_16x16x32_bf16 v[176:179], a[228:231], a[32:35], v[176:179]\n"
    "ds_read_b64_tr_b16 a[196:197], v5 offset:10304\n"
    "v_mfma_f32_16x16x32_bf16 v[176:179], a[232:235], a[64:67], v[176:179]\n"
    "buffer_load_dwordx4 v9, s[52:55], s90 offen offset:3072 sc1 lds\n"
    "ds_read_b64_tr_b16 a[198:199], v5 offset:15424\n"
    "v_mfma_f32_16x16x32_bf16 v[176:179], a[236:239], a[96:99], v[176:179]\n"
    "v_mfma_f32_16x16x32_bf16 v[180:183], a[236:239], a[100:103], v[180:183]\n"
    "ds_read_b64_tr_b16 a[200:201], v5 offset:20544\n"
    "v_cvt_pk_bf16_f32 v152, v152, v153\n"
    "v_mfma_f32_16x16x32_bf16 v[180:183], a[232:235], a[68:71], v[180:183]\n"
    "ds_read_b64_tr_b16 a[202:203], v5 offset:25664\n"
    "v_cvt_pk_bf16_f32 v153, v154, v155\n"
    "v_mfma_f32_16x16x32_bf16 v[180:183], a[228:231], a[36:39], v[180:183]\n"
    "v_cvt_pk_bf16_f32 v154, v168, v169\n"
    "v_cvt_pk_bf16_f32 v155, v170, v171\n"
    "v_mfma_f32_16x16x32_bf16 v[180:183], a[224:227], a[4:7], v[180:183]\n"
    "buffer_store_dwordx4 v[152:155], v58, s[52:55], s58 offen offset:0 sc1\n"
    "ds_read_b64_tr_b16 a[204:205], v5 offset:30784\n"
    "v_mfma_f32_16x16x32_bf16 v[180:183], a[220:223], a[180:183], v[180:183]\n"
    "ds_read_b64_tr_b16 a[206:207], v5 offset:35904\n"
    "v_mfma_f32_16x16x32_bf16 v[180:183], a[216:219], a[148:151], v[180:183]\n"
    "v_mfma_f32_16x16x32_bf16 v[184:187], a[216:219], a[152:155], v[184:187]\n"
    "ds_read_b64_tr_b16 a[208:209], v5 offset:41024\n"
    "v_mfma_f32_16x16x32_bf16 v[184:187], a[220:223], a[184:187], v[184:187]\n"
    "ds_read_b64_tr_b16 a[210:211], v5 offset:46144\n"
    "v_mfma_f32_16x16x32_bf16 v[184:187], a[224:227], a[8:11], v[184:187]\n"
    "s_add_u32 s90, s90, 4096\n"
    "v_mfma_f32_16x16x32_bf16 v[184:187], a[228:231], a[40:43], v[184:187]\n"
    "s_add_u32 m0, s91, 24576\n"
    "ds_read_b64_tr_b16 a[212:213], v5 offset:51264\n"
    "v_cvt_pk_bf16_f32 v156, v156, v157\n"
    "v_mfma_f32_16x16x32_bf16 v[184:187], a[232:235], a[72:75], v[184:187]\n"
    "buffer_load_dwordx4 v9, s[52:55], s90 offen offset:0 sc1 lds\n"
    "ds_read_b64_tr_b16 a[214:215], v5 offset:56384\n"
    "v_mfma_f32_16x16x32_bf16 v[184:187], a[236:239], a[104:107], v[184:187]\n"
    "v_cvt_pk_bf16_f32 v157, v158, v159\n"
    "v_cvt_pk_bf16_f32 v158, v172, v173\n"
    "v_mfma_f32_16x16x32_bf16 v[188:191], a[236:239], a[108:111], v[188:191]\n"
    "v_cvt_pk_bf16_f32 v159, v174, v175\n"
    "v_mfma_f32_16x16x32_bf16 v[188:191], a[232:235], a[76:79], v[188:191]\n"
    "buffer_store_dwordx4 v[156:159], v58, s[52:55], s59 offen offset:0 sc1\n"
    "v_mfma_f32_16x16x32_bf16 v[188:191], a[228:231], a[44:47], v[188:191]\n"
    "v_mfma_f32_16x16x32_bf16 v[188:191], a[224:227], a[12:15], v[188:191]\n"
    "v_mfma_f32_16x16x32_bf16 v[188:191], a[220:223], a[188:191], v[188:191]\n"
    "s_waitcnt lgkmcnt(0)\n"
    "v_mfma_f32_16x16x32_bf16 v[188:191], a[216:219], a[156:159], v[188:191]\n"
    "v_mfma_f32_16x16x32_bf16 v[192:195], a[192:195], a[144:147], v[192:195]\n"
    "s_cmp_ge_u32 s35, 6\n"
    "s_cselect_b32 s67, -6, 6\n"
    "s_add_u32 s67, s67, s35\n"
    "ds_read_b64_tr_b16 a[216:217], v4 offset:128\n"
    "v_mfma_f32_16x16x32_bf16 v[192:195], a[196:199], a[176:179], v[192:195]\n"
    "s_lshl_b32 s67, s67, 12\n"
    "ds_read_b64_tr_b16 a[218:219], v4 offset:5248\n"
    "v_mfma_f32_16x16x32_bf16 v[192:195], a[200:203], a[0:3], v[192:195]\n"
    "s_add_u32 s67, s67, s66\n"
    "v_mfma_f32_16x16x32_bf16 v[192:195], a[204:207], a[32:35], v[192:195]\n"
    "buffer_load_dwordx4 a[240:243], v9, s[52:55], s67 offen offset:0 sc1\n"
    "ds_read_b64_tr_b16 a[220:221], v4 offset:10368\n"
    "v_mfma_f32_16x16x32_bf16 v[192:195], a[208:211], a[64:67], v[192:195]\n"
    "ds_read_b64_tr_b16 a[222:223], v4 offset:15488\n"
    "v_mfma_f32_16x16x32_bf16 v[192:195], a[212:215], a[96:99], v[192:195]\n"
    "v_mfma_f32_16x16x32_bf16 v[196:199], a[212:215], a[100:103], v[196:199]\n"
    "ds_read_b64_tr_b16 a[224:225], v4 offset:20608\n"
    "v_mfma_f32_16x16x32_bf16 v[196:199], a[208:211], a[68:71], v[196:199]\n"
    "ds_read_b64_tr_b16 a[226:227], v4 offset:25728\n"
    "v_mfma_f32_16x16x32_bf16 v[196:199], a[204:207], a[36:39], v[196:199]\n"
    "v_mfma_f32_16x16x32_bf16 v[196:199], a[200:203], a[4:7], v[196:199]\n"
    "buffer_load_dwordx4 a[244:247], v9, s[52:55], s67 offen offset:1024 sc1\n"
    "ds_read_b64_tr_b16 a[228:229], v4 offset:30848\n"
    "v_mfma_f32_16x16x32_bf16 v[196:199], a[196:199], a[180:183], v[196:199]\n"
    "ds_read_b64_tr_b16 a[230:231], v4 offset:35968\n"
    "v_mfma_f32_16x16x32_bf16 v[196:199], a[192:195], a[148:151], v[196:199]\n"
    "v_mfma_f32_16x16x32_bf16 v[200:203], a[192:195], a[152:155], v[200:203]\n"
    "ds_read_b64_tr_b16 a[232:233], v4 offset:41088\n"
    "v_mfma_f32_16x16x32_bf16 v[200:203], a[196:199], a[184:187], v[200:203]\n"
    "ds_read_b64_tr_b16 a[234:235], v4 offset:46208\n"
    "v_cvt_pk_bf16_f32 v176, v176, v177\n"
    "v_mfma_f32_16x16x32_bf16 v[200:203], a[200:203], a[8:11], v[200:203]\n"
    "v_cvt_pk_bf16_f32 v177, v178, v179\n"
    "v_cvt_pk_bf16_f32 v178, v192, v193\n"
    "v_mfma_f32_16x16x32_bf16 v[200:203], a[204:207], a[40:43], v[200:203]\n"
    "ds_read_b64_tr_b16 a[236:237], v4 offset:51328\n"
    "v_cvt_pk_bf16_f32 v179, v194, v195\n"
    "v_mfma_f32_16x16x32_bf16 v[200:203], a[208:211], a[72:75], v[200:203]\n"
    "buffer_store_dwordx4 v[176:179], v58, s[52:55], s56 offen offset:1024 sc1\n"
    "ds_read_b64_tr_b16 a[238:239], v4 offset:56448\n"
    "v_mfma_f32_16x16x32_bf16 v[200:203], a[212:215], a[104:107], v[200:203]\n"
    "v_mfma_f32_16x16x32_bf16 v[204:207], a[212:215], a[108:111], v[204:207]\n"
    "buffer_load_dwordx4 a[248:251], v9, s[52:55], s67 offen offset:2048 sc1\n"
    "v_mfma_f32_16x16x32_bf16 v[204:207], a[208:211], a[76:79], v[204:207]\n"
    "v_mfma_f32_16x16x32_bf16 v[204:207], a[204:207], a[44:47], v[204:207]\n"
    "v_mfma_f32_16x16x32_bf16 v[204:207], a[200:203], a[12:15], v[204:207]\n"
    "v_mfma_f32_16x16x32_bf16 v[204:207], a[196:199], a[188:191], v[204:207]\n"
    "s_waitcnt lgkmcnt(0)\n"
    "v_cvt_pk_bf16_f32 v180, v180, v181\n"
    "v_cvt_pk_bf16_f32 v181, v182, v183\n"
    "v_mfma_f32_16x16x32_bf16 v[204:207], a[192:195], a[156:159], v[204:207]\n"
    "s_waitcnt vmcnt(14)\n"
    "s_barrier\n"
    "v_cvt_pk_bf16_f32 v182, v196, v197\n"
    "v_cvt_pk_bf16_f32 v183, v198, v199\n"
    "v_mfma_f32_16x16x32_bf16 v[224:227], a[216:219], a[144:147], v[224:227]\n"
    "buffer_store_dwordx4 v[180:183], v58, s[52:55], s57 offen offset:1024 sc1\n"
    "ds_read_b64_tr_b16 a[192:193], v60 offset:0\n"
    "v_mfma_f32_16x16x32_bf16 v[224:227], a[220:223], a[176:179], v[224:227]\n"
    "ds_read_b64_tr_b16 a[194:195], v60 offset:2560\n"
    "v_mfma_f32_16x16x32_bf16 v[224:227], a[224:227], a[0:3], v[224:227]\n"
    "s_lshl_b32 s73, s78, 2\n"
    "s_add_u32 s73, s73, s79\n"
    "s_lshl_b32 s73, s73, 3\n"
    "s_add_u32 s73, s73, s77\n"
    "s_lshl_b32 s95, s73, 6\n"
    "s_add_u32 s95, s95, 102400\n"
    "v_mov_b32 v62, s80\n"
    "v_mov_b32 v63, s81\n"
    "buffer_store_dwordx2 v[62:63], v10, s[52:55], s95 offen sc1\n"
    "v_mfma_f32_16x16x32_bf16 v[224:227], a[228:231], a[32:35], v[224:227]\n"
    "ds_read_b64_tr_b16 a[196:197], v60 offset:5120\n"
    "v_mfma_f32_16x16x32_bf16 v[224:227], a[232:235], a[64:67], v[224:227]\n"
    "buffer_load_dwordx4 a[252:255], v9, s[52:55], s67 offen offset:3072 sc1\n"
    "ds_read_b64_tr_b16 a[198:199], v60 offset:7680\n"
    "v_mfma_f32_16x16x32_bf16 v[224:227], a[236:239], a[96:99], v[224:227]\n"
    "v_mfma_f32_16x16x32_bf16 v[228:231], a[236:239], a[100:103], v[228:231]\n"
    "ds_read_b64_tr_b16 a[200:201], v60 offset:32\n"
    "v_cvt_pk_bf16_f32 v184, v184, v185\n"
    "v_cvt_pk_bf16_f32 v185, v186, v187\n"
    "v_mfma_f32_16x16x32_bf16 v[228:231], a[232:235], a[68:71], v[228:231]\n"
    "ds_read_b64_tr_b16 a[202:203], v60 offset:2592\n"
    "v_cvt_pk_bf16_f32 v186, v200, v201\n"
    "v_cvt_pk_bf16_f32 v187, v202, v203\n"
    "v_mfma_f32_16x16x32_bf16 v[228:231], a[228:231], a[36:39], v[228:231]\n"
    "buffer_store_dwordx4 v[184:187], v58, s[52:55], s58 offen offset:1024 sc1\n"
    "v_mfma_f32_16x16x32_bf16 v[228:231], a[224:227], a[4:7], v[228:231]\n"
    "ds_read_b64_tr_b16 a[204:205], v60 offset:5152\n"
    "v_mfma_f32_16x16x32_bf16 v[228:231], a[220:223], a[180:183], v[228:231]\n"
    "ds_read_b64_tr_b16 a[206:207], v60 offset:7712\n"
    "v_mfma_f32_16x16x32_bf16 v[228:231], a[216:219], a[148:151], v[228:231]\n"
    "v_mfma_f32_16x16x32_bf16 v[232:235], a[216:219], a[152:155], v[232:235]\n"
    "ds_read_b64_tr_b16 a[208:209], v60 offset:64\n"
    "v_mfma_f32_16x16x32_bf16 v[232:235], a[220:223], a[184:187], v[232:235]\n"
    "ds_read_b64_tr_b16 a[210:211], v60 offset:2624\n"
    "v_mfma_f32_16x16x32_bf16 v[232:235], a[224:227], a[8:11], v[232:235]\n"
    "v_cvt_pk_bf16_f32 v188, v188, v189\n"
    "v_cvt_pk_bf16_f32 v189, v190, v191\n"
    "v_mfma_f32_16x16x32_bf16 v[232:235], a[228:231], a[40:43], v[232:235]\n"
    "ds_read_b64_tr_b16 a[212:213], v60 offset:5184\n"
    "v_cvt_pk_bf16_f32 v190, v204, v205\n"
    "v_cvt_pk_bf16_f32 v191, v206, v207\n"
    "v_mfma_f32_16x16x32_bf16 v[232:235], a[232:235], a[72:75], v[232:235]\n"
    "buffer_store_dwordx4 v[188:191], v58, s[52:55], s59 offen offset:1024 sc1\n"
    "ds_read_b64_tr_b16 a[214:215], v60 offset:7744\n"
    "v_mfma_f32_16x16x32_bf16 v[232:235], a[236:239], a[104:107], v[232:235]\n"
    "v_mfma_f32_16x16x32_bf16 v[236:239], a[236:239], a[108:111], v[236:239]\n"
    "v_cvt_pk_bf16_f32 v224, v224, v225\n"
    "v_cvt_pk_bf16_f32 v225, v226, v227\n"
    "v_mfma_f32_16x16x32_bf16 v[236:239], a[232:235], a[76:79], v[236:239]\n"
    "buffer_store_dwordx2 v[224:225], v7, s[52:55], s56 offen sc1\n"
    "v_mfma_f32_16x16x32_bf16 v[236:239], a[228:231], a[44:47], v[236:239]\n"
    "v_mfma_f32_16x16x32_bf16 v[236:239], a[224:227], a[12:15], v[236:239]\n"
    "v_cvt_pk_bf16_f32 v228, v228, v229\n"
    "v_cvt_pk_bf16_f32 v229, v230, v231\n"
    "v_mfma_f32_16x16x32_bf16 v[236:239], a[220:223], a[188:191], v[236:239]\n"
    "buffer_store_dwordx2 v[228:229], v7, s[52:55], s57 offen sc1\n"
    "v_mfma_f32_16x16x32_bf16 v[236:239], a[216:219], a[156:159], v[236:239]\n"
    "L_g4:\n"
    "s_waitcnt lgkmcnt(10)\n"
    "v_mfma_f32_16x16x32_bf16 v[208:211], a[192:195], a[16:19], 0\n"
    "s_waitcnt lgkmcnt(8)\n"
    "ds_read_b64_tr_b16 a[216:217], v60 offset:96\n"
    "ds_read_b64_tr_b16 a[218:219], v60 offset:2656\n"
    "v_mfma_f32_16x16x32_bf16 v[208:211], a[196:199], a[20:23], v[208:211]\n"
    "s_waitcnt lgkmcnt(8)\n"
    "ds_read_b64_tr_b16 a[220:221], v60 offset:5216\n"
    "ds_read_b64_tr_b16 a[222:223], v60 offset:7776\n"
    "v_mfma_f32_16x16x32_bf16 v[212:215], a[200:203], a[16:19], 0\n"
    "s_waitcnt lgkmcnt(8)\n"
    "ds_read_b64_tr_b16 a[224:225], v60 offset:128\n"
    "ds_read_b64_tr_b16 a[226:227], v60 offset:2688\n"
    "v_mfma_f32_16x16x32_bf16 v[212:215], a[204:207], a[20:23], v[212:215]\n"
    "s_waitcnt lgkmcnt(8)\n"
    "ds_read_b64_tr_b16 a[228:229], v60 offset:5248\n"
    "ds_read_b64_tr_b16 a[230:231], v60 offset:7808\n"
    "v_cvt_pk_bf16_f32 v232, v232, v233\n"
    "v_cvt_pk_bf16_f32 v233, v234, v235\n"
    "buffer_store_dwordx2 v[232:233], v7, s[52:55], s58 offen sc1\n"
    "v_mfma_f32_16x16x32_bf16 v[216:219], a[208:211], a[16:19], 0\n"
    "s_waitcnt lgkmcnt(8)\n"
    "ds_read_b64_tr_b16 a[232:233], v60 offset:10240\n"
    "ds_read_b64_tr_b16 a[234:235], v60 offset:12800\n"
    "v_mfma_f32_16x16x32_bf16 v[216:219], a[212:215], a[20:23], v[216:219]\n"
    "s_waitcnt lgkmcnt(8)\n"
    "ds_read_b64_tr_b16 a[236:237], v60 offset:15360\n"
    "ds_read_b64_tr_b16 a[238:239], v60 offset:17920\n"
    "v_mfma_f32_16x16x32_bf16 v[220:223], a[216:219], a[16:19], 0\n"
    "s_waitcnt lgkmcnt(8)\n"
    "ds_read_b64_tr_b16 a[192:193], v60 offset:10272\n"
    "ds_read_b64_tr_b16 a[194:195], v60 offset:12832\n"
    "v_mfma_f32_16x16x32_bf16 v[220:223], a[220:223], a[20:23], v[220:223]\n"
    "s_waitcnt lgkmcnt(8)\n"
    "ds_read_b64_tr_b16 a[196:197], v60 offset:15392\n"
    "ds_read_b64_tr_b16 a[198:199], v60 offset:17952\n"
    "v_mfma_f32_16x16x32_bf16 v[240:243], a[224:227], a[16:19], 0\n"
    "s_waitcnt lgkmcnt(8)\n"
    "ds_read_b64_tr_b16 a[200:201], v60 offset:10304\n"
    "ds_read_b64_tr_b16 a[202:203], v60 offset:12864\n"
    "v_cvt_pk_bf16_f32 v236, v236, v237\n"
    "v_cvt_pk_bf16_f32 v237, v238, v239\n"
    "buffer_store_dwordx2 v[236:237], v7, s[52:55], s59 offen sc1\n"
    "v_mfma_f32_16x16x32_bf16 v[240:243], a[228:231], a[20:23], v[240:243]\n"
    "s_waitcnt lgkmcnt(8)\n"
    "ds_read_b64_tr_b16 a[204:205], v60 offset:15424\n"
    "ds_read_b64_tr_b16 a[206:207], v60 offset:17984\n"
    "v_mfma_f32_16x16x32_bf16 v[208:211], a[232:235], a[24:27], v[208:211]\n"
    "s_waitcnt lgkmcnt(8)\n"
    "ds_read_b64_tr_b16 a[208:209], v60 offset:10336\n"
    "ds_read_b64_tr_b16 a[210:211], v60 offset:12896\n"
    "v_mfma_f32_16x16x32_bf16 v[208:211], a[236:239], a[28:31], v[208:211]\n"
    "s_waitcnt lgkmcnt(8)\n"
    "ds_read_b64_tr_b16 a[212:213], v60 offset:15456\n"
    "ds_read_b64_tr_b16 a[214:215], v60 offset:18016\n"
    "v_mfma_f32_16x16x32_bf16 v[212:215], a[192:195], a[24:27], v[212:215]\n"
    "s_waitcnt lgkmcnt(8)\n"
    "ds_read_b64_tr_b16 a[216:217], v60 offset:10368\n"
    "ds_read_b64_tr_b16 a[218:219], v60 offset:12928\n"
    "v_mfma_f32_16x16x32_bf16 v[212:215], a[196:199], a[28:31], v[212:215]\n"
    "s_waitcnt lgkmcnt(8)\n"
    "ds_read_b64_tr_b16 a[220:221], v60 offset:15488\n"
    "ds_read_b64_tr_b16 a[222:223], v60 offset:18048\n"
    "s_barrier\n"
    "v_add_u32 v60, 20480, v60\n"
    "v_mfma_f32_16x16x32_bf16 v[216:219], a[200:203], a[24:27], v[216:219]\n"
    "s_waitcnt lgkmcnt(8)\n"
    "ds_read_b64_tr_b16 a[224:225], v60 offset:0\n"
    "ds_read_b64_tr_b16 a[226:227], v60 offset:2560\n"
    "s_cmp_ge_u32 s35, 7\n"
    "s_cselect_b32 s90, -7, 5\n"
    "s_add_u32 s90, s90, s35\n"
    "s_mul_i32 s90, s90, 20480\n"
    "s_add_u32 s90, s90, s71\n"
    "s_add_u32 m0, s91, 40960\n"
    "s_nop 0\n"
    "buffer_load_dwordx4 v9, s[52:55], s90 offen offset:0 sc1 lds\n"
    "v_mfma_f32_16x16x32_bf16 v[216:219], a[204:207], a[28:31], v[216:219]\n"
    "s_waitcnt lgkmcnt(8)\n"
    "ds_read_b64_tr_b16 a[228:229], v60 offset:5120\n"
    "ds_read_b64_tr_b16 a[230:231], v60 offset:7680\n"
    "v_mfma_f32_16x16x32_bf16 v[220:223], a[208:211], a[24:27], v[220:223]\n"
    "s_waitcnt lgkmcnt(8)\n"
    "ds_read_b64_tr_b16 a[232:233], v60 offset:32\n"
    "ds_read_b64_tr_b16 a[234:235], v60 offset:2592\n"
    "buffer_load_dwordx4 v9, s[52:55], s90 offen offset:1024 sc1 lds\n"
    "v_mfma_f32_16x16x32_bf16 v[220:223], a[212:215], a[28:31], v[220:223]\n"
    "s_waitcnt lgkmcnt(8)\n"
    "ds_read_b64_tr_b16 a[236:237], v60 offset:5152\n"
    "ds_read_b64_tr_b16 a[238:239], v60 offset:7712\n"
    "v_mfma_f32_16x16x32_bf16 v[240:243], a[216:219], a[24:27], v[240:243]\n"
    "s_waitcnt lgkmcnt(8)\n"
    "ds_read_b64_tr_b16 a[192:193], v60 offset:64\n"
    "ds_read_b64_tr_b16 a[194:195], v60 offset:2624\n"
    "buffer_load_dwordx4 v9, s[52:55], s90 offen offset:2048 sc1 lds\n"
    "v_mfma_f32_16x16x32_bf16 v[240:243], a[220:223], a[28:31], v[240:243]\n"
    "s_waitcnt lgkmcnt(8)\n"
    "ds_read_b64_tr_b16 a[196:197], v60 offset:5184\n"
    "ds_read_b64_tr_b16 a[198:199], v60 offset:7744\n"
    "v_mfma_f32_16x16x32_bf16 v[208:211], a[224:227], a[48:51], v[208:211]\n"
    "s_waitcnt lgkmcnt(8)\n"
    "ds_read_b64_tr_b16 a[200:201], v60 offset:96\n"
    "ds_read_b64_tr_b16 a[202:203], v60 offset:2656\n"
    "buffer_load_dwordx4 v9, s[52:55], s90 offen offset:3072 sc1 lds\n"
    "v_mfma_f32_16x16x32_bf16 v[208:211], a[228:231], a[52:55], v[208:211]\n"
    "s_waitcnt lgkmcnt(8)\n"
    "ds_read_b64_tr_b16 a[204:205], v60 offset:5216\n"
    "ds_read_b64_tr_b16 a[206:207], v60 offset:7776\n"
    "v_mfma_f32_16x16x32_bf16 v[212:215], a[232:235], a[48:51], v[212:215]\n"
    "s_waitcnt lgkmcnt(8)\n"
    "ds_read_b64_tr_b16 a[208:209], v60 offset:128\n"
    "ds_read_b64_tr_b16 a[210:211], v60 offset:2688\n"
    "s_add_u32 s90, s90, 4096\n"
    "s_add_u32 m0, s91, 45056\n"
    "s_nop 0\n"
    "buffer_load_dwordx4 v9, s[52:55], s90 offen offset:0 sc1 lds\n"
    "v_mfma_f32_16x16x32_bf16 v[212:215], a[236:239], a[52:55], v[212:215]\n"
    "s_waitcnt lgkmcnt(8)\n"
    "ds_read_b64_tr_b16 a[212:213], v60 offset:5248\n"
    "ds_read_b64_tr_b16 a[214:215], v60 offset:7808\n"
    "v_mfma_f32_16x16x32_bf16 v[216:219], a[192:195], a[48:51], v[216:219]\n"
    "s_waitcnt lgkmcnt(8)\n"
    "ds_read_b64_tr_b16 a[216:217], v60 offset:10240\n"
    "ds_read_b64_tr_b16 a[218:219], v60 offset:12800\n"
    "s_lshl_b32 s73, s77, 2\n"
    "s_lshr_b32 s74, s78, 1\n"
    "s_add_u32 s73, s73, s74\n"
    "s_lshl_b32 s73, s73, 3\n"
    "s_and_b32 s74, s78, 1\n"
    "s_xor_b32 s74, s74, 0\n"
    "s_add_u32 s73, s73, s74\n"
    "v_lshrrev_b32 v56, 4, v9\n"
    "v_min_u32 v56, 3, v56\n"
    "v_mul_u32_u24 v56, 2, v56\n"
    "v_add_u32 v56, s73, v56\n"
    "v_lshlrev_b32 v56, 6, v56\n"
    "v_add_u32 v56, 102400, v56\n"
    "buffer_load_dwordx2 v[62:63], v56, s[52:55], 0 offen sc1\n"
    "v_mfma_f32_16x16x32_bf16 v[216:219], a[196:199], a[52:55], v[216:219]\n"
    "s_waitcnt lgkmcnt(8)\n"
    "ds_read_b64_tr_b16 a[220:221], v60 offset:15360\n"
    "ds_read_b64_tr_b16 a[222:223], v60 offset:17920\n"
    "v_mfma_f32_16x16x32_bf16 v[220:223], a[200:203], a[48:51], v[220:223]\n"
    "s_waitcnt lgkmcnt(8)\n"
    "ds_read_b64_tr_b16 a[224:225], v60 offset:10272\n"
    "ds_read_b64_tr_b16 a[226:227], v60 offset:12832\n"
    "s_cmp_ge_u32 s35, 6\n"
    "s_cselect_b32 s90, -6, 6\n"
    "s_add_u32 s90, s90, s35\n"
    "s_mul_i32 s90, s90, 20480\n"
    "s_add_u32 s90, s90, s71\n"
    "s_add_u32 m0, s91, 61440\n"
    "s_nop 0\n"
    "buffer_load_dwordx4 v9, s[52:55], s90 offen offset:0 sc1 lds\n"
    "v_mfma_f32_16x16x32_bf16 v[220:223], a[204:207], a[52:55], v[220:223]\n"
    "s_waitcnt lgkmcnt(8)\n"
    "ds_read_b64_tr_b16 a[228:229], v60 offset:15392\n"
    "ds_read_b64_tr_b16 a[230:231], v60 offset:17952\n"
    "v_mfma_f32_16x16x32_bf16 v[240:243], a[208:211], a[48:51], v[240:243]\n"
    "s_waitcnt lgkmcnt(8)\n"
    "ds_read_b64_tr_b16 a[232:233], v60 offset:10304\n"
    "ds_read_b64_tr_b16 a[234:235], v60 offset:12864\n"
    "buffer_load_dwordx4 v9, s[52:55], s90 offen offset:1024 sc1 lds\n"
    "v_mfma_f32_16x16x32_bf16 v[240:243], a[212:215], a[52:55], v[240:243]\n"
    "s_waitcnt lgkmcnt(8)\n"
    "ds_read_b64_tr_b16 a[236:237], v60 offset:15424\n"
    "ds_read_b64_tr_b16 a[238:239], v60 offset:17984\n"
    "v_mfma_f32_16x16x32_bf16 v[208:211], a[216:219], a[56:59], v[208:211]\n"
    "s_waitcnt lgkmcnt(8)\n"
    "ds_read_b64_tr_b16 a[192:193], v60 offset:10336\n"
    "ds_read_b64_tr_b16 a[194:195], v60 offset:12896\n"
    "buffer_load_dwordx4 v9, s[52:55], s90 offen offset:2048 sc1 lds\n"
    "v_mfma_f32_16x16x32_bf16 v[208:211], a[220:223], a[60:63], v[208:211]\n"
    "s_waitcnt lgkmcnt(8)\n"
    "ds_read_b64_tr_b16 a[196:197], v60 offset:15456\n"
    "ds_read_b64_tr_b16 a[198:199], v60 offset:18016\n"
    "v_mfma_f32_16x16x32_bf16 v[212:215], a[224:227], a[56:59], v[212:215]\n"
    "s_waitcnt lgkmcnt(8)\n"
    "ds_read_b64_tr_b16 a[200:201], v60 offset:10368\n"
    "ds_read_b64_tr_b16 a[202:203], v60 offset:12928\n"
    "buffer_load_dwordx4 v9, s[52:55], s90 offen offset:3072 sc1 lds\n"
    "v_mfma_f32_16x16x32_bf16 v[212:215], a[228:231], a[60:63], v[212:215]\n"
    "s_waitcnt lgkmcnt(8)\n"
    "ds_read_b64_tr_b16 a[204:205], v60 offset:15488\n"
    "ds_read_b64_tr_b16 a[206:207], v60 offset:18048\n"
    "s_barrier\n"
    "v_add_u32 v60, 20480, v60\n"
    "v_mfma_f32_16x16x32_bf16 v[216:219], a[232:235], a[56:59], v[216:219]\n"
    "s_waitcnt lgkmcnt(8)\n"
    "ds_read_b64_tr_b16 a[208:209], v60 offset:0\n"
    "ds_read_b64_tr_b16 a[210:211], v60 offset:2560\n"
    "s_add_u32 s90, s90, 4096\n"
    "s_add_u32 m0, s91, 65536\n"
    "s_nop 0\n"
    "buffer_load_dwordx4 v9, s[52:55], s90 offen offset:0 sc1 lds\n"
    "v_mfma_f32_16x16x32_bf16 v[216:219], a[236:239], a[60:63], v[216:219]\n"
    "s_waitcnt lgkmcnt(8)\n"
    "ds_read_b64_tr_b16 a[212:213], v60 offset:5120\n"
    "ds_read_b64_tr_b16 a[214:215], v60 offset:7680\n"
    "v_mfma_f32_16x16x32_bf16 v[220:223], a[192:195], a[56:59], v[220:223]\n"
    "s_waitcnt lgkmcnt(8)\n"
    "ds_read_b64_tr_b16 a[216:217], v60 offset:32\n"
    "ds_read_b64_tr_b16 a[218:219], v60 offset:2592\n"
    "s_cmp_ge_u32 s35, 5\n"
    "s_cselect_b32 s67, -5, 7\n"
    "s_add_u32 s67, s67, s35\n"
    "s_lshl_b32 s67, s67, 12\n"
    "s_add_u32 s67, s67, s66\n"
    "buffer_load_dwordx4 a[144:147], v9, s[52:55], s67 offen offset:0 sc1\n"
    "v_mfma_f32_16x16x32_bf16 v[220:223], a[196:199], a[60:63], v[220:223]\n"
    "s_waitcnt lgkmcnt(8)\n"
    "ds_read_b64_tr_b16 a[220:221], v60 offset:5152\n"
    "ds_read_b64_tr_b16 a[222:223], v60 offset:7712\n"
    "v_mfma_f32_16x16x32_bf16 v[240:243], a[200:203], a[56:59], v[240:243]\n"
    "s_waitcnt lgkmcnt(8)\n"
    "ds_read_b64_tr_b16 a[224:225], v60 offset:64\n"
    "ds_read_b64_tr_b16 a[226:227], v60 offset:2624\n"
    "buffer_load_dwordx4 a[148:151], v9, s[52:55], s67 offen offset:1024 sc1\n"
    "v_mfma_f32_16x16x32_bf16 v[240:243], a[204:207], a[60:63], v[240:243]\n"
    "s_waitcnt lgkmcnt(8)\n"
    "ds_read_b64_tr_b16 a[228:229], v60 offset:5184\n"
    "ds_read_b64_tr_b16 a[230:231], v60 offset:7744\n"
    "v_mfma_f32_16x16x32_bf16 v[208:211], a[208:211], a[80:83], v[208:211]\n"
    "s_waitcnt lgkmcnt(8)\n"
    "ds_read_b64_tr_b16 a[232:233], v60 offset:96\n"
    "ds_read_b64_tr_b16 a[234:235], v60 offset:2656\n"
    "buffer_load_dwordx4 a[152:155], v9, s[52:55], s67 offen offset:2048 sc1\n"
    "v_mfma_f32_16x16x32_bf16 v[208:211], a[212:215], a[84:87], v[208:211]\n"
    "s_waitcnt lgkmcnt(8)\n"
    "ds_read_b64_tr_b16 a[236:237], v60 offset:5216\n"
    "ds_read_b64_tr_b16 a[238:239], v60 offset:7776\n"
    "v_mfma_f32_16x16x32_bf16 v[212:215], a[216:219], a[80:83], v[212:215]\n"
    "s_waitcnt lgkmcnt(8)\n"
    "ds_read_b64_tr_b16 a[192:193], v60 offset:128\n"
    "ds_read_b64_tr_b16 a[194:195], v60 offset:2688\n"
    "buffer_load_dwordx4 a[156:159], v9, s[52:55], s67 offen offset:3072 sc1\n"
    "v_mfma_f32_16x16x32_bf16 v[212:215], a[220:223], a[84:87], v[212:215]\n"
    "s_waitcnt lgkmcnt(8)\n"
    "ds_read_b64_tr_b16 a[196:197], v60 offset:5248\n"
    "ds_read_b64_tr_b16 a[198:199], v60 offset:7808\n"
    "v_mfma_f32_16x16x32_bf16 v[216:219], a[224:227], a[80:83], v[216:219]\n"
    "s_waitcnt lgkmcnt(8)\n"
    "ds_read_b64_tr_b16 a[200:201], v60 offset:10240\n"
    "ds_read_b64_tr_b16 a[202:203], v60 offset:12800\n"
    "s_cmp_ge_u32 s35, 5\n"
    "s_cselect_b32 s90, -5, 7\n"
    "s_add_u32 s90, s90, s35\n"
    "s_mul_i32 s90, s90, 20480\n"
    "s_add_u32 s90, s90, s71\n"
    "s_add_u32 m0, s91, 81920\n"
    "s_nop 0\n"
    "buffer_load_dwordx4 v9, s[52:55], s90 offen offset:0 sc1 lds\n"
    "v_mfma_f32_16x16x32_bf16 v[216:219], a[228:231], a[84:87], v[216:219]\n"
    "s_waitcnt lgkmcnt(8)\n"
    "ds_read_b64_tr_b16 a[204:205], v60 offset:15360\n"
    "ds_read_b64_tr_b16 a[206:207], v60 offset:17920\n"
    "v_mfma_f32_16x16x32_bf16 v[220:223], a[232:235], a[80:83], v[220:223]\n"
    "s_waitcnt lgkmcnt(8)\n"
    "ds_read_b64_tr_b16 a[208:209], v60 offset:10272\n"
    "ds_read_b64_tr_b16 a[210:211], v60 offset:12832\n"
    "buffer_load_dwordx4 v9, s[52:55], s90 offen offset:1024 sc1 lds\n"
    "v_mfma_f32_16x16x32_bf16 v[220:223], a[236:239], a[84:87], v[220:223]\n"
    "s_waitcnt lgkmcnt(8)\n"
    "ds_read_b64_tr_b16 a[212:213], v60 offset:15392\n"
    "ds_read_b64_tr_b16 a[214:215], v60 offset:17952\n"
    "v_mfma_f32_16x16x32_bf16 v[240:243], a[192:195], a[80:83], v[240:243]\n"
    "s_waitcnt lgkmcnt(8)\n"
    "ds_read_b64_tr_b16 a[216:217], v60 offset:10304\n"
    "ds_read_b64_tr_b16 a[218:219], v60 offset:12864\n"
    "buffer_load_dwordx4 v9, s[52:55], s90 offen offset:2048 sc1 lds\n"
    "v_mfma_f32_16x16x32_bf16 v[240:243], a[196:199], a[84:87], v[240:243]\n"
    "s_waitcnt lgkmcnt(8)\n"
    "ds_read_b64_tr_b16 a[220:221], v60 offset:15424\n"
    "ds_read_b64_tr_b16 a[222:223], v60 offset:17984\n"
    "v_mfma_f32_16x16x32_bf16 v[208:211], a[200:203], a[88:91], v[208:211]\n"
    "s_waitcnt lgkmcnt(8)\n"
    "ds_read_b64_tr_b16 a[224:225], v60 offset:10336\n"
    "ds_read_b64_tr_b16 a[226:227], v60 offset:12896\n"
    "buffer_load_dwordx4 v9, s[52:55], s90 offen offset:3072 sc1 lds\n"
    "v_mfma_f32_16x16x32_bf16 v[208:211], a[204:207], a[92:95], v[208:211]\n"
    "s_waitcnt lgkmcnt(8)\n"
    "ds_read_b64_tr_b16 a[228:229], v60 offset:15456\n"
    "ds_read_b64_tr_b16 a[230:231], v60 offset:18016\n"
    "v_mfma_f32_16x16x32_bf16 v[212:215], a[208:211], a[88:91], v[212:215]\n"
    "s_waitcnt lgkmcnt(8)\n"
    "ds_read_b64_tr_b16 a[232:233], v60 offset:10368\n"
    "ds_read_b64_tr_b16 a[234:235], v60 offset:12928\n"
    "s_add_u32 s90, s90, 4096\n"
    "s_add_u32 m0, s91, 86016\n"
    "s_nop 0\n"
    "buffer_load_dwordx4 v9, s[52:55], s90 offen offset:0 sc1 lds\n"
    "v_mfma_f32_16x16x32_bf16 v[212:215], a[212:215], a[92:95], v[212:215]\n"
    "s_waitcnt lgkmcnt(8)\n"
    "ds_read_b64_tr_b16 a[236:237], v60 offset:15488\n"
    "ds_read_b64_tr_b16 a[238:239], v60 offset:18048\n"
    "s_waitcnt vmcnt(20)\n"
    "s_barrier\n"
    "v_add_u32 v60, 4294823936, v60\n"
    "v_mfma_f32_16x16x32_bf16 v[216:219], a[216:219], a[88:91], v[216:219]\n"
    "s_waitcnt lgkmcnt(8)\n"
    "ds_read_b64_tr_b16 a[192:193], v60 offset:0\n"
    "ds_read_b64_tr_b16 a[194:195], v60 offset:2560\n"
    "s_lshl_b32 s73, s78, 2\n"
    "s_add_u32 s73, s73, s79\n"
    "s_lshl_b32 s73, s73, 3\n"
    "s_add_u32 s73, s73, s77\n"
    "s_lshl_b32 s95, s73, 6\n"
    "s_add_u32 s95, s95, 118784\n"
    "v_mov_b32 v50, s80\n"
    "v_mov_b32 v51, s81\n"
    "buffer_store_dwordx2 v[50:51], v10, s[52:55], s95 offen sc1\n"
    "v_mfma_f32_16x16x32_bf16 v[216:219], a[220:223], a[92:95], v[216:219]\n"
    "s_waitcnt lgkmcnt(8)\n"
    "ds_read_b64_tr_b16 a[196:197], v60 offset:5120\n"
    "ds_read_b64_tr_b16 a[198:199], v60 offset:7680\n"
    "v_mfma_f32_16x16x32_bf16 v[220:223], a[224:227], a[88:91], v[220:223]\n"
    "s_waitcnt lgkmcnt(8)\n"
    "ds_read_b64_tr_b16 a[200:201], v60 offset:32\n"
    "ds_read_b64_tr_b16 a[202:203], v60 offset:2592\n"
    "s_waitcnt vmcnt(15)\n"
    "v_cmp_ne_u64 vcc, s[80:81], v[62:63]\n"
    "s_nop 1\n"
    "s_cbranch_vccnz L_slow9\n"
    "L_back9:\n"
    "s_lshl_b32 s65, s83, 3\n"
    "s_add_u32 s65, s65, s78\n"
    "s_mul_i32 s65, s65, 20480\n"
    "s_add_u32 s65, s65, 44040192\n"
    "buffer_load_dwordx4 v[64:67], v9, s[52:55], s65 offen offset:0 sc1\n"
    "v_mfma_f32_16x16x32_bf16 v[220:223], a[228:231], a[92:95], v[220:223]\n"
    "s_waitcnt lgkmcnt(8)\n"
    "ds_read_b64_tr_b16 a[204:205], v60 offset:5152\n"
    "ds_read_b64_tr_b16 a[206:207], v60 offset:7712\n"
    "v_mfma_f32_16x16x32_bf16 v[240:243], a[232:235], a[88:91], v[240:243]\n"
    "s_waitcnt lgkmcnt(8)\n"
    "ds_read_b64_tr_b16 a[208:209], v60 offset:64\n"
    "ds_read_b64_tr_b16 a[210:211], v60 offset:2624\n"
    "buffer_load_dwordx4 v[68:71], v9, s[52:55], s65 offen offset:1024 sc1\n"
    "v_mfma_f32_16x16x32_bf16 v[240:243], a[236:239], a[92:95], v[240:243]\n"
    "s_waitcnt lgkmcnt(8)\n"
    "ds_read_b64_tr_b16 a[212:213], v60 offset:5184\n"
    "ds_read_b64_tr_b16 a[214:215], v60 offset:7744\n"
    "v_mfma_f32_16x16x32_bf16 v[208:211], a[192:195], a[112:115], v[208:211]\n"
    "s_waitcnt lgkmcnt(8)\n"
    "ds_read_b64_tr_b16 a[216:217], v60 offset:96\n"
    "ds_read_b64_tr_b16 a[218:219], v60 offset:2656\n"
    "buffer_load_dwordx2 v[128:129], v59, s[52:55], s65 offen sc1\n"
    "v_mfma_f32_16x16x32_bf16 v[208:211], a[196:199], a[116:119], v[208:211]\n"
    "s_waitcnt lgkmcnt(8)\n"
    "ds_read_b64_tr_b16 a[220:221], v60 offset:5216\n"
    "ds_read_b64_tr_b16 a[222:223], v60 offset:7776\n"
    "v_mfma_f32_16x16x32_bf16 v[212:215], a[200:203], a[112:115], v[212:215]\n"
    "s_waitcnt lgkmcnt(8)\n"
    "ds_read_b64_tr_b16 a[224:225], v60 offset:128\n"
    "ds_read_b64_tr_b16 a[226:227], v60 offset:2688\n"
    "s_cmp_ge_u32 s35, 4\n"
    "s_cselect_b32 s67, -4, 8\n"
    "s_add_u32 s67, s67, s35\n"
    "s_lshl_b32 s67, s67, 12\n"
    "s_add_u32 s67, s67, s66\n"
    "buffer_load_dwordx4 a[176:179], v9, s[52:55], s67 offen offset:0 sc1\n"
    "v_mfma_f32_16x16x32_bf16 v[212:215], a[204:207], a[116:119], v[212:215]\n"
    "s_waitcnt lgkmcnt(8)\n"
    "ds_read_b64_tr_b16 a[228:229], v60 offset:5248\n"
    "ds_read_b64_tr_b16 a[230:231], v60 offset:7808\n"
    "v_mfma_f32_16x16x32_bf16 v[216:219], a[208:211], a[112:115], v[216:219]\n"
    "s_waitcnt lgkmcnt(8)\n"
    "ds_read_b64_tr_b16 a[232:233], v60 offset:10240\n"
    "ds_read_b64_tr_b16 a[234:235], v60 offset:12800\n"
    "buffer_load_dwordx4 a[180:183], v9, s[52:55], s67 offen offset:1024 sc1\n"
    "v_mfma_f32_16x16x32_bf16 v[216:219], a[212:215], a[116:119], v[216:219]\n"
    "s_waitcnt lgkmcnt(8)\n"
    "ds_read_b64_tr_b16 a[236:237], v60 offset:15360\n"
    "ds_read_b64_tr_b16 a[238:239], v60 offset:17920\n"
    "v_mfma_f32_16x16x32_bf16 v[220:223], a[216:219], a[112:115], v[220:223]\n"
    "s_waitcnt lgkmcnt(8)\n"
    "ds_read_b64_tr_b16 a[192:193], v60 offset:10272\n"
    "ds_read_b64_tr_b16 a[194:195], v60 offset:12832\n"
    "buffer_load_dwordx4 a[184:187], v9, s[52:55], s67 offen offset:2048 sc1\n"
    "v_mfma_f32_16x16x32_bf16 v[220:223], a[220:223], a[116:119], v[220:223]\n"
    "s_waitcnt lgkmcnt(8)\n"
    "ds_read_b64_tr_b16 a[196:197], v60 offset:15392\n"
    "ds_read_b64_tr_b16 a[198:199], v60 offset:17952\n"
    "v_mfma_f32_16x16x32_bf16 v[240:243], a[224:227], a[112:115], v[240:243]\n"
    "s_waitcnt lgkmcnt(8)\n"
    "ds_read_b64_tr_b16 a[200:201], v60 offset:10304\n"
    "ds_read_b64_tr_b16 a[202:203], v60 offset:12864\n"
    "buffer_load_dwordx4 a[188:191], v9, s[52:55], s67 offen offset:3072 sc1\n"
    "v_mfma_f32_16x16x32_bf16 v[240:243], a[228:231], a[116:119], v[240:243]\n"
    "s_waitcnt lgkmcnt(8)\n"
    "ds_read_b64_tr_b16 a[204:205], v60 offset:15424\n"
    "ds_read_b64_tr_b16 a[206:207], v60 offset:17984\n"
    "v_mfma_f32_16x16x32_bf16 v[208:211], a[232:235], a[120:123], v[208:211]\n"
    "s_waitcnt lgkmcnt(8)\n"
    "ds_read_b64_tr_b16 a[208:209], v60 offset:10336\n"
    "ds_read_b64_tr_b16 a[210:211], v60 offset:12896\n"
    "s_cmp_ge_u32 s35, 4\n"
    "s_cselect_b32 s90, -4, 8\n"
    "s_add_u32 s90, s90, s35\n"
    "s_mul_i32 s90, s90, 20480\n"
    "s_add_u32 s90, s90, s71\n"
    "s_add_u32 m0, s91, 102400\n"
    "s_nop 0\n"
    "buffer_load_dwordx4 v9, s[52:55], s90 offen offset:0 sc1 lds\n"
    "v_mfma_f32_16x16x32_bf16 v[208:211], a[236:239], a[124:127], v[208:211]\n"
    "s_waitcnt lgkmcnt(8)\n"
    "ds_read_b64_tr_b16 a[212:213], v60 offset:15456\n"
    "ds_read_b64_tr_b16 a[214:215], v60 offset:18016\n"
    "v_mfma_f32_16x16x32_bf16 v[212:215], a[192:195], a[120:123], v[212:215]\n"
    "s_waitcnt lgkmcnt(8)\n"
    "ds_read_b64_tr_b16 a[216:217], v60 offset:10368\n"
    "ds_read_b64_tr_b16 a[218:219], v60 offset:12928\n"
    "v_mfma_f32_16x16x32_bf16 v[212:215], a[196:199], a[124:127], v[212:215]\n"
    "s_waitcnt lgkmcnt(8)\n"
    "ds_read_b64_tr_b16 a[220:221], v60 offset:15488\n"
    "ds_read_b64_tr_b16 a[222:223], v60 offset:18048\n"
    "s_barrier\n"
    "v_add_u32 v60, 20480, v60\n"
    "buffer_load_dwordx4 v9, s[52:55], s90 offen offset:1024 sc1 lds\n"
    "v_mfma_f32_16x16x32_bf16 v[216:219], a[200:203], a[120:123], v[216:219]\n"
    "s_waitcnt lgkmcnt(8)\n"
    "ds_read_b64_tr_b16 a[224:225], v60 offset:0\n"
    "ds_read_b64_tr_b16 a[226:227], v60 offset:2560\n"
    "v_mfma_f32_16x16x32_bf16 v[216:219], a[204:207], a[124:127], v[216:219]\n"
    "s_waitcnt lgkmcnt(8)\n"
    "ds_read_b64_tr_b16 a[228:229], v60 offset:5120\n"
    "ds_read_b64_tr_b16 a[230:231], v60 offset:7680\n"
    "buffer_load_dwordx4 v9, s[52:55], s90 offen offset:2048 sc1 lds\n"
    "v_mfma_f32_16x16x32_bf16 v[220:223], a[208:211], a[120:123], v[220:223]\n"
    "s_waitcnt lgkmcnt(8)\n"
    "ds_read_b64_tr_b16 a[232:233], v60 offset:32\n"
    "ds_read_b64_tr_b16 a[234:235], v60 offset:2592\n"
    "v_mfma_f32_16x16x32_bf16 v[220:223], a[212:215], a[124:127], v[220:223]\n"
    "s_waitcnt lgkmcnt(8)\n"
    "ds_read_b64_tr_b16 a[236:237], v60 offset:5152\n"
    "ds_read_b64_tr_b16 a[238:239], v60 offset:7712\n"
    "buffer_load_dwordx4 v9, s[52:55], s90 offen offset:3072 sc1 lds\n"
    "v_mfma_f32_16x16x32_bf16 v[240:243], a[216:219], a[120:123], v[240:243]\n"
    "s_waitcnt lgkmcnt(8)\n"
    "ds_read_b64_tr_b16 a[192:193], v60 offset:64\n"
    "ds_read_b64_tr_b16 a[194:195], v60 offset:2624\n"
    "v_mfma_f32_16x16x32_bf16 v[240:243], a[220:223], a[124:127], v[240:243]\n"
    "s_waitcnt lgkmcnt(8)\n"
    "ds_read_b64_tr_b16 a[196:197], v60 offset:5184\n"
    "ds_read_b64_tr_b16 a[198:199], v60 offset:7744\n"
    "s_add_u32 s90, s90, 4096\n"
    "s_add_u32 m0, s91, 106496\n"
    "s_nop 0\n"
    "buffer_load_dwordx4 v9, s[52:55], s90 offen offset:0 sc1 lds\n"
    "v_mfma_f32_16x16x32_bf16 v[208:211], a[224:227], a[128:131], v[208:211]\n"
    "s_waitcnt lgkmcnt(8)\n"
    "ds_read_b64_tr_b16 a[200:201], v60 offset:96\n"
    "ds_read_b64_tr_b16 a[202:203], v60 offset:2656\n"
    "v_mfma_f32_16x16x32_bf16 v[208:211], a[228:231], a[132:135], v[208:211]\n"
    "s_waitcnt lgkmcnt(8)\n"
    "ds_read_b64_tr_b16 a[204:205], v60 offset:5216\n"
    "ds_read_b64_tr_b16 a[206:207], v60 offset:7776\n"
    "s_add_u32 s65, s65, 2560\n"
    "buffer_load_dwordx4 v[72:75], v9, s[52:55], s65 offen offset:0 sc1\n"
    "v_mfma_f32_16x16x32_bf16 v[212:215], a[232:235], a[128:131], v[212:215]\n"
    "s_waitcnt lgkmcnt(8)\n"
    "ds_read_b64_tr_b16 a[208:209], v60 offset:128\n"
    "ds_read_b64_tr_b16 a[210:211], v60 offset:2688\n"
    "v_mfma_f32_16x16x32_bf16 v[212:215], a[236:239], a[132:135], v[212:215]\n"
    "s_waitcnt lgkmcnt(8)\n"
    "ds_read_b64_tr_b16 a[212:213], v60 offset:5248\n"
    "ds_read_b64_tr_b16 a[214:215], v60 offset:7808\n"
    "buffer_load_dwordx4 v[76:79], v9, s[52:55], s65 offen offset:1024 sc1\n"
    "v_mfma_f32_16x16x32_bf16 v[216:219], a[192:195], a[128:131], v[216:219]\n"
    "s_waitcnt lgkmcnt(8)\n"
    "ds_read_b64_tr_b16 a[216:217], v60 offset:10240\n"
    "ds_read_b64_tr_b16 a[218:219], v60 offset:12800\n"
    "v_mfma_f32_16x16x32_bf16 v[216:219], a[196:199], a[132:135], v[216:219]\n"
    "s_waitcnt lgkmcnt(8)\n"
    "ds_read_b64_tr_b16 a[220:221], v60 offset:15360\n"
    "ds_read_b64_tr_b16 a[222:223], v60 offset:17920\n"
    "buffer_load_dwordx2 v[130:131], v59, s[52:55], s65 offen sc1\n"
    "v_mfma_f32_16x16x32_bf16 v[220:223], a[200:203], a[128:131], v[220:223]\n"
    "s_waitcnt lgkmcnt(8)\n"
    "ds_read_b64_tr_b16 a[224:225], v60 offset:10272\n"
    "ds_read_b64_tr_b16 a[226:227], v60 offset:12832\n"
    "v_mfma_f32_16x16x32_bf16 v[220:223], a[204:207], a[132:135], v[220:223]\n"
    "s_waitcnt lgkmcnt(8)\n"
    "ds_read_b64_tr_b16 a[228:229], v60 offset:15392\n"
    "ds_read_b64_tr_b16 a[230:231], v60 offset:17952\n"
    "s_cmp_ge_u32 s35, 3\n"
    "s_cselect_b32 s67, -3, 9\n"
    "s_add_u32 s67, s67, s35\n"
    "s_lshl_b32 s67, s67, 12\n"
    "s_add_u32 s67, s67, s66\n"
    "buffer_load_dwordx4 a[64:67], v9, s[52:55], s67 offen offset:0 sc1\n"
    "v_mfma_f32_16x16x32_bf16 v[240:243], a[208:211], a[128:131], v[240:243]\n"
    "s_waitcnt lgkmcnt(8)\n"
    "ds_read_b64_tr_b16 a[232:233], v60 offset:10304\n"
    "ds_read_b64_tr_b16 a[234:235], v60 offset:12864\n"
    "v_mfma_f32_16x16x32_bf16 v[240:243], a[212:215], a[132:135], v[240:243]\n"
    "s_waitcnt lgkmcnt(8)\n"
    "ds_read_b64_tr_b16 a[236:237], v60 offset:15424\n"
    "ds_read_b64_tr_b16 a[238:239], v60 offset:17984\n"
    "v_mfma_f32_16x16x32_bf16 v[208:211], a[216:219], a[136:139], v[208:211]\n"
    "s_waitcnt lgkmcnt(8)\n"
    "ds_read_b64_tr_b16 a[192:193], v60 offset:10336\n"
    "ds_read_b64_tr_b16 a[194:195], v60 offset:12896\n"
    "buffer_load_dwordx4 a[68:71], v9, s[52:55], s67 offen offset:1024 sc1\n"
    "v_mfma_f32_16x16x32_bf16 v[208:211], a[220:223], a[140:143], v[208:211]\n"
    "s_waitcnt lgkmcnt(8)\n"
    "ds_read_b64_tr_b16 a[196:197], v60 offset:15456\n"
    "ds_read_b64_tr_b16 a[198:199], v60 offset:18016\n"
    "v_mfma_f32_16x16x32_bf16 v[212:215], a[224:227], a[136:139], v[212:215]\n"
    "s_waitcnt lgkmcnt(8)\n"
    "ds_read_b64_tr_b16 a[200:201], v60 offset:10368\n"
    "ds_read_b64_tr_b16 a[202:203], v60 offset:12928\n"
    "buffer_load_dwordx4 a[72:75], v9, s[52:55], s67 offen offset:2048 sc1\n"
    "v_mfma_f32_16x16x32_bf16 v[212:215], a[228:231], a[140:143], v[212:215]\n"
    "s_waitcnt lgkmcnt(8)\n"
    "ds_read_b64_tr_b16 a[204:205], v60 offset:15488\n"
    "ds_read_b64_tr_b16 a[206:207], v60 offset:18048\n"
    "s_barrier\n"
    "v_add_u32 v60, 20480, v60\n"
    "v_mfma_f32_16x16x32_bf16 v[216:219], a[232:235], a[136:139], v[216:219]\n"
    "s_waitcnt lgkmcnt(8)\n"
    "ds_read_b64_tr_b16 a[208:209], v60 offset:0\n"
    "ds_read_b64_tr_b16 a[210:211], v60 offset:2560\n"
    "buffer_load_dwordx4 a[76:79], v9, s[52:55], s67 offen offset:3072 sc1\n"
    "v_mfma_f32_16x16x32_bf16 v[216:219], a[236:239], a[140:143], v[216:219]\n"
    "s_waitcnt lgkmcnt(8)\n"
    "ds_read_b64_tr_b16 a[212:213], v60 offset:5120\n"
    "ds_read_b64_tr_b16 a[214:215], v60 offset:7680\n"
    "v_mfma_f32_16x16x32_bf16 v[220:223], a[192:195], a[136:139], v[220:223]\n"
    "s_waitcnt lgkmcnt(8)\n"
    "ds_read_b64_tr_b16 a[216:217], v60 offset:32\n"
    "ds_read_b64_tr_b16 a[218:219], v60 offset:2592\n"
    "s_cmp_ge_u32 s35, 3\n"
    "s_cselect_b32 s90, -3, 9\n"
    "s_add_u32 s90, s90, s35\n"
    "s_mul_i32 s90, s90, 20480\n"
    "s_add_u32 s90, s90, s71\n"
    "s_add_u32 m0, s91, 122880\n"
    "s_nop 0\n"
    "buffer_load_dwordx4 v9, s[52:55], s90 offen offset:0 sc1 lds\n"
    "v_mfma_f32_16x16x32_bf16 v[220:223], a[196:199], a[140:143], v[220:223]\n"
    "s_waitcnt lgkmcnt(8)\n"
    "ds_read_b64_tr_b16 a[220:221], v60 offset:5152\n"
    "ds_read_b64_tr_b16 a[222:223], v60 offset:7712\n"
    "v_mfma_f32_16x16x32_bf16 v[240:243], a[200:203], a[136:139], v[240:243]\n"
    "s_waitcnt lgkmcnt(8)\n"
    "ds_read_b64_tr_b16 a[224:225], v60 offset:64\n"
    "ds_read_b64_tr_b16 a[226:227], v60 offset:2624\n"
    "buffer_load_dwordx4 v9, s[52:55], s90 offen offset:1024 sc1 lds\n"
    "v_mfma_f32_16x16x32_bf16 v[240:243], a[204:207], a[140:143], v[240:243]\n"
    "s_waitcnt lgkmcnt(8)\n"
    "ds_read_b64_tr_b16 a[228:229], v60 offset:5184\n"
    "ds_read_b64_tr_b16 a[230:231], v60 offset:7744\n"
    "v_mfma_f32_16x16x32_bf16 v[208:211], a[208:211], a[160:163], v[208:211]\n"
    "s_waitcnt lgkmcnt(8)\n"
    "ds_read_b64_tr_b16 a[232:233], v60 offset:96\n"
    "ds_read_b64_tr_b16 a[234:235], v60 offset:2656\n"
    "buffer_load_dwordx4 v9, s[52:55], s90 offen offset:2048 sc1 lds\n"
    "v_mfma_f32_16x16x32_bf16 v[208:211], a[212:215], a[164:167], v[208:211]\n"
    "s_waitcnt lgkmcnt(8)\n"
    "ds_read_b64_tr_b16 a[236:237], v60 offset:5216\n"
    "ds_read_b64_tr_b16 a[238:239], v60 offset:7776\n"
    "v_mfma_f32_16x16x32_bf16 v[212:215], a[216:219], a[160:163], v[212:215]\n"
    "s_waitcnt lgkmcnt(8)\n"
    "ds_read_b64_tr_b16 a[192:193], v60 offset:128\n"
    "ds_read_b64_tr_b16 a[194:195], v60 offset:2688\n"
    "buffer_load_dwordx4 v9, s[52:55], s90 offen offset:3072 sc1 lds\n"
    "v_mfma_f32_16x16x32_bf16 v[212:215], a[220:223], a[164:167], v[212:215]\n"
    "s_waitcnt lgkmcnt(8)\n"
    "ds_read_b64_tr_b16 a[196:197], v60 offset:5248\n"
    "ds_read_b64_tr_b16 a[198:199], v60 offset:7808\n"
    "v_mfma_f32_16x16x32_bf16 v[216:219], a[224:227], a[160:163], v[216:219]\n"
    "s_waitcnt lgkmcnt(8)\n"
    "ds_read_b64_tr_b16 a[200:201], v60 offset:10240\n"
    "ds_read_b64_tr_b16 a[202:203], v60 offset:12800\n"
    "s_add_u32 s90, s90, 4096\n"
    "s_add_u32 m0, s91, 126976\n"
    "s_nop 0\n"
    "buffer_load_dwordx4 v9, s[52:55], s90 offen offset:0 sc1 lds\n"
    "v_mfma_f32_16x16x32_bf16 v[216:219], a[228:231], a[164:167], v[216:219]\n"
    "s_waitcnt lgkmcnt(8)\n"
    "ds_read_b64_tr_b16 a[204:205], v60 offset:15360\n"
    "ds_read_b64_tr_b16 a[206:207], v60 offset:17920\n"
    "v_mfma_f32_16x16x32_bf16 v[220:223], a[232:235], a[160:163], v[220:223]\n"
    "s_waitcnt lgkmcnt(8)\n"
    "ds_read_b64_tr_b16 a[208:209], v60 offset:10272\n"
    "ds_read_b64_tr_b16 a[210:211], v60 offset:12832\n"
    "v_mfma_f32_16x16x32_bf16 v[220:223], a[236:239], a[164:167], v[220:223]\n"
    "s_waitcnt lgkmcnt(8)\n"
    "ds_read_b64_tr_b16 a[212:213], v60 offset:15392\n"
    "ds_read_b64_tr_b16 a[214:215], v60 offset:17952\n"
    "s_add_u32 s65, s65, 2560\n"
    "buffer_load_dwordx4 v[80:83], v9, s[52:55], s65 offen offset:0 sc1\n"
    "v_mfma_f32_16x16x32_bf16 v[240:243], a[192:195], a[160:163], v[240:243]\n"
    "s_waitcnt lgkmcnt(8)\n"
    "ds_read_b64_tr_b16 a[216:217], v60 offset:10304\n"
    "ds_read_b64_tr_b16 a[218:219], v60 offset:12864\n"
    "v_mfma_f32_16x16x32_bf16 v[240:243], a[196:199], a[164:167], v[240:243]\n"
    "s_waitcnt lgkmcnt(8)\n"
    "ds_read_b64_tr_b16 a[220:221], v60 offset:15424\n"
    "ds_read_b64_tr_b16 a[222:223], v60 offset:17984\n"
    "buffer_load_dwordx4 v[84:87], v9, s[52:55], s65 offen offset:1024 sc1\n"
    "v_mfma_f32_16x16x32_bf16 v[208:211], a[200:203], a[168:171], v[208:211]\n"
    "s_waitcnt lgkmcnt(8)\n"
    "ds_read_b64_tr_b16 a[224:225], v60 offset:10336\n"
    "ds_read_b64_tr_b16 a[226:227], v60 offset:12896\n"
    "v_mfma_f32_16x16x32_bf16 v[208:211], a[204:207], a[172:175], v[208:211]\n"
    "s_waitcnt lgkmcnt(8)\n"
    "ds_read_b64_tr_b16 a[228:229], v60 offset:15456\n"
    "ds_read_b64_tr_b16 a[230:231], v60 offset:18016\n"
    "buffer_load_dwordx2 v[132:133], v59, s[52:55], s65 offen sc1\n"
    "v_mfma_f32_16x16x32_bf16 v[212:215], a[208:211], a[168:171], v[212:215]\n"
    "s_waitcnt lgkmcnt(8)\n"
    "ds_read_b64_tr_b16 a[232:233], v60 offset:10368\n"
    "ds_read_b64_tr_b16 a[234:235], v60 offset:12928\n"
    "v_mfma_f32_16x16x32_bf16 v[212:215], a[212:215], a[172:175], v[212:215]\n"
    "s_waitcnt lgkmcnt(8)\n"
    "ds_read_b64_tr_b16 a[236:237], v60 offset:15488\n"
    "ds_read_b64_tr_b16 a[238:239], v60 offset:18048\n"
    "s_waitcnt vmcnt(37)\n"
    "s_barrier\n"
    "v_add_u32 v60, 20480, v60\n"
    "s_cmp_ge_u32 s35, 2\n"
    "s_cselect_b32 s67, -2, 10\n"
    "s_add_u32 s67, s67, s35\n"
    "s_lshl_b32 s67, s67, 12\n"
    "s_add_u32 s67, s67, s66\n"
    "buffer_load_dwordx4 a[96:99], v9, s[52:55], s67 offen offset:0 sc1\n"
    "v_mfma_f32_16x16x32_bf16 v[216:219], a[216:219], a[168:171], v[216:219]\n"
    "s_waitcnt lgkmcnt(8)\n"
    "ds_read_b64_tr_b16 a[192:193], v60 offset:0\n"
    "ds_read_b64_tr_b16 a[194:195], v60 offset:2560\n"
    "v_mfma_f32_16x16x32_bf16 v[216:219], a[220:223], a[172:175], v[216:219]\n"
    "s_waitcnt lgkmcnt(8)\n"
    "ds_read_b64_tr_b16 a[196:197], v60 offset:5120\n"
    "ds_read_b64_tr_b16 a[198:199], v60 offset:7680\n"
    "buffer_load_dwordx4 a[100:103], v9, s[52:55], s67 offen offset:1024 sc1\n"
    "v_mfma_f32_16x16x32_bf16 v[220:223], a[224:227], a[168:171], v[220:223]\n"
    "s_waitcnt lgkmcnt(8)\n"
    "ds_read_b64_tr_b16 a[200:201], v60 offset:32\n"
    "ds_read_b64_tr_b16 a[202:203], v60 offset:2592\n"
    "v_mfma_f32_16x16x32_bf16 v[220:223], a[228:231], a[172:175], v[220:223]\n"
    "s_waitcnt lgkmcnt(8)\n"
    "ds_read_b64_tr_b16 a[204:205], v60 offset:5152\n"
    "ds_read_b64_tr_b16 a[206:207], v60 offset:7712\n"
    "buffer_load_dwordx4 a[104:107], v9, s[52:55], s67 offen offset:2048 sc1\n"
    "v_mfma_f32_16x16x32_bf16 v[240:243], a[232:235], a[168:171], v[240:243]\n"
    "s_waitcnt lgkmcnt(8)\n"
    "ds_read_b64_tr_b16 a[208:209], v60 offset:64\n"
    "ds_read_b64_tr_b16 a[210:211], v60 offset:2624\n"
    "v_mfma_f32_16x16x32_bf16 v[240:243], a[236:239], a[172:175], v[240:243]\n"
    "s_waitcnt lgkmcnt(8)\n"
    "ds_read_b64_tr_b16 a[212:213], v60 offset:5184\n"
    "ds_read_b64_tr_b16 a[214:215], v60 offset:7744\n"
    "buffer_load_dwordx4 a[108:111], v9, s[52:55], s67 offen offset:3072 sc1\n"
    "v_mfma_f32_16x16x32_bf16 v[208:211], a[192:195], a[240:243], v[208:211]\n"
    "s_waitcnt lgkmcnt(8)\n"
    "ds_read_b64_tr_b16 a[216:217], v60 offset:96\n"
    "ds_read_b64_tr_b16 a[218:219], v60 offset:2656\n"
    "v_mfma_f32_16x16x32_bf16 v[208:211], a[196:199], a[244:247], v[208:211]\n"
    "s_waitcnt lgkmcnt(8)\n"
    "ds_read_b64_tr_b16 a[220:221], v60 offset:5216\n"
    "ds_read_b64_tr_b16 a[222:223], v60 offset:7776\n"
    "s_cmp_ge_u32 s35, 2\n"
    "s_cselect_b32 s90, -2, 10\n"
    "s_add_u32 s90, s90, s35\n"
    "s_mul_i32 s90, s90, 20480\n"
    "s_add_u32 s90, s90, s71\n"
    "s_add_u32 m0, s91, 143360\n"
    "s_nop 0\n"
    "buffer_load_dwordx4 v9, s[52:55], s90 offen offset:0 sc1 lds\n"
    "v_mfma_f32_16x16x32_bf16 v[212:215], a[200:203], a[240:243], v[212:215]\n"
    "s_waitcnt lgkmcnt(8)\n"
    "ds_read_b64_tr_b16 a[224:225], v60 offset:128\n"
    "ds_read_b64_tr_b16 a[226:227], v60 offset:2688\n"
    "v_mfma_f32_16x16x32_bf16 v[212:215], a[204:207], a[244:247], v[212:215]\n"
    "s_waitcnt lgkmcnt(8)\n"
    "ds_read_b64_tr_b16 a[228:229], v60 offset:5248\n"
    "ds_read_b64_tr_b16 a[230:231], v60 offset:7808\n"
    "buffer_load_dwordx4 v9, s[52:55], s90 offen offset:1024 sc1 lds\n"
    "v_mfma_f32_16x16x32_bf16 v[216:219], a[208:211], a[240:243], v[216:219]\n"
    "s_waitcnt lgkmcnt(8)\n"
    "ds_read_b64_tr_b16 a[232:233], v60 offset:10240\n"
    "ds_read_b64_tr_b16 a[234:235], v60 offset:12800\n"
    "v_mfma_f32_16x16x32_bf16 v[216:219], a[212:215], a[244:247], v[216:219]\n"
    "s_waitcnt lgkmcnt(8)\n"
    "ds_read_b64_tr_b16 a[236:237], v60 offset:15360\n"
    "ds_read_b64_tr_b16 a[238:239], v60 offset:17920\n"
    "v_mfma_f32_16x16x32_bf16 v[220:223], a[216:219], a[240:243], v[220:223]\n"
    "s_waitcnt lgkmcnt(8)\n"
    "ds_read_b64_tr_b16 a[192:193], v60 offset:10272\n"
    "ds_read_b64_tr_b16 a[194:195], v60 offset:12832\n"
    "buffer_load_dwordx4 v9, s[52:55], s90 offen offset:2048 sc1 lds\n"
    "v_mfma_f32_16x16x32_bf16 v[220:223], a[220:223], a[244:247], v[220:223]\n"
    "s_waitcnt lgkmcnt(8)\n"
    "ds_read_b64_tr_b16 a[196:197], v60 offset:15392\n"
    "ds_read_b64_tr_b16 a[198:199], v60 offset:17952\n"
    "v_mfma_f32_16x16x32_bf16 v[240:243], a[224:227], a[240:243], v[240:243]\n"
    "s_waitcnt lgkmcnt(8)\n"
    "ds_read_b64_tr_b16 a[200:201], v60 offset:10304\n"
    "ds_read_b64_tr_b16 a[202:203], v60 offset:12864\n"
    "buffer_load_dwordx4 v9, s[52:55], s90 offen offset:3072 sc1 lds\n"
    "v_mfma_f32_16x16x32_bf16 v[240:243], a[228:231], a[244:247], v[240:243]\n"
    "s_waitcnt lgkmcnt(8)\n"
    "ds_read_b64_tr_b16 a[204:205], v60 offset:15424\n"
    "ds_read_b64_tr_b16 a[206:207], v60 offset:17984\n"
    "v_mfma_f32_16x16x32_bf16 v[208:211], a[232:235], a[248:251], v[208:211]\n"
    "s_waitcnt lgkmcnt(8)\n"
    "ds_read_b64_tr_b16 a[208:209], v60 offset:10336\n"
    "ds_read_b64_tr_b16 a[210:211], v60 offset:12896\n"
    "s_add_u32 s90, s90, 4096\n"
    "s_add_u32 m0, s91, 147456\n"
    "s_nop 0\n"
    "buffer_load_dwordx4 v9, s[52:55], s90 offen offset:0 sc1 lds\n"
    "v_mfma_f32_16x16x32_bf16 v[208:211], a[236:239], a[252:255], v[208:211]\n"
    "s_waitcnt lgkmcnt(8)\n"
    "ds_read_b64_tr_b16 a[212:213], v60 offset:15456\n"
    "ds_read_b64_tr_b16 a[214:215], v60 offset:18016\n"
    "v_mfma_f32_16x16x32_bf16 v[212:215], a[192:195], a[248:251], v[212:215]\n"
    "s_waitcnt lgkmcnt(8)\n"
    "ds_read_b64_tr_b16 a[216:217], v60 offset:10368\n"
    "ds_read_b64_tr_b16 a[218:219], v60 offset:12928\n"
    "s_lshl_b32 s73, s77, 2\n"
    "s_lshr_b32 s74, s78, 1\n"
    "s_add_u32 s73, s73, s74\n"
    "s_lshl_b32 s73, s73, 3\n"
    "s_and_b32 s74, s78, 1\n"
    "s_xor_b32 s74, s74, 1\n"
    "s_add_u32 s73, s73, s74\n"
    "v_lshrrev_b32 v56, 4, v9\n"
    "v_min_u32 v56, 3, v56\n"
    "v_mul_u32_u24 v56, 2, v56\n"
    "v_add_u32 v56, s73, v56\n"
    "v_lshlrev_b32 v56, 6, v56\n"
    "v_add_u32 v56, 118784, v56\n"
    "buffer_load_dwordx2 v[62:63], v56, s[52:55], 0 offen sc1\n"
    "v_mfma_f32_16x16x32_bf16 v[212:215], a[196:199], a[252:255], v[212:215]\n"
    "s_waitcnt lgkmcnt(8)\n"
    "ds_read_b64_tr_b16 a[220:221], v60 offset:15488\n"
    "ds_read_b64_tr_b16 a[222:223], v60 offset:18048\n"
    "s_waitcnt vmcnt(38)\n"
    "s_barrier\n"
    "v_add_u32 v60, 20480, v60\n"
    "v_mfma_f32_16x16x32_bf16 v[216:219], a[200:203], a[248:251], v[216:219]\n"
    "s_waitcnt lgkmcnt(8)\n"
    "ds_read_b64_tr_b16 a[224:225], v60 offset:0\n"
    "ds_read_b64_tr_b16 a[226:227], v60 offset:2560\n"
    "s_add_u32 s65, s65, 2560\n"
    "buffer_load_dwordx4 v[88:91], v9, s[52:55], s65 offen offset:0 sc1\n"
    "v_mfma_f32_16x16x32_bf16 v[216:219], a[204:207], a[252:255], v[216:219]\n"
    "s_waitcnt lgkmcnt(8)\n"
    "ds_read_b64_tr_b16 a[228:229], v60 offset:5120\n"
    "ds_read_b64_tr_b16 a[230:231], v60 offset:7680\n"
    "v_mfma_f32_16x16x32_bf16 v[220:223], a[208:211], a[248:251], v[220:223]\n"
    "s_waitcnt lgkmcnt(8)\n"
    "ds_read_b64_tr_b16 a[232:233], v60 offset:32\n"
    "ds_read_b64_tr_b16 a[234:235], v60 offset:2592\n"
    "buffer_load_dwordx4 v[92:95], v9, s[52:55], s65 offen offset:1024 sc1\n"
    "v_mfma_f32_16x16x32_bf16 v[220:223], a[212:215], a[252:255], v[220:223]\n"
    "s_waitcnt lgkmcnt(8)\n"
    "ds_read_b64_tr_b16 a[236:237], v60 offset:5152\n"
    "ds_read_b64_tr_b16 a[238:239], v60 offset:7712\n"
    "v_mfma_f32_16x16x32_bf16 v[240:243], a[216:219], a[248:251], v[240:243]\n"
    "s_waitcnt lgkmcnt(8)\n"
    "ds_read_b64_tr_b16 a[192:193], v60 offset:64\n"
    "ds_read_b64_tr_b16 a[194:195], v60 offset:2624\n"
    "buffer_load_dwordx2 v[134:135], v59, s[52:55], s65 offen sc1\n"
    "v_mfma_f32_16x16x32_bf16 v[240:243], a[220:223], a[252:255], v[240:243]\n"
    "s_waitcnt lgkmcnt(8)\n"
    "ds_read_b64_tr_b16 a[196:197], v60 offset:5184\n"
    "ds_read_b64_tr_b16 a[198:199], v60 offset:7744\n"
    "v_mfma_f32_16x16x32_bf16 v[208:211], a[224:227], a[144:147], v[208:211]\n"
    "s_waitcnt lgkmcnt(8)\n"
    "ds_read_b64_tr_b16 a[200:201], v60 offset:96\n"
    "ds_read_b64_tr_b16 a[202:203], v60 offset:2656\n"
    "s_cmp_ge_u32 s35, 1\n"
    "s_cselect_b32 s67, -1, 11\n"
    "s_add_u32 s67, s67, s35\n"
    "s_lshl_b32 s67, s67, 12\n"
    "s_add_u32 s67, s67, s66\n"
    "buffer_load_dwordx4 a[0:3], v9, s[52:55], s67 offen offset:0 sc1\n"
    "v_mfma_f32_16x16x32_bf16 v[208:211], a[228:231], a[148:151], v[208:211]\n"
    "s_waitcnt lgkmcnt(8)\n"
    "ds_read_b64_tr_b16 a[204:205], v60 offset:5216\n"
    "ds_read_b64_tr_b16 a[206:207], v60 offset:7776\n"
    "v_mfma_f32_16x16x32_bf16 v[212:215], a[232:235], a[144:147], v[212:215]\n"
    "s_waitcnt lgkmcnt(8)\n"
    "ds_read_b64_tr_b16 a[208:209], v60 offset:128\n"
    "ds_read_b64_tr_b16 a[210:211], v60 offset:2688\n"
    "v_mfma_f32_16x16x32_bf16 v[212:215], a[236:239], a[148:151], v[212:215]\n"
    "s_waitcnt lgkmcnt(8)\n"
    "ds_read_b64_tr_b16 a[212:213], v60 offset:5248\n"
    "ds_read_b64_tr_b16 a[214:215], v60 offset:7808\n"
    "buffer_load_dwordx4 a[4:7], v9, s[52:55], s67 offen offset:1024 sc1\n"
    "v_mfma_f32_16x16x32_bf16 v[216:219], a[192:195], a[144:147], v[216:219]\n"
    "s_waitcnt lgkmcnt(8)\n"
    "ds_read_b64_tr_b16 a[216:217], v60 offset:10240\n"
    "ds_read_b64_tr_b16 a[218:219], v60 offset:12800\n"
    "v_mfma_f32_16x16x32_bf16 v[216:219], a[196:199], a[148:151], v[216:219]\n"
    "s_waitcnt lgkmcnt(8)\n"
    "ds_read_b64_tr_b16 a[220:221], v60 offset:15360\n"
    "ds_read_b64_tr_b16 a[222:223], v60 offset:17920\n"
    "buffer_load_dwordx4 a[8:11], v9, s[52:55], s67 offen offset:2048 sc1\n"
    "v_mfma_f32_16x16x32_bf16 v[220:223], a[200:203], a[144:147], v[220:223]\n"
    "s_waitcnt lgkmcnt(8)\n"
    "ds_read_b64_tr_b16 a[224:225], v60 offset:10272\n"
    "ds_read_b64_tr_b16 a[226:227], v60 offset:12832\n"
    "v_mfma_f32_16x16x32_bf16 v[220:223], a[204:207], a[148:151], v[220:223]\n"
    "s_waitcnt lgkmcnt(8)\n"
    "ds_read_b64_tr_b16 a[228:229], v60 offset:15392\n"
    "ds_read_b64_tr_b16 a[230:231], v60 offset:17952\n"
    "buffer_load_dwordx4 a[12:15], v9, s[52:55], s67 offen offset:3072 sc1\n"
    "v_mfma_f32_16x16x32_bf16 v[240:243], a[208:211], a[144:147], v[240:243]\n"
    "s_waitcnt lgkmcnt(8)\n"
    "ds_read_b64_tr_b16 a[232:233], v60 offset:10304\n"
    "ds_read_b64_tr_b16 a[234:235], v60 offset:12864\n"
    "v_mfma_f32_16x16x32_bf16 v[240:243], a[212:215], a[148:151], v[240:243]\n"
    "s_waitcnt lgkmcnt(8)\n"
    "ds_read_b64_tr_b16 a[236:237], v60 offset:15424\n"
    "ds_read_b64_tr_b16 a[238:239], v60 offset:17984\n"
    "s_cmp_ge_u32 s35, 1\n"
    "s_cselect_b32 s90, -1, 11\n"
    "s_add_u32 s90, s90, s35\n"
    "s_mul_i32 s90, s90, 20480\n"
    "s_add_u32 s90, s90, s71\n"
    "s_add_u32 m0, s91, 0\n"
    "s_nop 0\n"
    "buffer_load_dwordx4 v9, s[52:55], s90 offen offset:0 sc1 lds\n"
    "v_mfma_f32_16x16x32_bf16 v[208:211], a[216:219], a[152:155], v[208:211]\n"
    "s_waitcnt lgkmcnt(8)\n"
    "ds_read_b64_tr_b16 a[192:193], v60 offset:10336\n"
    "ds_read_b64_tr_b16 a[194:195], v60 offset:12896\n"
    "v_mfma_f32_16x16x32_bf16 v[208:211], a[220:223], a[156:159], v[208:211]\n"
    "s_waitcnt lgkmcnt(8)\n"
    "ds_read_b64_tr_b16 a[196:197], v60 offset:15456\n"
    "ds_read_b64_tr_b16 a[198:199], v60 offset:18016\n"
    "buffer_load_dwordx4 v9, s[52:55], s90 offen offset:1024 sc1 lds\n"
    "v_mfma_f32_16x16x32_bf16 v[212:215], a[224:227], a[152:155], v[212:215]\n"
    "s_waitcnt lgkmcnt(8)\n"
    "ds_read_b64_tr_b16 a[200:201], v60 offset:10368\n"
    "ds_read_b64_tr_b16 a[202:203], v60 offset:12928\n"
    "v_mfma_f32_16x16x32_bf16 v[212:215], a[228:231], a[156:159], v[212:215]\n"
    "s_waitcnt lgkmcnt(8)\n"
    "ds_read_b64_tr_b16 a[204:205], v60 offset:15488\n"
    "ds_read_b64_tr_b16 a[206:207], v60 offset:18048\n"
    "s_waitcnt vmcnt(34)\n"
    "s_barrier\n"
    "v_add_u32 v60, 20480, v60\n"
    "buffer_load_dwordx4 v9, s[52:55], s90 offen offset:2048 sc1 lds\n"
    "v_mfma_f32_16x16x32_bf16 v[216:219], a[232:235], a[152:155], v[216:219]\n"
    "s_waitcnt lgkmcnt(8)\n"
    "ds_read_b64_tr_b16 a[208:209], v60 offset:0\n"
    "ds_read_b64_tr_b16 a[210:211], v60 offset:2560\n"
    "v_mfma_f32_16x16x32_bf16 v[216:219], a[236:239], a[156:159], v[216:219]\n"
    "s_waitcnt lgkmcnt(8)\n"
    "ds_read_b64_tr_b16 a[212:213], v60 offset:5120\n"
    "ds_read_b64_tr_b16 a[214:215], v60 offset:7680\n"
    "buffer_load_dwordx4 v9, s[52:55], s90 offen offset:3072 sc1 lds\n"
    "v_mfma_f32_16x16x32_bf16 v[220:223], a[192:195], a[152:155], v[220:223]\n"
    "s_waitcnt lgkmcnt(8)\n"
    "ds_read_b64_tr_b16 a[216:217], v60 offset:32\n"
    "ds_read_b64_tr_b16 a[218:219], v60 offset:2592\n"
    "v_mfma_f32_16x16x32_bf16 v[220:223], a[196:199], a[156:159], v[220:223]\n"
    "s_waitcnt lgkmcnt(8)\n"
    "ds_read_b64_tr_b16 a[220:221], v60 offset:5152\n"
    "ds_read_b64_tr_b16 a[222:223], v60 offset:7712\n"
    "s_add_u32 s90, s90, 4096\n"
    "s_add_u32 m0, s91, 4096\n"
    "s_nop 0\n"
    "buffer_load_dwordx4 v9, s[52:55], s90 offen offset:0 sc1 lds\n"
    "v_mfma_f32_16x16x32_bf16 v[240:243], a[200:203], a[152:155], v[240:243]\n"
    "s_waitcnt lgkmcnt(8)\n"
    "ds_read_b64_tr_b16 a[224:225], v60 offset:64\n"
    "ds_read_b64_tr_b16 a[226:227], v60 offset:2624\n"
    "v_mfma_f32_16x16x32_bf16 v[240:243], a[204:207], a[156:159], v[240:243]\n"
    "s_waitcnt lgkmcnt(8)\n"
    "ds_read_b64_tr_b16 a[228:229], v60 offset:5184\n"
    "ds_read_b64_tr_b16 a[230:231], v60 offset:7744\n"
    "v_mfma_f32_16x16x32_bf16 v[208:211], a[208:211], a[176:179], v[208:211]\n"
    "s_waitcnt lgkmcnt(8)\n"
    "ds_read_b64_tr_b16 a[232:233], v60 offset:96\n"
    "ds_read_b64_tr_b16 a[234:235], v60 offset:2656\n"
    "s_waitcnt vmcnt(12)\n"
    "v_cmp_ne_u64 vcc, s[80:81], v[62:63]\n"
    "s_nop 1\n"
    "s_cbranch_vccnz L_slow10\n"
    "L_back10:\n"
    "s_add_u32 s65, s65, 2560\n"
    "buffer_load_dwordx4 v[96:99], v9, s[52:55], s65 offen offset:0 sc1\n"
    "v_mfma_f32_16x16x32_bf16 v[208:211], a[212:215], a[180:183], v[208:211]\n"
    "s_waitcnt lgkmcnt(8)\n"
    "ds_read_b64_tr_b16 a[236:237], v60 offset:5216\n"
    "ds_read_b64_tr_b16 a[238:239], v60 offset:7776\n"
    "v_mfma_f32_16x16x32_bf16 v[212:215], a[216:219], a[176:179], v[212:215]\n"
    "s_waitcnt lgkmcnt(8)\n"
    "ds_read_b64_tr_b16 a[192:193], v60 offset:128\n"
    "ds_read_b64_tr_b16 a[194:195], v60 offset:2688\n"
    "v_mfma_f32_16x16x32_bf16 v[212:215], a[220:223], a[180:183], v[212:215]\n"
    "s_waitcnt lgkmcnt(8)\n"
    "ds_read_b64_tr_b16 a[196:197], v60 offset:5248\n"
    "ds_read_b64_tr_b16 a[198:199], v60 offset:7808\n"
    "buffer_load_dwordx4 v[100:103], v9, s[52:55], s65 offen offset:1024 sc1\n"
    "v_mfma_f32_16x16x32_bf16 v[216:219], a[224:227], a[176:179], v[216:219]\n"
    "s_waitcnt lgkmcnt(8)\n"
    "ds_read_b64_tr_b16 a[200:201], v60 offset:10240\n"
    "ds_read_b64_tr_b16 a[202:203], v60 offset:12800\n"
    "v_mfma_f32_16x16x32_bf16 v[216:219], a[228:231], a[180:183], v[216:219]\n"
    "s_waitcnt lgkmcnt(8)\n"
    "ds_read_b64_tr_b16 a[204:205], v60 offset:15360\n"
    "ds_read_b64_tr_b16 a[206:207], v60 offset:17920\n"
    "v_mfma_f32_16x16x32_bf16 v[220:223], a[232:235], a[176:179], v[220:223]\n"
    "s_waitcnt lgkmcnt(8)\n"
    "ds_read_b64_tr_b16 a[208:209], v60 offset:10272\n"
    "ds_read_b64_tr_b16 a[210:211], v60 offset:12832\n"
    "v_mfma_f32_16x16x32_bf16 v[220:223], a[236:239], a[180:183], v[220:223]\n"
    "s_waitcnt lgkmcnt(8)\n"
    "ds_read_b64_tr_b16 a[212:213], v60 offset:15392\n"
    "ds_read_b64_tr_b16 a[214:215], v60 offset:17952\n"
    "buffer_load_dwordx2 v[136:137], v59, s[52:55], s65 offen sc1\n"
    "v_mfma_f32_16x16x32_bf16 v[240:243], a[192:195], a[176:179], v[240:243]\n"
    "s_waitcnt lgkmcnt(8)\n"
    "ds_read_b64_tr_b16 a[216:217], v60 offset:10304\n"
    "ds_read_b64_tr_b16 a[218:219], v60 offset:12864\n"
    "v_mfma_f32_16x16x32_bf16 v[240:243], a[196:199], a[180:183], v[240:243]\n"
    "s_waitcnt lgkmcnt(8)\n"
    "ds_read_b64_tr_b16 a[220:221], v60 offset:15424\n"
    "ds_read_b64_tr_b16 a[222:223], v60 offset:17984\n"
    "v_mfma_f32_16x16x32_bf16 v[208:211], a[200:203], a[184:187], v[208:211]\n"
    "s_waitcnt lgkmcnt(8)\n"
    "ds_read_b64_tr_b16 a[224:225], v60 offset:10336\n"
    "ds_read_b64_tr_b16 a[226:227], v60 offset:12896\n"
    "v_mfma_f32_16x16x32_bf16 v[208:211], a[204:207], a[188:191], v[208:211]\n"
    "s_waitcnt lgkmcnt(8)\n"
    "ds_read_b64_tr_b16 a[228:229], v60 offset:15456\n"
    "ds_read_b64_tr_b16 a[230:231], v60 offset:18016\n"
    "s_add_u32 s65, s65, 2560\n"
    "buffer_load_dwordx4 v[104:107], v9, s[52:55], s65 offen offset:0 sc1\n"
    "v_mfma_f32_16x16x32_bf16 v[212:215], a[208:211], a[184:187], v[212:215]\n"
    "s_waitcnt lgkmcnt(8)\n"
    "ds_read_b64_tr_b16 a[232:233], v60 offset:10368\n"
    "ds_read_b64_tr_b16 a[234:235], v60 offset:12928\n"
    "v_mfma_f32_16x16x32_bf16 v[212:215], a[212:215], a[188:191], v[212:215]\n"
    "s_waitcnt lgkmcnt(8)\n"
    "ds_read_b64_tr_b16 a[236:237], v60 offset:15488\n"
    "ds_read_b64_tr_b16 a[238:239], v60 offset:18048\n"
    "s_barrier\n"
    "v_add_u32 v60, 20480, v60\n"
    "v_mfma_f32_16x16x32_bf16 v[216:219], a[216:219], a[184:187], v[216:219]\n"
    "s_waitcnt lgkmcnt(8)\n"
    "ds_read_b64_tr_b16 a[192:193], v60 offset:0\n"
    "ds_read_b64_tr_b16 a[194:195], v60 offset:2560\n"
    "buffer_load_dwordx4 v[108:111], v9, s[52:55], s65 offen offset:1024 sc1\n"
    "v_mfma_f32_16x16x32_bf16 v[216:219], a[220:223], a[188:191], v[216:219]\n"
    "s_waitcnt lgkmcnt(8)\n"
    "ds_read_b64_tr_b16 a[196:197], v60 offset:5120\n"
    "ds_read_b64_tr_b16 a[198:199], v60 offset:7680\n"
    "v_mfma_f32_16x16x32_bf16 v[220:223], a[224:227], a[184:187], v[220:223]\n"
    "s_waitcnt lgkmcnt(8)\n"
    "ds_read_b64_tr_b16 a[200:201], v60 offset:32\n"
    "ds_read_b64_tr_b16 a[202:203], v60 offset:2592\n"
    "v_mfma_f32_16x16x32_bf16 v[220:223], a[228:231], a[188:191], v[220:223]\n"
    "s_waitcnt lgkmcnt(8)\n"
    "ds_read_b64_tr_b16 a[204:205], v60 offset:5152\n"
    "ds_read_b64_tr_b16 a[206:207], v60 offset:7712\n"
    "v_mfma_f32_16x16x32_bf16 v[240:243], a[232:235], a[184:187], v[240:243]\n"
    "s_waitcnt lgkmcnt(8)\n"
    "ds_read_b64_tr_b16 a[208:209], v60 offset:64\n"
    "ds_read_b64_tr_b16 a[210:211], v60 offset:2624\n"
    "buffer_load_dwordx2 v[138:139], v59, s[52:55], s65 offen sc1\n"
    "v_mfma_f32_16x16x32_bf16 v[240:243], a[236:239], a[188:191], v[240:243]\n"
    "s_waitcnt lgkmcnt(8)\n"
    "ds_read_b64_tr_b16 a[212:213], v60 offset:5184\n"
    "ds_read_b64_tr_b16 a[214:215], v60 offset:7744\n"
    "v_mfma_f32_16x16x32_bf16 v[208:211], a[192:195], a[64:67], v[208:211]\n"
    "s_waitcnt lgkmcnt(8)\n"
    "ds_read_b64_tr_b16 a[216:217], v60 offset:96\n"
    "ds_read_b64_tr_b16 a[218:219], v60 offset:2656\n"
    "v_mfma_f32_16x16x32_bf16 v[208:211], a[196:199], a[68:71], v[208:211]\n"
    "s_waitcnt lgkmcnt(8)\n"
    "ds_read_b64_tr_b16 a[220:221], v60 offset:5216\n"
    "ds_read_b64_tr_b16 a[222:223], v60 offset:7776\n"
    "v_mfma_f32_16x16x32_bf16 v[212:215], a[200:203], a[64:67], v[212:215]\n"
    "s_waitcnt lgkmcnt(8)\n"
    "ds_read_b64_tr_b16 a[224:225], v60 offset:128\n"
    "ds_read_b64_tr_b16 a[226:227], v60 offset:2688\n"
    "s_add_u32 s65, s65, 2560\n"
    "buffer_load_dwordx4 v[112:115], v9, s[52:55], s65 offen offset:0 sc1\n"
    "v_mfma_f32_16x16x32_bf16 v[212:215], a[204:207], a[68:71], v[212:215]\n"
    "s_waitcnt lgkmcnt(8)\n"
    "ds_read_b64_tr_b16 a[228:229], v60 offset:5248\n"
    "ds_read_b64_tr_b16 a[230:231], v60 offset:7808\n"
    "v_mfma_f32_16x16x32_bf16 v[216:219], a[208:211], a[64:67], v[216:219]\n"
    "s_waitcnt lgkmcnt(8)\n"
    "ds_read_b64_tr_b16 a[232:233], v60 offset:10240\n"
    "ds_read_b64_tr_b16 a[234:235], v60 offset:12800\n"
    "v_mfma_f32_16x16x32_bf16 v[216:219], a[212:215], a[68:71], v[216:219]\n"
    "s_waitcnt lgkmcnt(8)\n"
    "ds_read_b64_tr_b16 a[236:237], v60 offset:15360\n"
    "ds_read_b64_tr_b16 a[238:239], v60 offset:17920\n"
    "buffer_load_dwordx4 v[116:119], v9, s[52:55], s65 offen offset:1024 sc1\n"
    "v_mfma_f32_16x16x32_bf16 v[220:223], a[216:219], a[64:67], v[220:223]\n"
    "s_waitcnt lgkmcnt(8)\n"
    "ds_read_b64_tr_b16 a[192:193], v60 offset:10272\n"
    "ds_read_b64_tr_b16 a[194:195], v60 offset:12832\n"
    "v_mfma_f32_16x16x32_bf16 v[220:223], a[220:223], a[68:71], v[220:223]\n"
    "s_waitcnt lgkmcnt(8)\n"
    "ds_read_b64_tr_b16 a[196:197], v60 offset:15392\n"
    "ds_read_b64_tr_b16 a[198:199], v60 offset:17952\n"
    "v_mfma_f32_16x16x32_bf16 v[240:243], a[224:227], a[64:67], v[240:243]\n"
    "s_waitcnt lgkmcnt(8)\n"
    "ds_read_b64_tr_b16 a[200:201], v60 offset:10304\n"
    "ds_read_b64_tr_b16 a[202:203], v60 offset:12864\n"
    "v_mfma_f32_16x16x32_bf16 v[240:243], a[228:231], a[68:71], v[240:243]\n"
    "s_waitcnt lgkmcnt(8)\n"
    "ds_read_b64_tr_b16 a[204:205], v60 offset:15424\n"
    "ds_read_b64_tr_b16 a[206:207], v60 offset:17984\n"
    "buffer_load_dwordx2 v[140:141], v59, s[52:55], s65 offen sc1\n"
    "v_mfma_f32_16x16x32_bf16 v[208:211], a[232:235], a[72:75], v[208:211]\n"
    "s_waitcnt lgkmcnt(8)\n"
    "ds_read_b64_tr_b16 a[208:209], v60 offset:10336\n"
    "ds_read_b64_tr_b16 a[210:211], v60 offset:12896\n"
    "v_mfma_f32_16x16x32_bf16 v[208:211], a[236:239], a[76:79], v[208:211]\n"
    "s_waitcnt lgkmcnt(8)\n"
    "ds_read_b64_tr_b16 a[212:213], v60 offset:15456\n"
    "ds_read_b64_tr_b16 a[214:215], v60 offset:18016\n"
    "v_mfma_f32_16x16x32_bf16 v[212:215], a[192:195], a[72:75], v[212:215]\n"
    "s_waitcnt lgkmcnt(8)\n"
    "ds_read_b64_tr_b16 a[216:217], v60 offset:10368\n"
    "ds_read_b64_tr_b16 a[218:219], v60 offset:12928\n"
    "v_mfma_f32_16x16x32_bf16 v[212:215], a[196:199], a[76:79], v[212:215]\n"
    "s_waitcnt lgkmcnt(8)\n"
    "ds_read_b64_tr_b16 a[220:221], v60 offset:15488\n"
    "ds_read_b64_tr_b16 a[222:223], v60 offset:18048\n"
    "s_barrier\n"
    "v_add_u32 v60, 20480, v60\n"
    "s_add_u32 s65, s65, 2560\n"
    "buffer_load_dwordx4 v[120:123], v9, s[52:55], s65 offen offset:0 sc1\n"
    "v_mfma_f32_16x16x32_bf16 v[216:219], a[200:203], a[72:75], v[216:219]\n"
    "s_waitcnt lgkmcnt(8)\n"
    "ds_read_b64_tr_b16 a[224:225], v60 offset:0\n"
    "ds_read_b64_tr_b16 a[226:227], v60 offset:2560\n"
    "v_mfma_f32_16x16x32_bf16 v[216:219], a[204:207], a[76:79], v[216:219]\n"
    "s_waitcnt lgkmcnt(8)\n"
    "ds_read_b64_tr_b16 a[228:229], v60 offset:5120\n"
    "ds_read_b64_tr_b16 a[230:231], v60 offset:7680\n"
    "v_mfma_f32_16x16x32_bf16 v[220:223], a[208:211], a[72:75], v[220:223]\n"
    "s_waitcnt lgkmcnt(8)\n"
    "ds_read_b64_tr_b16 a[232:233], v60 offset:32\n"
    "ds_read_b64_tr_b16 a[234:235], v60 offset:2592\n"
    "buffer_load_dwordx4 v[124:127], v9, s[52:55], s65 offen offset:1024 sc1\n"
    "v_mfma_f32_16x16x32_bf16 v[220:223], a[212:215], a[76:79], v[220:223]\n"
    "s_waitcnt lgkmcnt(8)\n"
    "ds_read_b64_tr_b16 a[236:237], v60 offset:5152\n"
    "ds_read_b64_tr_b16 a[238:239], v60 offset:7712\n"
    "v_mfma_f32_16x16x32_bf16 v[240:243], a[216:219], a[72:75], v[240:243]\n"
    "s_waitcnt lgkmcnt(8)\n"
    "ds_read_b64_tr_b16 a[192:193], v60 offset:64\n"
    "ds_read_b64_tr_b16 a[194:195], v60 offset:2624\n"
    "v_mfma_f32_16x16x32_bf16 v[240:243], a[220:223], a[76:79], v[240:243]\n"
    "s_waitcnt lgkmcnt(8)\n"
    "ds_read_b64_tr_b16 a[196:197], v60 offset:5184\n"
    "ds_read_b64_tr_b16 a[198:199], v60 offset:7744\n"
    "v_mfma_f32_16x16x32_bf16 v[208:211], a[224:227], a[96:99], v[208:211]\n"
    "s_waitcnt lgkmcnt(8)\n"
    "ds_read_b64_tr_b16 a[200:201], v60 offset:96\n"
    "ds_read_b64_tr_b16 a[202:203], v60 offset:2656\n"
    "buffer_load_dwordx2 v[142:143], v59, s[52:55], s65 offen sc1\n"
    "v_mfma_f32_16x16x32_bf16 v[208:211], a[228:231], a[100:103], v[208:211]\n"
    "s_waitcnt lgkmcnt(8)\n"
    "ds_read_b64_tr_b16 a[204:205], v60 offset:5216\n"
    "ds_read_b64_tr_b16 a[206:207], v60 offset:7776\n"
    "v_mfma_f32_16x16x32_bf16 v[212:215], a[232:235], a[96:99], v[212:215]\n"
    "s_waitcnt lgkmcnt(8)\n"
    "ds_read_b64_tr_b16 a[208:209], v60 offset:128\n"
    "ds_read_b64_tr_b16 a[210:211], v60 offset:2688\n"
    "v_mfma_f32_16x16x32_bf16 v[212:215], a[236:239], a[100:103], v[212:215]\n"
    "s_waitcnt lgkmcnt(8)\n"
    "ds_read_b64_tr_b16 a[212:213], v60 offset:5248\n"
    "ds_read_b64_tr_b16 a[214:215], v60 offset:7808\n"
    "v_mfma_f32_16x16x32_bf16 v[216:219], a[192:195], a[96:99], v[216:219]\n"
    "s_waitcnt lgkmcnt(8)\n"
    "ds_read_b64_tr_b16 a[216:217], v60 offset:10240\n"
    "ds_read_b64_tr_b16 a[218:219], v60 offset:12800\n"
    "s_lshl_b32 s74, s83, 3\n"
    "s_add_u32 s74, s74, s78\n"
    "s_mul_i32 s74, s74, 2560\n"
    "s_add_u32 s74, s74, 67108864\n"
    "buffer_load_dwordx4 v[16:19], v9, s[52:55], s74 offen offset:0 sc1\n"
    "v_mfma_f32_16x16x32_bf16 v[216:219], a[196:199], a[100:103], v[216:219]\n"
    "s_waitcnt lgkmcnt(8)\n"
    "ds_read_b64_tr_b16 a[220:221], v60 offset:15360\n"
    "ds_read_b64_tr_b16 a[222:223], v60 offset:17920\n"
    "v_mfma_f32_16x16x32_bf16 v[220:223], a[200:203], a[96:99], v[220:223]\n"
    "s_waitcnt lgkmcnt(8)\n"
    "ds_read_b64_tr_b16 a[224:225], v60 offset:10272\n"
    "ds_read_b64_tr_b16 a[226:227], v60 offset:12832\n"
    "v_mfma_f32_16x16x32_bf16 v[220:223], a[204:207], a[100:103], v[220:223]\n"
    "s_waitcnt lgkmcnt(8)\n"
    "ds_read_b64_tr_b16 a[228:229], v60 offset:15392\n"
    "ds_read_b64_tr_b16 a[230:231], v60 offset:17952\n"
    "buffer_load_dwordx4 v[20:23], v9, s[52:55], s74 offen offset:1024 sc1\n"
    "v_mfma_f32_16x16x32_bf16 v[240:243], a[208:211], a[96:99], v[240:243]\n"
    "s_waitcnt lgkmcnt(8)\n"
    "ds_read_b64_tr_b16 a[232:233], v60 offset:10304\n"
    "ds_read_b64_tr_b16 a[234:235], v60 offset:12864\n"
    "v_mfma_f32_16x16x32_bf16 v[240:243], a[212:215], a[100:103], v[240:243]\n"
    "s_waitcnt lgkmcnt(8)\n"
    "ds_read_b64_tr_b16 a[236:237], v60 offset:15424\n"
    "ds_read_b64_tr_b16 a[238:239], v60 offset:17984\n"
    "v_mfma_f32_16x16x32_bf16 v[208:211], a[216:219], a[104:107], v[208:211]\n"
    "s_waitcnt lgkmcnt(8)\n"
    "ds_read_b64_tr_b16 a[192:193], v60 offset:10336\n"
    "ds_read_b64_tr_b16 a[194:195], v60 offset:12896\n"
    "v_mfma_f32_16x16x32_bf16 v[208:211], a[220:223], a[108:111], v[208:211]\n"
    "s_waitcnt lgkmcnt(8)\n"
    "ds_read_b64_tr_b16 a[196:197], v60 offset:15456\n"
    "ds_read_b64_tr_b16 a[198:199], v60 offset:18016\n"
    "buffer_load_dwordx2 v[24:25], v59, s[52:55], s74 offen sc1\n"
    "v_mfma_f32_16x16x32_bf16 v[212:215], a[224:227], a[104:107], v[212:215]\n"
    "s_waitcnt lgkmcnt(8)\n"
    "ds_read_b64_tr_b16 a[200:201], v60 offset:10368\n"
    "ds_read_b64_tr_b16 a[202:203], v60 offset:12928\n"
    "v_mfma_f32_16x16x32_bf16 v[212:215], a[228:231], a[108:111], v[212:215]\n"
    "s_waitcnt lgkmcnt(8)\n"
    "ds_read_b64_tr_b16 a[204:205], v60 offset:15488\n"
    "ds_read_b64_tr_b16 a[206:207], v60 offset:18048\n"
    "s_waitcnt vmcnt(15)\n"
    "s_barrier\n"
    "v_add_u32 v60, 4294823936, v60\n"
    "v_mfma_f32_16x16x32_bf16 v[216:219], a[232:235], a[104:107], v[216:219]\n"
    "s_waitcnt lgkmcnt(8)\n"
    "ds_read_b64_tr_b16 a[208:209], v60 offset:0\n"
    "ds_read_b64_tr_b16 a[210:211], v60 offset:2560\n"
    "v_mfma_f32_16x16x32_bf16 v[216:219], a[236:239], a[108:111], v[216:219]\n"
    "s_waitcnt lgkmcnt(8)\n"
    "ds_read_b64_tr_b16 a[212:213], v60 offset:5120\n"
    "ds_read_b64_tr_b16 a[214:215], v60 offset:7680\n"
    "v_mfma_f32_16x16x32_bf16 v[220:223], a[192:195], a[104:107], v[220:223]\n"
    "s_waitcnt lgkmcnt(8)\n"
    "ds_read_b64_tr_b16 a[216:217], v60 offset:32\n"
    "ds_read_b64_tr_b16 a[218:219], v60 offset:2592\n"
    "v_mfma_f32_16x16x32_bf16 v[220:223], a[196:199], a[108:111], v[220:223]\n"
    "s_waitcnt lgkmcnt(8)\n"
    "ds_read_b64_tr_b16 a[220:221], v60 offset:5152\n"
    "ds_read_b64_tr_b16 a[222:223], v60 offset:7712\n"
    "v_mfma_f32_16x16x32_bf16 v[240:243], a[200:203], a[104:107], v[240:243]\n"
    "s_waitcnt lgkmcnt(8)\n"
    "ds_read_b64_tr_b16 a[224:225], v60 offset:64\n"
    "ds_read_b64_tr_b16 a[226:227], v60 offset:2624\n"
    "v_mfma_f32_16x16x32_bf16 v[240:243], a[204:207], a[108:111], v[240:243]\n"
    "s_waitcnt lgkmcnt(8)\n"
    "ds_read_b64_tr_b16 a[228:229], v60 offset:5184\n"
    "ds_read_b64_tr_b16 a[230:231], v60 offset:7744\n"
    "v_mfma_f32_16x16x32_bf16 v[208:211], a[208:211], a[0:3], v[208:211]\n"
    "s_waitcnt lgkmcnt(8)\n"
    "ds_read_b64_tr_b16 a[232:233], v60 offset:96\n"
    "ds_read_b64_tr_b16 a[234:235], v60 offset:2656\n"
    "v_mfma_f32_16x16x32_bf16 v[208:211], a[212:215], a[4:7], v[208:211]\n"
    "s_waitcnt lgkmcnt(8)\n"
    "ds_read_b64_tr_b16 a[236:237], v60 offset:5216\n"
    "ds_read_b64_tr_b16 a[238:239], v60 offset:7776\n"
    "v_mfma_f32_16x16x32_bf16 v[212:215], a[216:219], a[0:3], v[212:215]\n"
    "s_waitcnt lgkmcnt(8)\n"
    "ds_read_b64_tr_b16 a[192:193], v60 offset:128\n"
    "ds_read_b64_tr_b16 a[194:195], v60 offset:2688\n"
    "v_mfma_f32_16x16x32_bf16 v[212:215], a[220:223], a[4:7], v[212:215]\n"
    "s_waitcnt lgkmcnt(8)\n"
    "ds_read_b64_tr_b16 a[196:197], v60 offset:5248\n"
    "ds_read_b64_tr_b16 a[198:199], v60 offset:7808\n"
    "v_mfma_f32_16x16x32_bf16 v[216:219], a[224:227], a[0:3], v[216:219]\n"
    "s_waitcnt lgkmcnt(8)\n"
    "ds_read_b64_tr_b16 a[200:201], v60 offset:10240\n"
    "ds_read_b64_tr_b16 a[202:203], v60 offset:12800\n"
    "v_mfma_f32_16x16x32_bf16 v[216:219], a[228:231], a[4:7], v[216:219]\n"
    "s_waitcnt lgkmcnt(8)\n"
    "ds_read_b64_tr_b16 a[204:205], v60 offset:15360\n"
    "ds_read_b64_tr_b16 a[206:207], v60 offset:17920\n"
    "v_mfma_f32_16x16x32_bf16 v[220:223], a[232:235], a[0:3], v[220:223]\n"
    "s_waitcnt lgkmcnt(8)\n"
    "ds_read_b64_tr_b16 a[208:209], v60 offset:10272\n"
    "ds_read_b64_tr_b16 a[210:211], v60 offset:12832\n"
    "v_mfma_f32_16x16x32_bf16 v[220:223], a[236:239], a[4:7], v[220:223]\n"
    "s_waitcnt lgkmcnt(8)\n"
    "ds_read_b64_tr_b16 a[212:213], v60 offset:15392\n"
    "ds_read_b64_tr_b16 a[214:215], v60 offset:17952\n"
    "v_mfma_f32_16x16x32_bf16 v[240:243], a[192:195], a[0:3], v[240:243]\n"
    "s_waitcnt lgkmcnt(8)\n"
    "ds_read_b64_tr_b16 a[216:217], v60 offset:10304\n"
    "ds_read_b64_tr_b16 a[218:219], v60 offset:12864\n"
    "v_mfma_f32_16x16x32_bf16 v[240:243], a[196:199], a[4:7], v[240:243]\n"
    "s_waitcnt lgkmcnt(8)\n"
    "ds_read_b64_tr_b16 a[220:221], v60 offset:15424\n"
    "ds_read_b64_tr_b16 a[222:223], v60 offset:17984\n"
    "v_mfma_f32_16x16x32_bf16 v[208:211], a[200:203], a[8:11], v[208:211]\n"
    "s_waitcnt lgkmcnt(8)\n"
    "ds_read_b64_tr_b16 a[224:225], v60 offset:10336\n"
    "ds_read_b64_tr_b16 a[226:227], v60 offset:12896\n"
    "v_mfma_f32_16x16x32_bf16 v[208:211], a[204:207], a[12:15], v[208:211]\n"
    "s_waitcnt lgkmcnt(8)\n"
    "ds_read_b64_tr_b16 a[228:229], v60 offset:15456\n"
    "ds_read_b64_tr_b16 a[230:231], v60 offset:18016\n"
    "v_mfma_f32_16x16x32_bf16 v[212:215], a[208:211], a[8:11], v[212:215]\n"
    "s_waitcnt lgkmcnt(8)\n"
    "ds_read_b64_tr_b16 a[232:233], v60 offset:10368\n"
    "ds_read_b64_tr_b16 a[234:235], v60 offset:12928\n"
    "v_mfma_f32_16x16x32_bf16 v[212:215], a[212:215], a[12:15], v[212:215]\n"
    "s_waitcnt lgkmcnt(8)\n"
    "ds_read_b64_tr_b16 a[236:237], v60 offset:15488\n"
    "ds_read_b64_tr_b16 a[238:239], v60 offset:18048\n"
    "v_mfma_f32_16x16x32_bf16 v[216:219], a[216:219], a[8:11], v[216:219]\n"
    "s_waitcnt lgkmcnt(8)\n"
    "v_bfe_u32 v150, v9, 4, 4\n"
    "v_lshrrev_b32 v151, 8, v9\n"
    "v_mfma_f32_16x16x32_bf16 v[216:219], a[220:223], a[12:15], v[216:219]\n"
    "s_waitcnt lgkmcnt(6)\n"
    "v_lshlrev_b32 v151, 2, v151\n"
    "v_sub_u32 v150, v150, v151\n"
    "v_mfma_f32_16x16x32_bf16 v[220:223], a[224:227], a[8:11], v[220:223]\n"
    "s_waitcnt lgkmcnt(4)\n"
    "v_mov_b32 v144, 0\n"
    "v_mov_b32 v145, 0\n"
    "v_mfma_f32_16x16x32_bf16 v[220:223], a[228:231], a[12:15], v[220:223]\n"
    "s_waitcnt lgkmcnt(2)\n"
    "v_mov_b32 v146, 0\n"
    "v_mov_b32 v147, 0\n"
    "v_mfma_f32_16x16x32_bf16 v[240:243], a[232:235], a[8:11], v[240:243]\n"
    "s_waitcnt lgkmcnt(0)\n"
    "v_mov_b32 v148, 0\n"
    "v_mov_b32 v149, 0\n"
    "v_mfma_f32_16x16x32_bf16 v[240:243], a[236:239], a[12:15], v[240:243]\n"
    "v_cmp_eq_u32 vcc, 0, v150\n"
    "v_mov_b32 v151, 0x3f80\n"
    "s_nop 0\n"
    "v_cndmask_b32 v146, v146, v151, vcc\n"
    "v_cmp_eq_u32 vcc, 1, v150\n"
    "v_mov_b32 v151, 0x3f800000\n"
    "s_nop 0\n"
    "v_cndmask_b32 v146, v146, v151, vcc\n"
    "v_cmp_eq_u32 vcc, 2, v150\n"
    "v_mov_b32 v151, 0x3f80\n"
    "s_nop 0\n"
    "v_cndmask_b32 v147, v147, v151, vcc\n"
    "v_cmp_eq_u32 vcc, 3, v150\n"
    "v_mov_b32 v151, 0x3f800000\n"
    "s_nop 0\n"
    "v_cndmask_b32 v147, v147, v151, vcc\n"
    "s_mul_i32 s73, s83, 20480\n"
    "s_mov_b64 s[84:85], s[22:23]\n"
    "s_add_u32 s84, s84, s73\n"
    "s_addc_u32 s85, s85, 0\n"
    "s_mov_b32 s86, 20480\n"
    "s_mov_b32 s87, 0x20000\n"
    "v_mfma_f32_16x16x32_bf16 v[152:155], v[146:149], v[64:67], 0\n"
    "v_add_f32 v208, v208, v28\n"
    "v_add_f32 v209, v209, v29\n"
    "v_add_f32 v210, v210, v30\n"
    "v_add_f32 v211, v211, v31\n"
    "v_mfma_f32_16x16x32_bf16 v[156:159], v[144:147], v[64:67], 0\n"
    "v_mul_f32 v176, 0xbfb8aa3b, v208\n"
    "v_mul_f32 v177, 0xbfb8aa3b, v209\n"
    "v_mul_f32 v178, 0xbfb8aa3b, v210\n"
    "v_mfma_f32_16x16x32_bf16 v[160:163], v[146:149], v[68:71], 0\n"
    "v_mul_f32 v179, 0xbfb8aa3b, v211\n"
    "v_exp_f32 v176, v176\n"
    "v_exp_f32 v177, v177\n"
    "v_exp_f32 v178, v178\n"
    "v_mfma_f32_16x16x32_bf16 v[164:167], v[144:147], v[68:71], 0\n"
    "v_exp_f32 v179, v179\n"
    "v_add_f32 v176, 1.0, v176\n"
    "v_add_f32 v177, 1.0, v177\n"
    "v_mfma_f32_16x16x32_bf16 v[168:171], v[146:149], v[128:131], 0\n"
    "v_add_f32 v178, 1.0, v178\n"
    "v_add_f32 v179, 1.0, v179\n"
    "v_rcp_f32 v176, v176\n"
    "v_rcp_f32 v177, v177\n"
    "v_mfma_f32_16x16x32_bf16 v[152:155], v[146:149], v[72:75], v[152:155]\n"
    "v_rcp_f32 v178, v178\n"
    "v_rcp_f32 v179, v179\n"
    "v_mul_f32 v208, v208, v176\n"
    "v_mfma_f32_16x16x32_bf16 v[156:159], v[144:147], v[72:75], v[156:159]\n"
    "v_mul_f32 v209, v209, v177\n"
    "v_mul_f32 v210, v210, v178\n"
    "v_mul_f32 v211, v211, v179\n"
    "v_add_f32 v212, v212, v32\n"
    "v_mfma_f32_16x16x32_bf16 v[160:163], v[146:149], v[76:79], v[160:163]\n"
    "v_add_f32 v213, v213, v33\n"
    "v_add_f32 v214, v214, v34\n"
    "v_add_f32 v215, v215, v35\n"
    "v_mfma_f32_16x16x32_bf16 v[164:167], v[144:147], v[76:79], v[164:167]\n"
    "v_mul_f32 v176, 0xbfb8aa3b, v212\n"
    "v_mul_f32 v177, 0xbfb8aa3b, v213\n"
    "v_mul_f32 v178, 0xbfb8aa3b, v214\n"
    "v_mul_f32 v179, 0xbfb8aa3b, v215\n"
    "v_mfma_f32_16x16x32_bf16 v[168:171], v[144:147], v[128:131], v[168:171]\n"
    "v_exp_f32 v176, v176\n"
    "v_exp_f32 v177, v177\n"
    "v_exp_f32 v178, v178\n"
    "v_mfma_f32_16x16x32_bf16 v[152:155], v[146:149], v[80:83], v[152:155]\n"
    "v_exp_f32 v179, v179\n"
    "v_add_f32 v176, 1.0, v176\n"
    "v_add_f32 v177, 1.0, v177\n"
    "v_add_f32 v178, 1.0, v178\n"
    "v_mfma_f32_16x16x32_bf16 v[156:159], v[144:147], v[80:83], v[156:159]\n"
    "v_add_f32 v179, 1.0, v179\n"
    "v_rcp_f32 v176, v176\n"
    "v_rcp_f32 v177, v177\n"
    "v_mfma_f32_16x16x32_bf16 v[160:163], v[146:149], v[84:87], v[160:163]\n"
    "v_rcp_f32 v178, v178\n"
    "v_rcp_f32 v179, v179\n"
    "v_mul_f32 v212, v212, v176\n"
    "v_mul_f32 v213, v213, v177\n"
    "v_mfma_f32_16x16x32_bf16 v[164:167], v[144:147], v[84:87], v[164:167]\n"
    "v_mul_f32 v214, v214, v178\n"
    "v_mul_f32 v215, v215, v179\n"
    "v_add_f32 v216, v216, v36\n"
    "v_mfma_f32_16x16x32_bf16 v[168:171], v[146:149], v[132:135], v[168:171]\n"
    "v_add_f32 v217, v217, v37\n"
    "v_add_f32 v218, v218, v38\n"
    "v_add_f32 v219, v219, v39\n"
    "v_mul_f32 v176, 0xbfb8aa3b, v216\n"
    "v_mfma_f32_16x16x32_bf16 v[152:155], v[146:149], v[88:91], v[152:155]\n"
    "v_mul_f32 v177, 0xbfb8aa3b, v217\n"
    "v_mul_f32 v178, 0xbfb8aa3b, v218\n"
    "v_mul_f32 v179, 0xbfb8aa3b, v219\n"
    "v_mfma_f32_16x16x32_bf16 v[156:159], v[144:147], v[88:91], v[156:159]\n"
    "v_exp_f32 v176, v176\n"
    "v_exp_f32 v177, v177\n"
    "v_exp_f32 v178, v178\n"
    "v_exp_f32 v179, v179\n"
    "v_mfma_f32_16x16x32_bf16 v[160:163], v[146:149], v[92:95], v[160:163]\n"
    "v_add_f32 v176, 1.0, v176\n"
    "v_add_f32 v177, 1.0, v177\n"
    "v_add_f32 v178, 1.0, v178\n"
    "v_mfma_f32_16x16x32_bf16 v[164:167], v[144:147], v[92:95], v[164:167]\n"
    "v_add_f32 v179, 1.0, v179\n"
    "v_rcp_f32 v176, v176\n"
    "v_rcp_f32 v177, v177\n"
    "v_rcp_f32 v178, v178\n"
    "v_mfma_f32_16x16x32_bf16 v[168:171], v[144:147], v[132:135], v[168:171]\n"
    "s_waitcnt vmcnt(9)\n"
    "v_rcp_f32 v179, v179\n"
    "v_mul_f32 v216, v216, v176\n"
    "v_mul_f32 v217, v217, v177\n"
    "v_mfma_f32_16x16x32_bf16 v[152:155], v[146:149], v[96:99], v[152:155]\n"
    "v_mul_f32 v218, v218, v178\n"
    "v_mul_f32 v219, v219, v179\n"
    "v_add_f32 v220, v220, v40\n"
    "v_add_f32 v221, v221, v41\n"
    "v_mfma_f32_16x16x32_bf16 v[156:159], v[144:147], v[96:99], v[156:159]\n"
    "v_add_f32 v222, v222, v42\n"
    "v_add_f32 v223, v223, v43\n"
    "v_mul_f32 v176, 0xbfb8aa3b, v220\n"
    "v_mfma_f32_16x16x32_bf16 v[160:163], v[146:149], v[100:103], v[160:163]\n"
    "v_mul_f32 v177, 0xbfb8aa3b, v221\n"
    "v_mul_f32 v178, 0xbfb8aa3b, v222\n"
    "v_mul_f32 v179, 0xbfb8aa3b, v223\n"
    "v_exp_f32 v176, v176\n"
    "v_mfma_f32_16x16x32_bf16 v[164:167], v[144:147], v[100:103], v[164:167]\n"
    "v_exp_f32 v177, v177\n"
    "v_exp_f32 v178, v178\n"
    "v_exp_f32 v179, v179\n"
    "v_mfma_f32_16x16x32_bf16 v[168:171], v[146:149], v[136:139], v[168:171]\n"
    "v_add_f32 v176, 1.0, v176\n"
    "v_add_f32 v177, 1.0, v177\n"
    "v_add_f32 v178, 1.0, v178\n"
    "v_add_f32 v179, 1.0, v179\n"
    "v_mfma_f32_16x16x32_bf16 v[152:155], v[146:149], v[104:107], v[152:155]\n"
    "v_rcp_f32 v176, v176\n"
    "v_rcp_f32 v177, v177\n"
    "v_rcp_f32 v178, v178\n"
    "v_mfma_f32_16x16x32_bf16 v[156:159], v[144:147], v[104:107], v[156:159]\n"
    "v_rcp_f32 v179, v179\n"
    "v_mul_f32 v220, v220, v176\n"
    "v_mul_f32 v221, v221, v177\n"
    "v_mul_f32 v222, v222, v178\n"
    "v_mfma_f32_16x16x32_bf16 v[160:163], v[146:149], v[108:111], v[160:163]\n"
    "v_mul_f32 v223, v223, v179\n"
    "v_add_f32 v240, v240, v44\n"
    "v_add_f32 v241, v241, v45\n"
    "v_mfma_f32_16x16x32_bf16 v[164:167], v[144:147], v[108:111], v[164:167]\n"
    "v_add_f32 v242, v242, v46\n"
    "v_add_f32 v243, v243, v47\n"
    "v_mul_f32 v176, 0xbfb8aa3b, v240\n"
    "v_mul_f32 v177, 0xbfb8aa3b, v241\n"
    "v_mfma_f32_16x16x32_bf16 v[168:171], v[144:147], v[136:139], v[168:171]\n"
    "s_waitcnt vmcnt(0)\n"
    "v_mul_f32 v178, 0xbfb8aa3b, v242\n"
    "v_mul_f32 v179, 0xbfb8aa3b, v243\n"
    "v_exp_f32 v176, v176\n"
    "v_mfma_f32_16x16x32_bf16 v[152:155], v[146:149], v[112:115], v[152:155]\n"
    "v_exp_f32 v177, v177\n"
    "v_exp_f32 v178, v178\n"
    "v_exp_f32 v179, v179\n"
    "v_add_f32 v176, 1.0, v176\n"
    "v_mfma_f32_16x16x32_bf16 v[156:159], v[144:147], v[112:115], v[156:159]\n"
    "v_add_f32 v177, 1.0, v177\n"
    "v_add_f32 v178, 1.0, v178\n"
    "v_add_f32 v179, 1.0, v179\n"
    "v_mfma_f32_16x16x32_bf16 v[160:163], v[146:149], v[116:119], v[160:163]\n"
    "v_rcp_f32 v176, v176\n"
    "v_rcp_f32 v177, v177\n"
    "v_rcp_f32 v178, v178\n"
    "v_rcp_f32 v179, v179\n"
    "v_mfma_f32_16x16x32_bf16 v[164:167], v[144:147], v[116:119], v[164:167]\n"
    "v_mul_f32 v240, v240, v176\n"
    "v_mul_f32 v241, v241, v177\n"
    "v_mul_f32 v242, v242, v178\n"
    "v_mfma_f32_16x16x32_bf16 v[168:171], v[146:149], v[140:143], v[168:171]\n"
    "v_mul_f32 v243, v243, v179\n"
    "v_lshlrev_b32 v180, 16, v16\n"
    "v_and_b32 v181, 0xffff0000, v16\n"
    "v_lshlrev_b32 v182, 16, v17\n"
    "v_mfma_f32_16x16x32_bf16 v[152:155], v[146:149], v[120:123], v[152:155]\n"
    "v_and_b32 v183, 0xffff0000, v17\n"
    "v_lshlrev_b32 v184, 16, v18\n"
    "v_and_b32 v185, 0xffff0000, v18\n"
    "v_mfma_f32_16x16x32_bf16 v[156:159], v[144:147], v[120:123], v[156:159]\n"
    "v_lshlrev_b32 v186, 16, v19\n"
    "v_and_b32 v187, 0xffff0000, v19\n"
    "v_lshlrev_b32 v188, 16, v20\n"
    "v_and_b32 v189, 0xffff0000, v20\n"
    "v_mfma_f32_16x16x32_bf16 v[160:163], v[146:149], v[124:127], v[160:163]\n"
    "v_lshlrev_b32 v190, 16, v21\n"
    "v_and_b32 v191, 0xffff0000, v21\n"
    "v_lshlrev_b32 v192, 16, v22\n"
    "v_mfma_f32_16x16x32_bf16 v[164:167], v[144:147], v[124:127], v[164:167]\n"
    "v_and_b32 v193, 0xffff0000, v22\n"
    "v_lshlrev_b32 v194, 16, v23\n"
    "v_and_b32 v195, 0xffff0000, v23\n"
    "v_lshlrev_b32 v196, 16, v24\n"
    "v_mfma_f32_16x16x32_bf16 v[168:171], v[144:147], v[140:143], v[168:171]\n"
    "v_and_b32 v197, 0xffff0000, v24\n"
    "v_lshlrev_b32 v198, 16, v25\n"
    "v_and_b32 v199, 0xffff0000, v25\n"
    "v_fmac_f32 v180, v152, v208\n"
    "v_fmac_f32 v181, v153, v209\n"
    "v_fmac_f32 v182, v154, v210\n"
    "v_fmac_f32 v183, v155, v211\n"
    "v_cvt_pk_bf16_f32 v16, v180, v181\n"
    "v_cvt_pk_bf16_f32 v17, v182, v183\n"
    "ds_write_b64 v26, v[16:17] offset:0\n"
    "v_fmac_f32 v184, v156, v212\n"
    "v_fmac_f32 v185, v157, v213\n"
    "v_fmac_f32 v186, v158, v214\n"
    "v_fmac_f32 v187, v159, v215\n"
    "v_cvt_pk_bf16_f32 v18, v184, v185\n"
    "v_cvt_pk_bf16_f32 v19, v186, v187\n"
    "ds_write_b64 v26, v[18:19] offset:32\n"
    "v_fmac_f32 v188, v160, v216\n"
    "v_fmac_f32 v189, v161, v217\n"
    "v_fmac_f32 v190, v162, v218\n"
    "v_fmac_f32 v191, v163, v219\n"
    "v_cvt_pk_bf16_f32 v20, v188, v189\n"
    "v_cvt_pk_bf16_f32 v21, v190, v191\n"
    "ds_write_b64 v26, v[20:21] offset:64\n"
    "v_fmac_f32 v192, v164, v220\n"
    "v_fmac_f32 v193, v165, v221\n"
    "v_fmac_f32 v194, v166, v222\n"
    "v_fmac_f32 v195, v167, v223\n"
    "v_cvt_pk_bf16_f32 v22, v192, v193\n"
    "v_cvt_pk_bf16_f32 v23, v194, v195\n"
    "ds_write_b64 v26, v[22:23] offset:96\n"
    "v_fmac_f32 v196, v168, v240\n"
    "v_fmac_f32 v197, v169, v241\n"
    "v_fmac_f32 v198, v170, v242\n"
    "v_fmac_f32 v199, v171, v243\n"
    "v_cvt_pk_bf16_f32 v24, v196, v197\n"
    "v_cvt_pk_bf16_f32 v25, v198, v199\n"
    "ds_write_b64 v26, v[24:25] offset:128\n"
    "s_waitcnt lgkmcnt(0)\n"
    "ds_read_b128 v[64:67], v27 offset:0\n"
    "ds_read_b128 v[68:71], v27 offset:1024\n"
    "ds_read_b128 v[72:75], v27 offset:2048\n"
    "s_waitcnt lgkmcnt(2)\n"
    "buffer_store_dwordx4 v[64:67], v11, s[84:87], s94 offen offset:0 nt\n"
    "s_waitcnt lgkmcnt(1)\n"
    "buffer_store_dwordx4 v[68:71], v12, s[84:87], s94 offen offset:1024 nt\n"
    "s_waitcnt lgkmcnt(0)\n"
    "buffer_store_dwordx4 v[72:75], v13, s[84:87], s94 offen offset:2048 nt\n"
    "s_cmp_lg_u32 %1, 0\n"
    "s_cbranch_scc1 L_end\n"
    "s_lshl_b32 s73, s76, 6\n"
    "v_lshrrev_b32 v56, 4, v9\n"
    "v_min_u32 v56, 63, v56\n"
    "v_add_u32 v56, s73, v56\n"
    "v_lshlrev_b32 v56, 6, v56\n"
    "v_add_u32 v56, 4096, v56\n"
    "buffer_load_dwordx2 v[62:63], v56, s[52:55], 0 offen sc1\n"
    "s_waitcnt vmcnt(0)\n"
    "v_cmp_ne_u64 vcc, s[80:81], v[62:63]\n"
    "s_nop 1\n"
    "s_cbranch_vccnz L_slow11\n"
    "L_back11:\n"
    "s_barrier\n"
    "v_mov_b32 v62, s80\n"
    "v_mov_b32 v63, s81\n"
    "buffer_store_dwordx2 v[62:63], v10, s[52:55], 0 offen sc1\n"
    "L_end:\n"
    "s_branch L_exit\n"
    "L_retry:\n"
    "s_waitcnt vmcnt(0) lgkmcnt(0)\n"
    "s_barrier\n"
    "s_mov_b32 s75, -1\n"
    "s_sub_u32 s36, s36, 24576\n"
    "s_subb_u32 s37, s37, 0\n"
    "s_add_u32 s38, s38, 24576\n"
    "v_subrev_u32 v8, 811008, v8\n"
    "v_add_u32 v2, 49152, v2\n"
    "v_add_u32 v3, 49152, v3\n"
    "v_add_u32 v4, 98304, v4\n"
    "v_add_u32 v5, 98304, v5\n"
    "v_add_u32 v6, 98304, v6\n"
    "v_add_u32 v7, 98304, v7\n"
    "v_mov_b32 v16, 0xff800000\n"
    "v_mov_b32 v17, 0xff800000\n"
    "v_mov_b32 v18, 0xff800000\n"
    "v_mov_b32 v19, 0xff800000\n"
    "s_mov_b32 s72, 32\n"
    "L_mx:\n"
    "s_add_u32 m0, s68, 49152\n"
    "s_nop 0\n"
    "buffer_load_dwordx4 v8, s[36:39], s56 offen offset:0 lds\n"
    "buffer_load_dwordx4 v8, s[36:39], s57 offen offset:1024 lds\n"
    "s_add_u32 m0, s68, 51200\n"
    "s_nop 0\n"
    "buffer_load_dwordx4 v8, s[36:39], s58 offen offset:0 lds\n"
    "buffer_load_dwordx4 v8, s[36:39], s59 offen offset:1024 lds\n"
    "s_add_u32 m0, s68, 53248\n"
    "s_nop 0\n"
    "buffer_load_dwordx4 v8, s[36:39], s60 offen offset:0 lds\n"
    "buffer_load_dwordx4 v8, s[36:39], s61 offen offset:1024 lds\n"
    "v_add_u32 v8, 24576, v8\n"
    "s_waitcnt vmcnt(0)\n"
    "s_barrier\n"
    "ds_read_b128 a[192:195], v2 offset:0\n"
    "ds_read_b128 a[196:199], v3 offset:0\n"
    "ds_read_b128 a[200:203], v2 offset:2048\n"
    "ds_read_b128 a[204:207], v3 offset:2048\n"
    "ds_read_b128 a[208:211], v2 offset:4096\n"
    "ds_read_b128 a[212:215], v3 offset:4096\n"
    "s_waitcnt lgkmcnt(0)\n"
    "v_mfma_f32_16x16x32_bf16 v[80:83], a[192:195], v[64:67], 0\n"
    "v_mfma_f32_16x16x32_bf16 v[80:83], a[196:199], v[96:99], v[80:83]\n"
    "v_mfma_f32_16x16x32_bf16 v[80:83], a[200:203], v[128:131], v[80:83]\n"
    "v_mfma_f32_16x16x32_bf16 v[80:83], a[204:207], v[160:163], v[80:83]\n"
    "v_mfma_f32_16x16x32_bf16 v[80:83], a[208:211], v[192:195], v[80:83]\n"
    "v_mfma_f32_16x16x32_bf16 v[80:83], a[212:215], v[224:227], v[80:83]\n"
    "v_mfma_f32_16x16x32_bf16 v[84:87], a[212:215], v[228:231], 0\n"
    "v_mfma_f32_16x16x32_bf16 v[84:87], a[208:211], v[196:199], v[84:87]\n"
    "v_mfma_f32_16x16x32_bf16 v[84:87], a[204:207], v[164:167], v[84:87]\n"
    "v_mfma_f32_16x16x32_bf16 v[84:87], a[200:203], v[132:135], v[84:87]\n"
    "v_mfma_f32_16x16x32_bf16 v[84:87], a[196:199], v[100:103], v[84:87]\n"
    "v_mfma_f32_16x16x32_bf16 v[84:87], a[192:195], v[68:71], v[84:87]\n"
    "v_mfma_f32_16x16x32_bf16 v[88:91], a[192:195], v[72:75], 0\n"
    "v_mfma_f32_16x16x32_bf16 v[88:91], a[196:199], v[104:107], v[88:91]\n"
    "v_mfma_f32_16x16x32_bf16 v[88:91], a[200:203], v[136:139], v[88:91]\n"
    "v_mfma_f32_16x16x32_bf16 v[88:91], a[204:207], v[168:171], v[88:91]\n"
    "v_mfma_f32_16x16x32_bf16 v[88:91], a[208:211], v[200:203], v[88:91]\n"
    "v_mfma_f32_16x16x32_bf16 v[88:91], a[212:215], v[232:235], v[88:91]\n"
    "v_mfma_f32_16x16x32_bf16 v[92:95], a[212:215], v[236:239], 0\n"
    "v_mfma_f32_16x16x32_bf16 v[92:95], a[208:211], v[204:207], v[92:95]\n"
    "v_mfma_f32_16x16x32_bf16 v[92:95], a[204:207], v[172:175], v[92:95]\n"
    "v_mfma_f32_16x16x32_bf16 v[92:95], a[200:203], v[140:143], v[92:95]\n"
    "v_mfma_f32_16x16x32_bf16 v[92:95], a[196:199], v[108:111], v[92:95]\n"
    "v_mfma_f32_16x16x32_bf16 v[92:95], a[192:195], v[76:79], v[92:95]\n"
    "s_nop 7\n"
    "v_max3_f32 v16, v16, v80, v81\n"
    "v_max3_f32 v16, v16, v82, v83\n"
    "v_max3_f32 v17, v17, v84, v85\n"
    "v_max3_f32 v17, v17, v86, v87\n"
    "v_max3_f32 v18, v18, v88, v89\n"
    "v_max3_f32 v18, v18, v90, v91\n"
    "v_max3_f32 v19, v19, v92, v93\n"
    "v_max3_f32 v19, v19, v94, v95\n"
    "ds_read_b128 a[192:195], v2 offset:6144\n"
    "ds_read_b128 a[196:199], v3 offset:6144\n"
    "ds_read_b128 a[200:203], v2 offset:8192\n"
    "ds_read_b128 a[204:207], v3 offset:8192\n"
    "ds_read_b128 a[208:211], v2 offset:10240\n"
    "ds_read_b128 a[212:215], v3 offset:10240\n"
    "s_waitcnt lgkmcnt(0)\n"
    "v_mfma_f32_16x16x32_bf16 v[80:83], a[192:195], v[64:67], 0\n"
    "v_mfma_f32_16x16x32_bf16 v[80:83], a[196:199], v[96:99], v[80:83]\n"
    "v_mfma_f32_16x16x32_bf16 v[80:83], a[200:203], v[128:131], v[80:83]\n"
    "v_mfma_f32_16x16x32_bf16 v[80:83], a[204:207], v[160:163], v[80:83]\n"
    "v_mfma_f32_16x16x32_bf16 v[80:83], a[208:211], v[192:195], v[80:83]\n"
    "v_mfma_f32_16x16x32_bf16 v[80:83], a[212:215], v[224:227], v[80:83]\n"
    "v_mfma_f32_16x16x32_bf16 v[84:87], a[212:215], v[228:231], 0\n"
    "v_mfma_f32_16x16x32_bf16 v[84:87], a[208:211], v[196:199], v[84:87]\n"
    "v_mfma_f32_16x16x32_bf16 v[84:87], a[204:207], v[164:167], v[84:87]\n"
    "v_mfma_f32_16x16x32_bf16 v[84:87], a[200:203], v[132:135], v[84:87]\n"
    "v_mfma_f32_16x16x32_bf16 v[84:87], a[196:199], v[100:103], v[84:87]\n"
    "v_mfma_f32_16x16x32_bf16 v[84:87], a[192:195], v[68:71], v[84:87]\n"
    "v_mfma_f32_16x16x32_bf16 v[88:91], a[192:195], v[72:75], 0\n"
    "v_mfma_f32_16x16x32_bf16 v[88:91], a[196:199], v[104:107], v[88:91]\n"
    "v_mfma_f32_16x16x32_bf16 v[88:91], a[200:203], v[136:139], v[88:91]\n"
    "v_mfma_f32_16x16x32_bf16 v[88:91], a[204:207], v[168:171], v[88:91]\n"
    "v_mfma_f32_16x16x32_bf16 v[88:91], a[208:211], v[200:203], v[88:91]\n"
    "v_mfma_f32_16x16x32_bf16 v[88:91], a[212:215], v[232:235], v[88:91]\n"
    "v_mfma_f32_16x16x32_bf16 v[92:95], a[212:215], v[236:239], 0\n"
    "v_mfma_f32_16x16x32_bf16 v[92:95], a[208:211], v[204:207], v[92:95]\n"
    "v_mfma_f32_16x16x32_bf16 v[92:95], a[204:207], v[172:175], v[92:95]\n"
    "v_mfma_f32_16x16x32_bf16 v[92:95], a[200:203], v[140:143], v[92:95]\n"
    "v_mfma_f32_16x16x32_bf16 v[92:95], a[196:199], v[108:111], v[92:95]\n"
    "v_mfma_f32_16x16x32_bf16 v[92:95], a[192:195], v[76:79], v[92:95]\n"
    "s_nop 7\n"
    "v_max3_f32 v16, v16, v80, v81\n"
    "v_max3_f32 v16, v16, v82, v83\n"
    "v_max3_f32 v17, v17, v84, v85\n"
    "v_max3_f32 v17, v17, v86, v87\n"
    "v_max3_f32 v18, v18, v88, v89\n"
    "v_max3_f32 v18, v18, v90, v91\n"
    "v_max3_f32 v19, v19, v92, v93\n"
    "v_max3_f32 v19, v19, v94, v95\n"
    "ds_read_b128 a[192:195], v2 offset:12288\n"
    "ds_read_b128 a[196:199], v3 offset:12288\n"
    "ds_read_b128 a[200:203], v2 offset:14336\n"
    "ds_read_b128 a[204:207], v3 offset:14336\n"
    "ds_read_b128 a[208:211], v2 offset:16384\n"
    "ds_read_b128 a[212:215], v3 offset:16384\n"
    "s_waitcnt lgkmcnt(0)\n"
    "v_mfma_f32_16x16x32_bf16 v[80:83], a[192:195], v[64:67], 0\n"
    "v_mfma_f32_16x16x32_bf16 v[80:83], a[196:199], v[96:99], v[80:83]\n"
    "v_mfma_f32_16x16x32_bf16 v[80:83], a[200:203], v[128:131], v[80:83]\n"
    "v_mfma_f32_16x16x32_bf16 v[80:83], a[204:207], v[160:163], v[80:83]\n"
    "v_mfma_f32_16x16x32_bf16 v[80:83], a[208:211], v[192:195], v[80:83]\n"
    "v_mfma_f32_16x16x32_bf16 v[80:83], a[212:215], v[224:227], v[80:83]\n"
    "v_mfma_f32_16x16x32_bf16 v[84:87], a[212:215], v[228:231], 0\n"
    "v_mfma_f32_16x16x32_bf16 v[84:87], a[208:211], v[196:199], v[84:87]\n"
    "v_mfma_f32_16x16x32_bf16 v[84:87], a[204:207], v[164:167], v[84:87]\n"
    "v_mfma_f32_16x16x32_bf16 v[84:87], a[200:203], v[132:135], v[84:87]\n"
    "v_mfma_f32_16x16x32_bf16 v[84:87], a[196:199], v[100:103], v[84:87]\n"
    "v_mfma_f32_16x16x32_bf16 v[84:87], a[192:195], v[68:71], v[84:87]\n"
    "v_mfma_f32_16x16x32_bf16 v[88:91], a[192:195], v[72:75], 0\n"
    "v_mfma_f32_16x16x32_bf16 v[88:91], a[196:199], v[104:107], v[88:91]\n"
    "v_mfma_f32_16x16x32_bf16 v[88:91], a[200:203], v[136:139], v[88:91]\n"
    "v_mfma_f32_16x16x32_bf16 v[88:91], a[204:207], v[168:171], v[88:91]\n"
    "v_mfma_f32_16x16x32_bf16 v[88:91], a[208:211], v[200:203], v[88:91]\n"
    "v_mfma_f32_16x16x32_bf16 v[88:91], a[212:215], v[232:235], v[88:91]\n"
    "v_mfma_f32_16x16x32_bf16 v[92:95], a[212:215], v[236:239], 0\n"
    "v_mfma_f32_16x16x32_bf16 v[92:95], a[208:211], v[204:207], v[92:95]\n"
    "v_mfma_f32_16x16x32_bf16 v[92:95], a[204:207], v[172:175], v[92:95]\n"
    "v_mfma_f32_16x16x32_bf16 v[92:95], a[200:203], v[140:143], v[92:95]\n"
    "v_mfma_f32_16x16x32_bf16 v[92:95], a[196:199], v[108:111], v[92:95]\n"
    "v_mfma_f32_16x16x32_bf16 v[92:95], a[192:195], v[76:79], v[92:95]\n"
    "s_nop 7\n"
    "v_max3_f32 v16, v16, v80, v81\n"
    "v_max3_f32 v16, v16, v82, v83\n"
    "v_max3_f32 v17, v17, v84, v85\n"
    "v_max3_f32 v17, v17, v86, v87\n"
    "v_max3_f32 v18, v18, v88, v89\n"
    "v_max3_f32 v18, v18, v90, v91\n"
    "v_max3_f32 v19, v19, v92, v93\n"
    "v_max3_f32 v19, v19, v94, v95\n"
    "ds_read_b128 a[192:195], v2 offset:18432\n"
    "ds_read_b128 a[196:199], v3 offset:18432\n"
    "ds_read_b128 a[200:203], v2 offset:20480\n"
    "ds_read_b128 a[204:207], v3 offset:20480\n"
    "ds_read_b128 a[208:211], v2 offset:22528\n"
    "ds_read_b128 a[212:215], v3 offset:22528\n"
    "s_waitcnt lgkmcnt(0)\n"
    "v_mfma_f32_16x16x32_bf16 v[80:83], a[192:195], v[64:67], 0\n"
    "v_mfma_f32_16x16x32_bf16 v[80:83], a[196:199], v[96:99], v[80:83]\n"
    "v_mfma_f32_16x16x32_bf16 v[80:83], a[200:203], v[128:131], v[80:83]\n"
    "v_mfma_f32_16x16x32_bf16 v[80:83], a[204:207], v[160:163], v[80:83]\n"
    "v_mfma_f32_16x16x32_bf16 v[80:83], a[208:211], v[192:195], v[80:83]\n"
    "v_mfma_f32_16x16x32_bf16 v[80:83], a[212:215], v[224:227], v[80:83]\n"
    "v_mfma_f32_16x16x32_bf16 v[84:87], a[212:215], v[228:231], 0\n"
    "v_mfma_f32_16x16x32_bf16 v[84:87], a[208:211], v[196:199], v[84:87]\n"
    "v_mfma_f32_16x16x32_bf16 v[84:87], a[204:207], v[164:167], v[84:87]\n"
    "v_mfma_f32_16x16x32_bf16 v[84:87], a[200:203], v[132:135], v[84:87]\n"
    "v_mfma_f32_16x16x32_bf16 v[84:87], a[196:199], v[100:103], v[84:87]\n"
    "v_mfma_f32_16x16x32_bf16 v[84:87], a[192:195], v[68:71], v[84:87]\n"
    "v_mfma_f32_16x16x32_bf16 v[88:91], a[192:195], v[72:75], 0\n"
    "v_mfma_f32_16x16x32_bf16 v[88:91], a[196:199], v[104:107], v[88:91]\n"
    "v_mfma_f32_16x16x32_bf16 v[88:91], a[200:203], v[136:139], v[88:91]\n"
    "v_mfma_f32_16x16x32_bf16 v[88:91], a[204:207], v[168:171], v[88:91]\n"
    "v_mfma_f32_16x16x32_bf16 v[88:91], a[208:211], v[200:203], v[88:91]\n"
    "v_mfma_f32_16x16x32_bf16 v[88:91], a[212:215], v[232:235], v[88:91]\n"
    "v_mfma_f32_16x16x32_bf16 v[92:95], a[212:215], v[236:239], 0\n"
    "v_mfma_f32_16x16x32_bf16 v[92:95], a[208:211], v[204:207], v[92:95]\n"
    "v_mfma_f32_16x16x32_bf16 v[92:95], a[204:207], v[172:175], v[92:95]\n"
    "v_mfma_f32_16x16x32_bf16 v[92:95], a[200:203], v[140:143], v[92:95]\n"
    "v_mfma_f32_16x16x32_bf16 v[92:95], a[196:199], v[108:111], v[92:95]\n"
    "v_mfma_f32_16x16x32_bf16 v[92:95], a[192:195], v[76:79], v[92:95]\n"
    "s_nop 7\n"
    "v_max3_f32 v16, v16, v80, v81\n"
    "v_max3_f32 v16, v16, v82, v83\n"
    "v_max3_f32 v17, v17, v84, v85\n"
    "v_max3_f32 v17, v17, v86, v87\n"
    "v_max3_f32 v18, v18, v88, v89\n"
    "v_max3_f32 v18, v18, v90, v91\n"
    "v_max3_f32 v19, v19, v92, v93\n"
    "v_max3_f32 v19, v19, v94, v95\n"
    "s_barrier\n"
    "s_sub_u32 s72, s72, 1\n"
    "s_cmp_lg_u32 s72, 0\n"
    "s_cbranch_scc1 L_mx\n"
    "v_subrev_u32 v8, 786432, v8\n"
    "s_add_u32 m0, s68, 49152\n"
    "s_nop 0\n"
    "buffer_load_dwordx4 v8, s[36:39], s56 offen offset:0 lds\n"
    "buffer_load_dwordx4 v8, s[36:39], s57 offen offset:1024 lds\n"
    "s_add_u32 m0, s68, 51200\n"
    "s_nop 0\n"
    "buffer_load_dwordx4 v8, s[36:39], s58 offen offset:0 lds\n"
    "buffer_load_dwordx4 v8, s[36:39], s59 offen offset:1024 lds\n"
    "s_add_u32 m0, s68, 53248\n"
    "s_nop 0\n"
    "buffer_load_dwordx4 v8, s[36:39], s60 offen offset:0 lds\n"
    "buffer_load_dwordx4 v8, s[36:39], s61 offen offset:1024 lds\n"
    "s_add_u32 s36, s36, 24576\n"
    "s_addc_u32 s37, s37, 0\n"
    "s_sub_u32 s38, s38, 24576\n"
    "s_add_u32 m0, s68, 98304\n"
    "s_nop 0\n"
    "buffer_load_dwordx4 v8, s[36:39], s56 offen offset:0 lds\n"
    "buffer_load_dwordx4 v8, s[36:39], s57 offen offset:1024 lds\n"
    "s_add_u32 m0, s68, 100352\n"
    "s_nop 0\n"
    "buffer_load_dwordx4 v8, s[36:39], s58 offen offset:0 lds\n"
    "buffer_load_dwordx4 v8, s[36:39], s59 offen offset:1024 lds\n"
    "s_add_u32 m0, s68, 102400\n"
    "s_nop 0\n"
    "buffer_load_dwordx4 v8, s[36:39], s60 offen offset:0 lds\n"
    "buffer_load_dwordx4 v8, s[36:39], s61 offen offset:1024 lds\n"
    "s_add_u32 m0, s68, 122880\n"
    "s_nop 0\n"
    "buffer_load_dwordx4 v8, s[40:43], s56 offen offset:0 lds\n"
    "buffer_load_dwordx4 v8, s[40:43], s57 offen offset:1024 lds\n"
    "s_add_u32 m0, s68, 124928\n"
    "s_nop 0\n"
    "buffer_load_dwordx4 v8, s[40:43], s58 offen offset:0 lds\n"
    "buffer_load_dwordx4 v8, s[40:43], s59 offen offset:1024 lds\n"
    "s_add_u32 m0, s68, 126976\n"
    "s_nop 0\n"
    "buffer_load_dwordx4 v8, s[40:43], s60 offen offset:0 lds\n"
    "buffer_load_dwordx4 v8, s[40:43], s61 offen offset:1024 lds\n"
    "v_add_u32 v8, 24576, v8\n"
    "s_waitcnt vmcnt(0)\n"
    "s_barrier\n"
    "ds_read_b128 a[192:195], v2 offset:0\n"
    "ds_read_b128 a[196:199], v3 offset:0\n"
    "ds_read_b128 a[200:203], v2 offset:2048\n"
    "ds_read_b128 a[204:207], v3 offset:2048\n"
    "ds_read_b128 a[208:211], v2 offset:4096\n"
    "ds_read_b128 a[212:215], v3 offset:4096\n"
    "s_waitcnt lgkmcnt(0)\n"
    "s_branch L_setmax\n"
    "L_slow0:\n"
    "s_mov_b32 s34, 0\n"
    "L_spin0:\n"
    "s_sleep 4\n"
    "buffer_load_dwordx2 v[62:63], v56, s[52:55], 0 offen sc1\n"
    "s_waitcnt vmcnt(0)\n"
    "v_cmp_ne_u64 vcc, s[80:81], v[62:63]\n"
    "s_nop 1\n"
    "s_cbranch_vccz L_back0\n"
    "s_add_u32 s34, s34, 1\n"
    "s_cmp_lt_u32 s34, 0x40000\n"
    "s_cbranch_scc1 L_spin0\n"
    "s_branch L_back0\n"
    "L_slow1:\n"
    "s_mov_b32 s34, 0\n"
    "L_spin1:\n"
    "s_sleep 4\n"
    "buffer_load_dwordx2 v[62:63], v56, s[52:55], 0 offen sc1\n"
    "s_waitcnt vmcnt(0)\n"
    "v_cmp_ne_u64 vcc, s[80:81], v[62:63]\n"
    "s_nop 1\n"
    "s_cbranch_vccz L_back1\n"
    "s_add_u32 s34, s34, 1\n"
    "s_cmp_lt_u32 s34, 0x40000\n"
    "s_cbranch_scc1 L_spin1\n"
    "s_branch L_back1\n"
    "L_slow2:\n"
    "s_mov_b32 s34, 0\n"
    "L_spin2:\n"
    "s_sleep 4\n"
    "buffer_load_dwordx2 v[62:63], v56, s[52:55], 0 offen sc1\n"
    "s_waitcnt vmcnt(0)\n"
    "v_cmp_ne_u64 vcc, s[80:81], v[62:63]\n"
    "s_nop 1\n"
    "s_cbranch_vccz L_back2\n"
    "s_add_u32 s34, s34, 1\n"
    "s_cmp_lt_u32 s34, 0x40000\n"
    "s_cbranch_scc1 L_spin2\n"
    "s_branch L_back2\n"
    "L_slow3:\n"
    "s_mov_b32 s34, 0\n"
    "L_spin3:\n"
    "s_sleep 4\n"
    "buffer_load_dwordx2 v[62:63], v56, s[52:55], 0 offen sc1\n"
    "s_waitcnt vmcnt(0)\n"
    "v_cmp_ne_u64 vcc, s[80:81], v[62:63]\n"
    "s_nop 1\n"
    "s_cbranch_vccz L_back3\n"
    "s_add_u32 s34, s34, 1\n"
    "s_cmp_lt_u32 s34, 0x40000\n"
    "s_cbranch_scc1 L_spin3\n"
    "s_branch L_back3\n"
    "L_slow4:\n"
    "s_mov_b32 s34, 0\n"
    "L_spin4:\n"
    "s_sleep 4\n"
    "buffer_load_dwordx2 v[62:63], v56, s[52:55], 0 offen sc1\n"
    "s_waitcnt vmcnt(0)\n"
    "v_cmp_ne_u64 vcc, s[80:81], v[62:63]\n"
    "s_nop 1\n"
    "s_cbranch_vccz L_back4\n"
    "s_add_u32 s34, s34, 1\n"
    "s_cmp_lt_u32 s34, 0x40000\n"
    "s_cbranch_scc1 L_spin4\n"
    "s_branch L_back4\n"
    "L_slow5:\n"
    "s_mov_b32 s34, 0\n"
    "L_spin5:\n"
    "s_sleep 4\n"
    "buffer_load_dwordx2 v[62:63], v56, s[52:55], 0 offen sc1\n"
    "s_waitcnt vmcnt(0)\n"
    "v_cmp_ne_u64 vcc, s[80:81], v[62:63]\n"
    "s_nop 1\n"
    "s_cbranch_vccz L_back5\n"
    "s_add_u32 s34, s34, 1\n"
    "s_cmp_lt_u32 s34, 0x40000\n"
    "s_cbranch_scc1 L_spin5\n"
    "s_branch L_back5\n"
    "L_slow6:\n"
    "s_mov_b32 s34, 0\n"
    "L_spin6:\n"
    "s_sleep 4\n"
    "buffer_load_dwordx2 v[62:63], v56, s[52:55], 0 offen sc1\n"
    "s_waitcnt vmcnt(0)\n"
    "v_cmp_ne_u64 vcc, s[80:81], v[62:63]\n"
    "s_nop 1\n"
    "s_cbranch_vccz L_back6\n"
    "s_add_u32 s34, s34, 1\n"
    "s_cmp_lt_u32 s34, 0x40000\n"
    "s_cbranch_scc1 L_spin6\n"
    "s_branch L_back6\n"
    "L_slow7:\n"
    "s_mov_b32 s34, 0\n"
    "L_spin7:\n"
    "s_sleep 4\n"
    "buffer_load_dwordx2 v[62:63], v56, s[52:55], 0 offen sc1\n"
    "s_waitcnt vmcnt(0)\n"
    "v_cmp_ne_u64 vcc, s[80:81], v[62:63]\n"
    "s_nop 1\n"
    "s_cbranch_vccz L_back7\n"
    "s_add_u32 s34, s34, 1\n"
    "s_cmp_lt_u32 s34, 0x40000\n"
    "s_cbranch_scc1 L_spin7\n"
    "s_branch L_back7\n"
    "L_slow8:\n"
    "s_mov_b32 s34, 0\n"
    "L_spin8:\n"
    "s_sleep 4\n"
    "buffer_load_dwordx2 v[62:63], v56, s[52:55], 0 offen sc1\n"
    "s_waitcnt vmcnt(0)\n"
    "v_cmp_ne_u64 vcc, s[80:81], v[62:63]\n"
    "s_nop 1\n"
    "s_cbranch_vccz L_back8\n"
    "s_add_u32 s34, s34, 1\n"
    "s_cmp_lt_u32 s34, 0x40000\n"
    "s_cbranch_scc1 L_spin8\n"
    "s_branch L_back8\n"
    "L_slow9:\n"
    "s_mov_b32 s34, 0\n"
    "L_spin9:\n"
    "s_sleep 4\n"
    "buffer_load_dwordx2 v[62:63], v56, s[52:55], 0 offen sc1\n"
    "s_waitcnt vmcnt(0)\n"
    "v_cmp_ne_u64 vcc, s[80:81], v[62:63]\n"
    "s_nop 1\n"
    "s_cbranch_vccz L_back9\n"
    "s_add_u32 s34, s34, 1\n"
    "s_cmp_lt_u32 s34, 0x40000\n"
    "s_cbranch_scc1 L_spin9\n"
    "s_branch L_back9\n"
    "L_slow10:\n"
    "s_mov_b32 s34, 0\n"
    "L_spin10:\n"
    "s_sleep 4\n"
    "buffer_load_dwordx2 v[62:63], v56, s[52:55], 0 offen sc1\n"
    "s_waitcnt vmcnt(0)\n"
    "v_cmp_ne_u64 vcc, s[80:81], v[62:63]\n"
    "s_nop 1\n"
    "s_cbranch_vccz L_back10\n"
    "s_add_u32 s34, s34, 1\n"
    "s_cmp_lt_u32 s34, 0x40000\n"
    "s_cbranch_scc1 L_spin10\n"
    "s_branch L_back10\n"
    "L_slow11:\n"
    "s_mov_b32 s34, 0\n"
    "L_spin11:\n"
    "s_sleep 4\n"
    "buffer_load_dwordx2 v[62:63], v56, s[52:55], 0 offen sc1\n"
    "s_waitcnt vmcnt(0)\n"
    "v_cmp_ne_u64 vcc, s[80:81], v[62:63]\n"
    "s_nop 1\n"
    "s_cbranch_vccz L_back11\n"
    "s_add_u32 s34, s34, 1\n"
    "s_cmp_lt_u32 s34, 0x40000\n"
    "s_cbranch_scc1 L_spin11\n"
    "s_branch L_back11\n"
    "L_exit:\n"
    :: "s"(__builtin_amdgcn_kernarg_segment_ptr()), "s"(blockIdx.x), "v"(threadIdx.x), "s"((unsigned)(size_t)lds)
    : "v1","v2","v3","v4","v5","v6","v7","v8","v9","v10","v11","v12","v13","v14","v15","v16","v17","v18","v19","v20","v21","v22","v23","v24","v25","v26","v27","v28","v29","v30","v31","v32","v33","v34","v35","v36","v37","v38","v39","v40","v41","v42","v43","v44","v45","v46","v47","v48","v49","v50","v51","v52","v53","v54","v55","v56","v57","v58","v59","v60","v61","v62","v63","v64","v65","v66","v67","v68","v69","v70","v71","v72","v73","v74","v75","v76","v77","v78","v79","v80","v81","v82","v83","v84","v85","v86","v87","v88","v89","v90","v91","v92","v93","v94","v95","v96","v97","v98","v99","v100","v101","v102","v103","v104","v105","v106","v107","v108","v109","v110","v111","v112","v113","v114","v115","v116","v117","v118","v119","v120","v121","v122","v123","v124","v125","v126","v127","v128","v129","v130","v131","v132","v133","v134","v135","v136","v137","v138","v139","v140","v141","v142","v143","v144","v145","v146","v147","v148","v149","v150","v151","v152","v153","v154","v155","v156","v157","v158","v159","v160","v161","v162","v163","v164","v165","v166","v167","v168","v169","v170","v171","v172","v173","v174","v175","v176","v177","v178","v179","v180","v181","v182","v183","v184","v185","v186","v187","v188","v189","v190","v191","v192","v193","v194","v195","v196","v197","v198","v199","v200","v201","v202","v203","v204","v205","v206","v207","v208","v209","v210","v211","v212","v213","v214","v215","v216","v217","v218","v219","v220","v221","v222","v223","v224","v225","v226","v227","v228","v229","v230","v231","v232","v233","v234","v235","v236","v237","v238","v239","v240","v241","v242","v243","v244","v245","v246","v247","v248","v249","v250","v251","v252","v253","v254","v255","a0","a1","a2","a3","a4","a5","a6","a7","a8","a9","a10","a11","a12","a13","a14","a15","a16","a17","a18","a19","a20","a21","a22","a23","a24","a25","a26","a27","a28","a29","a30","a31","a32","a33","a34","a35","a36","a37","a38","a39","a40","a41","a42","a43","a44","a45","a46","a47","a48","a49","a50","a51","a52","a53","a54","a55","a56","a57","a58","a59","a60","a61","a62","a63","a64","a65","a66","a67","a68","a69","a70","a71","a72","a73","a74","a75","a76","a77","a78","a79","a80","a81","a82","a83","a84","a85","a86","a87","a88","a89","a90","a91","a92","a93","a94","a95","a96","a97","a98","a99","a100","a101","a102","a103","a104","a105","a106","a107","a108","a109","a110","a111","a112","a113","a114","a115","a116","a117","a118","a119","a120","a121","a122","a123","a124","a125","a126","a127","a128","a129","a130","a131","a132","a133","a134","a135","a136","a137","a138","a139","a140","a141","a142","a143","a144","a145","a146","a147","a148","a149","a150","a151","a152","a153","a154","a155","a156","a157","a158","a159","a160","a161","a162","a163","a164","a165","a166","a167","a168","a169","a170","a171","a172","a173","a174","a175","a176","a177","a178","a179","a180","a181","a182","a183","a184","a185","a186","a187","a188","a189","a190","a191","a192","a193","a194","a195","a196","a197","a198","a199","a200","a201","a202","a203","a204","a205","a206","a207","a208","a209","a210","a211","a212","a213","a214","a215","a216","a217","a218","a219","a220","a221","a222","a223","a224","a225","a226","a227","a228","a229","a230","a231","a232","a233","a234","a235","a236","a237","a238","a239","a240","a241","a242","a243","a244","a245","a246","a247","a248","a249","a250","a251","a252","a253","a254","a255","s4","s5","s6","s7","s8","s9","s10","s11","s12","s13","s14","s15","s16","s17","s18","s19","s20","s21","s22","s23","s24","s25","s34","s35","s36","s37","s38","s39","s40","s41","s42","s43","s44","s45","s46","s47","s48","s49","s50","s51","s52","s53","s54","s55","s56","s57","s58","s59","s60","s61","s62","s63","s64","s65","s66","s67","s68","s69","s70","s71","s72","s73","s74","s75","s76","s77","s78","s79","s80","s81","s82","s83","s84","s85","s86","s87","s88","s89","s90","s91","s92","s93","s94","s95","s96","s97","s98","s99","vcc","scc","memory");
}

extern "C" void kernel_launch(void* const* d_in, const int* in_sizes, int n_in, void* d_out, int out_size, void* d_ws, size_t ws_size,
                              hipStream_t stream) {
  fused_block_kernel<<<256, 256, 0, stream>>>(d_in[0], d_in[1], d_in[2], d_in[3], d_in[4], d_in[6], d_in[7], d_in[8], d_in[9], d_out, d_ws);
}
```
